```python
import torch

def _softmax(x, axis=-1):
    return torch.softmax(x, dim=axis)
def _gelu(x):
    return torch.nn.functional.gelu(x, approximate="tanh")
def _silu(x):
    return torch.nn.functional.silu(x)
def _sigmoid(x):
    return torch.sigmoid(x)
def _layernorm(x, axis=-1, eps=1e-5):
    m = x.mean(dim=axis, keepdim=True)
    v = x.var(dim=axis, keepdim=True, unbiased=False)
    return (x - m) / torch.sqrt(v + eps)
def _scatter_add(tgt, idx, src, axis=0):
    return tgt.clone().index_add_(axis, idx, src)
def _segment_sum(src, seg, n):
    out = torch.zeros((n,) + src.shape[1:], dtype=src.dtype)
    return out.index_add_(0, seg, src)
def _pad(x, pw):
    flat = tuple(v for pair in reversed(pw) for v in pair)
    return torch.nn.functional.pad(x, flat)

kernel_name = 'standalone__gqa_block_cached__near_ridge'

def setup_inputs():

    _w = (lambda a: (lambda t: t.to(torch.bfloat16) if t.is_floating_point() else t)(torch.as_tensor(a)))
    B, Sq, D = 8, 112, 448
    Hq, Hkv, Sk, Dh = 16, 8, 1024, 256
    assert Hq % Hkv == 0, (Hq, Hkv)
    return {
        "x": _w((lambda *s: torch.randn(*s, dtype=torch.bfloat16) * 0.8)(B, Sq, D)),
        "k_cache": _w((lambda *s: torch.randn(*s, dtype=torch.bfloat16) * 0.8)(B, Hkv, Sk, Dh)),
        "v_cache": _w((lambda *s: torch.randn(*s, dtype=torch.bfloat16) * 0.05)(B, Hkv, Sk, Dh)),
        "wq": _w((lambda *s: torch.randn(*s, dtype=torch.bfloat16) * 0.8)(D, Hq * Dh) * (D ** -0.5)),
        "wo": _w((lambda *s: torch.randn(*s, dtype=torch.bfloat16) * 0.05)(Hq * Dh, D) * ((Hq * Dh) ** -0.5)),
    }

def reference(x, k_cache, v_cache, wq, wo):
    B, Sq, D = x.shape
    Hq, Dh = 16, 256
    Hkv = k_cache.shape[1]
    rep = Hq // Hkv
    q = (lambda x, *d: x.permute(*d))((x @ wq).reshape(B, Sq, Hq, Dh), 0, 2, 1, 3)
    k = (lambda x, n, axis: torch.repeat_interleave(x, n, dim=axis))(k_cache, rep, axis=1)
    v = (lambda x, n, axis: torch.repeat_interleave(x, n, dim=axis))(v_cache, rep, axis=1)
    scores = torch.einsum("bhqd,bhkd->bhqk", q, k) * (Dh ** -0.5)
    p = _softmax(scores, axis=-1)
    o = torch.einsum("bhqk,bhkd->bhqd", p, v)
    return (lambda x, *d: x.permute(*d))(o, 0, 2, 1, 3).reshape(B, Sq, Hq * Dh) @ wo
```

```cpp
#include <hip/hip_runtime.h>
#include <hip/hip_bf16.h>

typedef unsigned short u16;
typedef __attribute__((ext_vector_type(8))) short bf16x8;
typedef __attribute__((ext_vector_type(4))) short s16x4;
typedef __attribute__((ext_vector_type(16))) float f32x16;
typedef __attribute__((ext_vector_type(4))) float f32x4;
typedef __attribute__((ext_vector_type(2))) float f32x2;
typedef __attribute__((ext_vector_type(2))) __bf16 bf16x2v;
typedef __attribute__((ext_vector_type(2))) unsigned u32x2;
typedef __attribute__((ext_vector_type(4))) unsigned u32x4;
#define DI __device__ __forceinline__
#define LDSP(p) ((__attribute__((address_space(3))) unsigned*)(p))

constexpr int NB = 8, SQ = 112, DM = 448, HQ = 16, HKV = 8, SK = 1024, DH = 256;
constexpr int NROW = NB * SQ;
constexpr int NQ = HQ * DH;

DI unsigned cvtpk(float lo, float hi) {
  f32x2 v = {lo, hi};
  bf16x2v b = __builtin_convertvector(v, bf16x2v);
  return __builtin_bit_cast(unsigned, b);
}
DI void glds16(const void* g, void* l) {
  __builtin_amdgcn_global_load_lds((const unsigned*)g, LDSP(l), 16, 0, 0);
}
#define WAITV(n) asm volatile("s_waitcnt vmcnt(" #n ")" ::: "memory")
#define WAITL0() asm volatile("s_waitcnt lgkmcnt(0)" ::: "memory")

DI s16x4 trread(unsigned addr) {
  s16x4 r;
  asm volatile("ds_read_b64_tr_b16 %0, %1" : "=v"(r) : "v"(addr) : "memory");
  return r;
}
template <int OFF> DI s16x4 trread_o(unsigned addr) {
  s16x4 r;
  asm volatile("ds_read_b64_tr_b16 %0, %1 offset:%2" : "=v"(r) : "v"(addr), "i"(OFF) : "memory");
  return r;
}
DI bf16x8 cat8(s16x4 a, s16x4 b) { return __builtin_shufflevector(a, b, 0, 1, 2, 3, 4, 5, 6, 7); }

template <int RB, int NCH>
DI void wave_gemm(const u16* __restrict__ act, int lda, const int (&arow)[RB], const u16* __restrict__ W, int ldw,
                  char* wl, f32x16 (&acc)[RB][2], int lane) {
  const int r = lane & 31, h = lane >> 5;
  const int g = lane >> 4, q = (lane >> 2) & 3, pp = lane & 3;
  const unsigned ldsb = (unsigned)(size_t)wl;
  const unsigned trb = ldsb + (8 * (g >> 1) + q) * 128 + ((32 * (g & 1) + 8 * pp) ^ ((q >> 1) << 6));
  const int drow = lane >> 3;
  const int dchk = (lane & 7) ^ (((drow >> 1) & 1) << 2);
  const u16* wsrc = W + (size_t)drow * ldw + dchk * 8;
  auto issue = [&](int c, int buf) {
#pragma unroll
    for (int p = 0; p < 8; ++p) glds16(wsrc + (size_t)(64 * c + 8 * p) * ldw, wl + buf * 8192 + p * 1024);
  };
  issue(0, 0);
#pragma unroll
  for (int c = 0; c < NCH; ++c) {
    const int buf = c & 1;
    bf16x8 bfr[4][RB];
#pragma unroll
    for (int s = 0; s < 4; ++s)
#pragma unroll
      for (int rb = 0; rb < RB; ++rb)
        bfr[s][rb] = *(const bf16x8*)(act + (size_t)arow[rb] * lda + 64 * c + 16 * s + 8 * h);
    WAITV(0);
    if (c + 1 < NCH) issue(c + 1, buf ^ 1);
#pragma unroll
    for (int s = 0; s < 4; ++s) {
      s16x4 a00 = trread(trb + buf * 8192 + s * 2048 + 0);
      s16x4 a01 = trread(trb + buf * 8192 + s * 2048 + 512);
      s16x4 a10 = trread((trb + buf * 8192 + s * 2048 + 0) ^ 64);
      s16x4 a11 = trread((trb + buf * 8192 + s * 2048 + 512) ^ 64);
      asm volatile("s_waitcnt lgkmcnt(0)" : "+v"(a00), "+v"(a01), "+v"(a10), "+v"(a11)::"memory");
      bf16x8 A0 = cat8(a00, a01), A1 = cat8(a10, a11);
#pragma unroll
      for (int rb = 0; rb < RB; ++rb) {
        acc[rb][0] = __builtin_amdgcn_mfma_f32_32x32x16_bf16(A0, bfr[s][rb], acc[rb][0], 0, 0, 0);
        acc[rb][1] = __builtin_amdgcn_mfma_f32_32x32x16_bf16(A1, bfr[s][rb], acc[rb][1], 0, 0, 0);
      }
    }
  }
  (void)r;
}

__global__ __launch_bounds__(256) void qproj_kernel(const u16* __restrict__ x, const u16* __restrict__ wq,
                                                    u16* __restrict__ qout) {
  extern __shared__ __attribute__((aligned(16))) char lds[];
  const int tid = threadIdx.x, lane = tid & 63;
  const int w = __builtin_amdgcn_readfirstlane(tid >> 6);
  const int r = lane & 31, h = lane >> 5;
  const int rt = blockIdx.x % 14, head = blockIdx.x / 14;
  const int col0 = head * 256 + 64 * w;
  f32x16 acc[2][2];
#pragma unroll
  for (int a = 0; a < 2; ++a)
#pragma unroll
    for (int b = 0; b < 2; ++b)
#pragma unroll
      for (int i = 0; i < 16; ++i) acc[a][b][i] = 0.f;
  const int arow[2] = {rt * 64 + r, rt * 64 + 32 + r};
  wave_gemm<2, 7>(x, DM, arow, wq + col0, NQ, lds + w * 16384, acc, lane);
#pragma unroll
  for (int rb = 0; rb < 2; ++rb)
#pragma unroll
    for (int cb = 0; cb < 2; ++cb)
#pragma unroll
      for (int g = 0; g < 4; ++g) {
        u32x2 v = {cvtpk(acc[rb][cb][4 * g + 0], acc[rb][cb][4 * g + 1]),
                   cvtpk(acc[rb][cb][4 * g + 2], acc[rb][cb][4 * g + 3])};
        *(u32x2*)(qout + (size_t)arow[rb] * NQ + col0 + 32 * cb + 8 * g + 4 * h) = v;
      }
}

__global__ __launch_bounds__(256) void oproj_kernel(const u16* __restrict__ o, const u16* __restrict__ wo,
                                                    u16* __restrict__ out) {
  extern __shared__ __attribute__((aligned(16))) char lds[];
  const int tid = threadIdx.x, lane = tid & 63;
  const int w = __builtin_amdgcn_readfirstlane(tid >> 6);
  const int r = lane & 31, h = lane >> 5;
  const int rt = blockIdx.x % 28, ct = blockIdx.x / 28;
  const int col0 = ct * 64;
  f32x16 acc[1][2];
#pragma unroll
  for (int b = 0; b < 2; ++b)
#pragma unroll
    for (int i = 0; i < 16; ++i) acc[0][b][i] = 0.f;
  const int arow[1] = {rt * 32 + r};
  wave_gemm<1, 16>(o + 1024 * w, NQ, arow, wo + (size_t)(1024 * w) * DM + col0, DM, lds + w * 16384, acc, lane);
  __syncthreads();
  float* red = (float*)lds;
#pragma unroll
  for (int cb = 0; cb < 2; ++cb)
#pragma unroll
    for (int g = 0; g < 4; ++g) {
      f32x4 v = {acc[0][cb][4 * g], acc[0][cb][4 * g + 1], acc[0][cb][4 * g + 2], acc[0][cb][4 * g + 3]};
      *(f32x4*)(red + ((w * 8 + cb * 4 + g) * 64 + lane) * 4) = v;
    }
  __syncthreads();
#pragma unroll
  for (int j = 0; j < 2; ++j) {
    const int cg = 2 * w + j, cb = cg >> 2, g = cg & 3;
    f32x4 s = {0.f, 0.f, 0.f, 0.f};
#pragma unroll
    for (int ww = 0; ww < 4; ++ww) {
      f32x4 v = *(const f32x4*)(red + ((ww * 8 + cg) * 64 + lane) * 4);
      s += v;
    }
    u32x2 v = {cvtpk(s[0], s[1]), cvtpk(s[2], s[3])};
    *(u32x2*)(out + (size_t)arow[0] * DM + col0 + 32 * cb + 8 * g + 4 * h) = v;
  }
}

constexpr float SCL = 0.0625f * 1.4426950408889634f;
constexpr float THR = 8.0f;

typedef __attribute__((address_space(3))) void* lds_vp;
struct DmaCtx {
  __amdgpu_buffer_rsrc_t rk, rv;
  int koff[4], voff[4];
  char* dst;
};
template <int U>
DI void issue_unit(const DmaCtx& c) {
  constexpr int t = U >> 2, kind = U & 3, slot = U % 5;
  if constexpr (kind < 2) {
#pragma unroll
    for (int pi = 0; pi < 4; ++pi)
      __builtin_amdgcn_raw_ptr_buffer_load_lds(c.rk, (lds_vp)(c.dst + slot * 8192 + pi * 1024), 16, c.koff[pi],
                                               32 * t * 512 + kind * 256, 0, 0);
  } else {
    constexpr int ks = kind - 2;
#pragma unroll
    for (int pi = 0; pi < 4; ++pi)
      __builtin_amdgcn_raw_ptr_buffer_load_lds(c.rv, (lds_vp)(c.dst + slot * 8192 + pi * 1024), 16, c.voff[pi],
                                               (32 * t + 16 * ks) * 512, 0, 0);
  }
}
template <int U> DI void wait_unit_bar() {
  constexpr int after = (U + 3 > 31 ? 31 : U + 3) - U;
  if constexpr (after == 3) WAITV(12);
  else if constexpr (after == 2) WAITV(8);
  else if constexpr (after == 1) WAITV(4);
  else WAITV(0);
  WAITL0();
  __builtin_amdgcn_s_barrier();
}

struct AttnState {
  bf16x8 qf[16];
  f32x16 oacc[8];
  float m, l;
};

template <int T, int DHF>
DI void qk_half(AttnState& st, f32x16& s, const DmaCtx& c, char* ring, unsigned kaddr) {
  constexpr int U = 4 * T + DHF, slot = U % 5;
  wait_unit_bar<U>();
  if constexpr (U + 4 < 32) issue_unit<U + 4>(c);
#pragma unroll
  for (int ss = 0; ss < 8; ++ss) {
    bf16x8 kf = *(const bf16x8*)(ring + slot * 8192 + (kaddr ^ (ss * 32)));
    s = __builtin_amdgcn_mfma_f32_32x32x16_bf16(kf, st.qf[DHF * 8 + ss], s, 0, 0, 0);
  }
}
template <int T, int KS, int HF>
DI void pv_quarter(AttnState& st, const bf16x8& pf, const unsigned (&vaddr)[4]) {
  constexpr int U = 4 * T + 2 + KS, slot = U % 5;
  constexpr int OFF = slot * 8192 + HF * 256;
  s16x4 v0[4], v1[4];
  v0[0] = trread_o<OFF>(vaddr[0]); v1[0] = trread_o<OFF + 4096>(vaddr[0]);
  v0[1] = trread_o<OFF>(vaddr[1]); v1[1] = trread_o<OFF + 4096>(vaddr[1]);
  v0[2] = trread_o<OFF>(vaddr[2]); v1[2] = trread_o<OFF + 4096>(vaddr[2]);
  v0[3] = trread_o<OFF>(vaddr[3]); v1[3] = trread_o<OFF + 4096>(vaddr[3]);
  asm volatile("s_waitcnt lgkmcnt(0)"
               : "+v"(v0[0]), "+v"(v0[1]), "+v"(v0[2]), "+v"(v0[3]), "+v"(v1[0]), "+v"(v1[1]), "+v"(v1[2]),
                 "+v"(v1[3])::"memory");
#pragma unroll
  for (int d = 0; d < 4; ++d) {
    bf16x8 vf = cat8(v0[d], v1[d]);
    st.oacc[HF * 4 + d] = __builtin_amdgcn_mfma_f32_32x32x16_bf16(vf, pf, st.oacc[HF * 4 + d], 0, 0, 0);
  }
}
template <int T, int KS>
DI void pv_half(AttnState& st, const bf16x8& pf, const DmaCtx& c, const unsigned (&vaddr)[4]) {
  constexpr int U = 4 * T + 2 + KS;
  wait_unit_bar<U>();
  if constexpr (U + 4 < 32) issue_unit<U + 4>(c);
  pv_quarter<T, KS, 0>(st, pf, vaddr);
  pv_quarter<T, KS, 1>(st, pf, vaddr);
}

template <int T>
DI void attn_tile(AttnState& st, const DmaCtx& c, char* ring, unsigned kaddr, const unsigned (&vaddr)[4]) {
  f32x16 s;
#pragma unroll
  for (int i = 0; i < 16; ++i) s[i] = 0.f;
  qk_half<T, 0>(st, s, c, ring, kaddr);
  qk_half<T, 1>(st, s, c, ring, kaddr);
  bf16x8 pf[2];
  {
    float a = s[0];
#pragma unroll
    for (int i = 1; i < 16; ++i) a = fmaxf(a, s[i]);
    auto rr = __builtin_amdgcn_permlane32_swap(__float_as_uint(a), __float_as_uint(a), false, false);
    const float mx = fmaxf(__uint_as_float(rr[0]), __uint_as_float(rr[1])) * SCL;
    const bool grew = (mx - st.m > THR);
    if (T == 0 || __any((int)grew)) {
      const float mn = fmaxf(st.m, mx);
      const float alpha = __builtin_amdgcn_exp2f(st.m - mn);
      st.m = mn;
      st.l *= alpha;
      if (T != 0) {
#pragma unroll
        for (int db = 0; db < 8; ++db)
#pragma unroll
          for (int i = 0; i < 16; ++i) st.oacc[db][i] *= alpha;
      }
    }
    const float nm = -st.m;
    float ps = 0.f;
#pragma unroll
    for (int i = 0; i < 16; ++i) {
      s[i] = __builtin_amdgcn_exp2f(fmaf(s[i], SCL, nm));
      ps += s[i];
    }
    st.l += ps;
#pragma unroll
    for (int ks = 0; ks < 2; ++ks) {
      u32x4 wv = {cvtpk(s[8 * ks + 0], s[8 * ks + 1]), cvtpk(s[8 * ks + 2], s[8 * ks + 3]),
                  cvtpk(s[8 * ks + 4], s[8 * ks + 5]), cvtpk(s[8 * ks + 6], s[8 * ks + 7])};
      pf[ks] = __builtin_bit_cast(bf16x8, wv);
    }
  }
  pv_half<T, 0>(st, pf[0], c, vaddr);
  pv_half<T, 1>(st, pf[1], c, vaddr);
}

__global__ __launch_bounds__(512, 2) void attn_kernel(const u16* __restrict__ q, const u16* __restrict__ kc,
                                                      const u16* __restrict__ vc, u16* __restrict__ o) {
  extern __shared__ __attribute__((aligned(16))) char lds[];
  const int tid = threadIdx.x, lane = tid & 63;
  const int w = __builtin_amdgcn_readfirstlane(tid >> 6);
  const int rb = w >> 2, kq = w & 3;
  const int r = lane & 31, h = lane >> 5;
  const int bid = blockIdx.x;
  const int xcd = bid & 7, j = bid >> 3;
  const int grp = (j >> 2) * 8 + xcd, sub = j & 3;
  const int b = grp >> 3, kvh = grp & 7;
  const int head = kvh * 2 + (sub >> 1), rhalf = sub & 1;
  const int row0 = b * SQ + rhalf * 56;

  const u16* Kw = kc + ((size_t)(b * HKV + kvh) * SK + 256 * kq) * DH;
  const u16* Vw = vc + ((size_t)(b * HKV + kvh) * SK + 256 * kq) * DH;
  char* ring = lds + kq * 40960;

  AttnState st;
  {
    int rr = 32 * rb + r;
    rr = rr > 55 ? 55 : rr;
    const u16* qrow = q + (size_t)(row0 + rr) * NQ + head * DH + 8 * h;
#pragma unroll
    for (int s = 0; s < 16; ++s) st.qf[s] = *(const bf16x8*)(qrow + 16 * s);
    st.m = -__builtin_inff();
    st.l = 0.f;
#pragma unroll
    for (int db = 0; db < 8; ++db)
#pragma unroll
      for (int i = 0; i < 16; ++i) st.oacc[db][i] = 0.f;
  }
#pragma unroll
  for (int s = 0; s < 16; ++s) asm volatile("" : "+v"(st.qf[s]));
  WAITV(0);

  DmaCtx c;
  c.rk = __builtin_amdgcn_make_buffer_rsrc((void*)Kw, 0, 256 * DH * 2, 0x00020000);
  c.rv = __builtin_amdgcn_make_buffer_rsrc((void*)Vw, 0, 256 * DH * 2, 0x00020000);
  c.dst = ring + rb * 4096;
#pragma unroll
  for (int pi = 0; pi < 4; ++pi) {
    const int krow = 16 * rb + 4 * pi + (lane >> 4);
    c.koff[pi] = krow * 512 + (((lane & 15) ^ (krow & 15)) * 16);
    const int vrow = 8 * rb + 2 * pi + (lane >> 5);
    c.voff[pi] = vrow * 512 + (((lane & 31) ^ ((vrow & 3) << 2)) * 16);
  }
  issue_unit<0>(c);
  issue_unit<1>(c);
  issue_unit<2>(c);
  issue_unit<3>(c);

  const unsigned kaddr = r * 256 + ((h ^ (r & 15)) * 16);
  const int g = lane >> 4, qq = (lane >> 2) & 3, pp = lane & 3;
  unsigned vaddr[4];
#pragma unroll
  for (int d = 0; d < 4; ++d)
    vaddr[d] = (unsigned)(size_t)ring + (4 * (g >> 1) + qq) * 512 + ((64 * d + 32 * (g & 1) + 8 * pp) ^ (qq << 6));

  attn_tile<0>(st, c, ring, kaddr, vaddr);
  attn_tile<1>(st, c, ring, kaddr, vaddr);
  attn_tile<2>(st, c, ring, kaddr, vaddr);
  attn_tile<3>(st, c, ring, kaddr, vaddr);
  attn_tile<4>(st, c, ring, kaddr, vaddr);
  attn_tile<5>(st, c, ring, kaddr, vaddr);
  attn_tile<6>(st, c, ring, kaddr, vaddr);
  attn_tile<7>(st, c, ring, kaddr, vaddr);

  __syncthreads();
  float* ml = (float*)lds;
  {
    auto rr = __builtin_amdgcn_permlane32_swap(__float_as_uint(st.l), __float_as_uint(st.l), false, false);
    const float lt = __uint_as_float(rr[0]) + __uint_as_float(rr[1]);
    if (h == 0) {
      ml[(w * 32 + r) * 2 + 0] = st.m;
      ml[(w * 32 + r) * 2 + 1] = lt;
    }
  }
  __syncthreads();
  float f;
  {
    float mm[4], ll[4];
#pragma unroll
    for (int k = 0; k < 4; ++k) {
      mm[k] = ml[((rb * 4 + k) * 32 + r) * 2 + 0];
      ll[k] = ml[((rb * 4 + k) * 32 + r) * 2 + 1];
    }
    const float M = fmaxf(fmaxf(mm[0], mm[1]), fmaxf(mm[2], mm[3]));
    float L = 0.f;
#pragma unroll
    for (int k = 0; k < 4; ++k) L += __builtin_amdgcn_exp2f(mm[k] - M) * ll[k];
    f = __builtin_amdgcn_exp2f(st.m - M) / L;
  }
  float* xch = (float*)(lds + 4096);
#pragma unroll
  for (int rnd = 0; rnd < 2; ++rnd) {
#pragma unroll
    for (int db = 0; db < 8; ++db) {
      if ((db & 1) != rnd) continue;
      const int owner = db >> 1;
      if (owner == kq) continue;
      const int dslot = owner < kq ? owner : owner - 1;
#pragma unroll
      for (int gg = 0; gg < 4; ++gg) {
        f32x4 v = {st.oacc[db][4 * gg] * f, st.oacc[db][4 * gg + 1] * f, st.oacc[db][4 * gg + 2] * f,
                   st.oacc[db][4 * gg + 3] * f};
        *(f32x4*)(xch + (((w * 3 + dslot) * 4 + gg) * 64 + lane) * 4) = v;
      }
    }
    __syncthreads();
#pragma unroll
    for (int db = 0; db < 8; ++db) {
      if ((db & 1) != rnd) continue;
      if ((db >> 1) != kq) continue;
#pragma unroll
      for (int gg = 0; gg < 4; ++gg) {
        f32x4 sum = {st.oacc[db][4 * gg] * f, st.oacc[db][4 * gg + 1] * f, st.oacc[db][4 * gg + 2] * f,
                     st.oacc[db][4 * gg + 3] * f};
#pragma unroll
        for (int sk = 0; sk < 4; ++sk) {
          if (sk == kq) continue;
          const int dslot = kq < sk ? kq : kq - 1;
          f32x4 v = *(const f32x4*)(xch + ((((rb * 4 + sk) * 3 + dslot) * 4 + gg) * 64 + lane) * 4);
          sum += v;
        }
        const int rr = 32 * rb + r;
        if (rr < 56) {
          u32x2 v = {cvtpk(sum[0], sum[1]), cvtpk(sum[2], sum[3])};
          *(u32x2*)(o + (size_t)(row0 + rr) * NQ + head * DH + 32 * db + 8 * gg + 4 * h) = v;
        }
      }
    }
    __syncthreads();
  }
}

extern "C" void kernel_launch(void* const* d_in, const int* in_sizes, int n_in, void* d_out, int out_size,
                              void* d_ws, size_t ws_size, hipStream_t stream) {
  const u16* x = (const u16*)d_in[0];
  const u16* kc = (const u16*)d_in[1];
  const u16* vc = (const u16*)d_in[2];
  const u16* wq = (const u16*)d_in[3];
  const u16* wo = (const u16*)d_in[4];
  u16* out = (u16*)d_out;
  u16* qws = (u16*)d_ws;
  u16* ows = (u16*)((char*)d_ws + (size_t)NROW * NQ * 2);
  (void)hipFuncSetAttribute((const void*)attn_kernel, hipFuncAttributeMaxDynamicSharedMemorySize, 163840);
  hipLaunchKernelGGL(qproj_kernel, dim3(14 * 16), dim3(256), 65536, stream, x, wq, qws);
  hipLaunchKernelGGL(attn_kernel, dim3(256), dim3(512), 163840, stream, (const u16*)qws, kc, vc, ows);
  hipLaunchKernelGGL(oproj_kernel, dim3(28 * 7), dim3(256), 65536, stream, (const u16*)ows, wo, out);
}
```

```cpp
#include <hip/hip_runtime.h>
#include <hip/hip_bf16.h>
#include <utility>

typedef unsigned short u16;
typedef __attribute__((ext_vector_type(8))) short bf16x8;
typedef __attribute__((ext_vector_type(4))) short s16x4;
typedef __attribute__((ext_vector_type(16))) float f32x16;
typedef __attribute__((ext_vector_type(4))) float f32x4;
typedef __attribute__((ext_vector_type(2))) float f32x2;
typedef __attribute__((ext_vector_type(2))) __bf16 bf16x2v;
typedef __attribute__((ext_vector_type(2))) unsigned u32x2;
typedef __attribute__((ext_vector_type(4))) unsigned u32x4;
#define DI __device__ __forceinline__
#define LDSP(p) ((__attribute__((address_space(3))) unsigned*)(p))

constexpr int NB = 8, SQ = 112, DM = 448, HQ = 16, HKV = 8, SK = 1024, DH = 256;
constexpr int NROW = NB * SQ;
constexpr int NQ = HQ * DH;

DI unsigned cvtpk(float lo, float hi) {
  f32x2 v = {lo, hi};
  bf16x2v b = __builtin_convertvector(v, bf16x2v);
  return __builtin_bit_cast(unsigned, b);
}
DI void glds16(const void* g, void* l) {
  __builtin_amdgcn_global_load_lds((const unsigned*)g, LDSP(l), 16, 0, 0);
}
#define WAITV(n) asm volatile("s_waitcnt vmcnt(" #n ")" ::: "memory")
#define WAITL0() asm volatile("s_waitcnt lgkmcnt(0)" ::: "memory")

DI s16x4 trread(unsigned addr) {
  s16x4 r;
  asm volatile("ds_read_b64_tr_b16 %0, %1" : "=v"(r) : "v"(addr) : "memory");
  return r;
}
template <int OFF> DI s16x4 trread_o(unsigned addr) {
  s16x4 r;
  asm volatile("ds_read_b64_tr_b16 %0, %1 offset:%2" : "=v"(r) : "v"(addr), "i"(OFF) : "memory");
  return r;
}
DI bf16x8 cat8(s16x4 a, s16x4 b) { return __builtin_shufflevector(a, b, 0, 1, 2, 3, 4, 5, 6, 7); }

template <int RB, int NCH>
DI void wave_gemm(const u16* __restrict__ act, int lda, const int (&arow)[RB], const u16* __restrict__ W, int ldw,
                  char* wl, f32x16 (&acc)[RB][2], int lane) {
  const int r = lane & 31, h = lane >> 5;
  const int g = lane >> 4, q = (lane >> 2) & 3, pp = lane & 3;
  const unsigned ldsb = (unsigned)(size_t)wl;
  const unsigned trb = ldsb + (8 * (g >> 1) + q) * 128 + ((32 * (g & 1) + 8 * pp) ^ ((q >> 1) << 6));
  const int drow = lane >> 3;
  const int dchk = (lane & 7) ^ (((drow >> 1) & 1) << 2);
  const u16* wsrc = W + (size_t)drow * ldw + dchk * 8;
  auto issue = [&](int c, int buf) {
#pragma unroll
    for (int p = 0; p < 8; ++p) glds16(wsrc + (size_t)(64 * c + 8 * p) * ldw, wl + buf * 8192 + p * 1024);
  };
  issue(0, 0);
#pragma unroll
  for (int c = 0; c < NCH; ++c) {
    const int buf = c & 1;
    bf16x8 bfr[4][RB];
#pragma unroll
    for (int s = 0; s < 4; ++s)
#pragma unroll
      for (int rb = 0; rb < RB; ++rb)
        bfr[s][rb] = *(const bf16x8*)(act + (size_t)arow[rb] * lda + 64 * c + 16 * s + 8 * h);
    WAITV(0);
    if (c + 1 < NCH) issue(c + 1, buf ^ 1);
#pragma unroll
    for (int s = 0; s < 4; ++s) {
      s16x4 a00 = trread(trb + buf * 8192 + s * 2048 + 0);
      s16x4 a01 = trread(trb + buf * 8192 + s * 2048 + 512);
      s16x4 a10 = trread((trb + buf * 8192 + s * 2048 + 0) ^ 64);
      s16x4 a11 = trread((trb + buf * 8192 + s * 2048 + 512) ^ 64);
      asm volatile("s_waitcnt lgkmcnt(0)" : "+v"(a00), "+v"(a01), "+v"(a10), "+v"(a11)::"memory");
      bf16x8 A0 = cat8(a00, a01), A1 = cat8(a10, a11);
#pragma unroll
      for (int rb = 0; rb < RB; ++rb) {
        acc[rb][0] = __builtin_amdgcn_mfma_f32_32x32x16_bf16(A0, bfr[s][rb], acc[rb][0], 0, 0, 0);
        acc[rb][1] = __builtin_amdgcn_mfma_f32_32x32x16_bf16(A1, bfr[s][rb], acc[rb][1], 0, 0, 0);
      }
    }
  }
  (void)r;
}

__global__ __launch_bounds__(256) void qproj_kernel(const u16* __restrict__ x, const u16* __restrict__ wq,
                                                    u16* __restrict__ qout) {
  extern __shared__ __attribute__((aligned(16))) char lds[];
  const int tid = threadIdx.x, lane = tid & 63;
  const int w = __builtin_amdgcn_readfirstlane(tid >> 6);
  const int r = lane & 31, h = lane >> 5;
  const int rt = blockIdx.x % 14, head = blockIdx.x / 14;
  const int col0 = head * 256 + 64 * w;
  f32x16 acc[2][2];
#pragma unroll
  for (int a = 0; a < 2; ++a)
#pragma unroll
    for (int b = 0; b < 2; ++b)
#pragma unroll
      for (int i = 0; i < 16; ++i) acc[a][b][i] = 0.f;
  const int arow[2] = {rt * 64 + r, rt * 64 + 32 + r};
  wave_gemm<2, 7>(x, DM, arow, wq + col0, NQ, lds + w * 16384, acc, lane);
#pragma unroll
  for (int rb = 0; rb < 2; ++rb)
#pragma unroll
    for (int cb = 0; cb < 2; ++cb)
#pragma unroll
      for (int g = 0; g < 4; ++g) {
        u32x2 v = {cvtpk(acc[rb][cb][4 * g + 0], acc[rb][cb][4 * g + 1]),
                   cvtpk(acc[rb][cb][4 * g + 2], acc[rb][cb][4 * g + 3])};
        *(u32x2*)(qout + (size_t)arow[rb] * NQ + col0 + 32 * cb + 8 * g + 4 * h) = v;
      }
}

__global__ __launch_bounds__(256) void oproj_kernel(const u16* __restrict__ o, const u16* __restrict__ wo,
                                                    u16* __restrict__ out) {
  extern __shared__ __attribute__((aligned(16))) char lds[];
  const int tid = threadIdx.x, lane = tid & 63;
  const int w = __builtin_amdgcn_readfirstlane(tid >> 6);
  const int r = lane & 31, h = lane >> 5;
  const int rt = blockIdx.x % 28, ct = blockIdx.x / 28;
  const int col0 = ct * 64;
  f32x16 acc[1][2];
#pragma unroll
  for (int b = 0; b < 2; ++b)
#pragma unroll
    for (int i = 0; i < 16; ++i) acc[0][b][i] = 0.f;
  const int arow[1] = {rt * 32 + r};
  wave_gemm<1, 16>(o + 1024 * w, NQ, arow, wo + (size_t)(1024 * w) * DM + col0, DM, lds + w * 16384, acc, lane);
  __syncthreads();
  float* red = (float*)lds;
#pragma unroll
  for (int cb = 0; cb < 2; ++cb)
#pragma unroll
    for (int g = 0; g < 4; ++g) {
      f32x4 v = {acc[0][cb][4 * g], acc[0][cb][4 * g + 1], acc[0][cb][4 * g + 2], acc[0][cb][4 * g + 3]};
      *(f32x4*)(red + ((w * 8 + cb * 4 + g) * 64 + lane) * 4) = v;
    }
  __syncthreads();
#pragma unroll
  for (int j = 0; j < 2; ++j) {
    const int cg = 2 * w + j, cb = cg >> 2, g = cg & 3;
    f32x4 s = {0.f, 0.f, 0.f, 0.f};
#pragma unroll
    for (int ww = 0; ww < 4; ++ww) {
      f32x4 v = *(const f32x4*)(red + ((ww * 8 + cg) * 64 + lane) * 4);
      s += v;
    }
    u32x2 v = {cvtpk(s[0], s[1]), cvtpk(s[2], s[3])};
    *(u32x2*)(out + (size_t)arow[0] * DM + col0 + 32 * cb + 8 * g + 4 * h) = v;
  }
}

constexpr float SCL = 0.0625f * 1.4426950408889634f;
constexpr float THR = 8.0f;

typedef __attribute__((address_space(3))) void* lds_vp;
struct DmaCtx {
  __amdgpu_buffer_rsrc_t rk, rv;
  int koff[4], voff[4];
  char* dst;
};
template <int U>
DI void issue_unit(const DmaCtx& c) {
  constexpr int t = U >> 2, kind = U & 3, slot = U % 5;
  if constexpr (kind < 2) {
#pragma unroll
    for (int pi = 0; pi < 4; ++pi)
      __builtin_amdgcn_raw_ptr_buffer_load_lds(c.rk, (lds_vp)(c.dst + slot * 8192 + pi * 1024), 16, c.koff[pi],
                                               32 * t * 512 + kind * 256, 0, 0);
  } else {
    constexpr int ks = kind - 2;
#pragma unroll
    for (int pi = 0; pi < 4; ++pi)
      __builtin_amdgcn_raw_ptr_buffer_load_lds(c.rv, (lds_vp)(c.dst + slot * 8192 + pi * 1024), 16, c.voff[pi],
                                               (32 * t + 16 * ks) * 512, 0, 0);
  }
}
template <int U> DI void wait_unit_bar() {
  constexpr int after = (U + 3 > 31 ? 31 : U + 3) - U;
  if constexpr (after == 3) WAITV(12);
  else if constexpr (after == 2) WAITV(8);
  else if constexpr (after == 1) WAITV(4);
  else WAITV(0);
  WAITL0();
  __builtin_amdgcn_s_barrier();
}

struct AttnState {
  bf16x8 qf[16];
  f32x16 oacc[8];
  float m, l;
};

template <int T, int DHF>
DI void qk_half(AttnState& st, f32x16& s, const DmaCtx& c, char* ring, unsigned kaddr) {
  constexpr int U = 4 * T + DHF, slot = U % 5;
  wait_unit_bar<U>();
  if constexpr (U + 4 < 32) issue_unit<U + 4>(c);
#pragma unroll
  for (int ss = 0; ss < 8; ++ss) {
    bf16x8 kf = *(const bf16x8*)(ring + slot * 8192 + (kaddr ^ (ss * 32)));
    s = __builtin_amdgcn_mfma_f32_32x32x16_bf16(kf, st.qf[DHF * 8 + ss], s, 0, 0, 0);
  }
}
template <int T, int KS, int HF>
DI void pv_quarter(AttnState& st, const bf16x8& pf, const unsigned (&vaddr)[4]) {
  constexpr int U = 4 * T + 2 + KS, slot = U % 5;
  constexpr int OFF = slot * 8192 + HF * 256;
  s16x4 v0[4], v1[4];
  v0[0] = trread_o<OFF>(vaddr[0]); v1[0] = trread_o<OFF + 4096>(vaddr[0]);
  v0[1] = trread_o<OFF>(vaddr[1]); v1[1] = trread_o<OFF + 4096>(vaddr[1]);
  v0[2] = trread_o<OFF>(vaddr[2]); v1[2] = trread_o<OFF + 4096>(vaddr[2]);
  v0[3] = trread_o<OFF>(vaddr[3]); v1[3] = trread_o<OFF + 4096>(vaddr[3]);
  asm volatile("s_waitcnt lgkmcnt(0)"
               : "+v"(v0[0]), "+v"(v0[1]), "+v"(v0[2]), "+v"(v0[3]), "+v"(v1[0]), "+v"(v1[1]), "+v"(v1[2]),
                 "+v"(v1[3])::"memory");
#pragma unroll
  for (int d = 0; d < 4; ++d) {
    bf16x8 vf = cat8(v0[d], v1[d]);
    st.oacc[HF * 4 + d] = __builtin_amdgcn_mfma_f32_32x32x16_bf16(vf, pf, st.oacc[HF * 4 + d], 0, 0, 0);
  }
}
template <int T, int KS>
DI void pv_half(AttnState& st, const bf16x8& pf, const DmaCtx& c, const unsigned (&vaddr)[4]) {
  constexpr int U = 4 * T + 2 + KS;
  wait_unit_bar<U>();
  if constexpr (U + 4 < 32) issue_unit<U + 4>(c);
  pv_quarter<T, KS, 0>(st, pf, vaddr);
  pv_quarter<T, KS, 1>(st, pf, vaddr);
}

template <int T>
DI void attn_tile(AttnState& st, const DmaCtx& c, char* ring, unsigned kaddr, const unsigned (&vaddr)[4]) {
  f32x16 s;
#pragma unroll
  for (int i = 0; i < 16; ++i) s[i] = 0.f;
  qk_half<T, 0>(st, s, c, ring, kaddr);
  qk_half<T, 1>(st, s, c, ring, kaddr);
  bf16x8 pf[2];
  {
    float a = s[0];
#pragma unroll
    for (int i = 1; i < 16; ++i) a = fmaxf(a, s[i]);
    auto rr = __builtin_amdgcn_permlane32_swap(__float_as_uint(a), __float_as_uint(a), false, false);
    const float mx = fmaxf(__uint_as_float(rr[0]), __uint_as_float(rr[1])) * SCL;
    const bool grew = (mx - st.m > THR);
    if (T == 0 || __any((int)grew)) {
      const float mn = fmaxf(st.m, mx);
      const float alpha = __builtin_amdgcn_exp2f(st.m - mn);
      st.m = mn;
      st.l *= alpha;
      if (T != 0) {
#pragma unroll
        for (int db = 0; db < 8; ++db)
#pragma unroll
          for (int i = 0; i < 16; ++i) st.oacc[db][i] *= alpha;
      }
    }
    const float nm = -st.m;
    float ps = 0.f;
#pragma unroll
    for (int i = 0; i < 16; ++i) {
      s[i] = __builtin_amdgcn_exp2f(fmaf(s[i], SCL, nm));
      ps += s[i];
    }
    st.l += ps;
#pragma unroll
    for (int ks = 0; ks < 2; ++ks) {
      u32x4 wv = {cvtpk(s[8 * ks + 0], s[8 * ks + 1]), cvtpk(s[8 * ks + 2], s[8 * ks + 3]),
                  cvtpk(s[8 * ks + 4], s[8 * ks + 5]), cvtpk(s[8 * ks + 6], s[8 * ks + 7])};
      pf[ks] = __builtin_bit_cast(bf16x8, wv);
    }
  }
  pv_half<T, 0>(st, pf[0], c, vaddr);
  pv_half<T, 1>(st, pf[1], c, vaddr);
}

#define STAMP(i)
#define DBGARG
__global__ __launch_bounds__(512, 2) void attn_kernel(const u16* __restrict__ q, const u16* __restrict__ kc,
                                                      const u16* __restrict__ vc, u16* __restrict__ o DBGARG) {
  extern __shared__ __attribute__((aligned(16))) char lds[];
  STAMP(0);
  const int tid = threadIdx.x, lane = tid & 63;
  const int w = __builtin_amdgcn_readfirstlane(tid >> 6);
  const int rb = w >> 2, kq = w & 3;
  const int r = lane & 31, h = lane >> 5;
  const int bid = blockIdx.x;
  const int xcd = bid & 7, j = bid >> 3;
  const int grp = (j >> 2) * 8 + xcd, sub = j & 3;
  const int b = grp >> 3, kvh = grp & 7;
  const int head = kvh * 2 + (sub >> 1), rhalf = sub & 1;
  const int row0 = b * SQ + rhalf * 56;

  const u16* Kw = kc + ((size_t)(b * HKV + kvh) * SK + 256 * kq) * DH;
  const u16* Vw = vc + ((size_t)(b * HKV + kvh) * SK + 256 * kq) * DH;
  char* ring = lds + kq * 40960;

  AttnState st;
  {
    int rr = 32 * rb + r;
    rr = rr > 55 ? 55 : rr;
    const u16* qrow = q + (size_t)(row0 + rr) * NQ + head * DH + 8 * h;
#pragma unroll
    for (int s = 0; s < 16; ++s) st.qf[s] = *(const bf16x8*)(qrow + 16 * s);
    st.m = -__builtin_inff();
    st.l = 0.f;
#pragma unroll
    for (int db = 0; db < 8; ++db)
#pragma unroll
      for (int i = 0; i < 16; ++i) st.oacc[db][i] = 0.f;
  }
#pragma unroll
  for (int s = 0; s < 16; ++s) asm volatile("" : "+v"(st.qf[s]));
  WAITV(0);

  STAMP(1);
  DmaCtx c;
  c.rk = __builtin_amdgcn_make_buffer_rsrc((void*)Kw, 0, 256 * DH * 2, 0x00020000);
  c.rv = __builtin_amdgcn_make_buffer_rsrc((void*)Vw, 0, 256 * DH * 2, 0x00020000);
  c.dst = ring + rb * 4096;
#pragma unroll
  for (int pi = 0; pi < 4; ++pi) {
    const int krow = 16 * rb + 4 * pi + (lane >> 4);
    c.koff[pi] = krow * 512 + (((lane & 15) ^ (krow & 15)) * 16);
    const int vrow = 8 * rb + 2 * pi + (lane >> 5);
    c.voff[pi] = vrow * 512 + (((lane & 31) ^ ((vrow & 3) << 2)) * 16);
  }
  issue_unit<0>(c);
  issue_unit<1>(c);
  issue_unit<2>(c);
  issue_unit<3>(c);

  const unsigned kaddr = r * 256 + ((h ^ (r & 15)) * 16);
  const int g = lane >> 4, qq = (lane >> 2) & 3, pp = lane & 3;
  unsigned vaddr[4];
#pragma unroll
  for (int d = 0; d < 4; ++d)
    vaddr[d] = (unsigned)(size_t)ring + (4 * (g >> 1) + qq) * 512 + ((64 * d + 32 * (g & 1) + 8 * pp) ^ (qq << 6));

  attn_tile<0>(st, c, ring, kaddr, vaddr);
  STAMP(2);
  attn_tile<1>(st, c, ring, kaddr, vaddr);
  STAMP(3);
  attn_tile<2>(st, c, ring, kaddr, vaddr);
  STAMP(4);
  attn_tile<3>(st, c, ring, kaddr, vaddr);
  STAMP(5);
  attn_tile<4>(st, c, ring, kaddr, vaddr);
  STAMP(6);
  attn_tile<5>(st, c, ring, kaddr, vaddr);
  STAMP(7);
  attn_tile<6>(st, c, ring, kaddr, vaddr);
  STAMP(8);
  attn_tile<7>(st, c, ring, kaddr, vaddr);
  STAMP(9);

  __syncthreads();
  float* ml = (float*)lds;
  {
    auto rr = __builtin_amdgcn_permlane32_swap(__float_as_uint(st.l), __float_as_uint(st.l), false, false);
    const float lt = __uint_as_float(rr[0]) + __uint_as_float(rr[1]);
    if (h == 0) {
      ml[(w * 32 + r) * 2 + 0] = st.m;
      ml[(w * 32 + r) * 2 + 1] = lt;
    }
  }
  __syncthreads();
  float f;
  {
    float mm[4], ll[4];
#pragma unroll
    for (int k = 0; k < 4; ++k) {
      mm[k] = ml[((rb * 4 + k) * 32 + r) * 2 + 0];
      ll[k] = ml[((rb * 4 + k) * 32 + r) * 2 + 1];
    }
    const float M = fmaxf(fmaxf(mm[0], mm[1]), fmaxf(mm[2], mm[3]));
    float L = 0.f;
#pragma unroll
    for (int k = 0; k < 4; ++k) L += __builtin_amdgcn_exp2f(mm[k] - M) * ll[k];
    f = __builtin_amdgcn_exp2f(st.m - M) / L;
  }
  float* xch = (float*)(lds + 4096);
#pragma unroll
  for (int rnd = 0; rnd < 2; ++rnd) {
#pragma unroll
    for (int db = 0; db < 8; ++db) {
      if ((db & 1) != rnd) continue;
      const int owner = db >> 1;
      if (owner == kq) continue;
      const int dslot = owner < kq ? owner : owner - 1;
#pragma unroll
      for (int gg = 0; gg < 4; ++gg) {
        f32x4 v = {st.oacc[db][4 * gg] * f, st.oacc[db][4 * gg + 1] * f, st.oacc[db][4 * gg + 2] * f,
                   st.oacc[db][4 * gg + 3] * f};
        *(f32x4*)(xch + (((w * 3 + dslot) * 4 + gg) * 64 + lane) * 4) = v;
      }
    }
    __syncthreads();
#pragma unroll
    for (int db = 0; db < 8; ++db) {
      if ((db & 1) != rnd) continue;
      if ((db >> 1) != kq) continue;
#pragma unroll
      for (int gg = 0; gg < 4; ++gg) {
        f32x4 sum = {st.oacc[db][4 * gg] * f, st.oacc[db][4 * gg + 1] * f, st.oacc[db][4 * gg + 2] * f,
                     st.oacc[db][4 * gg + 3] * f};
#pragma unroll
        for (int sk = 0; sk < 4; ++sk) {
          if (sk == kq) continue;
          const int dslot = kq < sk ? kq : kq - 1;
          f32x4 v = *(const f32x4*)(xch + ((((rb * 4 + sk) * 3 + dslot) * 4 + gg) * 64 + lane) * 4);
          sum += v;
        }
        const int rr = 32 * rb + r;
        if (rr < 56) {
          u32x2 v = {cvtpk(sum[0], sum[1]), cvtpk(sum[2], sum[3])};
          *(u32x2*)(o + (size_t)(row0 + rr) * NQ + head * DH + 32 * db + 8 * gg + 4 * h) = v;
        }
      }
    }
    __syncthreads();
  }
  STAMP(10);
}

#define GAP() __builtin_amdgcn_sched_barrier(0)
#define ACLOB "a0","a1","a2","a3","a4","a5","a6","a7","a8","a9","a10","a11","a12","a13","a14","a15","a16","a17","a18","a19","a20","a21","a22","a23","a24","a25","a26","a27","a28","a29","a30","a31","a32","a33","a34","a35","a36","a37","a38","a39","a40","a41","a42","a43","a44","a45","a46","a47","a48","a49","a50","a51","a52","a53","a54","a55","a56","a57","a58","a59","a60","a61","a62","a63","a64","a65","a66","a67","a68","a69","a70","a71","a72","a73","a74","a75","a76","a77","a78","a79","a80","a81","a82","a83","a84","a85","a86","a87","a88","a89","a90","a91","a92","a93","a94","a95","a96","a97","a98","a99","a100","a101","a102","a103","a104","a105","a106","a107","a108","a109","a110","a111","a112","a113","a114","a115","a116","a117","a118","a119","a120","a121","a122","a123","a124","a125","a126","a127","a128","a129","a130","a131","a132","a133","a134","a135","a136","a137","a138","a139","a140","a141","a142","a143","a144","a145","a146","a147","a148","a149","a150","a151","a152","a153","a154","a155","a156","a157","a158","a159","a160","a161","a162","a163","a164","a165","a166","a167","a168","a169","a170","a171","a172","a173","a174","a175","a176","a177","a178","a179","a180","a181","a182","a183","a184","a185","a186","a187","a188","a189","a190","a191","a192","a193","a194","a195","a196","a197","a198","a199","a200","a201","a202","a203","a204","a205","a206","a207","a208","a209","a210","a211","a212","a213","a214","a215","a216","a217","a218","a219","a220","a221","a222","a223","a224","a225","a226","a227","a228","a229","a230","a231","a232","a233","a234","a235","a236","a237","a238","a239","a240","a241","a242","a243","a244","a245","a246","a247","a248","a249","a250","a251","a252","a253","a254","a255"

template <int AO, bool ZERO> DI void pv_mfma(const bf16x8& vf, const bf16x8& pf) {
  if constexpr (ZERO)
    asm volatile("v_mfma_f32_32x32x16_bf16 a[%c2:%c3], %0, %1, 0" ::"v"(vf), "v"(pf), "i"(AO), "i"(AO + 15) : ACLOB);
  else
    asm volatile("v_mfma_f32_32x32x16_bf16 a[%c2:%c3], %0, %1, a[%c2:%c3]" ::"v"(vf), "v"(pf), "i"(AO), "i"(AO + 15)
                 : ACLOB);
}
template <bool FIRST> DI void qk_mfma(f32x16& s, const bf16x8& kf, const bf16x8& qf) {
  if constexpr (FIRST)
    asm volatile("v_mfma_f32_32x32x16_bf16 %0, %1, %2, 0" : "=&v"(s) : "v"(kf), "v"(qf) : ACLOB);
  else
    asm volatile("v_mfma_f32_32x32x16_bf16 %0, %1, %2, %0" : "+v"(s) : "v"(kf), "v"(qf) : ACLOB);
}
DI float max3f(float a, float b, float c) { float r; asm("v_max3_f32 %0, %1, %2, %3" : "=v"(r) : "v"(a), "v"(b), "v"(c)); return r; }
DI float max2f(float a, float b) { float r; asm("v_max_f32_e32 %0, %1, %2" : "=v"(r) : "v"(a), "v"(b)); return r; }
DI float fadd_s(float a, float b) { float r; asm("v_add_f32_e32 %0, %1, %2" : "=v"(r) : "v"(a), "v"(b)); return r; }
DI float ffma_s(float a, float b, float c) { float r; asm("v_fma_f32 %0, %1, %2, %3" : "=v"(r) : "v"(a), "v"(b), "v"(c)); return r; }

constexpr int unit_is_v(int n) { return n < 2 ? 0 : (n >= 30 ? 1 : (((n - 2) & 3) >= 2)); }
constexpr int unit_tile(int n) { return n < 2 ? 0 : (n >= 30 ? 7 : (((n - 2) & 3) >= 2 ? (n - 2) / 4 : (n - 2) / 4 + 1)); }
constexpr int unit_half(int n) { return n & 1; }
constexpr int unit_k(int t, int hf) { return t == 0 ? hf : 4 * (t - 1) + 2 + hf; }
constexpr int unit_v(int t, int hf) { return t == 7 ? 30 + hf : 4 * t + 4 + hf; }

struct Ctx4 {
  __amdgpu_buffer_rsrc_t rk, rv;
  int koff[4], voff[2];
  char* ring;
  unsigned kaddr[8];
  unsigned vaddr[4];
  bf16x8 qf[2][16];
  f32x16 s[2];
  bf16x8 pf[2][2];
  unsigned pn[2][8];
  float m[2], l[2], nm[2], mx[2], fct[2];
  float rs[2];
  bool pend;
};

template <int N, int P> DI void dma_piece(const Ctx4& c) {
  if constexpr (N < 32) {
    constexpr int slot = N % 5, t = unit_tile(N), hf = unit_half(N);
    if constexpr (!unit_is_v(N))
      __builtin_amdgcn_raw_ptr_buffer_load_lds(c.rk, (lds_vp)(c.ring + slot * 8192 + P * 1024), 16, c.koff[P & 3],
                                               (32 * t + 16 * (P >> 2)) * 512 + hf * 256, 0, 0);
    else
      __builtin_amdgcn_raw_ptr_buffer_load_lds(c.rv, (lds_vp)(c.ring + slot * 8192 + P * 1024), 16, c.voff[P & 1],
                                               (32 * t + 16 * hf + 4 * (P >> 1)) * 512, 0, 0);
  }
}
template <int N> DI void dma_unit(const Ctx4& c) {
  dma_piece<N, 0>(c); dma_piece<N, 1>(c); dma_piece<N, 2>(c); dma_piece<N, 3>(c);
  dma_piece<N, 4>(c); dma_piece<N, 5>(c); dma_piece<N, 6>(c); dma_piece<N, 7>(c);
}
template <int N, int AHEAD> DI void wait_unit4() {
  constexpr int last = N + AHEAD > 31 ? 31 : N + AHEAD;
  constexpr int after = last - N;
  if constexpr (after == 4) WAITV(32);
  else if constexpr (after == 3) WAITV(24);
  else if constexpr (after == 2) WAITV(16);
  else if constexpr (after == 1) WAITV(8);
  else WAITV(0);
}

template <int T, int J> DI bf16x8 kread(const Ctx4& c) {
  constexpr int n = unit_k(T, J >> 3), slot = n % 5;
  bf16x8 r;
  asm volatile("ds_read_b128 %0, %1 offset:%2" : "=v"(r) : "v"(c.kaddr[J & 7]), "i"(slot * 8192) : "memory");
  return r;
}
template <int T, int J> DI void phaseA_step(Ctx4& c, bf16x8 (&kf)[4]) {
  constexpr int n0 = unit_k(T, 0), n1 = unit_k(T, 1);
  if constexpr (J + 3 < 16) asm volatile("s_waitcnt lgkmcnt(3)" : "+v"(kf[J & 3])::"memory");
  else if constexpr (J + 3 == 16) asm volatile("s_waitcnt lgkmcnt(2)" : "+v"(kf[J & 3])::"memory");
  else if constexpr (J + 3 == 17) asm volatile("s_waitcnt lgkmcnt(1)" : "+v"(kf[J & 3])::"memory");
  else asm volatile("s_waitcnt lgkmcnt(0)" : "+v"(kf[J & 3])::"memory");
  qk_mfma<J == 0>(c.s[0], kf[J & 3], c.qf[0][J]);
  if constexpr (J >= 8) { dma_piece<n0 + 5, J - 8>(c); }
  GAP();
  qk_mfma<J == 0>(c.s[1], kf[J & 3], c.qf[1][J]);
  if constexpr (J + 4 == 8) wait_unit4<n1, 3>();
  if constexpr (J + 4 < 16) kf[J & 3] = kread<T, J + 4>(c);
  GAP();
}
template <int T, int... Js> DI void phaseA_(Ctx4& c, std::integer_sequence<int, Js...>) {
  bf16x8 kf[4];
  wait_unit4<unit_k(T, 0), 4>();
  kf[0] = kread<T, 0>(c); kf[1] = kread<T, 1>(c); kf[2] = kread<T, 2>(c); kf[3] = kread<T, 3>(c);
  GAP();
  (phaseA_step<T, Js>(c, kf), ...);
}
template <int T> DI void phaseA(Ctx4& c) { phaseA_<T>(c, std::make_integer_sequence<int, 16>{}); }

constexpr int egap(int e) { return 6 + (e * 25) / 32; }
template <int T, int G> DI void sm_slice(Ctx4& c) {
  if constexpr (G < 4) {
    constexpr int qb = G >> 1;
    if constexpr ((G & 1) == 0) {
      float a = max3f(c.s[qb][0], c.s[qb][1], c.s[qb][2]);
      a = max3f(a, c.s[qb][3], c.s[qb][4]);
      a = max3f(a, c.s[qb][5], c.s[qb][6]);
      c.mx[qb] = max3f(a, c.s[qb][7], c.s[qb][8]);
    } else {
      float a = max3f(c.mx[qb], c.s[qb][9], c.s[qb][10]);
      a = max3f(a, c.s[qb][11], c.s[qb][12]);
      a = max3f(a, c.s[qb][13], c.s[qb][14]);
      c.mx[qb] = max2f(a, c.s[qb][15]);
    }
  } else if constexpr (G == 4) {
#pragma unroll
    for (int qb = 0; qb < 2; ++qb) {
      auto rr = __builtin_amdgcn_permlane32_swap(__float_as_uint(c.mx[qb]), __float_as_uint(c.mx[qb]), false, false);
      c.mx[qb] = max2f(__uint_as_float(rr[0]), __uint_as_float(rr[1])) * SCL;
    }
  } else if constexpr (G == 5) {
    if constexpr (T == 0) {
      c.m[0] = c.mx[0]; c.m[1] = c.mx[1];
      c.l[0] = 0.f; c.l[1] = 0.f;
    } else {
      const bool grew = (c.mx[0] - c.m[0] > THR) | (c.mx[1] - c.m[1] > THR);
      if (__builtin_expect(__any((int)grew), 0)) {
#pragma unroll
        for (int qb = 0; qb < 2; ++qb) {
          const float mn = fmaxf(c.m[qb], c.mx[qb]);
          const float al = __builtin_amdgcn_exp2f(c.m[qb] - mn);
          c.m[qb] = mn;
          c.l[qb] *= al;
          c.fct[qb] = al;
        }
        c.pend = true;
      }
    }
    c.nm[0] = -c.m[0]; c.nm[1] = -c.m[1];
    c.rs[0] = 0.f; c.rs[1] = 0.f;
  }
  if constexpr (G >= 6) {
#pragma unroll
    for (int e = 0; e < 32; ++e) {
      if (egap(e) == G) {
        const int qb = e >> 4, i = e & 15;
        c.s[qb][i] = __builtin_amdgcn_exp2f(ffma_s(c.s[qb][i], SCL, c.nm[qb]));
      }
    }
#pragma unroll
    for (int e = 0; e < 32; ++e) {
      const int ge = egap(e);
      if (ge == G - 1 || (G == 31 && ge == 31)) {
        const int qb = e >> 4, i = e & 15;
        c.rs[qb] += c.s[qb][i];
      }
    }
#pragma unroll
    for (int d = 0; d < 16; ++d) {
      const int ge = egap(2 * d + 1);
      if (ge == G - 1 || (G == 31 && ge == 31)) {
        const int qb = d >> 3, i = (2 * d) & 15;
        c.pn[qb][d & 7] = cvtpk(c.s[qb][i], c.s[qb][i + 1]);
      }
    }
  }
}

template <int TV, int S> DI void vread_set(const Ctx4& c, s16x4 (&v)[4]) {
  constexpr int ks = S >> 2, dbp = S & 3;
  constexpr int n = unit_v(TV, ks), slot = n % 5;
  constexpr int db0 = 2 * dbp, db1 = 2 * dbp + 1;
  constexpr int o0 = slot * 8192 + (db0 >> 2) * 256, o1 = slot * 8192 + (db1 >> 2) * 256;
  v[0] = trread_o<o0>(c.vaddr[db0 & 3]);
  v[1] = trread_o<o0 + 4096>(c.vaddr[db0 & 3]);
  v[2] = trread_o<o1>(c.vaddr[db1 & 3]);
  v[3] = trread_o<o1 + 4096>(c.vaddr[db1 & 3]);
}
template <int T, bool HASPV, bool HASSM, int M> DI void phaseB_gap(Ctx4& c, s16x4 (&va)[4], s16x4 (&vb)[4]) {
  constexpr int TV = T - 1;
  constexpr int S = M >> 2, ks = S >> 2, dbp = S & 3, dd = (M >> 1) & 1, qb = M & 1;
  if constexpr (HASPV) {
    if constexpr ((M & 3) == 0) {
      if constexpr (S + 1 < 8) {
        if constexpr (S & 1) asm volatile("s_waitcnt lgkmcnt(4)" : "+v"(vb[0]), "+v"(vb[1]), "+v"(vb[2]), "+v"(vb[3])::"memory");
        else asm volatile("s_waitcnt lgkmcnt(4)" : "+v"(va[0]), "+v"(va[1]), "+v"(va[2]), "+v"(va[3])::"memory");
      } else {
        asm volatile("s_waitcnt lgkmcnt(0)" : "+v"(vb[0]), "+v"(vb[1]), "+v"(vb[2]), "+v"(vb[3])::"memory");
      }
    }
    {
      constexpr int db = 2 * dbp + dd;
      const bf16x8 vf = (S & 1) ? cat8(vb[2 * dd], vb[2 * dd + 1]) : cat8(va[2 * dd], va[2 * dd + 1]);
      pv_mfma<(qb * 8 + db) * 16, (TV == 0 && ks == 0)>(vf, c.pf[qb][ks]);
    }
    if constexpr ((M & 3) == 3 && S + 2 < 8) {
      if constexpr (S + 2 == 4) wait_unit4<unit_v(TV, 1), 3>();
      if constexpr (S & 1) vread_set<TV, S + 2>(c, vb); else vread_set<TV, S + 2>(c, va);
    }
    if constexpr (M >= 16 && M < 24) dma_piece<unit_v(TV, 0) + 5, M - 16>(c);
  }
  if constexpr (HASSM) sm_slice<T, M>(c);
  GAP();
}
template <int T, bool HASPV, bool HASSM, int... Ms> DI void phaseB_(Ctx4& c, std::integer_sequence<int, Ms...>) {
  constexpr int TV = T - 1;
  s16x4 va[4], vb[4];
  if constexpr (HASPV) {
    wait_unit4<unit_v(TV, 0), 3>();
    vread_set<TV, 0>(c, va);
    vread_set<TV, 1>(c, vb);
  }
  if constexpr (HASSM) dma_unit<unit_k(T, 1) + 5>(c);
  if constexpr (HASSM) {
    asm volatile("s_nop 7\n\ts_nop 7" : "+v"(c.s[0]), "+v"(c.s[1]));
  }
  GAP();
  (phaseB_gap<T, HASPV, HASSM, Ms>(c, va, vb), ...);
  if constexpr (HASPV) dma_unit<unit_v(TV, 1) + 5>(c);
  if constexpr (HASSM) {
    c.l[0] += c.rs[0]; c.l[1] += c.rs[1];
#pragma unroll
    for (int qb = 0; qb < 2; ++qb)
#pragma unroll
      for (int ks = 0; ks < 2; ++ks) {
        u32x4 wv = {c.pn[qb][4 * ks], c.pn[qb][4 * ks + 1], c.pn[qb][4 * ks + 2], c.pn[qb][4 * ks + 3]};
        c.pf[qb][ks] = __builtin_bit_cast(bf16x8, wv);
      }
  }
  if constexpr (HASPV && HASSM) {
    if (__builtin_expect(c.pend, 0)) {
      float t0, t1;
      asm volatile("s_nop 7\n\ts_nop 7\n\t"
                   "v_accvgpr_read_b32 %0, a0\n\tv_mul_f32 %0, %2, %0\n\tv_accvgpr_write_b32 a0, %0\n\t"
                   "v_accvgpr_read_b32 %1, a1\n\tv_mul_f32 %1, %2, %1\n\tv_accvgpr_write_b32 a1, %1\n\t"
                   "v_accvgpr_read_b32 %0, a2\n\tv_mul_f32 %0, %2, %0\n\tv_accvgpr_write_b32 a2, %0\n\t"
                   "v_accvgpr_read_b32 %1, a3\n\tv_mul_f32 %1, %2, %1\n\tv_accvgpr_write_b32 a3, %1\n\t"
                   "v_accvgpr_read_b32 %0, a4\n\tv_mul_f32 %0, %2, %0\n\tv_accvgpr_write_b32 a4, %0\n\t"
                   "v_accvgpr_read_b32 %1, a5\n\tv_mul_f32 %1, %2, %1\n\tv_accvgpr_write_b32 a5, %1\n\t"
                   "v_accvgpr_read_b32 %0, a6\n\tv_mul_f32 %0, %2, %0\n\tv_accvgpr_write_b32 a6, %0\n\t"
                   "v_accvgpr_read_b32 %1, a7\n\tv_mul_f32 %1, %2, %1\n\tv_accvgpr_write_b32 a7, %1\n\t"
                   "v_accvgpr_read_b32 %0, a8\n\tv_mul_f32 %0, %2, %0\n\tv_accvgpr_write_b32 a8, %0\n\t"
                   "v_accvgpr_read_b32 %1, a9\n\tv_mul_f32 %1, %2, %1\n\tv_accvgpr_write_b32 a9, %1\n\t"
                   "v_accvgpr_read_b32 %0, a10\n\tv_mul_f32 %0, %2, %0\n\tv_accvgpr_write_b32 a10, %0\n\t"
                   "v_accvgpr_read_b32 %1, a11\n\tv_mul_f32 %1, %2, %1\n\tv_accvgpr_write_b32 a11, %1\n\t"
                   "v_accvgpr_read_b32 %0, a12\n\tv_mul_f32 %0, %2, %0\n\tv_accvgpr_write_b32 a12, %0\n\t"
                   "v_accvgpr_read_b32 %1, a13\n\tv_mul_f32 %1, %2, %1\n\tv_accvgpr_write_b32 a13, %1\n\t"
                   "v_accvgpr_read_b32 %0, a14\n\tv_mul_f32 %0, %2, %0\n\tv_accvgpr_write_b32 a14, %0\n\t"
                   "v_accvgpr_read_b32 %1, a15\n\tv_mul_f32 %1, %2, %1\n\tv_accvgpr_write_b32 a15, %1\n\t"
                   "v_accvgpr_read_b32 %0, a16\n\tv_mul_f32 %0, %2, %0\n\tv_accvgpr_write_b32 a16, %0\n\t"
                   "v_accvgpr_read_b32 %1, a17\n\tv_mul_f32 %1, %2, %1\n\tv_accvgpr_write_b32 a17, %1\n\t"
                   "v_accvgpr_read_b32 %0, a18\n\tv_mul_f32 %0, %2, %0\n\tv_accvgpr_write_b32 a18, %0\n\t"
                   "v_accvgpr_read_b32 %1, a19\n\tv_mul_f32 %1, %2, %1\n\tv_accvgpr_write_b32 a19, %1\n\t"
                   "v_accvgpr_read_b32 %0, a20\n\tv_mul_f32 %0, %2, %0\n\tv_accvgpr_write_b32 a20, %0\n\t"
                   "v_accvgpr_read_b32 %1, a21\n\tv_mul_f32 %1, %2, %1\n\tv_accvgpr_write_b32 a21, %1\n\t"
                   "v_accvgpr_read_b32 %0, a22\n\tv_mul_f32 %0, %2, %0\n\tv_accvgpr_write_b32 a22, %0\n\t"
                   "v_accvgpr_read_b32 %1, a23\n\tv_mul_f32 %1, %2, %1\n\tv_accvgpr_write_b32 a23, %1\n\t"
                   "v_accvgpr_read_b32 %0, a24\n\tv_mul_f32 %0, %2, %0\n\tv_accvgpr_write_b32 a24, %0\n\t"
                   "v_accvgpr_read_b32 %1, a25\n\tv_mul_f32 %1, %2, %1\n\tv_accvgpr_write_b32 a25, %1\n\t"
                   "v_accvgpr_read_b32 %0, a26\n\tv_mul_f32 %0, %2, %0\n\tv_accvgpr_write_b32 a26, %0\n\t"
                   "v_accvgpr_read_b32 %1, a27\n\tv_mul_f32 %1, %2, %1\n\tv_accvgpr_write_b32 a27, %1\n\t"
                   "v_accvgpr_read_b32 %0, a28\n\tv_mul_f32 %0, %2, %0\n\tv_accvgpr_write_b32 a28, %0\n\t"
                   "v_accvgpr_read_b32 %1, a29\n\tv_mul_f32 %1, %2, %1\n\tv_accvgpr_write_b32 a29, %1\n\t"
                   "v_accvgpr_read_b32 %0, a30\n\tv_mul_f32 %0, %2, %0\n\tv_accvgpr_write_b32 a30, %0\n\t"
                   "v_accvgpr_read_b32 %1, a31\n\tv_mul_f32 %1, %2, %1\n\tv_accvgpr_write_b32 a31, %1\n\t"
                   "v_accvgpr_read_b32 %0, a32\n\tv_mul_f32 %0, %2, %0\n\tv_accvgpr_write_b32 a32, %0\n\t"
                   "v_accvgpr_read_b32 %1, a33\n\tv_mul_f32 %1, %2, %1\n\tv_accvgpr_write_b32 a33, %1\n\t"
                   "v_accvgpr_read_b32 %0, a34\n\tv_mul_f32 %0, %2, %0\n\tv_accvgpr_write_b32 a34, %0\n\t"
                   "v_accvgpr_read_b32 %1, a35\n\tv_mul_f32 %1, %2, %1\n\tv_accvgpr_write_b32 a35, %1\n\t"
                   "v_accvgpr_read_b32 %0, a36\n\tv_mul_f32 %0, %2, %0\n\tv_accvgpr_write_b32 a36, %0\n\t"
                   "v_accvgpr_read_b32 %1, a37\n\tv_mul_f32 %1, %2, %1\n\tv_accvgpr_write_b32 a37, %1\n\t"
                   "v_accvgpr_read_b32 %0, a38\n\tv_mul_f32 %0, %2, %0\n\tv_accvgpr_write_b32 a38, %0\n\t"
                   "v_accvgpr_read_b32 %1, a39\n\tv_mul_f32 %1, %2, %1\n\tv_accvgpr_write_b32 a39, %1\n\t"
                   "v_accvgpr_read_b32 %0, a40\n\tv_mul_f32 %0, %2, %0\n\tv_accvgpr_write_b32 a40, %0\n\t"
                   "v_accvgpr_read_b32 %1, a41\n\tv_mul_f32 %1, %2, %1\n\tv_accvgpr_write_b32 a41, %1\n\t"
                   "v_accvgpr_read_b32 %0, a42\n\tv_mul_f32 %0, %2, %0\n\tv_accvgpr_write_b32 a42, %0\n\t"
                   "v_accvgpr_read_b32 %1, a43\n\tv_mul_f32 %1, %2, %1\n\tv_accvgpr_write_b32 a43, %1\n\t"
                   "v_accvgpr_read_b32 %0, a44\n\tv_mul_f32 %0, %2, %0\n\tv_accvgpr_write_b32 a44, %0\n\t"
                   "v_accvgpr_read_b32 %1, a45\n\tv_mul_f32 %1, %2, %1\n\tv_accvgpr_write_b32 a45, %1\n\t"
                   "v_accvgpr_read_b32 %0, a46\n\tv_mul_f32 %0, %2, %0\n\tv_accvgpr_write_b32 a46, %0\n\t"
                   "v_accvgpr_read_b32 %1, a47\n\tv_mul_f32 %1, %2, %1\n\tv_accvgpr_write_b32 a47, %1\n\t"
                   "v_accvgpr_read_b32 %0, a48\n\tv_mul_f32 %0, %2, %0\n\tv_accvgpr_write_b32 a48, %0\n\t"
                   "v_accvgpr_read_b32 %1, a49\n\tv_mul_f32 %1, %2, %1\n\tv_accvgpr_write_b32 a49, %1\n\t"
                   "v_accvgpr_read_b32 %0, a50\n\tv_mul_f32 %0, %2, %0\n\tv_accvgpr_write_b32 a50, %0\n\t"
                   "v_accvgpr_read_b32 %1, a51\n\tv_mul_f32 %1, %2, %1\n\tv_accvgpr_write_b32 a51, %1\n\t"
                   "v_accvgpr_read_b32 %0, a52\n\tv_mul_f32 %0, %2, %0\n\tv_accvgpr_write_b32 a52, %0\n\t"
                   "v_accvgpr_read_b32 %1, a53\n\tv_mul_f32 %1, %2, %1\n\tv_accvgpr_write_b32 a53, %1\n\t"
                   "v_accvgpr_read_b32 %0, a54\n\tv_mul_f32 %0, %2, %0\n\tv_accvgpr_write_b32 a54, %0\n\t"
                   "v_accvgpr_read_b32 %1, a55\n\tv_mul_f32 %1, %2, %1\n\tv_accvgpr_write_b32 a55, %1\n\t"
                   "v_accvgpr_read_b32 %0, a56\n\tv_mul_f32 %0, %2, %0\n\tv_accvgpr_write_b32 a56, %0\n\t"
                   "v_accvgpr_read_b32 %1, a57\n\tv_mul_f32 %1, %2, %1\n\tv_accvgpr_write_b32 a57, %1\n\t"
                   "v_accvgpr_read_b32 %0, a58\n\tv_mul_f32 %0, %2, %0\n\tv_accvgpr_write_b32 a58, %0\n\t"
                   "v_accvgpr_read_b32 %1, a59\n\tv_mul_f32 %1, %2, %1\n\tv_accvgpr_write_b32 a59, %1\n\t"
                   "v_accvgpr_read_b32 %0, a60\n\tv_mul_f32 %0, %2, %0\n\tv_accvgpr_write_b32 a60, %0\n\t"
                   "v_accvgpr_read_b32 %1, a61\n\tv_mul_f32 %1, %2, %1\n\tv_accvgpr_write_b32 a61, %1\n\t"
                   "v_accvgpr_read_b32 %0, a62\n\tv_mul_f32 %0, %2, %0\n\tv_accvgpr_write_b32 a62, %0\n\t"
                   "v_accvgpr_read_b32 %1, a63\n\tv_mul_f32 %1, %2, %1\n\tv_accvgpr_write_b32 a63, %1\n\t"
                   "v_accvgpr_read_b32 %0, a64\n\tv_mul_f32 %0, %2, %0\n\tv_accvgpr_write_b32 a64, %0\n\t"
                   "v_accvgpr_read_b32 %1, a65\n\tv_mul_f32 %1, %2, %1\n\tv_accvgpr_write_b32 a65, %1\n\t"
                   "v_accvgpr_read_b32 %0, a66\n\tv_mul_f32 %0, %2, %0\n\tv_accvgpr_write_b32 a66, %0\n\t"
                   "v_accvgpr_read_b32 %1, a67\n\tv_mul_f32 %1, %2, %1\n\tv_accvgpr_write_b32 a67, %1\n\t"
                   "v_accvgpr_read_b32 %0, a68\n\tv_mul_f32 %0, %2, %0\n\tv_accvgpr_write_b32 a68, %0\n\t"
                   "v_accvgpr_read_b32 %1, a69\n\tv_mul_f32 %1, %2, %1\n\tv_accvgpr_write_b32 a69, %1\n\t"
                   "v_accvgpr_read_b32 %0, a70\n\tv_mul_f32 %0, %2, %0\n\tv_accvgpr_write_b32 a70, %0\n\t"
                   "v_accvgpr_read_b32 %1, a71\n\tv_mul_f32 %1, %2, %1\n\tv_accvgpr_write_b32 a71, %1\n\t"
                   "v_accvgpr_read_b32 %0, a72\n\tv_mul_f32 %0, %2, %0\n\tv_accvgpr_write_b32 a72, %0\n\t"
                   "v_accvgpr_read_b32 %1, a73\n\tv_mul_f32 %1, %2, %1\n\tv_accvgpr_write_b32 a73, %1\n\t"
                   "v_accvgpr_read_b32 %0, a74\n\tv_mul_f32 %0, %2, %0\n\tv_accvgpr_write_b32 a74, %0\n\t"
                   "v_accvgpr_read_b32 %1, a75\n\tv_mul_f32 %1, %2, %1\n\tv_accvgpr_write_b32 a75, %1\n\t"
                   "v_accvgpr_read_b32 %0, a76\n\tv_mul_f32 %0, %2, %0\n\tv_accvgpr_write_b32 a76, %0\n\t"
                   "v_accvgpr_read_b32 %1, a77\n\tv_mul_f32 %1, %2, %1\n\tv_accvgpr_write_b32 a77, %1\n\t"
                   "v_accvgpr_read_b32 %0, a78\n\tv_mul_f32 %0, %2, %0\n\tv_accvgpr_write_b32 a78, %0\n\t"
                   "v_accvgpr_read_b32 %1, a79\n\tv_mul_f32 %1, %2, %1\n\tv_accvgpr_write_b32 a79, %1\n\t"
                   "v_accvgpr_read_b32 %0, a80\n\tv_mul_f32 %0, %2, %0\n\tv_accvgpr_write_b32 a80, %0\n\t"
                   "v_accvgpr_read_b32 %1, a81\n\tv_mul_f32 %1, %2, %1\n\tv_accvgpr_write_b32 a81, %1\n\t"
                   "v_accvgpr_read_b32 %0, a82\n\tv_mul_f32 %0, %2, %0\n\tv_accvgpr_write_b32 a82, %0\n\t"
                   "v_accvgpr_read_b32 %1, a83\n\tv_mul_f32 %1, %2, %1\n\tv_accvgpr_write_b32 a83, %1\n\t"
                   "v_accvgpr_read_b32 %0, a84\n\tv_mul_f32 %0, %2, %0\n\tv_accvgpr_write_b32 a84, %0\n\t"
                   "v_accvgpr_read_b32 %1, a85\n\tv_mul_f32 %1, %2, %1\n\tv_accvgpr_write_b32 a85, %1\n\t"
                   "v_accvgpr_read_b32 %0, a86\n\tv_mul_f32 %0, %2, %0\n\tv_accvgpr_write_b32 a86, %0\n\t"
                   "v_accvgpr_read_b32 %1, a87\n\tv_mul_f32 %1, %2, %1\n\tv_accvgpr_write_b32 a87, %1\n\t"
                   "v_accvgpr_read_b32 %0, a88\n\tv_mul_f32 %0, %2, %0\n\tv_accvgpr_write_b32 a88, %0\n\t"
                   "v_accvgpr_read_b32 %1, a89\n\tv_mul_f32 %1, %2, %1\n\tv_accvgpr_write_b32 a89, %1\n\t"
                   "v_accvgpr_read_b32 %0, a90\n\tv_mul_f32 %0, %2, %0\n\tv_accvgpr_write_b32 a90, %0\n\t"
                   "v_accvgpr_read_b32 %1, a91\n\tv_mul_f32 %1, %2, %1\n\tv_accvgpr_write_b32 a91, %1\n\t"
                   "v_accvgpr_read_b32 %0, a92\n\tv_mul_f32 %0, %2, %0\n\tv_accvgpr_write_b32 a92, %0\n\t"
                   "v_accvgpr_read_b32 %1, a93\n\tv_mul_f32 %1, %2, %1\n\tv_accvgpr_write_b32 a93, %1\n\t"
                   "v_accvgpr_read_b32 %0, a94\n\tv_mul_f32 %0, %2, %0\n\tv_accvgpr_write_b32 a94, %0\n\t"
                   "v_accvgpr_read_b32 %1, a95\n\tv_mul_f32 %1, %2, %1\n\tv_accvgpr_write_b32 a95, %1\n\t"
                   "v_accvgpr_read_b32 %0, a96\n\tv_mul_f32 %0, %2, %0\n\tv_accvgpr_write_b32 a96, %0\n\t"
                   "v_accvgpr_read_b32 %1, a97\n\tv_mul_f32 %1, %2, %1\n\tv_accvgpr_write_b32 a97, %1\n\t"
                   "v_accvgpr_read_b32 %0, a98\n\tv_mul_f32 %0, %2, %0\n\tv_accvgpr_write_b32 a98, %0\n\t"
                   "v_accvgpr_read_b32 %1, a99\n\tv_mul_f32 %1, %2, %1\n\tv_accvgpr_write_b32 a99, %1\n\t"
                   "v_accvgpr_read_b32 %0, a100\n\tv_mul_f32 %0, %2, %0\n\tv_accvgpr_write_b32 a100, %0\n\t"
                   "v_accvgpr_read_b32 %1, a101\n\tv_mul_f32 %1, %2, %1\n\tv_accvgpr_write_b32 a101, %1\n\t"
                   "v_accvgpr_read_b32 %0, a102\n\tv_mul_f32 %0, %2, %0\n\tv_accvgpr_write_b32 a102, %0\n\t"
                   "v_accvgpr_read_b32 %1, a103\n\tv_mul_f32 %1, %2, %1\n\tv_accvgpr_write_b32 a103, %1\n\t"
                   "v_accvgpr_read_b32 %0, a104\n\tv_mul_f32 %0, %2, %0\n\tv_accvgpr_write_b32 a104, %0\n\t"
                   "v_accvgpr_read_b32 %1, a105\n\tv_mul_f32 %1, %2, %1\n\tv_accvgpr_write_b32 a105, %1\n\t"
                   "v_accvgpr_read_b32 %0, a106\n\tv_mul_f32 %0, %2, %0\n\tv_accvgpr_write_b32 a106, %0\n\t"
                   "v_accvgpr_read_b32 %1, a107\n\tv_mul_f32 %1, %2, %1\n\tv_accvgpr_write_b32 a107, %1\n\t"
                   "v_accvgpr_read_b32 %0, a108\n\tv_mul_f32 %0, %2, %0\n\tv_accvgpr_write_b32 a108, %0\n\t"
                   "v_accvgpr_read_b32 %1, a109\n\tv_mul_f32 %1, %2, %1\n\tv_accvgpr_write_b32 a109, %1\n\t"
                   "v_accvgpr_read_b32 %0, a110\n\tv_mul_f32 %0, %2, %0\n\tv_accvgpr_write_b32 a110, %0\n\t"
                   "v_accvgpr_read_b32 %1, a111\n\tv_mul_f32 %1, %2, %1\n\tv_accvgpr_write_b32 a111, %1\n\t"
                   "v_accvgpr_read_b32 %0, a112\n\tv_mul_f32 %0, %2, %0\n\tv_accvgpr_write_b32 a112, %0\n\t"
                   "v_accvgpr_read_b32 %1, a113\n\tv_mul_f32 %1, %2, %1\n\tv_accvgpr_write_b32 a113, %1\n\t"
                   "v_accvgpr_read_b32 %0, a114\n\tv_mul_f32 %0, %2, %0\n\tv_accvgpr_write_b32 a114, %0\n\t"
                   "v_accvgpr_read_b32 %1, a115\n\tv_mul_f32 %1, %2, %1\n\tv_accvgpr_write_b32 a115, %1\n\t"
                   "v_accvgpr_read_b32 %0, a116\n\tv_mul_f32 %0, %2, %0\n\tv_accvgpr_write_b32 a116, %0\n\t"
                   "v_accvgpr_read_b32 %1, a117\n\tv_mul_f32 %1, %2, %1\n\tv_accvgpr_write_b32 a117, %1\n\t"
                   "v_accvgpr_read_b32 %0, a118\n\tv_mul_f32 %0, %2, %0\n\tv_accvgpr_write_b32 a118, %0\n\t"
                   "v_accvgpr_read_b32 %1, a119\n\tv_mul_f32 %1, %2, %1\n\tv_accvgpr_write_b32 a119, %1\n\t"
                   "v_accvgpr_read_b32 %0, a120\n\tv_mul_f32 %0, %2, %0\n\tv_accvgpr_write_b32 a120, %0\n\t"
                   "v_accvgpr_read_b32 %1, a121\n\tv_mul_f32 %1, %2, %1\n\tv_accvgpr_write_b32 a121, %1\n\t"
                   "v_accvgpr_read_b32 %0, a122\n\tv_mul_f32 %0, %2, %0\n\tv_accvgpr_write_b32 a122, %0\n\t"
                   "v_accvgpr_read_b32 %1, a123\n\tv_mul_f32 %1, %2, %1\n\tv_accvgpr_write_b32 a123, %1\n\t"
                   "v_accvgpr_read_b32 %0, a124\n\tv_mul_f32 %0, %2, %0\n\tv_accvgpr_write_b32 a124, %0\n\t"
                   "v_accvgpr_read_b32 %1, a125\n\tv_mul_f32 %1, %2, %1\n\tv_accvgpr_write_b32 a125, %1\n\t"
                   "v_accvgpr_read_b32 %0, a126\n\tv_mul_f32 %0, %2, %0\n\tv_accvgpr_write_b32 a126, %0\n\t"
                   "v_accvgpr_read_b32 %1, a127\n\tv_mul_f32 %1, %2, %1\n\tv_accvgpr_write_b32 a127, %1\n\t"
                   "v_accvgpr_read_b32 %0, a128\n\tv_mul_f32 %0, %3, %0\n\tv_accvgpr_write_b32 a128, %0\n\t"
                   "v_accvgpr_read_b32 %1, a129\n\tv_mul_f32 %1, %3, %1\n\tv_accvgpr_write_b32 a129, %1\n\t"
                   "v_accvgpr_read_b32 %0, a130\n\tv_mul_f32 %0, %3, %0\n\tv_accvgpr_write_b32 a130, %0\n\t"
                   "v_accvgpr_read_b32 %1, a131\n\tv_mul_f32 %1, %3, %1\n\tv_accvgpr_write_b32 a131, %1\n\t"
                   "v_accvgpr_read_b32 %0, a132\n\tv_mul_f32 %0, %3, %0\n\tv_accvgpr_write_b32 a132, %0\n\t"
                   "v_accvgpr_read_b32 %1, a133\n\tv_mul_f32 %1, %3, %1\n\tv_accvgpr_write_b32 a133, %1\n\t"
                   "v_accvgpr_read_b32 %0, a134\n\tv_mul_f32 %0, %3, %0\n\tv_accvgpr_write_b32 a134, %0\n\t"
                   "v_accvgpr_read_b32 %1, a135\n\tv_mul_f32 %1, %3, %1\n\tv_accvgpr_write_b32 a135, %1\n\t"
                   "v_accvgpr_read_b32 %0, a136\n\tv_mul_f32 %0, %3, %0\n\tv_accvgpr_write_b32 a136, %0\n\t"
                   "v_accvgpr_read_b32 %1, a137\n\tv_mul_f32 %1, %3, %1\n\tv_accvgpr_write_b32 a137, %1\n\t"
                   "v_accvgpr_read_b32 %0, a138\n\tv_mul_f32 %0, %3, %0\n\tv_accvgpr_write_b32 a138, %0\n\t"
                   "v_accvgpr_read_b32 %1, a139\n\tv_mul_f32 %1, %3, %1\n\tv_accvgpr_write_b32 a139, %1\n\t"
                   "v_accvgpr_read_b32 %0, a140\n\tv_mul_f32 %0, %3, %0\n\tv_accvgpr_write_b32 a140, %0\n\t"
                   "v_accvgpr_read_b32 %1, a141\n\tv_mul_f32 %1, %3, %1\n\tv_accvgpr_write_b32 a141, %1\n\t"
                   "v_accvgpr_read_b32 %0, a142\n\tv_mul_f32 %0, %3, %0\n\tv_accvgpr_write_b32 a142, %0\n\t"
                   "v_accvgpr_read_b32 %1, a143\n\tv_mul_f32 %1, %3, %1\n\tv_accvgpr_write_b32 a143, %1\n\t"
                   "v_accvgpr_read_b32 %0, a144\n\tv_mul_f32 %0, %3, %0\n\tv_accvgpr_write_b32 a144, %0\n\t"
                   "v_accvgpr_read_b32 %1, a145\n\tv_mul_f32 %1, %3, %1\n\tv_accvgpr_write_b32 a145, %1\n\t"
                   "v_accvgpr_read_b32 %0, a146\n\tv_mul_f32 %0, %3, %0\n\tv_accvgpr_write_b32 a146, %0\n\t"
                   "v_accvgpr_read_b32 %1, a147\n\tv_mul_f32 %1, %3, %1\n\tv_accvgpr_write_b32 a147, %1\n\t"
                   "v_accvgpr_read_b32 %0, a148\n\tv_mul_f32 %0, %3, %0\n\tv_accvgpr_write_b32 a148, %0\n\t"
                   "v_accvgpr_read_b32 %1, a149\n\tv_mul_f32 %1, %3, %1\n\tv_accvgpr_write_b32 a149, %1\n\t"
                   "v_accvgpr_read_b32 %0, a150\n\tv_mul_f32 %0, %3, %0\n\tv_accvgpr_write_b32 a150, %0\n\t"
                   "v_accvgpr_read_b32 %1, a151\n\tv_mul_f32 %1, %3, %1\n\tv_accvgpr_write_b32 a151, %1\n\t"
                   "v_accvgpr_read_b32 %0, a152\n\tv_mul_f32 %0, %3, %0\n\tv_accvgpr_write_b32 a152, %0\n\t"
                   "v_accvgpr_read_b32 %1, a153\n\tv_mul_f32 %1, %3, %1\n\tv_accvgpr_write_b32 a153, %1\n\t"
                   "v_accvgpr_read_b32 %0, a154\n\tv_mul_f32 %0, %3, %0\n\tv_accvgpr_write_b32 a154, %0\n\t"
                   "v_accvgpr_read_b32 %1, a155\n\tv_mul_f32 %1, %3, %1\n\tv_accvgpr_write_b32 a155, %1\n\t"
                   "v_accvgpr_read_b32 %0, a156\n\tv_mul_f32 %0, %3, %0\n\tv_accvgpr_write_b32 a156, %0\n\t"
                   "v_accvgpr_read_b32 %1, a157\n\tv_mul_f32 %1, %3, %1\n\tv_accvgpr_write_b32 a157, %1\n\t"
                   "v_accvgpr_read_b32 %0, a158\n\tv_mul_f32 %0, %3, %0\n\tv_accvgpr_write_b32 a158, %0\n\t"
                   "v_accvgpr_read_b32 %1, a159\n\tv_mul_f32 %1, %3, %1\n\tv_accvgpr_write_b32 a159, %1\n\t"
                   "v_accvgpr_read_b32 %0, a160\n\tv_mul_f32 %0, %3, %0\n\tv_accvgpr_write_b32 a160, %0\n\t"
                   "v_accvgpr_read_b32 %1, a161\n\tv_mul_f32 %1, %3, %1\n\tv_accvgpr_write_b32 a161, %1\n\t"
                   "v_accvgpr_read_b32 %0, a162\n\tv_mul_f32 %0, %3, %0\n\tv_accvgpr_write_b32 a162, %0\n\t"
                   "v_accvgpr_read_b32 %1, a163\n\tv_mul_f32 %1, %3, %1\n\tv_accvgpr_write_b32 a163, %1\n\t"
                   "v_accvgpr_read_b32 %0, a164\n\tv_mul_f32 %0, %3, %0\n\tv_accvgpr_write_b32 a164, %0\n\t"
                   "v_accvgpr_read_b32 %1, a165\n\tv_mul_f32 %1, %3, %1\n\tv_accvgpr_write_b32 a165, %1\n\t"
                   "v_accvgpr_read_b32 %0, a166\n\tv_mul_f32 %0, %3, %0\n\tv_accvgpr_write_b32 a166, %0\n\t"
                   "v_accvgpr_read_b32 %1, a167\n\tv_mul_f32 %1, %3, %1\n\tv_accvgpr_write_b32 a167, %1\n\t"
                   "v_accvgpr_read_b32 %0, a168\n\tv_mul_f32 %0, %3, %0\n\tv_accvgpr_write_b32 a168, %0\n\t"
                   "v_accvgpr_read_b32 %1, a169\n\tv_mul_f32 %1, %3, %1\n\tv_accvgpr_write_b32 a169, %1\n\t"
                   "v_accvgpr_read_b32 %0, a170\n\tv_mul_f32 %0, %3, %0\n\tv_accvgpr_write_b32 a170, %0\n\t"
                   "v_accvgpr_read_b32 %1, a171\n\tv_mul_f32 %1, %3, %1\n\tv_accvgpr_write_b32 a171, %1\n\t"
                   "v_accvgpr_read_b32 %0, a172\n\tv_mul_f32 %0, %3, %0\n\tv_accvgpr_write_b32 a172, %0\n\t"
                   "v_accvgpr_read_b32 %1, a173\n\tv_mul_f32 %1, %3, %1\n\tv_accvgpr_write_b32 a173, %1\n\t"
                   "v_accvgpr_read_b32 %0, a174\n\tv_mul_f32 %0, %3, %0\n\tv_accvgpr_write_b32 a174, %0\n\t"
                   "v_accvgpr_read_b32 %1, a175\n\tv_mul_f32 %1, %3, %1\n\tv_accvgpr_write_b32 a175, %1\n\t"
                   "v_accvgpr_read_b32 %0, a176\n\tv_mul_f32 %0, %3, %0\n\tv_accvgpr_write_b32 a176, %0\n\t"
                   "v_accvgpr_read_b32 %1, a177\n\tv_mul_f32 %1, %3, %1\n\tv_accvgpr_write_b32 a177, %1\n\t"
                   "v_accvgpr_read_b32 %0, a178\n\tv_mul_f32 %0, %3, %0\n\tv_accvgpr_write_b32 a178, %0\n\t"
                   "v_accvgpr_read_b32 %1, a179\n\tv_mul_f32 %1, %3, %1\n\tv_accvgpr_write_b32 a179, %1\n\t"
                   "v_accvgpr_read_b32 %0, a180\n\tv_mul_f32 %0, %3, %0\n\tv_accvgpr_write_b32 a180, %0\n\t"
                   "v_accvgpr_read_b32 %1, a181\n\tv_mul_f32 %1, %3, %1\n\tv_accvgpr_write_b32 a181, %1\n\t"
                   "v_accvgpr_read_b32 %0, a182\n\tv_mul_f32 %0, %3, %0\n\tv_accvgpr_write_b32 a182, %0\n\t"
                   "v_accvgpr_read_b32 %1, a183\n\tv_mul_f32 %1, %3, %1\n\tv_accvgpr_write_b32 a183, %1\n\t"
                   "v_accvgpr_read_b32 %0, a184\n\tv_mul_f32 %0, %3, %0\n\tv_accvgpr_write_b32 a184, %0\n\t"
                   "v_accvgpr_read_b32 %1, a185\n\tv_mul_f32 %1, %3, %1\n\tv_accvgpr_write_b32 a185, %1\n\t"
                   "v_accvgpr_read_b32 %0, a186\n\tv_mul_f32 %0, %3, %0\n\tv_accvgpr_write_b32 a186, %0\n\t"
                   "v_accvgpr_read_b32 %1, a187\n\tv_mul_f32 %1, %3, %1\n\tv_accvgpr_write_b32 a187, %1\n\t"
                   "v_accvgpr_read_b32 %0, a188\n\tv_mul_f32 %0, %3, %0\n\tv_accvgpr_write_b32 a188, %0\n\t"
                   "v_accvgpr_read_b32 %1, a189\n\tv_mul_f32 %1, %3, %1\n\tv_accvgpr_write_b32 a189, %1\n\t"
                   "v_accvgpr_read_b32 %0, a190\n\tv_mul_f32 %0, %3, %0\n\tv_accvgpr_write_b32 a190, %0\n\t"
                   "v_accvgpr_read_b32 %1, a191\n\tv_mul_f32 %1, %3, %1\n\tv_accvgpr_write_b32 a191, %1\n\t"
                   "v_accvgpr_read_b32 %0, a192\n\tv_mul_f32 %0, %3, %0\n\tv_accvgpr_write_b32 a192, %0\n\t"
                   "v_accvgpr_read_b32 %1, a193\n\tv_mul_f32 %1, %3, %1\n\tv_accvgpr_write_b32 a193, %1\n\t"
                   "v_accvgpr_read_b32 %0, a194\n\tv_mul_f32 %0, %3, %0\n\tv_accvgpr_write_b32 a194, %0\n\t"
                   "v_accvgpr_read_b32 %1, a195\n\tv_mul_f32 %1, %3, %1\n\tv_accvgpr_write_b32 a195, %1\n\t"
                   "v_accvgpr_read_b32 %0, a196\n\tv_mul_f32 %0, %3, %0\n\tv_accvgpr_write_b32 a196, %0\n\t"
                   "v_accvgpr_read_b32 %1, a197\n\tv_mul_f32 %1, %3, %1\n\tv_accvgpr_write_b32 a197, %1\n\t"
                   "v_accvgpr_read_b32 %0, a198\n\tv_mul_f32 %0, %3, %0\n\tv_accvgpr_write_b32 a198, %0\n\t"
                   "v_accvgpr_read_b32 %1, a199\n\tv_mul_f32 %1, %3, %1\n\tv_accvgpr_write_b32 a199, %1\n\t"
                   "v_accvgpr_read_b32 %0, a200\n\tv_mul_f32 %0, %3, %0\n\tv_accvgpr_write_b32 a200, %0\n\t"
                   "v_accvgpr_read_b32 %1, a201\n\tv_mul_f32 %1, %3, %1\n\tv_accvgpr_write_b32 a201, %1\n\t"
                   "v_accvgpr_read_b32 %0, a202\n\tv_mul_f32 %0, %3, %0\n\tv_accvgpr_write_b32 a202, %0\n\t"
                   "v_accvgpr_read_b32 %1, a203\n\tv_mul_f32 %1, %3, %1\n\tv_accvgpr_write_b32 a203, %1\n\t"
                   "v_accvgpr_read_b32 %0, a204\n\tv_mul_f32 %0, %3, %0\n\tv_accvgpr_write_b32 a204, %0\n\t"
                   "v_accvgpr_read_b32 %1, a205\n\tv_mul_f32 %1, %3, %1\n\tv_accvgpr_write_b32 a205, %1\n\t"
                   "v_accvgpr_read_b32 %0, a206\n\tv_mul_f32 %0, %3, %0\n\tv_accvgpr_write_b32 a206, %0\n\t"
                   "v_accvgpr_read_b32 %1, a207\n\tv_mul_f32 %1, %3, %1\n\tv_accvgpr_write_b32 a207, %1\n\t"
                   "v_accvgpr_read_b32 %0, a208\n\tv_mul_f32 %0, %3, %0\n\tv_accvgpr_write_b32 a208, %0\n\t"
                   "v_accvgpr_read_b32 %1, a209\n\tv_mul_f32 %1, %3, %1\n\tv_accvgpr_write_b32 a209, %1\n\t"
                   "v_accvgpr_read_b32 %0, a210\n\tv_mul_f32 %0, %3, %0\n\tv_accvgpr_write_b32 a210, %0\n\t"
                   "v_accvgpr_read_b32 %1, a211\n\tv_mul_f32 %1, %3, %1\n\tv_accvgpr_write_b32 a211, %1\n\t"
                   "v_accvgpr_read_b32 %0, a212\n\tv_mul_f32 %0, %3, %0\n\tv_accvgpr_write_b32 a212, %0\n\t"
                   "v_accvgpr_read_b32 %1, a213\n\tv_mul_f32 %1, %3, %1\n\tv_accvgpr_write_b32 a213, %1\n\t"
                   "v_accvgpr_read_b32 %0, a214\n\tv_mul_f32 %0, %3, %0\n\tv_accvgpr_write_b32 a214, %0\n\t"
                   "v_accvgpr_read_b32 %1, a215\n\tv_mul_f32 %1, %3, %1\n\tv_accvgpr_write_b32 a215, %1\n\t"
                   "v_accvgpr_read_b32 %0, a216\n\tv_mul_f32 %0, %3, %0\n\tv_accvgpr_write_b32 a216, %0\n\t"
                   "v_accvgpr_read_b32 %1, a217\n\tv_mul_f32 %1, %3, %1\n\tv_accvgpr_write_b32 a217, %1\n\t"
                   "v_accvgpr_read_b32 %0, a218\n\tv_mul_f32 %0, %3, %0\n\tv_accvgpr_write_b32 a218, %0\n\t"
                   "v_accvgpr_read_b32 %1, a219\n\tv_mul_f32 %1, %3, %1\n\tv_accvgpr_write_b32 a219, %1\n\t"
                   "v_accvgpr_read_b32 %0, a220\n\tv_mul_f32 %0, %3, %0\n\tv_accvgpr_write_b32 a220, %0\n\t"
                   "v_accvgpr_read_b32 %1, a221\n\tv_mul_f32 %1, %3, %1\n\tv_accvgpr_write_b32 a221, %1\n\t"
                   "v_accvgpr_read_b32 %0, a222\n\tv_mul_f32 %0, %3, %0\n\tv_accvgpr_write_b32 a222, %0\n\t"
                   "v_accvgpr_read_b32 %1, a223\n\tv_mul_f32 %1, %3, %1\n\tv_accvgpr_write_b32 a223, %1\n\t"
                   "v_accvgpr_read_b32 %0, a224\n\tv_mul_f32 %0, %3, %0\n\tv_accvgpr_write_b32 a224, %0\n\t"
                   "v_accvgpr_read_b32 %1, a225\n\tv_mul_f32 %1, %3, %1\n\tv_accvgpr_write_b32 a225, %1\n\t"
                   "v_accvgpr_read_b32 %0, a226\n\tv_mul_f32 %0, %3, %0\n\tv_accvgpr_write_b32 a226, %0\n\t"
                   "v_accvgpr_read_b32 %1, a227\n\tv_mul_f32 %1, %3, %1\n\tv_accvgpr_write_b32 a227, %1\n\t"
                   "v_accvgpr_read_b32 %0, a228\n\tv_mul_f32 %0, %3, %0\n\tv_accvgpr_write_b32 a228, %0\n\t"
                   "v_accvgpr_read_b32 %1, a229\n\tv_mul_f32 %1, %3, %1\n\tv_accvgpr_write_b32 a229, %1\n\t"
                   "v_accvgpr_read_b32 %0, a230\n\tv_mul_f32 %0, %3, %0\n\tv_accvgpr_write_b32 a230, %0\n\t"
                   "v_accvgpr_read_b32 %1, a231\n\tv_mul_f32 %1, %3, %1\n\tv_accvgpr_write_b32 a231, %1\n\t"
                   "v_accvgpr_read_b32 %0, a232\n\tv_mul_f32 %0, %3, %0\n\tv_accvgpr_write_b32 a232, %0\n\t"
                   "v_accvgpr_read_b32 %1, a233\n\tv_mul_f32 %1, %3, %1\n\tv_accvgpr_write_b32 a233, %1\n\t"
                   "v_accvgpr_read_b32 %0, a234\n\tv_mul_f32 %0, %3, %0\n\tv_accvgpr_write_b32 a234, %0\n\t"
                   "v_accvgpr_read_b32 %1, a235\n\tv_mul_f32 %1, %3, %1\n\tv_accvgpr_write_b32 a235, %1\n\t"
                   "v_accvgpr_read_b32 %0, a236\n\tv_mul_f32 %0, %3, %0\n\tv_accvgpr_write_b32 a236, %0\n\t"
                   "v_accvgpr_read_b32 %1, a237\n\tv_mul_f32 %1, %3, %1\n\tv_accvgpr_write_b32 a237, %1\n\t"
                   "v_accvgpr_read_b32 %0, a238\n\tv_mul_f32 %0, %3, %0\n\tv_accvgpr_write_b32 a238, %0\n\t"
                   "v_accvgpr_read_b32 %1, a239\n\tv_mul_f32 %1, %3, %1\n\tv_accvgpr_write_b32 a239, %1\n\t"
                   "v_accvgpr_read_b32 %0, a240\n\tv_mul_f32 %0, %3, %0\n\tv_accvgpr_write_b32 a240, %0\n\t"
                   "v_accvgpr_read_b32 %1, a241\n\tv_mul_f32 %1, %3, %1\n\tv_accvgpr_write_b32 a241, %1\n\t"
                   "v_accvgpr_read_b32 %0, a242\n\tv_mul_f32 %0, %3, %0\n\tv_accvgpr_write_b32 a242, %0\n\t"
                   "v_accvgpr_read_b32 %1, a243\n\tv_mul_f32 %1, %3, %1\n\tv_accvgpr_write_b32 a243, %1\n\t"
                   "v_accvgpr_read_b32 %0, a244\n\tv_mul_f32 %0, %3, %0\n\tv_accvgpr_write_b32 a244, %0\n\t"
                   "v_accvgpr_read_b32 %1, a245\n\tv_mul_f32 %1, %3, %1\n\tv_accvgpr_write_b32 a245, %1\n\t"
                   "v_accvgpr_read_b32 %0, a246\n\tv_mul_f32 %0, %3, %0\n\tv_accvgpr_write_b32 a246, %0\n\t"
                   "v_accvgpr_read_b32 %1, a247\n\tv_mul_f32 %1, %3, %1\n\tv_accvgpr_write_b32 a247, %1\n\t"
                   "v_accvgpr_read_b32 %0, a248\n\tv_mul_f32 %0, %3, %0\n\tv_accvgpr_write_b32 a248, %0\n\t"
                   "v_accvgpr_read_b32 %1, a249\n\tv_mul_f32 %1, %3, %1\n\tv_accvgpr_write_b32 a249, %1\n\t"
                   "v_accvgpr_read_b32 %0, a250\n\tv_mul_f32 %0, %3, %0\n\tv_accvgpr_write_b32 a250, %0\n\t"
                   "v_accvgpr_read_b32 %1, a251\n\tv_mul_f32 %1, %3, %1\n\tv_accvgpr_write_b32 a251, %1\n\t"
                   "v_accvgpr_read_b32 %0, a252\n\tv_mul_f32 %0, %3, %0\n\tv_accvgpr_write_b32 a252, %0\n\t"
                   "v_accvgpr_read_b32 %1, a253\n\tv_mul_f32 %1, %3, %1\n\tv_accvgpr_write_b32 a253, %1\n\t"
                   "v_accvgpr_read_b32 %0, a254\n\tv_mul_f32 %0, %3, %0\n\tv_accvgpr_write_b32 a254, %0\n\t"
                   "v_accvgpr_read_b32 %1, a255\n\tv_mul_f32 %1, %3, %1\n\tv_accvgpr_write_b32 a255, %1\n\t"
                   "s_nop 1"
                   : "=&v"(t0), "=&v"(t1) : "v"(c.fct[0]), "v"(c.fct[1]) : ACLOB);
      c.pend = false;
    }
  }
  asm volatile("s_nop 1" ::: "memory");
  GAP();
}
template <int T, bool HASPV, bool HASSM> DI void phaseB(Ctx4& c) {
  phaseB_<T, HASPV, HASSM>(c, std::make_integer_sequence<int, 32>{});
}

template <int A, int OFF> DI void acc_dswrite4(unsigned addr) {
  asm volatile("ds_write_b128 %0, a[%c1:%c2] offset:%c3" ::"v"(addr), "i"(A), "i"(A + 3), "i"(OFF) : "memory", ACLOB);
}
template <int A> DI f32x4 acc_read4() {
  f32x4 r;
  asm volatile("v_accvgpr_read_b32 %0, a[%c4]\n\tv_accvgpr_read_b32 %1, a[%c5]\n\tv_accvgpr_read_b32 %2, a[%c6]\n\t"
               "v_accvgpr_read_b32 %3, a[%c7]"
               : "=v"(r[0]), "=v"(r[1]), "=v"(r[2]), "=v"(r[3]) : "i"(A), "i"(A + 1), "i"(A + 2), "i"(A + 3) : ACLOB);
  return r;
}

__global__ __launch_bounds__(256, 1) void attn4_kernel(const u16* __restrict__ x, const u16* __restrict__ wq,
                                                       const u16* __restrict__ kc, const u16* __restrict__ vc,
                                                       u16* __restrict__ o DBGARG) {
  extern __shared__ __attribute__((aligned(16))) char lds[];
  STAMP(0);
  const int tid = threadIdx.x, lane = tid & 63;
  const int w = __builtin_amdgcn_readfirstlane(tid >> 6);
  const int r = lane & 31, h = lane >> 5;
  const int bid = blockIdx.x;
  const int xcd = bid & 7, j = bid >> 3;
  const int grp = (j >> 2) * 8 + xcd, sub = j & 3;
  const int b = grp >> 3, kvh = grp & 7;
  const int head = kvh * 2 + (sub >> 1), rhalf = sub & 1;
  const int row0 = b * SQ + rhalf * 56;

  const u16* Kw = kc + ((size_t)(b * HKV + kvh) * SK + 256 * w) * DH;
  const u16* Vw = vc + ((size_t)(b * HKV + kvh) * SK + 256 * w) * DH;

  Ctx4 c;
  c.ring = lds + w * 40960;
  c.rk = __builtin_amdgcn_make_buffer_rsrc((void*)Kw, 0, 256 * DH * 2, 0x00020000);
  c.rv = __builtin_amdgcn_make_buffer_rsrc((void*)Vw, 0, 256 * DH * 2, 0x00020000);
#pragma unroll
  for (int pi = 0; pi < 4; ++pi) {
    const int krow = 4 * pi + (lane >> 4);
    c.koff[pi] = krow * 512 + (((lane & 15) ^ (krow & 15)) * 16);
  }
#pragma unroll
  for (int pi = 0; pi < 2; ++pi) {
    const int vrow = 2 * pi + (lane >> 5);
    c.voff[pi] = vrow * 512 + (((lane & 31) ^ ((vrow & 3) << 2)) * 16);
  }
  {
    __amdgpu_buffer_rsrc_t rx =
        __builtin_amdgcn_make_buffer_rsrc((void*)(x + (size_t)row0 * DM), 0, 56 * DM * 2, 0x00020000);
#pragma unroll
    for (int p = 0; p < 14; ++p) {
      const int ci = 64 * p + lane;
      const int lr = ci / 56, cp = ci - lr * 56;
      int gr = 16 * w + lr; gr = gr > 55 ? 55 : gr;
      const int cs = (cp & ~7) | ((cp ^ (lr >> 1)) & 7);
      __builtin_amdgcn_raw_ptr_buffer_load_lds(rx, (lds_vp)(c.ring + 3 * 8192 + p * 1024), 16, gr * (DM * 2) + cs * 16,
                                               0, 0, 0);
    }
  }
  {
    const int colw = head * DH + 64 * w;
    __amdgpu_buffer_rsrc_t rw =
        __builtin_amdgcn_make_buffer_rsrc((void*)(wq + colw), 0, (DM * NQ - colw) * 2, 0x00020000);
    const int drow = lane >> 3;
    const int wvoff = drow * (NQ * 2) + (((lane & 7) ^ (((drow >> 1) & 1) << 2)) * 16);
    auto wissue = [&](int ch) {
#pragma unroll
      for (int p = 0; p < 8; ++p)
        __builtin_amdgcn_raw_ptr_buffer_load_lds(rw, (lds_vp)(c.ring + ((ch + 2) % 3) * 8192 + p * 1024), 16, wvoff,
                                                 (64 * ch + 8 * p) * (NQ * 2), 0, 0);
    };
    wissue(0); wissue(1); wissue(2);
    const int g = lane >> 4, qq = (lane >> 2) & 3, pp = lane & 3;
    const unsigned trb = (unsigned)(size_t)c.ring + (8 * (g >> 1) + qq) * 128 + ((32 * (g & 1) + 8 * pp) ^ ((qq >> 1) << 6));
    const char* xa[2][4];
#pragma unroll
    for (int qb = 0; qb < 2; ++qb) {
      const int R = 32 * qb + r, lr = R & 15;
#pragma unroll
      for (int v = 0; v < 4; ++v)
        xa[qb][v] = lds + (R >> 4) * 40960 + 3 * 8192 + lr * 896 + ((((2 * v + h) ^ (lr >> 1)) & 7) * 16);
    }
    f32x16 qa[2][2];
#pragma unroll
    for (int a2 = 0; a2 < 2; ++a2)
#pragma unroll
      for (int b2 = 0; b2 < 2; ++b2)
#pragma unroll
        for (int i = 0; i < 16; ++i) qa[a2][b2][i] = 0.f;
    s16x4 fa[2][4];
    bf16x8 fx[2][2];
    auto rdstep = [&](int i, int bsel) {
      const int ch = i >> 2, s4 = i & 3;
      const unsigned tb = trb + ((ch + 2) % 3) * 8192 + s4 * 2048;
      fa[bsel][0] = trread(tb);
      fa[bsel][1] = trread(tb + 512);
      fa[bsel][2] = trread(tb ^ 64);
      fa[bsel][3] = trread((tb + 512) ^ 64);
      asm volatile("ds_read_b128 %0, %1" : "=v"(fx[bsel][0]) : "v"((unsigned)(size_t)(xa[0][s4] + ch * 128)) : "memory");
      asm volatile("ds_read_b128 %0, %1" : "=v"(fx[bsel][1]) : "v"((unsigned)(size_t)(xa[1][s4] + ch * 128)) : "memory");
    };
    auto refill_piece = [&](int k, int p) {
      if (k <= 3) {
        __builtin_amdgcn_raw_ptr_buffer_load_lds(rw, (lds_vp)(c.ring + ((k + 5) % 3) * 8192 + p * 1024), 16, wvoff,
                                                 (64 * (k + 3) + 8 * p) * (NQ * 2), 0, 0);
      }
    };
    WAITV(16);
    __builtin_amdgcn_s_barrier();
    STAMP(11);
    rdstep(0, 0);
#pragma unroll
    for (int i = 0; i < 28; ++i) {
      const int bs = i & 1, ch = i >> 2, s4 = i & 3;
      if (i + 1 < 28) {
        if (s4 == 3) { if (ch == 0) WAITV(8); else WAITV(6); }
        rdstep(i + 1, bs ^ 1);
        asm volatile("s_waitcnt lgkmcnt(6)" : "+v"(fa[bs][0]), "+v"(fa[bs][1]), "+v"(fa[bs][2]), "+v"(fa[bs][3]), "+v"(fx[bs][0]), "+v"(fx[bs][1])::"memory");
      } else {
        asm volatile("s_waitcnt lgkmcnt(0)" : "+v"(fa[bs][0]), "+v"(fa[bs][1]), "+v"(fa[bs][2]), "+v"(fa[bs][3]), "+v"(fx[bs][0]), "+v"(fx[bs][1])::"memory");
      }
      bf16x8 A0 = cat8(fa[bs][0], fa[bs][1]), A1 = cat8(fa[bs][2], fa[bs][3]);
      qa[0][0] = __builtin_amdgcn_mfma_f32_32x32x16_bf16(A0, fx[bs][0], qa[0][0], 0, 0, 0);
      GAP();
      if (ch >= 1) {
        if (ch - 1 <= 3) refill_piece(ch - 1, 2 * s4);
        else if (ch == 5) { if (s4 == 0) dma_piece<0, 0>(c); else if (s4 == 1) dma_piece<0, 2>(c); else if (s4 == 2) dma_piece<0, 4>(c); else dma_piece<0, 6>(c); }
        else { if (s4 == 0) dma_piece<1, 0>(c); else if (s4 == 1) dma_piece<1, 2>(c); else if (s4 == 2) dma_piece<1, 4>(c); else dma_piece<1, 6>(c); }
      }
      GAP();
      qa[0][1] = __builtin_amdgcn_mfma_f32_32x32x16_bf16(A1, fx[bs][0], qa[0][1], 0, 0, 0);
      qa[1][0] = __builtin_amdgcn_mfma_f32_32x32x16_bf16(A0, fx[bs][1], qa[1][0], 0, 0, 0);
      GAP();
      if (ch >= 1) {
        if (ch - 1 <= 3) refill_piece(ch - 1, 2 * s4 + 1);
        else if (ch == 5) { if (s4 == 0) dma_piece<0, 1>(c); else if (s4 == 1) dma_piece<0, 3>(c); else if (s4 == 2) dma_piece<0, 5>(c); else dma_piece<0, 7>(c); }
        else { if (s4 == 0) dma_piece<1, 1>(c); else if (s4 == 1) dma_piece<1, 3>(c); else if (s4 == 2) dma_piece<1, 5>(c); else dma_piece<1, 7>(c); }
      }
      GAP();
      qa[1][1] = __builtin_amdgcn_mfma_f32_32x32x16_bf16(A1, fx[bs][1], qa[1][1], 0, 0, 0);
    }
    dma_unit<2>(c);
    STAMP(12);
    __builtin_amdgcn_s_barrier();
#pragma unroll
    for (int qb = 0; qb < 2; ++qb) {
      const int R = 32 * qb + r;
      char* base = lds + (R >> 4) * 40960 + 4 * 8192 + (R & 15) * 512 + 8 * h;
#pragma unroll
      for (int cb = 0; cb < 2; ++cb)
#pragma unroll
        for (int gg = 0; gg < 4; ++gg) {
          u32x2 v = {cvtpk(qa[qb][cb][4 * gg], qa[qb][cb][4 * gg + 1]), cvtpk(qa[qb][cb][4 * gg + 2], qa[qb][cb][4 * gg + 3])};
          *(u32x2*)(base + (((8 * w + 4 * cb + gg) ^ (R & 15)) * 16)) = v;
        }
    }
    WAITL0();
    __builtin_amdgcn_s_barrier();
  }
  {
#pragma unroll
    for (int qb = 0; qb < 2; ++qb) {
      const int R = 32 * qb + r;
      const char* base = lds + (R >> 4) * 40960 + 4 * 8192 + (R & 15) * 512;
#pragma unroll
      for (int s = 0; s < 16; ++s) c.qf[qb][s] = *(const bf16x8*)(base + (((2 * s + h) ^ (R & 15)) * 16));
    }
#pragma unroll
    for (int qb = 0; qb < 2; ++qb)
#pragma unroll
      for (int s = 0; s < 16; ++s) asm volatile("" : "+v"(c.qf[qb][s]));
  }
  WAITL0();
  __builtin_amdgcn_s_barrier();
  dma_unit<3>(c);
  dma_unit<4>(c);
  STAMP(1);

  {
    const unsigned rb = (unsigned)(size_t)c.ring;
#pragma unroll
    for (int ss = 0; ss < 8; ++ss) c.kaddr[ss] = rb + r * 256 + ((((2 * ss + h) ^ (r & 15))) * 16);
    const int g = lane >> 4, qq = (lane >> 2) & 3, pp = lane & 3;
#pragma unroll
    for (int d = 0; d < 4; ++d)
      c.vaddr[d] = rb + (4 * (g >> 1) + qq) * 512 + ((64 * d + 32 * (g & 1) + 8 * pp) ^ (qq << 6));
  }
  c.pend = false;
  c.fct[0] = 1.f; c.fct[1] = 1.f;

  phaseA<0>(c); phaseB<0, false, true>(c); STAMP(2);
  phaseA<1>(c); phaseB<1, true, true>(c); STAMP(3);
  phaseA<2>(c); phaseB<2, true, true>(c); STAMP(4);
  phaseA<3>(c); phaseB<3, true, true>(c); STAMP(5);
  phaseA<4>(c); phaseB<4, true, true>(c); STAMP(6);
  phaseA<5>(c); phaseB<5, true, true>(c); STAMP(7);
  phaseA<6>(c); phaseB<6, true, true>(c); STAMP(8);
  phaseA<7>(c); phaseB<7, true, true>(c); STAMP(9);
  phaseB<8, true, false>(c);
  asm volatile("s_nop 7\n\ts_nop 7" ::: "memory");
  STAMP(13);

  __syncthreads();
  float* ml = (float*)lds;
#pragma unroll
  for (int qb = 0; qb < 2; ++qb) {
    auto rr = __builtin_amdgcn_permlane32_swap(__float_as_uint(c.l[qb]), __float_as_uint(c.l[qb]), false, false);
    const float lt = __uint_as_float(rr[0]) + __uint_as_float(rr[1]);
    if (h == 0) {
      ml[((w * 2 + qb) * 32 + r) * 2 + 0] = c.m[qb];
      ml[((w * 2 + qb) * 32 + r) * 2 + 1] = lt;
    }
  }
  __syncthreads();
  float fw[2][4];
#pragma unroll
  for (int qb = 0; qb < 2; ++qb) {
    float mm[4], ll[4];
#pragma unroll
    for (int ww = 0; ww < 4; ++ww) {
      mm[ww] = ml[((ww * 2 + qb) * 32 + r) * 2 + 0];
      ll[ww] = ml[((ww * 2 + qb) * 32 + r) * 2 + 1];
    }
    const float M = fmaxf(fmaxf(mm[0], mm[1]), fmaxf(mm[2], mm[3]));
    float e[4], L = 0.f;
#pragma unroll
    for (int ww = 0; ww < 4; ++ww) { e[ww] = __builtin_amdgcn_exp2f(mm[ww] - M); L += e[ww] * ll[ww]; }
    const float inv = 1.f / L;
#pragma unroll
    for (int ww = 0; ww < 4; ++ww) fw[qb][ww] = e[ww] * inv;
  }
  STAMP(14);
  const unsigned xw = (unsigned)(size_t)lds + 4096 + lane * 16;
  const f32x4* xr = (const f32x4*)(lds + 4096) + lane;
  char* otile = lds + 102400;
  {
    switch (w) {
      case 0: {
        acc_dswrite4<32, 0>(xw + 0);
        acc_dswrite4<36, 1024>(xw + 0);
        acc_dswrite4<40, 2048>(xw + 0);
        acc_dswrite4<44, 3072>(xw + 0);
        acc_dswrite4<160, 4096>(xw + 0);
        acc_dswrite4<164, 5120>(xw + 0);
        acc_dswrite4<168, 6144>(xw + 0);
        acc_dswrite4<172, 7168>(xw + 0);
        acc_dswrite4<64, 8192>(xw + 0);
        acc_dswrite4<68, 9216>(xw + 0);
        acc_dswrite4<72, 10240>(xw + 0);
        acc_dswrite4<76, 11264>(xw + 0);
        acc_dswrite4<192, 12288>(xw + 0);
        acc_dswrite4<196, 13312>(xw + 0);
        acc_dswrite4<200, 14336>(xw + 0);
        acc_dswrite4<204, 15360>(xw + 0);
        acc_dswrite4<96, 16384>(xw + 0);
        acc_dswrite4<100, 17408>(xw + 0);
        acc_dswrite4<104, 18432>(xw + 0);
        acc_dswrite4<108, 19456>(xw + 0);
        acc_dswrite4<224, 20480>(xw + 0);
        acc_dswrite4<228, 21504>(xw + 0);
        acc_dswrite4<232, 22528>(xw + 0);
        acc_dswrite4<236, 23552>(xw + 0);
      } break;
      case 1: {
        acc_dswrite4<0, 24576>(xw + 0);
        acc_dswrite4<4, 25600>(xw + 0);
        acc_dswrite4<8, 26624>(xw + 0);
        acc_dswrite4<12, 27648>(xw + 0);
        acc_dswrite4<128, 28672>(xw + 0);
        acc_dswrite4<132, 29696>(xw + 0);
        acc_dswrite4<136, 30720>(xw + 0);
        acc_dswrite4<140, 31744>(xw + 0);
        acc_dswrite4<64, 32768>(xw + 0);
        acc_dswrite4<68, 33792>(xw + 0);
        acc_dswrite4<72, 34816>(xw + 0);
        acc_dswrite4<76, 35840>(xw + 0);
        acc_dswrite4<192, 36864>(xw + 0);
        acc_dswrite4<196, 37888>(xw + 0);
        acc_dswrite4<200, 38912>(xw + 0);
        acc_dswrite4<204, 39936>(xw + 0);
        acc_dswrite4<96, 40960>(xw + 0);
        acc_dswrite4<100, 41984>(xw + 0);
        acc_dswrite4<104, 43008>(xw + 0);
        acc_dswrite4<108, 44032>(xw + 0);
        acc_dswrite4<224, 45056>(xw + 0);
        acc_dswrite4<228, 46080>(xw + 0);
        acc_dswrite4<232, 47104>(xw + 0);
        acc_dswrite4<236, 48128>(xw + 0);
      } break;
      case 2: {
        acc_dswrite4<0, 49152>(xw + 0);
        acc_dswrite4<4, 50176>(xw + 0);
        acc_dswrite4<8, 51200>(xw + 0);
        acc_dswrite4<12, 52224>(xw + 0);
        acc_dswrite4<128, 53248>(xw + 0);
        acc_dswrite4<132, 54272>(xw + 0);
        acc_dswrite4<136, 55296>(xw + 0);
        acc_dswrite4<140, 56320>(xw + 0);
        acc_dswrite4<32, 57344>(xw + 0);
        acc_dswrite4<36, 58368>(xw + 0);
        acc_dswrite4<40, 59392>(xw + 0);
        acc_dswrite4<44, 60416>(xw + 0);
        acc_dswrite4<160, 61440>(xw + 0);
        acc_dswrite4<164, 62464>(xw + 0);
        acc_dswrite4<168, 63488>(xw + 0);
        acc_dswrite4<172, 64512>(xw + 0);
        acc_dswrite4<96, 0>(xw + 65536);
        acc_dswrite4<100, 1024>(xw + 65536);
        acc_dswrite4<104, 2048>(xw + 65536);
        acc_dswrite4<108, 3072>(xw + 65536);
        acc_dswrite4<224, 4096>(xw + 65536);
        acc_dswrite4<228, 5120>(xw + 65536);
        acc_dswrite4<232, 6144>(xw + 65536);
        acc_dswrite4<236, 7168>(xw + 65536);
      } break;
      case 3: {
        acc_dswrite4<0, 8192>(xw + 65536);
        acc_dswrite4<4, 9216>(xw + 65536);
        acc_dswrite4<8, 10240>(xw + 65536);
        acc_dswrite4<12, 11264>(xw + 65536);
        acc_dswrite4<128, 12288>(xw + 65536);
        acc_dswrite4<132, 13312>(xw + 65536);
        acc_dswrite4<136, 14336>(xw + 65536);
        acc_dswrite4<140, 15360>(xw + 65536);
        acc_dswrite4<32, 16384>(xw + 65536);
        acc_dswrite4<36, 17408>(xw + 65536);
        acc_dswrite4<40, 18432>(xw + 65536);
        acc_dswrite4<44, 19456>(xw + 65536);
        acc_dswrite4<160, 20480>(xw + 65536);
        acc_dswrite4<164, 21504>(xw + 65536);
        acc_dswrite4<168, 22528>(xw + 65536);
        acc_dswrite4<172, 23552>(xw + 65536);
        acc_dswrite4<64, 24576>(xw + 65536);
        acc_dswrite4<68, 25600>(xw + 65536);
        acc_dswrite4<72, 26624>(xw + 65536);
        acc_dswrite4<76, 27648>(xw + 65536);
        acc_dswrite4<192, 28672>(xw + 65536);
        acc_dswrite4<196, 29696>(xw + 65536);
        acc_dswrite4<200, 30720>(xw + 65536);
        acc_dswrite4<204, 31744>(xw + 65536);
      } break;
    }
    WAITL0();
    __builtin_amdgcn_s_barrier();
    switch (w) {
      case 0: {
        { f32x4 sum = acc_read4<0>(); sum *= fw[0][0];
          sum += xr[1536] * fw[0][1];
          sum += xr[3072] * fw[0][2];
          sum += xr[4608] * fw[0][3];
          u32x2 v = {cvtpk(sum[0], sum[1]), cvtpk(sum[2], sum[3])};
          *(u32x2*)(otile + (0 + r) * 512 + (((0) ^ ((0 + r) & 31)) * 16) + 8 * h) = v; }
        { f32x4 sum = acc_read4<4>(); sum *= fw[0][0];
          sum += xr[1600] * fw[0][1];
          sum += xr[3136] * fw[0][2];
          sum += xr[4672] * fw[0][3];
          u32x2 v = {cvtpk(sum[0], sum[1]), cvtpk(sum[2], sum[3])};
          *(u32x2*)(otile + (0 + r) * 512 + (((1) ^ ((0 + r) & 31)) * 16) + 8 * h) = v; }
        { f32x4 sum = acc_read4<8>(); sum *= fw[0][0];
          sum += xr[1664] * fw[0][1];
          sum += xr[3200] * fw[0][2];
          sum += xr[4736] * fw[0][3];
          u32x2 v = {cvtpk(sum[0], sum[1]), cvtpk(sum[2], sum[3])};
          *(u32x2*)(otile + (0 + r) * 512 + (((2) ^ ((0 + r) & 31)) * 16) + 8 * h) = v; }
        { f32x4 sum = acc_read4<12>(); sum *= fw[0][0];
          sum += xr[1728] * fw[0][1];
          sum += xr[3264] * fw[0][2];
          sum += xr[4800] * fw[0][3];
          u32x2 v = {cvtpk(sum[0], sum[1]), cvtpk(sum[2], sum[3])};
          *(u32x2*)(otile + (0 + r) * 512 + (((3) ^ ((0 + r) & 31)) * 16) + 8 * h) = v; }
        { f32x4 sum = acc_read4<128>(); sum *= fw[1][0];
          sum += xr[1792] * fw[1][1];
          sum += xr[3328] * fw[1][2];
          sum += xr[4864] * fw[1][3];
          u32x2 v = {cvtpk(sum[0], sum[1]), cvtpk(sum[2], sum[3])};
          *(u32x2*)(otile + (32 + r) * 512 + (((0) ^ ((32 + r) & 31)) * 16) + 8 * h) = v; }
        { f32x4 sum = acc_read4<132>(); sum *= fw[1][0];
          sum += xr[1856] * fw[1][1];
          sum += xr[3392] * fw[1][2];
          sum += xr[4928] * fw[1][3];
          u32x2 v = {cvtpk(sum[0], sum[1]), cvtpk(sum[2], sum[3])};
          *(u32x2*)(otile + (32 + r) * 512 + (((1) ^ ((32 + r) & 31)) * 16) + 8 * h) = v; }
        { f32x4 sum = acc_read4<136>(); sum *= fw[1][0];
          sum += xr[1920] * fw[1][1];
          sum += xr[3456] * fw[1][2];
          sum += xr[4992] * fw[1][3];
          u32x2 v = {cvtpk(sum[0], sum[1]), cvtpk(sum[2], sum[3])};
          *(u32x2*)(otile + (32 + r) * 512 + (((2) ^ ((32 + r) & 31)) * 16) + 8 * h) = v; }
        { f32x4 sum = acc_read4<140>(); sum *= fw[1][0];
          sum += xr[1984] * fw[1][1];
          sum += xr[3520] * fw[1][2];
          sum += xr[5056] * fw[1][3];
          u32x2 v = {cvtpk(sum[0], sum[1]), cvtpk(sum[2], sum[3])};
          *(u32x2*)(otile + (32 + r) * 512 + (((3) ^ ((32 + r) & 31)) * 16) + 8 * h) = v; }
      } break;
      case 1: {
        { f32x4 sum = acc_read4<32>(); sum *= fw[0][1];
          sum += xr[0] * fw[0][0];
          sum += xr[3584] * fw[0][2];
          sum += xr[5120] * fw[0][3];
          u32x2 v = {cvtpk(sum[0], sum[1]), cvtpk(sum[2], sum[3])};
          *(u32x2*)(otile + (0 + r) * 512 + (((8) ^ ((0 + r) & 31)) * 16) + 8 * h) = v; }
        { f32x4 sum = acc_read4<36>(); sum *= fw[0][1];
          sum += xr[64] * fw[0][0];
          sum += xr[3648] * fw[0][2];
          sum += xr[5184] * fw[0][3];
          u32x2 v = {cvtpk(sum[0], sum[1]), cvtpk(sum[2], sum[3])};
          *(u32x2*)(otile + (0 + r) * 512 + (((9) ^ ((0 + r) & 31)) * 16) + 8 * h) = v; }
        { f32x4 sum = acc_read4<40>(); sum *= fw[0][1];
          sum += xr[128] * fw[0][0];
          sum += xr[3712] * fw[0][2];
          sum += xr[5248] * fw[0][3];
          u32x2 v = {cvtpk(sum[0], sum[1]), cvtpk(sum[2], sum[3])};
          *(u32x2*)(otile + (0 + r) * 512 + (((10) ^ ((0 + r) & 31)) * 16) + 8 * h) = v; }
        { f32x4 sum = acc_read4<44>(); sum *= fw[0][1];
          sum += xr[192] * fw[0][0];
          sum += xr[3776] * fw[0][2];
          sum += xr[5312] * fw[0][3];
          u32x2 v = {cvtpk(sum[0], sum[1]), cvtpk(sum[2], sum[3])};
          *(u32x2*)(otile + (0 + r) * 512 + (((11) ^ ((0 + r) & 31)) * 16) + 8 * h) = v; }
        { f32x4 sum = acc_read4<160>(); sum *= fw[1][1];
          sum += xr[256] * fw[1][0];
          sum += xr[3840] * fw[1][2];
          sum += xr[5376] * fw[1][3];
          u32x2 v = {cvtpk(sum[0], sum[1]), cvtpk(sum[2], sum[3])};
          *(u32x2*)(otile + (32 + r) * 512 + (((8) ^ ((32 + r) & 31)) * 16) + 8 * h) = v; }
        { f32x4 sum = acc_read4<164>(); sum *= fw[1][1];
          sum += xr[320] * fw[1][0];
          sum += xr[3904] * fw[1][2];
          sum += xr[5440] * fw[1][3];
          u32x2 v = {cvtpk(sum[0], sum[1]), cvtpk(sum[2], sum[3])};
          *(u32x2*)(otile + (32 + r) * 512 + (((9) ^ ((32 + r) & 31)) * 16) + 8 * h) = v; }
        { f32x4 sum = acc_read4<168>(); sum *= fw[1][1];
          sum += xr[384] * fw[1][0];
          sum += xr[3968] * fw[1][2];
          sum += xr[5504] * fw[1][3];
          u32x2 v = {cvtpk(sum[0], sum[1]), cvtpk(sum[2], sum[3])};
          *(u32x2*)(otile + (32 + r) * 512 + (((10) ^ ((32 + r) & 31)) * 16) + 8 * h) = v; }
        { f32x4 sum = acc_read4<172>(); sum *= fw[1][1];
          sum += xr[448] * fw[1][0];
          sum += xr[4032] * fw[1][2];
          sum += xr[5568] * fw[1][3];
          u32x2 v = {cvtpk(sum[0], sum[1]), cvtpk(sum[2], sum[3])};
          *(u32x2*)(otile + (32 + r) * 512 + (((11) ^ ((32 + r) & 31)) * 16) + 8 * h) = v; }
      } break;
      case 2: {
        { f32x4 sum = acc_read4<64>(); sum *= fw[0][2];
          sum += xr[512] * fw[0][0];
          sum += xr[2048] * fw[0][1];
          sum += xr[5632] * fw[0][3];
          u32x2 v = {cvtpk(sum[0], sum[1]), cvtpk(sum[2], sum[3])};
          *(u32x2*)(otile + (0 + r) * 512 + (((16) ^ ((0 + r) & 31)) * 16) + 8 * h) = v; }
        { f32x4 sum = acc_read4<68>(); sum *= fw[0][2];
          sum += xr[576] * fw[0][0];
          sum += xr[2112] * fw[0][1];
          sum += xr[5696] * fw[0][3];
          u32x2 v = {cvtpk(sum[0], sum[1]), cvtpk(sum[2], sum[3])};
          *(u32x2*)(otile + (0 + r) * 512 + (((17) ^ ((0 + r) & 31)) * 16) + 8 * h) = v; }
        { f32x4 sum = acc_read4<72>(); sum *= fw[0][2];
          sum += xr[640] * fw[0][0];
          sum += xr[2176] * fw[0][1];
          sum += xr[5760] * fw[0][3];
          u32x2 v = {cvtpk(sum[0], sum[1]), cvtpk(sum[2], sum[3])};
          *(u32x2*)(otile + (0 + r) * 512 + (((18) ^ ((0 + r) & 31)) * 16) + 8 * h) = v; }
        { f32x4 sum = acc_read4<76>(); sum *= fw[0][2];
          sum += xr[704] * fw[0][0];
          sum += xr[2240] * fw[0][1];
          sum += xr[5824] * fw[0][3];
          u32x2 v = {cvtpk(sum[0], sum[1]), cvtpk(sum[2], sum[3])};
          *(u32x2*)(otile + (0 + r) * 512 + (((19) ^ ((0 + r) & 31)) * 16) + 8 * h) = v; }
        { f32x4 sum = acc_read4<192>(); sum *= fw[1][2];
          sum += xr[768] * fw[1][0];
          sum += xr[2304] * fw[1][1];
          sum += xr[5888] * fw[1][3];
          u32x2 v = {cvtpk(sum[0], sum[1]), cvtpk(sum[2], sum[3])};
          *(u32x2*)(otile + (32 + r) * 512 + (((16) ^ ((32 + r) & 31)) * 16) + 8 * h) = v; }
        { f32x4 sum = acc_read4<196>(); sum *= fw[1][2];
          sum += xr[832] * fw[1][0];
          sum += xr[2368] * fw[1][1];
          sum += xr[5952] * fw[1][3];
          u32x2 v = {cvtpk(sum[0], sum[1]), cvtpk(sum[2], sum[3])};
          *(u32x2*)(otile + (32 + r) * 512 + (((17) ^ ((32 + r) & 31)) * 16) + 8 * h) = v; }
        { f32x4 sum = acc_read4<200>(); sum *= fw[1][2];
          sum += xr[896] * fw[1][0];
          sum += xr[2432] * fw[1][1];
          sum += xr[6016] * fw[1][3];
          u32x2 v = {cvtpk(sum[0], sum[1]), cvtpk(sum[2], sum[3])};
          *(u32x2*)(otile + (32 + r) * 512 + (((18) ^ ((32 + r) & 31)) * 16) + 8 * h) = v; }
        { f32x4 sum = acc_read4<204>(); sum *= fw[1][2];
          sum += xr[960] * fw[1][0];
          sum += xr[2496] * fw[1][1];
          sum += xr[6080] * fw[1][3];
          u32x2 v = {cvtpk(sum[0], sum[1]), cvtpk(sum[2], sum[3])};
          *(u32x2*)(otile + (32 + r) * 512 + (((19) ^ ((32 + r) & 31)) * 16) + 8 * h) = v; }
      } break;
      case 3: {
        { f32x4 sum = acc_read4<96>(); sum *= fw[0][3];
          sum += xr[1024] * fw[0][0];
          sum += xr[2560] * fw[0][1];
          sum += xr[4096] * fw[0][2];
          u32x2 v = {cvtpk(sum[0], sum[1]), cvtpk(sum[2], sum[3])};
          *(u32x2*)(otile + (0 + r) * 512 + (((24) ^ ((0 + r) & 31)) * 16) + 8 * h) = v; }
        { f32x4 sum = acc_read4<100>(); sum *= fw[0][3];
          sum += xr[1088] * fw[0][0];
          sum += xr[2624] * fw[0][1];
          sum += xr[4160] * fw[0][2];
          u32x2 v = {cvtpk(sum[0], sum[1]), cvtpk(sum[2], sum[3])};
          *(u32x2*)(otile + (0 + r) * 512 + (((25) ^ ((0 + r) & 31)) * 16) + 8 * h) = v; }
        { f32x4 sum = acc_read4<104>(); sum *= fw[0][3];
          sum += xr[1152] * fw[0][0];
          sum += xr[2688] * fw[0][1];
          sum += xr[4224] * fw[0][2];
          u32x2 v = {cvtpk(sum[0], sum[1]), cvtpk(sum[2], sum[3])};
          *(u32x2*)(otile + (0 + r) * 512 + (((26) ^ ((0 + r) & 31)) * 16) + 8 * h) = v; }
        { f32x4 sum = acc_read4<108>(); sum *= fw[0][3];
          sum += xr[1216] * fw[0][0];
          sum += xr[2752] * fw[0][1];
          sum += xr[4288] * fw[0][2];
          u32x2 v = {cvtpk(sum[0], sum[1]), cvtpk(sum[2], sum[3])};
          *(u32x2*)(otile + (0 + r) * 512 + (((27) ^ ((0 + r) & 31)) * 16) + 8 * h) = v; }
        { f32x4 sum = acc_read4<224>(); sum *= fw[1][3];
          sum += xr[1280] * fw[1][0];
          sum += xr[2816] * fw[1][1];
          sum += xr[4352] * fw[1][2];
          u32x2 v = {cvtpk(sum[0], sum[1]), cvtpk(sum[2], sum[3])};
          *(u32x2*)(otile + (32 + r) * 512 + (((24) ^ ((32 + r) & 31)) * 16) + 8 * h) = v; }
        { f32x4 sum = acc_read4<228>(); sum *= fw[1][3];
          sum += xr[1344] * fw[1][0];
          sum += xr[2880] * fw[1][1];
          sum += xr[4416] * fw[1][2];
          u32x2 v = {cvtpk(sum[0], sum[1]), cvtpk(sum[2], sum[3])};
          *(u32x2*)(otile + (32 + r) * 512 + (((25) ^ ((32 + r) & 31)) * 16) + 8 * h) = v; }
        { f32x4 sum = acc_read4<232>(); sum *= fw[1][3];
          sum += xr[1408] * fw[1][0];
          sum += xr[2944] * fw[1][1];
          sum += xr[4480] * fw[1][2];
          u32x2 v = {cvtpk(sum[0], sum[1]), cvtpk(sum[2], sum[3])};
          *(u32x2*)(otile + (32 + r) * 512 + (((26) ^ ((32 + r) & 31)) * 16) + 8 * h) = v; }
        { f32x4 sum = acc_read4<236>(); sum *= fw[1][3];
          sum += xr[1472] * fw[1][0];
          sum += xr[3008] * fw[1][1];
          sum += xr[4544] * fw[1][2];
          u32x2 v = {cvtpk(sum[0], sum[1]), cvtpk(sum[2], sum[3])};
          *(u32x2*)(otile + (32 + r) * 512 + (((27) ^ ((32 + r) & 31)) * 16) + 8 * h) = v; }
      } break;
    }
    WAITL0();
    __builtin_amdgcn_s_barrier();
  }
  {
    switch (w) {
      case 0: {
        acc_dswrite4<48, 0>(xw + 0);
        acc_dswrite4<52, 1024>(xw + 0);
        acc_dswrite4<56, 2048>(xw + 0);
        acc_dswrite4<60, 3072>(xw + 0);
        acc_dswrite4<176, 4096>(xw + 0);
        acc_dswrite4<180, 5120>(xw + 0);
        acc_dswrite4<184, 6144>(xw + 0);
        acc_dswrite4<188, 7168>(xw + 0);
        acc_dswrite4<80, 8192>(xw + 0);
        acc_dswrite4<84, 9216>(xw + 0);
        acc_dswrite4<88, 10240>(xw + 0);
        acc_dswrite4<92, 11264>(xw + 0);
        acc_dswrite4<208, 12288>(xw + 0);
        acc_dswrite4<212, 13312>(xw + 0);
        acc_dswrite4<216, 14336>(xw + 0);
        acc_dswrite4<220, 15360>(xw + 0);
        acc_dswrite4<112, 16384>(xw + 0);
        acc_dswrite4<116, 17408>(xw + 0);
        acc_dswrite4<120, 18432>(xw + 0);
        acc_dswrite4<124, 19456>(xw + 0);
        acc_dswrite4<240, 20480>(xw + 0);
        acc_dswrite4<244, 21504>(xw + 0);
        acc_dswrite4<248, 22528>(xw + 0);
        acc_dswrite4<252, 23552>(xw + 0);
      } break;
      case 1: {
        acc_dswrite4<16, 24576>(xw + 0);
        acc_dswrite4<20, 25600>(xw + 0);
        acc_dswrite4<24, 26624>(xw + 0);
        acc_dswrite4<28, 27648>(xw + 0);
        acc_dswrite4<144, 28672>(xw + 0);
        acc_dswrite4<148, 29696>(xw + 0);
        acc_dswrite4<152, 30720>(xw + 0);
        acc_dswrite4<156, 31744>(xw + 0);
        acc_dswrite4<80, 32768>(xw + 0);
        acc_dswrite4<84, 33792>(xw + 0);
        acc_dswrite4<88, 34816>(xw + 0);
        acc_dswrite4<92, 35840>(xw + 0);
        acc_dswrite4<208, 36864>(xw + 0);
        acc_dswrite4<212, 37888>(xw + 0);
        acc_dswrite4<216, 38912>(xw + 0);
        acc_dswrite4<220, 39936>(xw + 0);
        acc_dswrite4<112, 40960>(xw + 0);
        acc_dswrite4<116, 41984>(xw + 0);
        acc_dswrite4<120, 43008>(xw + 0);
        acc_dswrite4<124, 44032>(xw + 0);
        acc_dswrite4<240, 45056>(xw + 0);
        acc_dswrite4<244, 46080>(xw + 0);
        acc_dswrite4<248, 47104>(xw + 0);
        acc_dswrite4<252, 48128>(xw + 0);
      } break;
      case 2: {
        acc_dswrite4<16, 49152>(xw + 0);
        acc_dswrite4<20, 50176>(xw + 0);
        acc_dswrite4<24, 51200>(xw + 0);
        acc_dswrite4<28, 52224>(xw + 0);
        acc_dswrite4<144, 53248>(xw + 0);
        acc_dswrite4<148, 54272>(xw + 0);
        acc_dswrite4<152, 55296>(xw + 0);
        acc_dswrite4<156, 56320>(xw + 0);
        acc_dswrite4<48, 57344>(xw + 0);
        acc_dswrite4<52, 58368>(xw + 0);
        acc_dswrite4<56, 59392>(xw + 0);
        acc_dswrite4<60, 60416>(xw + 0);
        acc_dswrite4<176, 61440>(xw + 0);
        acc_dswrite4<180, 62464>(xw + 0);
        acc_dswrite4<184, 63488>(xw + 0);
        acc_dswrite4<188, 64512>(xw + 0);
        acc_dswrite4<112, 0>(xw + 65536);
        acc_dswrite4<116, 1024>(xw + 65536);
        acc_dswrite4<120, 2048>(xw + 65536);
        acc_dswrite4<124, 3072>(xw + 65536);
        acc_dswrite4<240, 4096>(xw + 65536);
        acc_dswrite4<244, 5120>(xw + 65536);
        acc_dswrite4<248, 6144>(xw + 65536);
        acc_dswrite4<252, 7168>(xw + 65536);
      } break;
      case 3: {
        acc_dswrite4<16, 8192>(xw + 65536);
        acc_dswrite4<20, 9216>(xw + 65536);
        acc_dswrite4<24, 10240>(xw + 65536);
        acc_dswrite4<28, 11264>(xw + 65536);
        acc_dswrite4<144, 12288>(xw + 65536);
        acc_dswrite4<148, 13312>(xw + 65536);
        acc_dswrite4<152, 14336>(xw + 65536);
        acc_dswrite4<156, 15360>(xw + 65536);
        acc_dswrite4<48, 16384>(xw + 65536);
        acc_dswrite4<52, 17408>(xw + 65536);
        acc_dswrite4<56, 18432>(xw + 65536);
        acc_dswrite4<60, 19456>(xw + 65536);
        acc_dswrite4<176, 20480>(xw + 65536);
        acc_dswrite4<180, 21504>(xw + 65536);
        acc_dswrite4<184, 22528>(xw + 65536);
        acc_dswrite4<188, 23552>(xw + 65536);
        acc_dswrite4<80, 24576>(xw + 65536);
        acc_dswrite4<84, 25600>(xw + 65536);
        acc_dswrite4<88, 26624>(xw + 65536);
        acc_dswrite4<92, 27648>(xw + 65536);
        acc_dswrite4<208, 28672>(xw + 65536);
        acc_dswrite4<212, 29696>(xw + 65536);
        acc_dswrite4<216, 30720>(xw + 65536);
        acc_dswrite4<220, 31744>(xw + 65536);
      } break;
    }
    WAITL0();
    __builtin_amdgcn_s_barrier();
    switch (w) {
      case 0: {
        { f32x4 sum = acc_read4<16>(); sum *= fw[0][0];
          sum += xr[1536] * fw[0][1];
          sum += xr[3072] * fw[0][2];
          sum += xr[4608] * fw[0][3];
          u32x2 v = {cvtpk(sum[0], sum[1]), cvtpk(sum[2], sum[3])};
          *(u32x2*)(otile + (0 + r) * 512 + (((4) ^ ((0 + r) & 31)) * 16) + 8 * h) = v; }
        { f32x4 sum = acc_read4<20>(); sum *= fw[0][0];
          sum += xr[1600] * fw[0][1];
          sum += xr[3136] * fw[0][2];
          sum += xr[4672] * fw[0][3];
          u32x2 v = {cvtpk(sum[0], sum[1]), cvtpk(sum[2], sum[3])};
          *(u32x2*)(otile + (0 + r) * 512 + (((5) ^ ((0 + r) & 31)) * 16) + 8 * h) = v; }
        { f32x4 sum = acc_read4<24>(); sum *= fw[0][0];
          sum += xr[1664] * fw[0][1];
          sum += xr[3200] * fw[0][2];
          sum += xr[4736] * fw[0][3];
          u32x2 v = {cvtpk(sum[0], sum[1]), cvtpk(sum[2], sum[3])};
          *(u32x2*)(otile + (0 + r) * 512 + (((6) ^ ((0 + r) & 31)) * 16) + 8 * h) = v; }
        { f32x4 sum = acc_read4<28>(); sum *= fw[0][0];
          sum += xr[1728] * fw[0][1];
          sum += xr[3264] * fw[0][2];
          sum += xr[4800] * fw[0][3];
          u32x2 v = {cvtpk(sum[0], sum[1]), cvtpk(sum[2], sum[3])};
          *(u32x2*)(otile + (0 + r) * 512 + (((7) ^ ((0 + r) & 31)) * 16) + 8 * h) = v; }
        { f32x4 sum = acc_read4<144>(); sum *= fw[1][0];
          sum += xr[1792] * fw[1][1];
          sum += xr[3328] * fw[1][2];
          sum += xr[4864] * fw[1][3];
          u32x2 v = {cvtpk(sum[0], sum[1]), cvtpk(sum[2], sum[3])};
          *(u32x2*)(otile + (32 + r) * 512 + (((4) ^ ((32 + r) & 31)) * 16) + 8 * h) = v; }
        { f32x4 sum = acc_read4<148>(); sum *= fw[1][0];
          sum += xr[1856] * fw[1][1];
          sum += xr[3392] * fw[1][2];
          sum += xr[4928] * fw[1][3];
          u32x2 v = {cvtpk(sum[0], sum[1]), cvtpk(sum[2], sum[3])};
          *(u32x2*)(otile + (32 + r) * 512 + (((5) ^ ((32 + r) & 31)) * 16) + 8 * h) = v; }
        { f32x4 sum = acc_read4<152>(); sum *= fw[1][0];
          sum += xr[1920] * fw[1][1];
          sum += xr[3456] * fw[1][2];
          sum += xr[4992] * fw[1][3];
          u32x2 v = {cvtpk(sum[0], sum[1]), cvtpk(sum[2], sum[3])};
          *(u32x2*)(otile + (32 + r) * 512 + (((6) ^ ((32 + r) & 31)) * 16) + 8 * h) = v; }
        { f32x4 sum = acc_read4<156>(); sum *= fw[1][0];
          sum += xr[1984] * fw[1][1];
          sum += xr[3520] * fw[1][2];
          sum += xr[5056] * fw[1][3];
          u32x2 v = {cvtpk(sum[0], sum[1]), cvtpk(sum[2], sum[3])};
          *(u32x2*)(otile + (32 + r) * 512 + (((7) ^ ((32 + r) & 31)) * 16) + 8 * h) = v; }
      } break;
      case 1: {
        { f32x4 sum = acc_read4<48>(); sum *= fw[0][1];
          sum += xr[0] * fw[0][0];
          sum += xr[3584] * fw[0][2];
          sum += xr[5120] * fw[0][3];
          u32x2 v = {cvtpk(sum[0], sum[1]), cvtpk(sum[2], sum[3])};
          *(u32x2*)(otile + (0 + r) * 512 + (((12) ^ ((0 + r) & 31)) * 16) + 8 * h) = v; }
        { f32x4 sum = acc_read4<52>(); sum *= fw[0][1];
          sum += xr[64] * fw[0][0];
          sum += xr[3648] * fw[0][2];
          sum += xr[5184] * fw[0][3];
          u32x2 v = {cvtpk(sum[0], sum[1]), cvtpk(sum[2], sum[3])};
          *(u32x2*)(otile + (0 + r) * 512 + (((13) ^ ((0 + r) & 31)) * 16) + 8 * h) = v; }
        { f32x4 sum = acc_read4<56>(); sum *= fw[0][1];
          sum += xr[128] * fw[0][0];
          sum += xr[3712] * fw[0][2];
          sum += xr[5248] * fw[0][3];
          u32x2 v = {cvtpk(sum[0], sum[1]), cvtpk(sum[2], sum[3])};
          *(u32x2*)(otile + (0 + r) * 512 + (((14) ^ ((0 + r) & 31)) * 16) + 8 * h) = v; }
        { f32x4 sum = acc_read4<60>(); sum *= fw[0][1];
          sum += xr[192] * fw[0][0];
          sum += xr[3776] * fw[0][2];
          sum += xr[5312] * fw[0][3];
          u32x2 v = {cvtpk(sum[0], sum[1]), cvtpk(sum[2], sum[3])};
          *(u32x2*)(otile + (0 + r) * 512 + (((15) ^ ((0 + r) & 31)) * 16) + 8 * h) = v; }
        { f32x4 sum = acc_read4<176>(); sum *= fw[1][1];
          sum += xr[256] * fw[1][0];
          sum += xr[3840] * fw[1][2];
          sum += xr[5376] * fw[1][3];
          u32x2 v = {cvtpk(sum[0], sum[1]), cvtpk(sum[2], sum[3])};
          *(u32x2*)(otile + (32 + r) * 512 + (((12) ^ ((32 + r) & 31)) * 16) + 8 * h) = v; }
        { f32x4 sum = acc_read4<180>(); sum *= fw[1][1];
          sum += xr[320] * fw[1][0];
          sum += xr[3904] * fw[1][2];
          sum += xr[5440] * fw[1][3];
          u32x2 v = {cvtpk(sum[0], sum[1]), cvtpk(sum[2], sum[3])};
          *(u32x2*)(otile + (32 + r) * 512 + (((13) ^ ((32 + r) & 31)) * 16) + 8 * h) = v; }
        { f32x4 sum = acc_read4<184>(); sum *= fw[1][1];
          sum += xr[384] * fw[1][0];
          sum += xr[3968] * fw[1][2];
          sum += xr[5504] * fw[1][3];
          u32x2 v = {cvtpk(sum[0], sum[1]), cvtpk(sum[2], sum[3])};
          *(u32x2*)(otile + (32 + r) * 512 + (((14) ^ ((32 + r) & 31)) * 16) + 8 * h) = v; }
        { f32x4 sum = acc_read4<188>(); sum *= fw[1][1];
          sum += xr[448] * fw[1][0];
          sum += xr[4032] * fw[1][2];
          sum += xr[5568] * fw[1][3];
          u32x2 v = {cvtpk(sum[0], sum[1]), cvtpk(sum[2], sum[3])};
          *(u32x2*)(otile + (32 + r) * 512 + (((15) ^ ((32 + r) & 31)) * 16) + 8 * h) = v; }
      } break;
      case 2: {
        { f32x4 sum = acc_read4<80>(); sum *= fw[0][2];
          sum += xr[512] * fw[0][0];
          sum += xr[2048] * fw[0][1];
          sum += xr[5632] * fw[0][3];
          u32x2 v = {cvtpk(sum[0], sum[1]), cvtpk(sum[2], sum[3])};
          *(u32x2*)(otile + (0 + r) * 512 + (((20) ^ ((0 + r) & 31)) * 16) + 8 * h) = v; }
        { f32x4 sum = acc_read4<84>(); sum *= fw[0][2];
          sum += xr[576] * fw[0][0];
          sum += xr[2112] * fw[0][1];
          sum += xr[5696] * fw[0][3];
          u32x2 v = {cvtpk(sum[0], sum[1]), cvtpk(sum[2], sum[3])};
          *(u32x2*)(otile + (0 + r) * 512 + (((21) ^ ((0 + r) & 31)) * 16) + 8 * h) = v; }
        { f32x4 sum = acc_read4<88>(); sum *= fw[0][2];
          sum += xr[640] * fw[0][0];
          sum += xr[2176] * fw[0][1];
          sum += xr[5760] * fw[0][3];
          u32x2 v = {cvtpk(sum[0], sum[1]), cvtpk(sum[2], sum[3])};
          *(u32x2*)(otile + (0 + r) * 512 + (((22) ^ ((0 + r) & 31)) * 16) + 8 * h) = v; }
        { f32x4 sum = acc_read4<92>(); sum *= fw[0][2];
          sum += xr[704] * fw[0][0];
          sum += xr[2240] * fw[0][1];
          sum += xr[5824] * fw[0][3];
          u32x2 v = {cvtpk(sum[0], sum[1]), cvtpk(sum[2], sum[3])};
          *(u32x2*)(otile + (0 + r) * 512 + (((23) ^ ((0 + r) & 31)) * 16) + 8 * h) = v; }
        { f32x4 sum = acc_read4<208>(); sum *= fw[1][2];
          sum += xr[768] * fw[1][0];
          sum += xr[2304] * fw[1][1];
          sum += xr[5888] * fw[1][3];
          u32x2 v = {cvtpk(sum[0], sum[1]), cvtpk(sum[2], sum[3])};
          *(u32x2*)(otile + (32 + r) * 512 + (((20) ^ ((32 + r) & 31)) * 16) + 8 * h) = v; }
        { f32x4 sum = acc_read4<212>(); sum *= fw[1][2];
          sum += xr[832] * fw[1][0];
          sum += xr[2368] * fw[1][1];
          sum += xr[5952] * fw[1][3];
          u32x2 v = {cvtpk(sum[0], sum[1]), cvtpk(sum[2], sum[3])};
          *(u32x2*)(otile + (32 + r) * 512 + (((21) ^ ((32 + r) & 31)) * 16) + 8 * h) = v; }
        { f32x4 sum = acc_read4<216>(); sum *= fw[1][2];
          sum += xr[896] * fw[1][0];
          sum += xr[2432] * fw[1][1];
          sum += xr[6016] * fw[1][3];
          u32x2 v = {cvtpk(sum[0], sum[1]), cvtpk(sum[2], sum[3])};
          *(u32x2*)(otile + (32 + r) * 512 + (((22) ^ ((32 + r) & 31)) * 16) + 8 * h) = v; }
        { f32x4 sum = acc_read4<220>(); sum *= fw[1][2];
          sum += xr[960] * fw[1][0];
          sum += xr[2496] * fw[1][1];
          sum += xr[6080] * fw[1][3];
          u32x2 v = {cvtpk(sum[0], sum[1]), cvtpk(sum[2], sum[3])};
          *(u32x2*)(otile + (32 + r) * 512 + (((23) ^ ((32 + r) & 31)) * 16) + 8 * h) = v; }
      } break;
      case 3: {
        { f32x4 sum = acc_read4<112>(); sum *= fw[0][3];
          sum += xr[1024] * fw[0][0];
          sum += xr[2560] * fw[0][1];
          sum += xr[4096] * fw[0][2];
          u32x2 v = {cvtpk(sum[0], sum[1]), cvtpk(sum[2], sum[3])};
          *(u32x2*)(otile + (0 + r) * 512 + (((28) ^ ((0 + r) & 31)) * 16) + 8 * h) = v; }
        { f32x4 sum = acc_read4<116>(); sum *= fw[0][3];
          sum += xr[1088] * fw[0][0];
          sum += xr[2624] * fw[0][1];
          sum += xr[4160] * fw[0][2];
          u32x2 v = {cvtpk(sum[0], sum[1]), cvtpk(sum[2], sum[3])};
          *(u32x2*)(otile + (0 + r) * 512 + (((29) ^ ((0 + r) & 31)) * 16) + 8 * h) = v; }
        { f32x4 sum = acc_read4<120>(); sum *= fw[0][3];
          sum += xr[1152] * fw[0][0];
          sum += xr[2688] * fw[0][1];
          sum += xr[4224] * fw[0][2];
          u32x2 v = {cvtpk(sum[0], sum[1]), cvtpk(sum[2], sum[3])};
          *(u32x2*)(otile + (0 + r) * 512 + (((30) ^ ((0 + r) & 31)) * 16) + 8 * h) = v; }
        { f32x4 sum = acc_read4<124>(); sum *= fw[0][3];
          sum += xr[1216] * fw[0][0];
          sum += xr[2752] * fw[0][1];
          sum += xr[4288] * fw[0][2];
          u32x2 v = {cvtpk(sum[0], sum[1]), cvtpk(sum[2], sum[3])};
          *(u32x2*)(otile + (0 + r) * 512 + (((31) ^ ((0 + r) & 31)) * 16) + 8 * h) = v; }
        { f32x4 sum = acc_read4<240>(); sum *= fw[1][3];
          sum += xr[1280] * fw[1][0];
          sum += xr[2816] * fw[1][1];
          sum += xr[4352] * fw[1][2];
          u32x2 v = {cvtpk(sum[0], sum[1]), cvtpk(sum[2], sum[3])};
          *(u32x2*)(otile + (32 + r) * 512 + (((28) ^ ((32 + r) & 31)) * 16) + 8 * h) = v; }
        { f32x4 sum = acc_read4<244>(); sum *= fw[1][3];
          sum += xr[1344] * fw[1][0];
          sum += xr[2880] * fw[1][1];
          sum += xr[4416] * fw[1][2];
          u32x2 v = {cvtpk(sum[0], sum[1]), cvtpk(sum[2], sum[3])};
          *(u32x2*)(otile + (32 + r) * 512 + (((29) ^ ((32 + r) & 31)) * 16) + 8 * h) = v; }
        { f32x4 sum = acc_read4<248>(); sum *= fw[1][3];
          sum += xr[1408] * fw[1][0];
          sum += xr[2944] * fw[1][1];
          sum += xr[4480] * fw[1][2];
          u32x2 v = {cvtpk(sum[0], sum[1]), cvtpk(sum[2], sum[3])};
          *(u32x2*)(otile + (32 + r) * 512 + (((30) ^ ((32 + r) & 31)) * 16) + 8 * h) = v; }
        { f32x4 sum = acc_read4<252>(); sum *= fw[1][3];
          sum += xr[1472] * fw[1][0];
          sum += xr[3008] * fw[1][1];
          sum += xr[4544] * fw[1][2];
          u32x2 v = {cvtpk(sum[0], sum[1]), cvtpk(sum[2], sum[3])};
          *(u32x2*)(otile + (32 + r) * 512 + (((31) ^ ((32 + r) & 31)) * 16) + 8 * h) = v; }
      } break;
    }
  }
  __syncthreads();
  STAMP(15);
#pragma unroll
  for (int k = 0; k < 7; ++k) {
    const int R = 14 * w + 2 * k + h;
    u32x4 v = *(const u32x4*)(otile + R * 512 + ((r ^ (R & 31)) * 16));
    *(u32x4*)(o + (size_t)(row0 + R) * NQ + head * DH + r * 8) = v;
  }
  STAMP(10);
}
__global__ __launch_bounds__(512, 2) void oproj2_kernel(const u16* __restrict__ o, const u16* __restrict__ wo,
                                                        u16* __restrict__ out) {
  extern __shared__ __attribute__((aligned(16))) char lds[];
  const int tid = threadIdx.x, lane = tid & 63;
  const int w = __builtin_amdgcn_readfirstlane(tid >> 6);
  const int r = lane & 31, h = lane >> 5;
  const int xcd = blockIdx.x & 7, idx = blockIdx.x >> 3;
  const int rt = xcd + 8 * (idx / 7), ct = idx % 7;
  if (rt >= 28) return;
  const int col0 = ct * 64;
  char* ring = lds + w * 18432;
  __amdgpu_buffer_rsrc_t rw = __builtin_amdgcn_make_buffer_rsrc((void*)(wo + (size_t)(512 * w) * DM + col0), 0,
                                                                 (512 * DM - col0) * 2, 0x00020000);
  __amdgpu_buffer_rsrc_t ra = __builtin_amdgcn_make_buffer_rsrc((void*)(o + (size_t)(rt * 32) * NQ + 512 * w), 0,
                                                                 (32 * NQ - 512 * w) * 2, 0x00020000);
  const int drow = lane >> 3;
  const int wvoff = drow * (DM * 2) + (((lane & 7) ^ (((drow >> 1) & 1) << 2)) * 16);
  const int arow = lane >> 2;
  const int avoff = arow * (NQ * 2) + (((lane & 3) ^ ((arow >> 2) & 3)) * 16);
  auto issue = [&](int u) {
    char* dst = ring + (u % 3) * 6144;
#pragma unroll
    for (int p = 0; p < 4; ++p)
      __builtin_amdgcn_raw_ptr_buffer_load_lds(rw, (lds_vp)(dst + p * 1024), 16, wvoff, (32 * u + 8 * p) * (DM * 2), 0, 0);
#pragma unroll
    for (int p = 0; p < 2; ++p)
      __builtin_amdgcn_raw_ptr_buffer_load_lds(ra, (lds_vp)(dst + 4096 + p * 1024), 16, avoff, 16 * p * (NQ * 2) + 64 * u, 0, 0);
  };
  issue(0); issue(1); issue(2);
  const int g = lane >> 4, qq = (lane >> 2) & 3, pp = lane & 3;
  const unsigned rb = (unsigned)(size_t)ring;
  const unsigned trb = rb + (8 * (g >> 1) + qq) * 128 + ((32 * (g & 1) + 8 * pp) ^ ((qq >> 1) << 6));
  const unsigned ab0 = rb + 4096 + r * 64 + (((0 + h) ^ ((r >> 2) & 3)) * 16);
  const unsigned ab1 = rb + 4096 + r * 64 + (((2 + h) ^ ((r >> 2) & 3)) * 16);
  f32x16 acc[2];
#pragma unroll
  for (int b2 = 0; b2 < 2; ++b2)
#pragma unroll
    for (int i = 0; i < 16; ++i) acc[b2][i] = 0.f;
#pragma unroll
  for (int u = 0; u < 16; ++u) {
    if (u <= 13) WAITV(12); else if (u == 14) WAITV(6); else WAITV(0);
    const unsigned so = (u % 3) * 6144;
    s16x4 a[2][4];
    bf16x8 xb[2];
#pragma unroll
    for (int s = 0; s < 2; ++s) {
      a[s][0] = trread(trb + so + s * 2048);
      a[s][1] = trread(trb + so + s * 2048 + 512);
      a[s][2] = trread((trb + so + s * 2048) ^ 64);
      a[s][3] = trread((trb + so + s * 2048 + 512) ^ 64);
    }
    asm volatile("ds_read_b128 %0, %1" : "=v"(xb[0]) : "v"(ab0 + so) : "memory");
    asm volatile("ds_read_b128 %0, %1" : "=v"(xb[1]) : "v"(ab1 + so) : "memory");
    asm volatile("s_waitcnt lgkmcnt(0)"
                 : "+v"(a[0][0]), "+v"(a[0][1]), "+v"(a[0][2]), "+v"(a[0][3]), "+v"(a[1][0]), "+v"(a[1][1]),
                   "+v"(a[1][2]), "+v"(a[1][3]), "+v"(xb[0]), "+v"(xb[1])::"memory");
    if (u + 3 < 16) issue(u + 3);
#pragma unroll
    for (int s = 0; s < 2; ++s) {
      acc[0] = __builtin_amdgcn_mfma_f32_32x32x16_bf16(cat8(a[s][0], a[s][1]), xb[s], acc[0], 0, 0, 0);
      acc[1] = __builtin_amdgcn_mfma_f32_32x32x16_bf16(cat8(a[s][2], a[s][3]), xb[s], acc[1], 0, 0, 0);
    }
  }
  __syncthreads();
  float* red = (float*)lds;
#pragma unroll
  for (int cb = 0; cb < 2; ++cb)
#pragma unroll
    for (int gg = 0; gg < 4; ++gg) {
      f32x4 v = {acc[cb][4 * gg], acc[cb][4 * gg + 1], acc[cb][4 * gg + 2], acc[cb][4 * gg + 3]};
      *(f32x4*)(red + ((w * 8 + cb * 4 + gg) * 64 + lane) * 4) = v;
    }
  __syncthreads();
  {
    const int cg = w, cb = cg >> 2, gg = cg & 3;
    f32x4 s = {0.f, 0.f, 0.f, 0.f};
#pragma unroll
    for (int ww = 0; ww < 8; ++ww) s += *(const f32x4*)(red + ((ww * 8 + cg) * 64 + lane) * 4);
    u32x2 v = {cvtpk(s[0], s[1]), cvtpk(s[2], s[3])};
    *(u32x2*)(out + (size_t)(rt * 32 + r) * DM + col0 + 32 * cb + 8 * gg + 4 * h) = v;
  }
}
extern "C" void kernel_launch(void* const* d_in, const int* in_sizes, int n_in, void* d_out, int out_size,
                              void* d_ws, size_t ws_size, hipStream_t stream) {
  const u16* x = (const u16*)d_in[0];
  const u16* kc = (const u16*)d_in[1];
  const u16* vc = (const u16*)d_in[2];
  const u16* wq = (const u16*)d_in[3];
  const u16* wo = (const u16*)d_in[4];
  u16* out = (u16*)d_out;
  u16* qws = (u16*)d_ws;
  u16* ows = (u16*)((char*)d_ws + (size_t)NROW * NQ * 2);
  (void)hipFuncSetAttribute((const void*)attn4_kernel, hipFuncAttributeMaxDynamicSharedMemorySize, 163840);
  hipLaunchKernelGGL(attn4_kernel, dim3(256), dim3(256), 163840, stream, x, wq, kc, vc, ows
  );
  (void)hipFuncSetAttribute((const void*)oproj2_kernel, hipFuncAttributeMaxDynamicSharedMemorySize, 147456);
  hipLaunchKernelGGL(oproj2_kernel, dim3(8 * 28), dim3(512), 147456, stream, (const u16*)ows, wo, out);
}
```

```cpp
#include <hip/hip_runtime.h>
#include <hip/hip_bf16.h>
#include <utility>

typedef unsigned short u16;
typedef __attribute__((ext_vector_type(8))) short bf16x8;
typedef __attribute__((ext_vector_type(4))) short s16x4;
typedef __attribute__((ext_vector_type(16))) float f32x16;
typedef __attribute__((ext_vector_type(4))) float f32x4;
typedef __attribute__((ext_vector_type(2))) float f32x2;
typedef __attribute__((ext_vector_type(2))) __bf16 bf16x2v;
typedef __attribute__((ext_vector_type(2))) unsigned u32x2;
typedef __attribute__((ext_vector_type(4))) unsigned u32x4;
#define DI __device__ __forceinline__
#define LDSP(p) ((__attribute__((address_space(3))) unsigned*)(p))

constexpr int NB = 8, SQ = 112, DM = 448, HQ = 16, HKV = 8, SK = 1024, DH = 256;
constexpr int NROW = NB * SQ;
constexpr int NQ = HQ * DH;

DI unsigned cvtpk(float lo, float hi) {
  f32x2 v = {lo, hi};
  bf16x2v b = __builtin_convertvector(v, bf16x2v);
  return __builtin_bit_cast(unsigned, b);
}
DI void glds16(const void* g, void* l) {
  __builtin_amdgcn_global_load_lds((const unsigned*)g, LDSP(l), 16, 0, 0);
}
#define WAITV(n) asm volatile("s_waitcnt vmcnt(" #n ")" ::: "memory")
#define WAITL0() asm volatile("s_waitcnt lgkmcnt(0)" ::: "memory")

DI s16x4 trread(unsigned addr) {
  s16x4 r;
  asm volatile("ds_read_b64_tr_b16 %0, %1" : "=v"(r) : "v"(addr) : "memory");
  return r;
}
template <int OFF> DI s16x4 trread_o(unsigned addr) {
  s16x4 r;
  asm volatile("ds_read_b64_tr_b16 %0, %1 offset:%2" : "=v"(r) : "v"(addr), "i"(OFF) : "memory");
  return r;
}
DI bf16x8 cat8(s16x4 a, s16x4 b) { return __builtin_shufflevector(a, b, 0, 1, 2, 3, 4, 5, 6, 7); }

template <int RB, int NCH>
DI void wave_gemm(const u16* __restrict__ act, int lda, const int (&arow)[RB], const u16* __restrict__ W, int ldw,
                  char* wl, f32x16 (&acc)[RB][2], int lane) {
  const int r = lane & 31, h = lane >> 5;
  const int g = lane >> 4, q = (lane >> 2) & 3, pp = lane & 3;
  const unsigned ldsb = (unsigned)(size_t)wl;
  const unsigned trb = ldsb + (8 * (g >> 1) + q) * 128 + ((32 * (g & 1) + 8 * pp) ^ ((q >> 1) << 6));
  const int drow = lane >> 3;
  const int dchk = (lane & 7) ^ (((drow >> 1) & 1) << 2);
  const u16* wsrc = W + (size_t)drow * ldw + dchk * 8;
  auto issue = [&](int c, int buf) {
#pragma unroll
    for (int p = 0; p < 8; ++p) glds16(wsrc + (size_t)(64 * c + 8 * p) * ldw, wl + buf * 8192 + p * 1024);
  };
  issue(0, 0);
#pragma unroll
  for (int c = 0; c < NCH; ++c) {
    const int buf = c & 1;
    bf16x8 bfr[4][RB];
#pragma unroll
    for (int s = 0; s < 4; ++s)
#pragma unroll
      for (int rb = 0; rb < RB; ++rb)
        bfr[s][rb] = *(const bf16x8*)(act + (size_t)arow[rb] * lda + 64 * c + 16 * s + 8 * h);
    WAITV(0);
    if (c + 1 < NCH) issue(c + 1, buf ^ 1);
#pragma unroll
    for (int s = 0; s < 4; ++s) {
      s16x4 a00 = trread(trb + buf * 8192 + s * 2048 + 0);
      s16x4 a01 = trread(trb + buf * 8192 + s * 2048 + 512);
      s16x4 a10 = trread((trb + buf * 8192 + s * 2048 + 0) ^ 64);
      s16x4 a11 = trread((trb + buf * 8192 + s * 2048 + 512) ^ 64);
      asm volatile("s_waitcnt lgkmcnt(0)" : "+v"(a00), "+v"(a01), "+v"(a10), "+v"(a11)::"memory");
      bf16x8 A0 = cat8(a00, a01), A1 = cat8(a10, a11);
#pragma unroll
      for (int rb = 0; rb < RB; ++rb) {
        acc[rb][0] = __builtin_amdgcn_mfma_f32_32x32x16_bf16(A0, bfr[s][rb], acc[rb][0], 0, 0, 0);
        acc[rb][1] = __builtin_amdgcn_mfma_f32_32x32x16_bf16(A1, bfr[s][rb], acc[rb][1], 0, 0, 0);
      }
    }
  }
  (void)r;
}

__global__ __launch_bounds__(256) void qproj_kernel(const u16* __restrict__ x, const u16* __restrict__ wq,
                                                    u16* __restrict__ qout) {
  extern __shared__ __attribute__((aligned(16))) char lds[];
  const int tid = threadIdx.x, lane = tid & 63;
  const int w = __builtin_amdgcn_readfirstlane(tid >> 6);
  const int r = lane & 31, h = lane >> 5;
  const int rt = blockIdx.x % 14, head = blockIdx.x / 14;
  const int col0 = head * 256 + 64 * w;
  f32x16 acc[2][2];
#pragma unroll
  for (int a = 0; a < 2; ++a)
#pragma unroll
    for (int b = 0; b < 2; ++b)
#pragma unroll
      for (int i = 0; i < 16; ++i) acc[a][b][i] = 0.f;
  const int arow[2] = {rt * 64 + r, rt * 64 + 32 + r};
  wave_gemm<2, 7>(x, DM, arow, wq + col0, NQ, lds + w * 16384, acc, lane);
#pragma unroll
  for (int rb = 0; rb < 2; ++rb)
#pragma unroll
    for (int cb = 0; cb < 2; ++cb)
#pragma unroll
      for (int g = 0; g < 4; ++g) {
        u32x2 v = {cvtpk(acc[rb][cb][4 * g + 0], acc[rb][cb][4 * g + 1]),
                   cvtpk(acc[rb][cb][4 * g + 2], acc[rb][cb][4 * g + 3])};
        *(u32x2*)(qout + (size_t)arow[rb] * NQ + col0 + 32 * cb + 8 * g + 4 * h) = v;
      }
}

__global__ __launch_bounds__(256) void oproj_kernel(const u16* __restrict__ o, const u16* __restrict__ wo,
                                                    u16* __restrict__ out) {
  extern __shared__ __attribute__((aligned(16))) char lds[];
  const int tid = threadIdx.x, lane = tid & 63;
  const int w = __builtin_amdgcn_readfirstlane(tid >> 6);
  const int r = lane & 31, h = lane >> 5;
  const int rt = blockIdx.x % 28, ct = blockIdx.x / 28;
  const int col0 = ct * 64;
  f32x16 acc[1][2];
#pragma unroll
  for (int b = 0; b < 2; ++b)
#pragma unroll
    for (int i = 0; i < 16; ++i) acc[0][b][i] = 0.f;
  const int arow[1] = {rt * 32 + r};
  wave_gemm<1, 16>(o + 1024 * w, NQ, arow, wo + (size_t)(1024 * w) * DM + col0, DM, lds + w * 16384, acc, lane);
  __syncthreads();
  float* red = (float*)lds;
#pragma unroll
  for (int cb = 0; cb < 2; ++cb)
#pragma unroll
    for (int g = 0; g < 4; ++g) {
      f32x4 v = {acc[0][cb][4 * g], acc[0][cb][4 * g + 1], acc[0][cb][4 * g + 2], acc[0][cb][4 * g + 3]};
      *(f32x4*)(red + ((w * 8 + cb * 4 + g) * 64 + lane) * 4) = v;
    }
  __syncthreads();
#pragma unroll
  for (int j = 0; j < 2; ++j) {
    const int cg = 2 * w + j, cb = cg >> 2, g = cg & 3;
    f32x4 s = {0.f, 0.f, 0.f, 0.f};
#pragma unroll
    for (int ww = 0; ww < 4; ++ww) {
      f32x4 v = *(const f32x4*)(red + ((ww * 8 + cg) * 64 + lane) * 4);
      s += v;
    }
    u32x2 v = {cvtpk(s[0], s[1]), cvtpk(s[2], s[3])};
    *(u32x2*)(out + (size_t)arow[0] * DM + col0 + 32 * cb + 8 * g + 4 * h) = v;
  }
}

constexpr float SCL = 0.0625f * 1.4426950408889634f;
constexpr float THR = 8.0f;

typedef __attribute__((address_space(3))) void* lds_vp;
struct DmaCtx {
  __amdgpu_buffer_rsrc_t rk, rv;
  int koff[4], voff[4];
  char* dst;
};
template <int U>
DI void issue_unit(const DmaCtx& c) {
  constexpr int t = U >> 2, kind = U & 3, slot = U % 5;
  if constexpr (kind < 2) {
#pragma unroll
    for (int pi = 0; pi < 4; ++pi)
      __builtin_amdgcn_raw_ptr_buffer_load_lds(c.rk, (lds_vp)(c.dst + slot * 8192 + pi * 1024), 16, c.koff[pi],
                                               32 * t * 512 + kind * 256, 0, 0);
  } else {
    constexpr int ks = kind - 2;
#pragma unroll
    for (int pi = 0; pi < 4; ++pi)
      __builtin_amdgcn_raw_ptr_buffer_load_lds(c.rv, (lds_vp)(c.dst + slot * 8192 + pi * 1024), 16, c.voff[pi],
                                               (32 * t + 16 * ks) * 512, 0, 0);
  }
}
template <int U> DI void wait_unit_bar() {
  constexpr int after = (U + 3 > 31 ? 31 : U + 3) - U;
  if constexpr (after == 3) WAITV(12);
  else if constexpr (after == 2) WAITV(8);
  else if constexpr (after == 1) WAITV(4);
  else WAITV(0);
  WAITL0();
  __builtin_amdgcn_s_barrier();
}

struct AttnState {
  bf16x8 qf[16];
  f32x16 oacc[8];
  float m, l;
};

template <int T, int DHF>
DI void qk_half(AttnState& st, f32x16& s, const DmaCtx& c, char* ring, unsigned kaddr) {
  constexpr int U = 4 * T + DHF, slot = U % 5;
  wait_unit_bar<U>();
  if constexpr (U + 4 < 32) issue_unit<U + 4>(c);
#pragma unroll
  for (int ss = 0; ss < 8; ++ss) {
    bf16x8 kf = *(const bf16x8*)(ring + slot * 8192 + (kaddr ^ (ss * 32)));
    s = __builtin_amdgcn_mfma_f32_32x32x16_bf16(kf, st.qf[DHF * 8 + ss], s, 0, 0, 0);
  }
}
template <int T, int KS, int HF>
DI void pv_quarter(AttnState& st, const bf16x8& pf, const unsigned (&vaddr)[4]) {
  constexpr int U = 4 * T + 2 + KS, slot = U % 5;
  constexpr int OFF = slot * 8192 + HF * 256;
  s16x4 v0[4], v1[4];
  v0[0] = trread_o<OFF>(vaddr[0]); v1[0] = trread_o<OFF + 4096>(vaddr[0]);
  v0[1] = trread_o<OFF>(vaddr[1]); v1[1] = trread_o<OFF + 4096>(vaddr[1]);
  v0[2] = trread_o<OFF>(vaddr[2]); v1[2] = trread_o<OFF + 4096>(vaddr[2]);
  v0[3] = trread_o<OFF>(vaddr[3]); v1[3] = trread_o<OFF + 4096>(vaddr[3]);
  asm volatile("s_waitcnt lgkmcnt(0)"
               : "+v"(v0[0]), "+v"(v0[1]), "+v"(v0[2]), "+v"(v0[3]), "+v"(v1[0]), "+v"(v1[1]), "+v"(v1[2]),
                 "+v"(v1[3])::"memory");
#pragma unroll
  for (int d = 0; d < 4; ++d) {
    bf16x8 vf = cat8(v0[d], v1[d]);
    st.oacc[HF * 4 + d] = __builtin_amdgcn_mfma_f32_32x32x16_bf16(vf, pf, st.oacc[HF * 4 + d], 0, 0, 0);
  }
}
template <int T, int KS>
DI void pv_half(AttnState& st, const bf16x8& pf, const DmaCtx& c, const unsigned (&vaddr)[4]) {
  constexpr int U = 4 * T + 2 + KS;
  wait_unit_bar<U>();
  if constexpr (U + 4 < 32) issue_unit<U + 4>(c);
  pv_quarter<T, KS, 0>(st, pf, vaddr);
  pv_quarter<T, KS, 1>(st, pf, vaddr);
}

template <int T>
DI void attn_tile(AttnState& st, const DmaCtx& c, char* ring, unsigned kaddr, const unsigned (&vaddr)[4]) {
  f32x16 s;
#pragma unroll
  for (int i = 0; i < 16; ++i) s[i] = 0.f;
  qk_half<T, 0>(st, s, c, ring, kaddr);
  qk_half<T, 1>(st, s, c, ring, kaddr);
  bf16x8 pf[2];
  {
    float a = s[0];
#pragma unroll
    for (int i = 1; i < 16; ++i) a = fmaxf(a, s[i]);
    auto rr = __builtin_amdgcn_permlane32_swap(__float_as_uint(a), __float_as_uint(a), false, false);
    const float mx = fmaxf(__uint_as_float(rr[0]), __uint_as_float(rr[1])) * SCL;
    const bool grew = (mx - st.m > THR);
    if (T == 0 || __any((int)grew)) {
      const float mn = fmaxf(st.m, mx);
      const float alpha = __builtin_amdgcn_exp2f(st.m - mn);
      st.m = mn;
      st.l *= alpha;
      if (T != 0) {
#pragma unroll
        for (int db = 0; db < 8; ++db)
#pragma unroll
          for (int i = 0; i < 16; ++i) st.oacc[db][i] *= alpha;
      }
    }
    const float nm = -st.m;
    float ps = 0.f;
#pragma unroll
    for (int i = 0; i < 16; ++i) {
      s[i] = __builtin_amdgcn_exp2f(fmaf(s[i], SCL, nm));
      ps += s[i];
    }
    st.l += ps;
#pragma unroll
    for (int ks = 0; ks < 2; ++ks) {
      u32x4 wv = {cvtpk(s[8 * ks + 0], s[8 * ks + 1]), cvtpk(s[8 * ks + 2], s[8 * ks + 3]),
                  cvtpk(s[8 * ks + 4], s[8 * ks + 5]), cvtpk(s[8 * ks + 6], s[8 * ks + 7])};
      pf[ks] = __builtin_bit_cast(bf16x8, wv);
    }
  }
  pv_half<T, 0>(st, pf[0], c, vaddr);
  pv_half<T, 1>(st, pf[1], c, vaddr);
}

#define STAMP(i)
#define DBGARG
__global__ __launch_bounds__(512, 2) void attn_kernel(const u16* __restrict__ q, const u16* __restrict__ kc,
                                                      const u16* __restrict__ vc, u16* __restrict__ o DBGARG) {
  extern __shared__ __attribute__((aligned(16))) char lds[];
  STAMP(0);
  const int tid = threadIdx.x, lane = tid & 63;
  const int w = __builtin_amdgcn_readfirstlane(tid >> 6);
  const int rb = w >> 2, kq = w & 3;
  const int r = lane & 31, h = lane >> 5;
  const int bid = blockIdx.x;
  const int xcd = bid & 7, j = bid >> 3;
  const int grp = (j >> 2) * 8 + xcd, sub = j & 3;
  const int b = grp >> 3, kvh = grp & 7;
  const int head = kvh * 2 + (sub >> 1), rhalf = sub & 1;
  const int row0 = b * SQ + rhalf * 56;

  const u16* Kw = kc + ((size_t)(b * HKV + kvh) * SK + 256 * kq) * DH;
  const u16* Vw = vc + ((size_t)(b * HKV + kvh) * SK + 256 * kq) * DH;
  char* ring = lds + kq * 40960;

  AttnState st;
  {
    int rr = 32 * rb + r;
    rr = rr > 55 ? 55 : rr;
    const u16* qrow = q + (size_t)(row0 + rr) * NQ + head * DH + 8 * h;
#pragma unroll
    for (int s = 0; s < 16; ++s) st.qf[s] = *(const bf16x8*)(qrow + 16 * s);
    st.m = -__builtin_inff();
    st.l = 0.f;
#pragma unroll
    for (int db = 0; db < 8; ++db)
#pragma unroll
      for (int i = 0; i < 16; ++i) st.oacc[db][i] = 0.f;
  }
#pragma unroll
  for (int s = 0; s < 16; ++s) asm volatile("" : "+v"(st.qf[s]));
  WAITV(0);

  STAMP(1);
  DmaCtx c;
  c.rk = __builtin_amdgcn_make_buffer_rsrc((void*)Kw, 0, 256 * DH * 2, 0x00020000);
  c.rv = __builtin_amdgcn_make_buffer_rsrc((void*)Vw, 0, 256 * DH * 2, 0x00020000);
  c.dst = ring + rb * 4096;
#pragma unroll
  for (int pi = 0; pi < 4; ++pi) {
    const int krow = 16 * rb + 4 * pi + (lane >> 4);
    c.koff[pi] = krow * 512 + (((lane & 15) ^ (krow & 15)) * 16);
    const int vrow = 8 * rb + 2 * pi + (lane >> 5);
    c.voff[pi] = vrow * 512 + (((lane & 31) ^ ((vrow & 3) << 2)) * 16);
  }
  issue_unit<0>(c);
  issue_unit<1>(c);
  issue_unit<2>(c);
  issue_unit<3>(c);

  const unsigned kaddr = r * 256 + ((h ^ (r & 15)) * 16);
  const int g = lane >> 4, qq = (lane >> 2) & 3, pp = lane & 3;
  unsigned vaddr[4];
#pragma unroll
  for (int d = 0; d < 4; ++d)
    vaddr[d] = (unsigned)(size_t)ring + (4 * (g >> 1) + qq) * 512 + ((64 * d + 32 * (g & 1) + 8 * pp) ^ (qq << 6));

  attn_tile<0>(st, c, ring, kaddr, vaddr);
  STAMP(2);
  attn_tile<1>(st, c, ring, kaddr, vaddr);
  STAMP(3);
  attn_tile<2>(st, c, ring, kaddr, vaddr);
  STAMP(4);
  attn_tile<3>(st, c, ring, kaddr, vaddr);
  STAMP(5);
  attn_tile<4>(st, c, ring, kaddr, vaddr);
  STAMP(6);
  attn_tile<5>(st, c, ring, kaddr, vaddr);
  STAMP(7);
  attn_tile<6>(st, c, ring, kaddr, vaddr);
  STAMP(8);
  attn_tile<7>(st, c, ring, kaddr, vaddr);
  STAMP(9);

  __syncthreads();
  float* ml = (float*)lds;
  {
    auto rr = __builtin_amdgcn_permlane32_swap(__float_as_uint(st.l), __float_as_uint(st.l), false, false);
    const float lt = __uint_as_float(rr[0]) + __uint_as_float(rr[1]);
    if (h == 0) {
      ml[(w * 32 + r) * 2 + 0] = st.m;
      ml[(w * 32 + r) * 2 + 1] = lt;
    }
  }
  __syncthreads();
  float f;
  {
    float mm[4], ll[4];
#pragma unroll
    for (int k = 0; k < 4; ++k) {
      mm[k] = ml[((rb * 4 + k) * 32 + r) * 2 + 0];
      ll[k] = ml[((rb * 4 + k) * 32 + r) * 2 + 1];
    }
    const float M = fmaxf(fmaxf(mm[0], mm[1]), fmaxf(mm[2], mm[3]));
    float L = 0.f;
#pragma unroll
    for (int k = 0; k < 4; ++k) L += __builtin_amdgcn_exp2f(mm[k] - M) * ll[k];
    f = __builtin_amdgcn_exp2f(st.m - M) / L;
  }
  float* xch = (float*)(lds + 4096);
#pragma unroll
  for (int rnd = 0; rnd < 2; ++rnd) {
#pragma unroll
    for (int db = 0; db < 8; ++db) {
      if ((db & 1) != rnd) continue;
      const int owner = db >> 1;
      if (owner == kq) continue;
      const int dslot = owner < kq ? owner : owner - 1;
#pragma unroll
      for (int gg = 0; gg < 4; ++gg) {
        f32x4 v = {st.oacc[db][4 * gg] * f, st.oacc[db][4 * gg + 1] * f, st.oacc[db][4 * gg + 2] * f,
                   st.oacc[db][4 * gg + 3] * f};
        *(f32x4*)(xch + (((w * 3 + dslot) * 4 + gg) * 64 + lane) * 4) = v;
      }
    }
    __syncthreads();
#pragma unroll
    for (int db = 0; db < 8; ++db) {
      if ((db & 1) != rnd) continue;
      if ((db >> 1) != kq) continue;
#pragma unroll
      for (int gg = 0; gg < 4; ++gg) {
        f32x4 sum = {st.oacc[db][4 * gg] * f, st.oacc[db][4 * gg + 1] * f, st.oacc[db][4 * gg + 2] * f,
                     st.oacc[db][4 * gg + 3] * f};
#pragma unroll
        for (int sk = 0; sk < 4; ++sk) {
          if (sk == kq) continue;
          const int dslot = kq < sk ? kq : kq - 1;
          f32x4 v = *(const f32x4*)(xch + ((((rb * 4 + sk) * 3 + dslot) * 4 + gg) * 64 + lane) * 4);
          sum += v;
        }
        const int rr = 32 * rb + r;
        if (rr < 56) {
          u32x2 v = {cvtpk(sum[0], sum[1]), cvtpk(sum[2], sum[3])};
          *(u32x2*)(o + (size_t)(row0 + rr) * NQ + head * DH + 32 * db + 8 * gg + 4 * h) = v;
        }
      }
    }
    __syncthreads();
  }
  STAMP(10);
}

#define GAP() __builtin_amdgcn_sched_barrier(0)
#define ACLOB "a0","a1","a2","a3","a4","a5","a6","a7","a8","a9","a10","a11","a12","a13","a14","a15","a16","a17","a18","a19","a20","a21","a22","a23","a24","a25","a26","a27","a28","a29","a30","a31","a32","a33","a34","a35","a36","a37","a38","a39","a40","a41","a42","a43","a44","a45","a46","a47","a48","a49","a50","a51","a52","a53","a54","a55","a56","a57","a58","a59","a60","a61","a62","a63","a64","a65","a66","a67","a68","a69","a70","a71","a72","a73","a74","a75","a76","a77","a78","a79","a80","a81","a82","a83","a84","a85","a86","a87","a88","a89","a90","a91","a92","a93","a94","a95","a96","a97","a98","a99","a100","a101","a102","a103","a104","a105","a106","a107","a108","a109","a110","a111","a112","a113","a114","a115","a116","a117","a118","a119","a120","a121","a122","a123","a124","a125","a126","a127","a128","a129","a130","a131","a132","a133","a134","a135","a136","a137","a138","a139","a140","a141","a142","a143","a144","a145","a146","a147","a148","a149","a150","a151","a152","a153","a154","a155","a156","a157","a158","a159","a160","a161","a162","a163","a164","a165","a166","a167","a168","a169","a170","a171","a172","a173","a174","a175","a176","a177","a178","a179","a180","a181","a182","a183","a184","a185","a186","a187","a188","a189","a190","a191","a192","a193","a194","a195","a196","a197","a198","a199","a200","a201","a202","a203","a204","a205","a206","a207","a208","a209","a210","a211","a212","a213","a214","a215","a216","a217","a218","a219","a220","a221","a222","a223","a224","a225","a226","a227","a228","a229","a230","a231","a232","a233","a234","a235","a236","a237","a238","a239","a240","a241","a242","a243","a244","a245","a246","a247","a248","a249","a250","a251","a252","a253","a254","a255"

template <int AO, bool ZERO> DI void pv_mfma(const bf16x8& vf, const bf16x8& pf) {
  if constexpr (ZERO)
    asm volatile("v_mfma_f32_32x32x16_bf16 a[%c2:%c3], %0, %1, 0" ::"v"(vf), "v"(pf), "i"(AO), "i"(AO + 15) : ACLOB);
  else
    asm volatile("v_mfma_f32_32x32x16_bf16 a[%c2:%c3], %0, %1, a[%c2:%c3]" ::"v"(vf), "v"(pf), "i"(AO), "i"(AO + 15)
                 : ACLOB);
}
template <bool FIRST> DI void qk_mfma(f32x16& s, const bf16x8& kf, const bf16x8& qf) {
  if constexpr (FIRST)
    asm volatile("v_mfma_f32_32x32x16_bf16 %0, %1, %2, 0" : "=&v"(s) : "v"(kf), "v"(qf) : ACLOB);
  else
    asm volatile("v_mfma_f32_32x32x16_bf16 %0, %1, %2, %0" : "+v"(s) : "v"(kf), "v"(qf) : ACLOB);
}
DI float max3f(float a, float b, float c) { float r; asm("v_max3_f32 %0, %1, %2, %3" : "=v"(r) : "v"(a), "v"(b), "v"(c)); return r; }
DI float max2f(float a, float b) { float r; asm("v_max_f32_e32 %0, %1, %2" : "=v"(r) : "v"(a), "v"(b)); return r; }
DI float fadd_s(float a, float b) { float r; asm("v_add_f32_e32 %0, %1, %2" : "=v"(r) : "v"(a), "v"(b)); return r; }
DI float ffma_s(float a, float b, float c) { float r; asm("v_fma_f32 %0, %1, %2, %3" : "=v"(r) : "v"(a), "v"(b), "v"(c)); return r; }

constexpr int unit_is_v(int n) { return n < 2 ? 0 : (n >= 30 ? 1 : (((n - 2) & 3) >= 2)); }
constexpr int unit_tile(int n) { return n < 2 ? 0 : (n >= 30 ? 7 : (((n - 2) & 3) >= 2 ? (n - 2) / 4 : (n - 2) / 4 + 1)); }
constexpr int unit_half(int n) { return n & 1; }
constexpr int unit_k(int t, int hf) { return t == 0 ? hf : 4 * (t - 1) + 2 + hf; }
constexpr int unit_v(int t, int hf) { return t == 7 ? 30 + hf : 4 * t + 4 + hf; }

struct Ctx4 {
  __amdgpu_buffer_rsrc_t rk, rv;
  int koff[4], voff[2];
  char* ring;
  unsigned kaddr[8];
  unsigned vaddr[4];
  bf16x8 qf[2][16];
  f32x16 s[2];
  bf16x8 pf[2][2];
  unsigned pn[2][8];
  float m[2], l[2], nm[2], mx[2], fct[2];
  float rs[2];
  bool pend;
  const u16* Kp; const u16* Vp;
  unsigned pfoff;
  int sub;
  unsigned pfd;
};
DI void prefetch_unit(Ctx4& c, int n) {
}

template <int N, int P> DI void dma_piece(const Ctx4& c) {
  if constexpr (N < 32) {
    constexpr int slot = N % 5, t = unit_tile(N), hf = unit_half(N);
    if constexpr (!unit_is_v(N))
      __builtin_amdgcn_raw_ptr_buffer_load_lds(c.rk, (lds_vp)(c.ring + slot * 8192 + P * 1024), 16, c.koff[P & 3],
                                               (32 * t + 16 * (P >> 2)) * 512 + hf * 256, 0, 0);
    else
      __builtin_amdgcn_raw_ptr_buffer_load_lds(c.rv, (lds_vp)(c.ring + slot * 8192 + P * 1024), 16, c.voff[P & 1],
                                               (32 * t + 16 * hf + 4 * (P >> 1)) * 512, 0, 0);
  }
}
template <int N> DI void dma_unit(const Ctx4& c) {
  dma_piece<N, 0>(c); dma_piece<N, 1>(c); dma_piece<N, 2>(c); dma_piece<N, 3>(c);
  dma_piece<N, 4>(c); dma_piece<N, 5>(c); dma_piece<N, 6>(c); dma_piece<N, 7>(c);
}
template <int N, int AHEAD> DI void wait_unit4() {
  constexpr int last = N + AHEAD > 31 ? 31 : N + AHEAD;
  constexpr int after = last - N;
  if constexpr (after == 4) WAITV(32);
  else if constexpr (after == 3) WAITV(24);
  else if constexpr (after == 2) WAITV(16);
  else if constexpr (after == 1) WAITV(8);
  else WAITV(0);
}

template <int T, int J> DI bf16x8 kread(const Ctx4& c) {
  constexpr int n = unit_k(T, J >> 3), slot = n % 5;
  bf16x8 r;
  asm volatile("ds_read_b128 %0, %1 offset:%2" : "=v"(r) : "v"(c.kaddr[J & 7]), "i"(slot * 8192) : "memory");
  return r;
}
template <int T, int J> DI void phaseA_step(Ctx4& c, bf16x8 (&kf)[4]) {
  constexpr int n0 = unit_k(T, 0), n1 = unit_k(T, 1);
  if constexpr (J + 3 < 16) asm volatile("s_waitcnt lgkmcnt(3)" : "+v"(kf[J & 3])::"memory");
  else if constexpr (J + 3 == 16) asm volatile("s_waitcnt lgkmcnt(2)" : "+v"(kf[J & 3])::"memory");
  else if constexpr (J + 3 == 17) asm volatile("s_waitcnt lgkmcnt(1)" : "+v"(kf[J & 3])::"memory");
  else asm volatile("s_waitcnt lgkmcnt(0)" : "+v"(kf[J & 3])::"memory");
  qk_mfma<J == 0>(c.s[0], kf[J & 3], c.qf[0][J]);
  if constexpr (J == 8 && T <= 4) prefetch_unit(c, 14 + 4 * T + c.sub);
  if constexpr (J >= 8) { dma_piece<n0 + 5, J - 8>(c); }
  GAP();
  qk_mfma<J == 0>(c.s[1], kf[J & 3], c.qf[1][J]);
  if constexpr (J + 4 == 8) wait_unit4<n1, 3>();
  if constexpr (J + 4 < 16) kf[J & 3] = kread<T, J + 4>(c);
  GAP();
}
template <int T, int... Js> DI void phaseA_(Ctx4& c, std::integer_sequence<int, Js...>) {
  bf16x8 kf[4];
  wait_unit4<unit_k(T, 0), 4>();
  kf[0] = kread<T, 0>(c); kf[1] = kread<T, 1>(c); kf[2] = kread<T, 2>(c); kf[3] = kread<T, 3>(c);
  GAP();
  (phaseA_step<T, Js>(c, kf), ...);
}
template <int T> DI void phaseA(Ctx4& c) { phaseA_<T>(c, std::make_integer_sequence<int, 16>{}); }

constexpr int egap(int e) { return 6 + (e * 25) / 32; }
template <int T, int G> DI void sm_slice(Ctx4& c) {
  if constexpr (G < 4) {
    constexpr int qb = G >> 1;
    if constexpr ((G & 1) == 0) {
      float a = max3f(c.s[qb][0], c.s[qb][1], c.s[qb][2]);
      a = max3f(a, c.s[qb][3], c.s[qb][4]);
      a = max3f(a, c.s[qb][5], c.s[qb][6]);
      c.mx[qb] = max3f(a, c.s[qb][7], c.s[qb][8]);
    } else {
      float a = max3f(c.mx[qb], c.s[qb][9], c.s[qb][10]);
      a = max3f(a, c.s[qb][11], c.s[qb][12]);
      a = max3f(a, c.s[qb][13], c.s[qb][14]);
      c.mx[qb] = max2f(a, c.s[qb][15]);
    }
  } else if constexpr (G == 4) {
#pragma unroll
    for (int qb = 0; qb < 2; ++qb) {
      auto rr = __builtin_amdgcn_permlane32_swap(__float_as_uint(c.mx[qb]), __float_as_uint(c.mx[qb]), false, false);
      c.mx[qb] = max2f(__uint_as_float(rr[0]), __uint_as_float(rr[1])) * SCL;
    }
  } else if constexpr (G == 5) {
    if constexpr (T == 0) {
      c.m[0] = c.mx[0]; c.m[1] = c.mx[1];
      c.l[0] = 0.f; c.l[1] = 0.f;
    } else {
      const bool grew = (c.mx[0] - c.m[0] > THR) | (c.mx[1] - c.m[1] > THR);
      if (__builtin_expect(__any((int)grew), 0)) {
#pragma unroll
        for (int qb = 0; qb < 2; ++qb) {
          const float mn = fmaxf(c.m[qb], c.mx[qb]);
          const float al = __builtin_amdgcn_exp2f(c.m[qb] - mn);
          c.m[qb] = mn;
          c.l[qb] *= al;
          c.fct[qb] = al;
        }
        c.pend = true;
      }
    }
    c.nm[0] = -c.m[0]; c.nm[1] = -c.m[1];
    c.rs[0] = 0.f; c.rs[1] = 0.f;
  }
  if constexpr (G >= 6) {
#pragma unroll
    for (int e = 0; e < 32; ++e) {
      if (egap(e) == G) {
        const int qb = e >> 4, i = e & 15;
        c.s[qb][i] = __builtin_amdgcn_exp2f(ffma_s(c.s[qb][i], SCL, c.nm[qb]));
      }
    }
#pragma unroll
    for (int e = 0; e < 32; ++e) {
      const int ge = egap(e);
      if (ge == G - 1 || (G == 31 && ge == 31)) {
        const int qb = e >> 4, i = e & 15;
        c.rs[qb] += c.s[qb][i];
      }
    }
#pragma unroll
    for (int d = 0; d < 16; ++d) {
      const int ge = egap(2 * d + 1);
      if (ge == G - 1 || (G == 31 && ge == 31)) {
        const int qb = d >> 3, i = (2 * d) & 15;
        c.pn[qb][d & 7] = cvtpk(c.s[qb][i], c.s[qb][i + 1]);
      }
    }
  }
}

template <int TV, int S> DI void vread_set(const Ctx4& c, s16x4 (&v)[4]) {
  constexpr int ks = S >> 2, dbp = S & 3;
  constexpr int n = unit_v(TV, ks), slot = n % 5;
  constexpr int db0 = 2 * dbp, db1 = 2 * dbp + 1;
  constexpr int o0 = slot * 8192 + (db0 >> 2) * 256, o1 = slot * 8192 + (db1 >> 2) * 256;
  v[0] = trread_o<o0>(c.vaddr[db0 & 3]);
  v[1] = trread_o<o0 + 4096>(c.vaddr[db0 & 3]);
  v[2] = trread_o<o1>(c.vaddr[db1 & 3]);
  v[3] = trread_o<o1 + 4096>(c.vaddr[db1 & 3]);
}
template <int T, bool HASPV, bool HASSM, int M> DI void phaseB_gap(Ctx4& c, s16x4 (&va)[4], s16x4 (&vb)[4]) {
  constexpr int TV = T - 1;
  constexpr int S = M >> 2, ks = S >> 2, dbp = S & 3, dd = (M >> 1) & 1, qb = M & 1;
  if constexpr (HASPV) {
    if constexpr ((M & 3) == 0) {
      if constexpr (S + 1 < 8) {
        if constexpr (S & 1) asm volatile("s_waitcnt lgkmcnt(4)" : "+v"(vb[0]), "+v"(vb[1]), "+v"(vb[2]), "+v"(vb[3])::"memory");
        else asm volatile("s_waitcnt lgkmcnt(4)" : "+v"(va[0]), "+v"(va[1]), "+v"(va[2]), "+v"(va[3])::"memory");
      } else {
        asm volatile("s_waitcnt lgkmcnt(0)" : "+v"(vb[0]), "+v"(vb[1]), "+v"(vb[2]), "+v"(vb[3])::"memory");
      }
    }
    {
      constexpr int db = 2 * dbp + dd;
      const bf16x8 vf = (S & 1) ? cat8(vb[2 * dd], vb[2 * dd + 1]) : cat8(va[2 * dd], va[2 * dd + 1]);
      pv_mfma<(qb * 8 + db) * 16, (TV == 0 && ks == 0)>(vf, c.pf[qb][ks]);
    }
    if constexpr ((M & 3) == 3 && S + 2 < 8) {
      if constexpr (S + 2 == 4) wait_unit4<unit_v(TV, 1), 3>();
      if constexpr (S & 1) vread_set<TV, S + 2>(c, vb); else vread_set<TV, S + 2>(c, va);
    }
    if constexpr (M >= 16 && M < 24) dma_piece<unit_v(TV, 0) + 5, M - 16>(c);
  }
  if constexpr (HASSM) sm_slice<T, M>(c);
  GAP();
}
template <int T, bool HASPV, bool HASSM, int... Ms> DI void phaseB_(Ctx4& c, std::integer_sequence<int, Ms...>) {
  constexpr int TV = T - 1;
  s16x4 va[4], vb[4];
  if constexpr (HASPV) {
    wait_unit4<unit_v(TV, 0), 3>();
    vread_set<TV, 0>(c, va);
    vread_set<TV, 1>(c, vb);
  }
  if constexpr (HASSM) dma_unit<unit_k(T, 1) + 5>(c);
  if constexpr (HASSM) {
    asm volatile("s_nop 7\n\ts_nop 7" : "+v"(c.s[0]), "+v"(c.s[1]));
  }
  GAP();
  (phaseB_gap<T, HASPV, HASSM, Ms>(c, va, vb), ...);
  if constexpr (HASPV) dma_unit<unit_v(TV, 1) + 5>(c);
  if constexpr (HASSM) {
    c.l[0] += c.rs[0]; c.l[1] += c.rs[1];
#pragma unroll
    for (int qb = 0; qb < 2; ++qb)
#pragma unroll
      for (int ks = 0; ks < 2; ++ks) {
        u32x4 wv = {c.pn[qb][4 * ks], c.pn[qb][4 * ks + 1], c.pn[qb][4 * ks + 2], c.pn[qb][4 * ks + 3]};
        c.pf[qb][ks] = __builtin_bit_cast(bf16x8, wv);
      }
  }
  if constexpr (HASPV && HASSM) {
    if (__builtin_expect(c.pend, 0)) {
      float t0, t1;
      asm volatile("s_nop 7\n\ts_nop 7\n\t"
                   "v_accvgpr_read_b32 %0, a0\n\tv_mul_f32 %0, %2, %0\n\tv_accvgpr_write_b32 a0, %0\n\t"
                   "v_accvgpr_read_b32 %1, a1\n\tv_mul_f32 %1, %2, %1\n\tv_accvgpr_write_b32 a1, %1\n\t"
                   "v_accvgpr_read_b32 %0, a2\n\tv_mul_f32 %0, %2, %0\n\tv_accvgpr_write_b32 a2, %0\n\t"
                   "v_accvgpr_read_b32 %1, a3\n\tv_mul_f32 %1, %2, %1\n\tv_accvgpr_write_b32 a3, %1\n\t"
                   "v_accvgpr_read_b32 %0, a4\n\tv_mul_f32 %0, %2, %0\n\tv_accvgpr_write_b32 a4, %0\n\t"
                   "v_accvgpr_read_b32 %1, a5\n\tv_mul_f32 %1, %2, %1\n\tv_accvgpr_write_b32 a5, %1\n\t"
                   "v_accvgpr_read_b32 %0, a6\n\tv_mul_f32 %0, %2, %0\n\tv_accvgpr_write_b32 a6, %0\n\t"
                   "v_accvgpr_read_b32 %1, a7\n\tv_mul_f32 %1, %2, %1\n\tv_accvgpr_write_b32 a7, %1\n\t"
                   "v_accvgpr_read_b32 %0, a8\n\tv_mul_f32 %0, %2, %0\n\tv_accvgpr_write_b32 a8, %0\n\t"
                   "v_accvgpr_read_b32 %1, a9\n\tv_mul_f32 %1, %2, %1\n\tv_accvgpr_write_b32 a9, %1\n\t"
                   "v_accvgpr_read_b32 %0, a10\n\tv_mul_f32 %0, %2, %0\n\tv_accvgpr_write_b32 a10, %0\n\t"
                   "v_accvgpr_read_b32 %1, a11\n\tv_mul_f32 %1, %2, %1\n\tv_accvgpr_write_b32 a11, %1\n\t"
                   "v_accvgpr_read_b32 %0, a12\n\tv_mul_f32 %0, %2, %0\n\tv_accvgpr_write_b32 a12, %0\n\t"
                   "v_accvgpr_read_b32 %1, a13\n\tv_mul_f32 %1, %2, %1\n\tv_accvgpr_write_b32 a13, %1\n\t"
                   "v_accvgpr_read_b32 %0, a14\n\tv_mul_f32 %0, %2, %0\n\tv_accvgpr_write_b32 a14, %0\n\t"
                   "v_accvgpr_read_b32 %1, a15\n\tv_mul_f32 %1, %2, %1\n\tv_accvgpr_write_b32 a15, %1\n\t"
                   "v_accvgpr_read_b32 %0, a16\n\tv_mul_f32 %0, %2, %0\n\tv_accvgpr_write_b32 a16, %0\n\t"
                   "v_accvgpr_read_b32 %1, a17\n\tv_mul_f32 %1, %2, %1\n\tv_accvgpr_write_b32 a17, %1\n\t"
                   "v_accvgpr_read_b32 %0, a18\n\tv_mul_f32 %0, %2, %0\n\tv_accvgpr_write_b32 a18, %0\n\t"
                   "v_accvgpr_read_b32 %1, a19\n\tv_mul_f32 %1, %2, %1\n\tv_accvgpr_write_b32 a19, %1\n\t"
                   "v_accvgpr_read_b32 %0, a20\n\tv_mul_f32 %0, %2, %0\n\tv_accvgpr_write_b32 a20, %0\n\t"
                   "v_accvgpr_read_b32 %1, a21\n\tv_mul_f32 %1, %2, %1\n\tv_accvgpr_write_b32 a21, %1\n\t"
                   "v_accvgpr_read_b32 %0, a22\n\tv_mul_f32 %0, %2, %0\n\tv_accvgpr_write_b32 a22, %0\n\t"
                   "v_accvgpr_read_b32 %1, a23\n\tv_mul_f32 %1, %2, %1\n\tv_accvgpr_write_b32 a23, %1\n\t"
                   "v_accvgpr_read_b32 %0, a24\n\tv_mul_f32 %0, %2, %0\n\tv_accvgpr_write_b32 a24, %0\n\t"
                   "v_accvgpr_read_b32 %1, a25\n\tv_mul_f32 %1, %2, %1\n\tv_accvgpr_write_b32 a25, %1\n\t"
                   "v_accvgpr_read_b32 %0, a26\n\tv_mul_f32 %0, %2, %0\n\tv_accvgpr_write_b32 a26, %0\n\t"
                   "v_accvgpr_read_b32 %1, a27\n\tv_mul_f32 %1, %2, %1\n\tv_accvgpr_write_b32 a27, %1\n\t"
                   "v_accvgpr_read_b32 %0, a28\n\tv_mul_f32 %0, %2, %0\n\tv_accvgpr_write_b32 a28, %0\n\t"
                   "v_accvgpr_read_b32 %1, a29\n\tv_mul_f32 %1, %2, %1\n\tv_accvgpr_write_b32 a29, %1\n\t"
                   "v_accvgpr_read_b32 %0, a30\n\tv_mul_f32 %0, %2, %0\n\tv_accvgpr_write_b32 a30, %0\n\t"
                   "v_accvgpr_read_b32 %1, a31\n\tv_mul_f32 %1, %2, %1\n\tv_accvgpr_write_b32 a31, %1\n\t"
                   "v_accvgpr_read_b32 %0, a32\n\tv_mul_f32 %0, %2, %0\n\tv_accvgpr_write_b32 a32, %0\n\t"
                   "v_accvgpr_read_b32 %1, a33\n\tv_mul_f32 %1, %2, %1\n\tv_accvgpr_write_b32 a33, %1\n\t"
                   "v_accvgpr_read_b32 %0, a34\n\tv_mul_f32 %0, %2, %0\n\tv_accvgpr_write_b32 a34, %0\n\t"
                   "v_accvgpr_read_b32 %1, a35\n\tv_mul_f32 %1, %2, %1\n\tv_accvgpr_write_b32 a35, %1\n\t"
                   "v_accvgpr_read_b32 %0, a36\n\tv_mul_f32 %0, %2, %0\n\tv_accvgpr_write_b32 a36, %0\n\t"
                   "v_accvgpr_read_b32 %1, a37\n\tv_mul_f32 %1, %2, %1\n\tv_accvgpr_write_b32 a37, %1\n\t"
                   "v_accvgpr_read_b32 %0, a38\n\tv_mul_f32 %0, %2, %0\n\tv_accvgpr_write_b32 a38, %0\n\t"
                   "v_accvgpr_read_b32 %1, a39\n\tv_mul_f32 %1, %2, %1\n\tv_accvgpr_write_b32 a39, %1\n\t"
                   "v_accvgpr_read_b32 %0, a40\n\tv_mul_f32 %0, %2, %0\n\tv_accvgpr_write_b32 a40, %0\n\t"
                   "v_accvgpr_read_b32 %1, a41\n\tv_mul_f32 %1, %2, %1\n\tv_accvgpr_write_b32 a41, %1\n\t"
                   "v_accvgpr_read_b32 %0, a42\n\tv_mul_f32 %0, %2, %0\n\tv_accvgpr_write_b32 a42, %0\n\t"
                   "v_accvgpr_read_b32 %1, a43\n\tv_mul_f32 %1, %2, %1\n\tv_accvgpr_write_b32 a43, %1\n\t"
                   "v_accvgpr_read_b32 %0, a44\n\tv_mul_f32 %0, %2, %0\n\tv_accvgpr_write_b32 a44, %0\n\t"
                   "v_accvgpr_read_b32 %1, a45\n\tv_mul_f32 %1, %2, %1\n\tv_accvgpr_write_b32 a45, %1\n\t"
                   "v_accvgpr_read_b32 %0, a46\n\tv_mul_f32 %0, %2, %0\n\tv_accvgpr_write_b32 a46, %0\n\t"
                   "v_accvgpr_read_b32 %1, a47\n\tv_mul_f32 %1, %2, %1\n\tv_accvgpr_write_b32 a47, %1\n\t"
                   "v_accvgpr_read_b32 %0, a48\n\tv_mul_f32 %0, %2, %0\n\tv_accvgpr_write_b32 a48, %0\n\t"
                   "v_accvgpr_read_b32 %1, a49\n\tv_mul_f32 %1, %2, %1\n\tv_accvgpr_write_b32 a49, %1\n\t"
                   "v_accvgpr_read_b32 %0, a50\n\tv_mul_f32 %0, %2, %0\n\tv_accvgpr_write_b32 a50, %0\n\t"
                   "v_accvgpr_read_b32 %1, a51\n\tv_mul_f32 %1, %2, %1\n\tv_accvgpr_write_b32 a51, %1\n\t"
                   "v_accvgpr_read_b32 %0, a52\n\tv_mul_f32 %0, %2, %0\n\tv_accvgpr_write_b32 a52, %0\n\t"
                   "v_accvgpr_read_b32 %1, a53\n\tv_mul_f32 %1, %2, %1\n\tv_accvgpr_write_b32 a53, %1\n\t"
                   "v_accvgpr_read_b32 %0, a54\n\tv_mul_f32 %0, %2, %0\n\tv_accvgpr_write_b32 a54, %0\n\t"
                   "v_accvgpr_read_b32 %1, a55\n\tv_mul_f32 %1, %2, %1\n\tv_accvgpr_write_b32 a55, %1\n\t"
                   "v_accvgpr_read_b32 %0, a56\n\tv_mul_f32 %0, %2, %0\n\tv_accvgpr_write_b32 a56, %0\n\t"
                   "v_accvgpr_read_b32 %1, a57\n\tv_mul_f32 %1, %2, %1\n\tv_accvgpr_write_b32 a57, %1\n\t"
                   "v_accvgpr_read_b32 %0, a58\n\tv_mul_f32 %0, %2, %0\n\tv_accvgpr_write_b32 a58, %0\n\t"
                   "v_accvgpr_read_b32 %1, a59\n\tv_mul_f32 %1, %2, %1\n\tv_accvgpr_write_b32 a59, %1\n\t"
                   "v_accvgpr_read_b32 %0, a60\n\tv_mul_f32 %0, %2, %0\n\tv_accvgpr_write_b32 a60, %0\n\t"
                   "v_accvgpr_read_b32 %1, a61\n\tv_mul_f32 %1, %2, %1\n\tv_accvgpr_write_b32 a61, %1\n\t"
                   "v_accvgpr_read_b32 %0, a62\n\tv_mul_f32 %0, %2, %0\n\tv_accvgpr_write_b32 a62, %0\n\t"
                   "v_accvgpr_read_b32 %1, a63\n\tv_mul_f32 %1, %2, %1\n\tv_accvgpr_write_b32 a63, %1\n\t"
                   "v_accvgpr_read_b32 %0, a64\n\tv_mul_f32 %0, %2, %0\n\tv_accvgpr_write_b32 a64, %0\n\t"
                   "v_accvgpr_read_b32 %1, a65\n\tv_mul_f32 %1, %2, %1\n\tv_accvgpr_write_b32 a65, %1\n\t"
                   "v_accvgpr_read_b32 %0, a66\n\tv_mul_f32 %0, %2, %0\n\tv_accvgpr_write_b32 a66, %0\n\t"
                   "v_accvgpr_read_b32 %1, a67\n\tv_mul_f32 %1, %2, %1\n\tv_accvgpr_write_b32 a67, %1\n\t"
                   "v_accvgpr_read_b32 %0, a68\n\tv_mul_f32 %0, %2, %0\n\tv_accvgpr_write_b32 a68, %0\n\t"
                   "v_accvgpr_read_b32 %1, a69\n\tv_mul_f32 %1, %2, %1\n\tv_accvgpr_write_b32 a69, %1\n\t"
                   "v_accvgpr_read_b32 %0, a70\n\tv_mul_f32 %0, %2, %0\n\tv_accvgpr_write_b32 a70, %0\n\t"
                   "v_accvgpr_read_b32 %1, a71\n\tv_mul_f32 %1, %2, %1\n\tv_accvgpr_write_b32 a71, %1\n\t"
                   "v_accvgpr_read_b32 %0, a72\n\tv_mul_f32 %0, %2, %0\n\tv_accvgpr_write_b32 a72, %0\n\t"
                   "v_accvgpr_read_b32 %1, a73\n\tv_mul_f32 %1, %2, %1\n\tv_accvgpr_write_b32 a73, %1\n\t"
                   "v_accvgpr_read_b32 %0, a74\n\tv_mul_f32 %0, %2, %0\n\tv_accvgpr_write_b32 a74, %0\n\t"
                   "v_accvgpr_read_b32 %1, a75\n\tv_mul_f32 %1, %2, %1\n\tv_accvgpr_write_b32 a75, %1\n\t"
                   "v_accvgpr_read_b32 %0, a76\n\tv_mul_f32 %0, %2, %0\n\tv_accvgpr_write_b32 a76, %0\n\t"
                   "v_accvgpr_read_b32 %1, a77\n\tv_mul_f32 %1, %2, %1\n\tv_accvgpr_write_b32 a77, %1\n\t"
                   "v_accvgpr_read_b32 %0, a78\n\tv_mul_f32 %0, %2, %0\n\tv_accvgpr_write_b32 a78, %0\n\t"
                   "v_accvgpr_read_b32 %1, a79\n\tv_mul_f32 %1, %2, %1\n\tv_accvgpr_write_b32 a79, %1\n\t"
                   "v_accvgpr_read_b32 %0, a80\n\tv_mul_f32 %0, %2, %0\n\tv_accvgpr_write_b32 a80, %0\n\t"
                   "v_accvgpr_read_b32 %1, a81\n\tv_mul_f32 %1, %2, %1\n\tv_accvgpr_write_b32 a81, %1\n\t"
                   "v_accvgpr_read_b32 %0, a82\n\tv_mul_f32 %0, %2, %0\n\tv_accvgpr_write_b32 a82, %0\n\t"
                   "v_accvgpr_read_b32 %1, a83\n\tv_mul_f32 %1, %2, %1\n\tv_accvgpr_write_b32 a83, %1\n\t"
                   "v_accvgpr_read_b32 %0, a84\n\tv_mul_f32 %0, %2, %0\n\tv_accvgpr_write_b32 a84, %0\n\t"
                   "v_accvgpr_read_b32 %1, a85\n\tv_mul_f32 %1, %2, %1\n\tv_accvgpr_write_b32 a85, %1\n\t"
                   "v_accvgpr_read_b32 %0, a86\n\tv_mul_f32 %0, %2, %0\n\tv_accvgpr_write_b32 a86, %0\n\t"
                   "v_accvgpr_read_b32 %1, a87\n\tv_mul_f32 %1, %2, %1\n\tv_accvgpr_write_b32 a87, %1\n\t"
                   "v_accvgpr_read_b32 %0, a88\n\tv_mul_f32 %0, %2, %0\n\tv_accvgpr_write_b32 a88, %0\n\t"
                   "v_accvgpr_read_b32 %1, a89\n\tv_mul_f32 %1, %2, %1\n\tv_accvgpr_write_b32 a89, %1\n\t"
                   "v_accvgpr_read_b32 %0, a90\n\tv_mul_f32 %0, %2, %0\n\tv_accvgpr_write_b32 a90, %0\n\t"
                   "v_accvgpr_read_b32 %1, a91\n\tv_mul_f32 %1, %2, %1\n\tv_accvgpr_write_b32 a91, %1\n\t"
                   "v_accvgpr_read_b32 %0, a92\n\tv_mul_f32 %0, %2, %0\n\tv_accvgpr_write_b32 a92, %0\n\t"
                   "v_accvgpr_read_b32 %1, a93\n\tv_mul_f32 %1, %2, %1\n\tv_accvgpr_write_b32 a93, %1\n\t"
                   "v_accvgpr_read_b32 %0, a94\n\tv_mul_f32 %0, %2, %0\n\tv_accvgpr_write_b32 a94, %0\n\t"
                   "v_accvgpr_read_b32 %1, a95\n\tv_mul_f32 %1, %2, %1\n\tv_accvgpr_write_b32 a95, %1\n\t"
                   "v_accvgpr_read_b32 %0, a96\n\tv_mul_f32 %0, %2, %0\n\tv_accvgpr_write_b32 a96, %0\n\t"
                   "v_accvgpr_read_b32 %1, a97\n\tv_mul_f32 %1, %2, %1\n\tv_accvgpr_write_b32 a97, %1\n\t"
                   "v_accvgpr_read_b32 %0, a98\n\tv_mul_f32 %0, %2, %0\n\tv_accvgpr_write_b32 a98, %0\n\t"
                   "v_accvgpr_read_b32 %1, a99\n\tv_mul_f32 %1, %2, %1\n\tv_accvgpr_write_b32 a99, %1\n\t"
                   "v_accvgpr_read_b32 %0, a100\n\tv_mul_f32 %0, %2, %0\n\tv_accvgpr_write_b32 a100, %0\n\t"
                   "v_accvgpr_read_b32 %1, a101\n\tv_mul_f32 %1, %2, %1\n\tv_accvgpr_write_b32 a101, %1\n\t"
                   "v_accvgpr_read_b32 %0, a102\n\tv_mul_f32 %0, %2, %0\n\tv_accvgpr_write_b32 a102, %0\n\t"
                   "v_accvgpr_read_b32 %1, a103\n\tv_mul_f32 %1, %2, %1\n\tv_accvgpr_write_b32 a103, %1\n\t"
                   "v_accvgpr_read_b32 %0, a104\n\tv_mul_f32 %0, %2, %0\n\tv_accvgpr_write_b32 a104, %0\n\t"
                   "v_accvgpr_read_b32 %1, a105\n\tv_mul_f32 %1, %2, %1\n\tv_accvgpr_write_b32 a105, %1\n\t"
                   "v_accvgpr_read_b32 %0, a106\n\tv_mul_f32 %0, %2, %0\n\tv_accvgpr_write_b32 a106, %0\n\t"
                   "v_accvgpr_read_b32 %1, a107\n\tv_mul_f32 %1, %2, %1\n\tv_accvgpr_write_b32 a107, %1\n\t"
                   "v_accvgpr_read_b32 %0, a108\n\tv_mul_f32 %0, %2, %0\n\tv_accvgpr_write_b32 a108, %0\n\t"
                   "v_accvgpr_read_b32 %1, a109\n\tv_mul_f32 %1, %2, %1\n\tv_accvgpr_write_b32 a109, %1\n\t"
                   "v_accvgpr_read_b32 %0, a110\n\tv_mul_f32 %0, %2, %0\n\tv_accvgpr_write_b32 a110, %0\n\t"
                   "v_accvgpr_read_b32 %1, a111\n\tv_mul_f32 %1, %2, %1\n\tv_accvgpr_write_b32 a111, %1\n\t"
                   "v_accvgpr_read_b32 %0, a112\n\tv_mul_f32 %0, %2, %0\n\tv_accvgpr_write_b32 a112, %0\n\t"
                   "v_accvgpr_read_b32 %1, a113\n\tv_mul_f32 %1, %2, %1\n\tv_accvgpr_write_b32 a113, %1\n\t"
                   "v_accvgpr_read_b32 %0, a114\n\tv_mul_f32 %0, %2, %0\n\tv_accvgpr_write_b32 a114, %0\n\t"
                   "v_accvgpr_read_b32 %1, a115\n\tv_mul_f32 %1, %2, %1\n\tv_accvgpr_write_b32 a115, %1\n\t"
                   "v_accvgpr_read_b32 %0, a116\n\tv_mul_f32 %0, %2, %0\n\tv_accvgpr_write_b32 a116, %0\n\t"
                   "v_accvgpr_read_b32 %1, a117\n\tv_mul_f32 %1, %2, %1\n\tv_accvgpr_write_b32 a117, %1\n\t"
                   "v_accvgpr_read_b32 %0, a118\n\tv_mul_f32 %0, %2, %0\n\tv_accvgpr_write_b32 a118, %0\n\t"
                   "v_accvgpr_read_b32 %1, a119\n\tv_mul_f32 %1, %2, %1\n\tv_accvgpr_write_b32 a119, %1\n\t"
                   "v_accvgpr_read_b32 %0, a120\n\tv_mul_f32 %0, %2, %0\n\tv_accvgpr_write_b32 a120, %0\n\t"
                   "v_accvgpr_read_b32 %1, a121\n\tv_mul_f32 %1, %2, %1\n\tv_accvgpr_write_b32 a121, %1\n\t"
                   "v_accvgpr_read_b32 %0, a122\n\tv_mul_f32 %0, %2, %0\n\tv_accvgpr_write_b32 a122, %0\n\t"
                   "v_accvgpr_read_b32 %1, a123\n\tv_mul_f32 %1, %2, %1\n\tv_accvgpr_write_b32 a123, %1\n\t"
                   "v_accvgpr_read_b32 %0, a124\n\tv_mul_f32 %0, %2, %0\n\tv_accvgpr_write_b32 a124, %0\n\t"
                   "v_accvgpr_read_b32 %1, a125\n\tv_mul_f32 %1, %2, %1\n\tv_accvgpr_write_b32 a125, %1\n\t"
                   "v_accvgpr_read_b32 %0, a126\n\tv_mul_f32 %0, %2, %0\n\tv_accvgpr_write_b32 a126, %0\n\t"
                   "v_accvgpr_read_b32 %1, a127\n\tv_mul_f32 %1, %2, %1\n\tv_accvgpr_write_b32 a127, %1\n\t"
                   "v_accvgpr_read_b32 %0, a128\n\tv_mul_f32 %0, %3, %0\n\tv_accvgpr_write_b32 a128, %0\n\t"
                   "v_accvgpr_read_b32 %1, a129\n\tv_mul_f32 %1, %3, %1\n\tv_accvgpr_write_b32 a129, %1\n\t"
                   "v_accvgpr_read_b32 %0, a130\n\tv_mul_f32 %0, %3, %0\n\tv_accvgpr_write_b32 a130, %0\n\t"
                   "v_accvgpr_read_b32 %1, a131\n\tv_mul_f32 %1, %3, %1\n\tv_accvgpr_write_b32 a131, %1\n\t"
                   "v_accvgpr_read_b32 %0, a132\n\tv_mul_f32 %0, %3, %0\n\tv_accvgpr_write_b32 a132, %0\n\t"
                   "v_accvgpr_read_b32 %1, a133\n\tv_mul_f32 %1, %3, %1\n\tv_accvgpr_write_b32 a133, %1\n\t"
                   "v_accvgpr_read_b32 %0, a134\n\tv_mul_f32 %0, %3, %0\n\tv_accvgpr_write_b32 a134, %0\n\t"
                   "v_accvgpr_read_b32 %1, a135\n\tv_mul_f32 %1, %3, %1\n\tv_accvgpr_write_b32 a135, %1\n\t"
                   "v_accvgpr_read_b32 %0, a136\n\tv_mul_f32 %0, %3, %0\n\tv_accvgpr_write_b32 a136, %0\n\t"
                   "v_accvgpr_read_b32 %1, a137\n\tv_mul_f32 %1, %3, %1\n\tv_accvgpr_write_b32 a137, %1\n\t"
                   "v_accvgpr_read_b32 %0, a138\n\tv_mul_f32 %0, %3, %0\n\tv_accvgpr_write_b32 a138, %0\n\t"
                   "v_accvgpr_read_b32 %1, a139\n\tv_mul_f32 %1, %3, %1\n\tv_accvgpr_write_b32 a139, %1\n\t"
                   "v_accvgpr_read_b32 %0, a140\n\tv_mul_f32 %0, %3, %0\n\tv_accvgpr_write_b32 a140, %0\n\t"
                   "v_accvgpr_read_b32 %1, a141\n\tv_mul_f32 %1, %3, %1\n\tv_accvgpr_write_b32 a141, %1\n\t"
                   "v_accvgpr_read_b32 %0, a142\n\tv_mul_f32 %0, %3, %0\n\tv_accvgpr_write_b32 a142, %0\n\t"
                   "v_accvgpr_read_b32 %1, a143\n\tv_mul_f32 %1, %3, %1\n\tv_accvgpr_write_b32 a143, %1\n\t"
                   "v_accvgpr_read_b32 %0, a144\n\tv_mul_f32 %0, %3, %0\n\tv_accvgpr_write_b32 a144, %0\n\t"
                   "v_accvgpr_read_b32 %1, a145\n\tv_mul_f32 %1, %3, %1\n\tv_accvgpr_write_b32 a145, %1\n\t"
                   "v_accvgpr_read_b32 %0, a146\n\tv_mul_f32 %0, %3, %0\n\tv_accvgpr_write_b32 a146, %0\n\t"
                   "v_accvgpr_read_b32 %1, a147\n\tv_mul_f32 %1, %3, %1\n\tv_accvgpr_write_b32 a147, %1\n\t"
                   "v_accvgpr_read_b32 %0, a148\n\tv_mul_f32 %0, %3, %0\n\tv_accvgpr_write_b32 a148, %0\n\t"
                   "v_accvgpr_read_b32 %1, a149\n\tv_mul_f32 %1, %3, %1\n\tv_accvgpr_write_b32 a149, %1\n\t"
                   "v_accvgpr_read_b32 %0, a150\n\tv_mul_f32 %0, %3, %0\n\tv_accvgpr_write_b32 a150, %0\n\t"
                   "v_accvgpr_read_b32 %1, a151\n\tv_mul_f32 %1, %3, %1\n\tv_accvgpr_write_b32 a151, %1\n\t"
                   "v_accvgpr_read_b32 %0, a152\n\tv_mul_f32 %0, %3, %0\n\tv_accvgpr_write_b32 a152, %0\n\t"
                   "v_accvgpr_read_b32 %1, a153\n\tv_mul_f32 %1, %3, %1\n\tv_accvgpr_write_b32 a153, %1\n\t"
                   "v_accvgpr_read_b32 %0, a154\n\tv_mul_f32 %0, %3, %0\n\tv_accvgpr_write_b32 a154, %0\n\t"
                   "v_accvgpr_read_b32 %1, a155\n\tv_mul_f32 %1, %3, %1\n\tv_accvgpr_write_b32 a155, %1\n\t"
                   "v_accvgpr_read_b32 %0, a156\n\tv_mul_f32 %0, %3, %0\n\tv_accvgpr_write_b32 a156, %0\n\t"
                   "v_accvgpr_read_b32 %1, a157\n\tv_mul_f32 %1, %3, %1\n\tv_accvgpr_write_b32 a157, %1\n\t"
                   "v_accvgpr_read_b32 %0, a158\n\tv_mul_f32 %0, %3, %0\n\tv_accvgpr_write_b32 a158, %0\n\t"
                   "v_accvgpr_read_b32 %1, a159\n\tv_mul_f32 %1, %3, %1\n\tv_accvgpr_write_b32 a159, %1\n\t"
                   "v_accvgpr_read_b32 %0, a160\n\tv_mul_f32 %0, %3, %0\n\tv_accvgpr_write_b32 a160, %0\n\t"
                   "v_accvgpr_read_b32 %1, a161\n\tv_mul_f32 %1, %3, %1\n\tv_accvgpr_write_b32 a161, %1\n\t"
                   "v_accvgpr_read_b32 %0, a162\n\tv_mul_f32 %0, %3, %0\n\tv_accvgpr_write_b32 a162, %0\n\t"
                   "v_accvgpr_read_b32 %1, a163\n\tv_mul_f32 %1, %3, %1\n\tv_accvgpr_write_b32 a163, %1\n\t"
                   "v_accvgpr_read_b32 %0, a164\n\tv_mul_f32 %0, %3, %0\n\tv_accvgpr_write_b32 a164, %0\n\t"
                   "v_accvgpr_read_b32 %1, a165\n\tv_mul_f32 %1, %3, %1\n\tv_accvgpr_write_b32 a165, %1\n\t"
                   "v_accvgpr_read_b32 %0, a166\n\tv_mul_f32 %0, %3, %0\n\tv_accvgpr_write_b32 a166, %0\n\t"
                   "v_accvgpr_read_b32 %1, a167\n\tv_mul_f32 %1, %3, %1\n\tv_accvgpr_write_b32 a167, %1\n\t"
                   "v_accvgpr_read_b32 %0, a168\n\tv_mul_f32 %0, %3, %0\n\tv_accvgpr_write_b32 a168, %0\n\t"
                   "v_accvgpr_read_b32 %1, a169\n\tv_mul_f32 %1, %3, %1\n\tv_accvgpr_write_b32 a169, %1\n\t"
                   "v_accvgpr_read_b32 %0, a170\n\tv_mul_f32 %0, %3, %0\n\tv_accvgpr_write_b32 a170, %0\n\t"
                   "v_accvgpr_read_b32 %1, a171\n\tv_mul_f32 %1, %3, %1\n\tv_accvgpr_write_b32 a171, %1\n\t"
                   "v_accvgpr_read_b32 %0, a172\n\tv_mul_f32 %0, %3, %0\n\tv_accvgpr_write_b32 a172, %0\n\t"
                   "v_accvgpr_read_b32 %1, a173\n\tv_mul_f32 %1, %3, %1\n\tv_accvgpr_write_b32 a173, %1\n\t"
                   "v_accvgpr_read_b32 %0, a174\n\tv_mul_f32 %0, %3, %0\n\tv_accvgpr_write_b32 a174, %0\n\t"
                   "v_accvgpr_read_b32 %1, a175\n\tv_mul_f32 %1, %3, %1\n\tv_accvgpr_write_b32 a175, %1\n\t"
                   "v_accvgpr_read_b32 %0, a176\n\tv_mul_f32 %0, %3, %0\n\tv_accvgpr_write_b32 a176, %0\n\t"
                   "v_accvgpr_read_b32 %1, a177\n\tv_mul_f32 %1, %3, %1\n\tv_accvgpr_write_b32 a177, %1\n\t"
                   "v_accvgpr_read_b32 %0, a178\n\tv_mul_f32 %0, %3, %0\n\tv_accvgpr_write_b32 a178, %0\n\t"
                   "v_accvgpr_read_b32 %1, a179\n\tv_mul_f32 %1, %3, %1\n\tv_accvgpr_write_b32 a179, %1\n\t"
                   "v_accvgpr_read_b32 %0, a180\n\tv_mul_f32 %0, %3, %0\n\tv_accvgpr_write_b32 a180, %0\n\t"
                   "v_accvgpr_read_b32 %1, a181\n\tv_mul_f32 %1, %3, %1\n\tv_accvgpr_write_b32 a181, %1\n\t"
                   "v_accvgpr_read_b32 %0, a182\n\tv_mul_f32 %0, %3, %0\n\tv_accvgpr_write_b32 a182, %0\n\t"
                   "v_accvgpr_read_b32 %1, a183\n\tv_mul_f32 %1, %3, %1\n\tv_accvgpr_write_b32 a183, %1\n\t"
                   "v_accvgpr_read_b32 %0, a184\n\tv_mul_f32 %0, %3, %0\n\tv_accvgpr_write_b32 a184, %0\n\t"
                   "v_accvgpr_read_b32 %1, a185\n\tv_mul_f32 %1, %3, %1\n\tv_accvgpr_write_b32 a185, %1\n\t"
                   "v_accvgpr_read_b32 %0, a186\n\tv_mul_f32 %0, %3, %0\n\tv_accvgpr_write_b32 a186, %0\n\t"
                   "v_accvgpr_read_b32 %1, a187\n\tv_mul_f32 %1, %3, %1\n\tv_accvgpr_write_b32 a187, %1\n\t"
                   "v_accvgpr_read_b32 %0, a188\n\tv_mul_f32 %0, %3, %0\n\tv_accvgpr_write_b32 a188, %0\n\t"
                   "v_accvgpr_read_b32 %1, a189\n\tv_mul_f32 %1, %3, %1\n\tv_accvgpr_write_b32 a189, %1\n\t"
                   "v_accvgpr_read_b32 %0, a190\n\tv_mul_f32 %0, %3, %0\n\tv_accvgpr_write_b32 a190, %0\n\t"
                   "v_accvgpr_read_b32 %1, a191\n\tv_mul_f32 %1, %3, %1\n\tv_accvgpr_write_b32 a191, %1\n\t"
                   "v_accvgpr_read_b32 %0, a192\n\tv_mul_f32 %0, %3, %0\n\tv_accvgpr_write_b32 a192, %0\n\t"
                   "v_accvgpr_read_b32 %1, a193\n\tv_mul_f32 %1, %3, %1\n\tv_accvgpr_write_b32 a193, %1\n\t"
                   "v_accvgpr_read_b32 %0, a194\n\tv_mul_f32 %0, %3, %0\n\tv_accvgpr_write_b32 a194, %0\n\t"
                   "v_accvgpr_read_b32 %1, a195\n\tv_mul_f32 %1, %3, %1\n\tv_accvgpr_write_b32 a195, %1\n\t"
                   "v_accvgpr_read_b32 %0, a196\n\tv_mul_f32 %0, %3, %0\n\tv_accvgpr_write_b32 a196, %0\n\t"
                   "v_accvgpr_read_b32 %1, a197\n\tv_mul_f32 %1, %3, %1\n\tv_accvgpr_write_b32 a197, %1\n\t"
                   "v_accvgpr_read_b32 %0, a198\n\tv_mul_f32 %0, %3, %0\n\tv_accvgpr_write_b32 a198, %0\n\t"
                   "v_accvgpr_read_b32 %1, a199\n\tv_mul_f32 %1, %3, %1\n\tv_accvgpr_write_b32 a199, %1\n\t"
                   "v_accvgpr_read_b32 %0, a200\n\tv_mul_f32 %0, %3, %0\n\tv_accvgpr_write_b32 a200, %0\n\t"
                   "v_accvgpr_read_b32 %1, a201\n\tv_mul_f32 %1, %3, %1\n\tv_accvgpr_write_b32 a201, %1\n\t"
                   "v_accvgpr_read_b32 %0, a202\n\tv_mul_f32 %0, %3, %0\n\tv_accvgpr_write_b32 a202, %0\n\t"
                   "v_accvgpr_read_b32 %1, a203\n\tv_mul_f32 %1, %3, %1\n\tv_accvgpr_write_b32 a203, %1\n\t"
                   "v_accvgpr_read_b32 %0, a204\n\tv_mul_f32 %0, %3, %0\n\tv_accvgpr_write_b32 a204, %0\n\t"
                   "v_accvgpr_read_b32 %1, a205\n\tv_mul_f32 %1, %3, %1\n\tv_accvgpr_write_b32 a205, %1\n\t"
                   "v_accvgpr_read_b32 %0, a206\n\tv_mul_f32 %0, %3, %0\n\tv_accvgpr_write_b32 a206, %0\n\t"
                   "v_accvgpr_read_b32 %1, a207\n\tv_mul_f32 %1, %3, %1\n\tv_accvgpr_write_b32 a207, %1\n\t"
                   "v_accvgpr_read_b32 %0, a208\n\tv_mul_f32 %0, %3, %0\n\tv_accvgpr_write_b32 a208, %0\n\t"
                   "v_accvgpr_read_b32 %1, a209\n\tv_mul_f32 %1, %3, %1\n\tv_accvgpr_write_b32 a209, %1\n\t"
                   "v_accvgpr_read_b32 %0, a210\n\tv_mul_f32 %0, %3, %0\n\tv_accvgpr_write_b32 a210, %0\n\t"
                   "v_accvgpr_read_b32 %1, a211\n\tv_mul_f32 %1, %3, %1\n\tv_accvgpr_write_b32 a211, %1\n\t"
                   "v_accvgpr_read_b32 %0, a212\n\tv_mul_f32 %0, %3, %0\n\tv_accvgpr_write_b32 a212, %0\n\t"
                   "v_accvgpr_read_b32 %1, a213\n\tv_mul_f32 %1, %3, %1\n\tv_accvgpr_write_b32 a213, %1\n\t"
                   "v_accvgpr_read_b32 %0, a214\n\tv_mul_f32 %0, %3, %0\n\tv_accvgpr_write_b32 a214, %0\n\t"
                   "v_accvgpr_read_b32 %1, a215\n\tv_mul_f32 %1, %3, %1\n\tv_accvgpr_write_b32 a215, %1\n\t"
                   "v_accvgpr_read_b32 %0, a216\n\tv_mul_f32 %0, %3, %0\n\tv_accvgpr_write_b32 a216, %0\n\t"
                   "v_accvgpr_read_b32 %1, a217\n\tv_mul_f32 %1, %3, %1\n\tv_accvgpr_write_b32 a217, %1\n\t"
                   "v_accvgpr_read_b32 %0, a218\n\tv_mul_f32 %0, %3, %0\n\tv_accvgpr_write_b32 a218, %0\n\t"
                   "v_accvgpr_read_b32 %1, a219\n\tv_mul_f32 %1, %3, %1\n\tv_accvgpr_write_b32 a219, %1\n\t"
                   "v_accvgpr_read_b32 %0, a220\n\tv_mul_f32 %0, %3, %0\n\tv_accvgpr_write_b32 a220, %0\n\t"
                   "v_accvgpr_read_b32 %1, a221\n\tv_mul_f32 %1, %3, %1\n\tv_accvgpr_write_b32 a221, %1\n\t"
                   "v_accvgpr_read_b32 %0, a222\n\tv_mul_f32 %0, %3, %0\n\tv_accvgpr_write_b32 a222, %0\n\t"
                   "v_accvgpr_read_b32 %1, a223\n\tv_mul_f32 %1, %3, %1\n\tv_accvgpr_write_b32 a223, %1\n\t"
                   "v_accvgpr_read_b32 %0, a224\n\tv_mul_f32 %0, %3, %0\n\tv_accvgpr_write_b32 a224, %0\n\t"
                   "v_accvgpr_read_b32 %1, a225\n\tv_mul_f32 %1, %3, %1\n\tv_accvgpr_write_b32 a225, %1\n\t"
                   "v_accvgpr_read_b32 %0, a226\n\tv_mul_f32 %0, %3, %0\n\tv_accvgpr_write_b32 a226, %0\n\t"
                   "v_accvgpr_read_b32 %1, a227\n\tv_mul_f32 %1, %3, %1\n\tv_accvgpr_write_b32 a227, %1\n\t"
                   "v_accvgpr_read_b32 %0, a228\n\tv_mul_f32 %0, %3, %0\n\tv_accvgpr_write_b32 a228, %0\n\t"
                   "v_accvgpr_read_b32 %1, a229\n\tv_mul_f32 %1, %3, %1\n\tv_accvgpr_write_b32 a229, %1\n\t"
                   "v_accvgpr_read_b32 %0, a230\n\tv_mul_f32 %0, %3, %0\n\tv_accvgpr_write_b32 a230, %0\n\t"
                   "v_accvgpr_read_b32 %1, a231\n\tv_mul_f32 %1, %3, %1\n\tv_accvgpr_write_b32 a231, %1\n\t"
                   "v_accvgpr_read_b32 %0, a232\n\tv_mul_f32 %0, %3, %0\n\tv_accvgpr_write_b32 a232, %0\n\t"
                   "v_accvgpr_read_b32 %1, a233\n\tv_mul_f32 %1, %3, %1\n\tv_accvgpr_write_b32 a233, %1\n\t"
                   "v_accvgpr_read_b32 %0, a234\n\tv_mul_f32 %0, %3, %0\n\tv_accvgpr_write_b32 a234, %0\n\t"
                   "v_accvgpr_read_b32 %1, a235\n\tv_mul_f32 %1, %3, %1\n\tv_accvgpr_write_b32 a235, %1\n\t"
                   "v_accvgpr_read_b32 %0, a236\n\tv_mul_f32 %0, %3, %0\n\tv_accvgpr_write_b32 a236, %0\n\t"
                   "v_accvgpr_read_b32 %1, a237\n\tv_mul_f32 %1, %3, %1\n\tv_accvgpr_write_b32 a237, %1\n\t"
                   "v_accvgpr_read_b32 %0, a238\n\tv_mul_f32 %0, %3, %0\n\tv_accvgpr_write_b32 a238, %0\n\t"
                   "v_accvgpr_read_b32 %1, a239\n\tv_mul_f32 %1, %3, %1\n\tv_accvgpr_write_b32 a239, %1\n\t"
                   "v_accvgpr_read_b32 %0, a240\n\tv_mul_f32 %0, %3, %0\n\tv_accvgpr_write_b32 a240, %0\n\t"
                   "v_accvgpr_read_b32 %1, a241\n\tv_mul_f32 %1, %3, %1\n\tv_accvgpr_write_b32 a241, %1\n\t"
                   "v_accvgpr_read_b32 %0, a242\n\tv_mul_f32 %0, %3, %0\n\tv_accvgpr_write_b32 a242, %0\n\t"
                   "v_accvgpr_read_b32 %1, a243\n\tv_mul_f32 %1, %3, %1\n\tv_accvgpr_write_b32 a243, %1\n\t"
                   "v_accvgpr_read_b32 %0, a244\n\tv_mul_f32 %0, %3, %0\n\tv_accvgpr_write_b32 a244, %0\n\t"
                   "v_accvgpr_read_b32 %1, a245\n\tv_mul_f32 %1, %3, %1\n\tv_accvgpr_write_b32 a245, %1\n\t"
                   "v_accvgpr_read_b32 %0, a246\n\tv_mul_f32 %0, %3, %0\n\tv_accvgpr_write_b32 a246, %0\n\t"
                   "v_accvgpr_read_b32 %1, a247\n\tv_mul_f32 %1, %3, %1\n\tv_accvgpr_write_b32 a247, %1\n\t"
                   "v_accvgpr_read_b32 %0, a248\n\tv_mul_f32 %0, %3, %0\n\tv_accvgpr_write_b32 a248, %0\n\t"
                   "v_accvgpr_read_b32 %1, a249\n\tv_mul_f32 %1, %3, %1\n\tv_accvgpr_write_b32 a249, %1\n\t"
                   "v_accvgpr_read_b32 %0, a250\n\tv_mul_f32 %0, %3, %0\n\tv_accvgpr_write_b32 a250, %0\n\t"
                   "v_accvgpr_read_b32 %1, a251\n\tv_mul_f32 %1, %3, %1\n\tv_accvgpr_write_b32 a251, %1\n\t"
                   "v_accvgpr_read_b32 %0, a252\n\tv_mul_f32 %0, %3, %0\n\tv_accvgpr_write_b32 a252, %0\n\t"
                   "v_accvgpr_read_b32 %1, a253\n\tv_mul_f32 %1, %3, %1\n\tv_accvgpr_write_b32 a253, %1\n\t"
                   "v_accvgpr_read_b32 %0, a254\n\tv_mul_f32 %0, %3, %0\n\tv_accvgpr_write_b32 a254, %0\n\t"
                   "v_accvgpr_read_b32 %1, a255\n\tv_mul_f32 %1, %3, %1\n\tv_accvgpr_write_b32 a255, %1\n\t"
                   "s_nop 1"
                   : "=&v"(t0), "=&v"(t1) : "v"(c.fct[0]), "v"(c.fct[1]) : ACLOB);
      c.pend = false;
    }
  }
  asm volatile("s_nop 1" ::: "memory");
  GAP();
}
template <int T, bool HASPV, bool HASSM> DI void phaseB(Ctx4& c) {
  phaseB_<T, HASPV, HASSM>(c, std::make_integer_sequence<int, 32>{});
}

template <int A, int OFF> DI void acc_dswrite4(unsigned addr) {
  asm volatile("ds_write_b128 %0, a[%c1:%c2] offset:%c3" ::"v"(addr), "i"(A), "i"(A + 3), "i"(OFF) : "memory", ACLOB);
}
template <int A> DI f32x4 acc_read4() {
  f32x4 r;
  asm volatile("v_accvgpr_read_b32 %0, a[%c4]\n\tv_accvgpr_read_b32 %1, a[%c5]\n\tv_accvgpr_read_b32 %2, a[%c6]\n\t"
               "v_accvgpr_read_b32 %3, a[%c7]"
               : "=v"(r[0]), "=v"(r[1]), "=v"(r[2]), "=v"(r[3]) : "i"(A), "i"(A + 1), "i"(A + 2), "i"(A + 3) : ACLOB);
  return r;
}

__global__ __launch_bounds__(256, 1) void attn4_kernel(const u16* __restrict__ x, const u16* __restrict__ wq,
                                                       const u16* __restrict__ kc, const u16* __restrict__ vc,
                                                       const u16* __restrict__ wo, u16* __restrict__ o,
                                                       unsigned* __restrict__ cnt DBGARG) {
  extern __shared__ __attribute__((aligned(16))) char lds[];
  STAMP(0);
  if (blockIdx.x == 0 && threadIdx.x < 128) cnt[threadIdx.x] = 0u;
  const int tid = threadIdx.x, lane = tid & 63;
  const int w = __builtin_amdgcn_readfirstlane(tid >> 6);
  const int r = lane & 31, h = lane >> 5;
  const int bid = blockIdx.x;
  const int xcd = bid & 7, j = bid >> 3;
  const int grp = (j >> 2) * 8 + xcd, sub = j & 3;
  const int b = grp >> 3, kvh = grp & 7;
  const int head = kvh * 2 + (sub >> 1), rhalf = sub & 1;
  const int row0 = b * SQ + rhalf * 56;

  const u16* Kw = kc + ((size_t)(b * HKV + kvh) * SK + 256 * w) * DH;
  const u16* Vw = vc + ((size_t)(b * HKV + kvh) * SK + 256 * w) * DH;

  Ctx4 c;
  c.pfd = 0; c.sub = sub;
  c.Kp = Kw; c.Vp = Vw; c.pfoff = lane * 128;
  c.ring = lds + w * 40960;
  c.rk = __builtin_amdgcn_make_buffer_rsrc((void*)Kw, 0, 256 * DH * 2, 0x00020000);
  c.rv = __builtin_amdgcn_make_buffer_rsrc((void*)Vw, 0, 256 * DH * 2, 0x00020000);
#pragma unroll
  for (int pi = 0; pi < 4; ++pi) {
    const int krow = 4 * pi + (lane >> 4);
    c.koff[pi] = krow * 512 + (((lane & 15) ^ (krow & 15)) * 16);
  }
#pragma unroll
  for (int pi = 0; pi < 2; ++pi) {
    const int vrow = 2 * pi + (lane >> 5);
    c.voff[pi] = vrow * 512 + (((lane & 31) ^ ((vrow & 3) << 2)) * 16);
  }
  {
    __amdgpu_buffer_rsrc_t rx =
        __builtin_amdgcn_make_buffer_rsrc((void*)(x + (size_t)row0 * DM), 0, 56 * DM * 2, 0x00020000);
#pragma unroll
    for (int p = 0; p < 14; ++p) {
      const int ci = 64 * p + lane;
      const int lr = ci / 56, cp = ci - lr * 56;
      int gr = 16 * w + lr; gr = gr > 55 ? 55 : gr;
      const int cs = (cp & ~7) | ((cp ^ (lr >> 1)) & 7);
      __builtin_amdgcn_raw_ptr_buffer_load_lds(rx, (lds_vp)(c.ring + 3 * 8192 + p * 1024), 16, gr * (DM * 2) + cs * 16,
                                               0, 0, 0);
    }
  }
  {
    const int colw = head * DH + 64 * w;
    __amdgpu_buffer_rsrc_t rw =
        __builtin_amdgcn_make_buffer_rsrc((void*)(wq + colw), 0, (DM * NQ - colw) * 2, 0x00020000);
    const int drow = lane >> 3;
    const int wvoff = drow * (NQ * 2) + (((lane & 7) ^ (((drow >> 1) & 1) << 2)) * 16);
    auto wissue = [&](int ch) {
#pragma unroll
      for (int p = 0; p < 8; ++p)
        __builtin_amdgcn_raw_ptr_buffer_load_lds(rw, (lds_vp)(c.ring + ((ch + 2) % 3) * 8192 + p * 1024), 16, wvoff,
                                                 (64 * ch + 8 * p) * (NQ * 2), 0, 0);
    };
    wissue(0); wissue(1); wissue(2);
    const int g = lane >> 4, qq = (lane >> 2) & 3, pp = lane & 3;
    const unsigned trb = (unsigned)(size_t)c.ring + (8 * (g >> 1) + qq) * 128 + ((32 * (g & 1) + 8 * pp) ^ ((qq >> 1) << 6));
    const char* xa[2][4];
#pragma unroll
    for (int qb = 0; qb < 2; ++qb) {
      const int R = 32 * qb + r, lr = R & 15;
#pragma unroll
      for (int v = 0; v < 4; ++v)
        xa[qb][v] = lds + (R >> 4) * 40960 + 3 * 8192 + lr * 896 + ((((2 * v + h) ^ (lr >> 1)) & 7) * 16);
    }
    f32x16 qa[2][2];
#pragma unroll
    for (int a2 = 0; a2 < 2; ++a2)
#pragma unroll
      for (int b2 = 0; b2 < 2; ++b2)
#pragma unroll
        for (int i = 0; i < 16; ++i) qa[a2][b2][i] = 0.f;
    s16x4 fa[2][4];
    bf16x8 fx[2][2];
    auto rdstep = [&](int i, int bsel) {
      const int ch = i >> 2, s4 = i & 3;
      const unsigned tb = trb + ((ch + 2) % 3) * 8192 + s4 * 2048;
      fa[bsel][0] = trread(tb);
      fa[bsel][1] = trread(tb + 512);
      fa[bsel][2] = trread(tb ^ 64);
      fa[bsel][3] = trread((tb + 512) ^ 64);
      asm volatile("ds_read_b128 %0, %1" : "=v"(fx[bsel][0]) : "v"((unsigned)(size_t)(xa[0][s4] + ch * 128)) : "memory");
      asm volatile("ds_read_b128 %0, %1" : "=v"(fx[bsel][1]) : "v"((unsigned)(size_t)(xa[1][s4] + ch * 128)) : "memory");
    };
    auto refill_piece = [&](int k, int p) {
      if (k <= 3) {
        __builtin_amdgcn_raw_ptr_buffer_load_lds(rw, (lds_vp)(c.ring + ((k + 5) % 3) * 8192 + p * 1024), 16, wvoff,
                                                 (64 * (k + 3) + 8 * p) * (NQ * 2), 0, 0);
      }
    };
    WAITV(16);
    __builtin_amdgcn_s_barrier();
    STAMP(11);
    rdstep(0, 0);
#pragma unroll
    for (int i = 0; i < 28; ++i) {
      const int bs = i & 1, ch = i >> 2, s4 = i & 3;
      if (i + 1 < 28) {
        if (s4 == 3) { if (ch == 0) WAITV(8); else WAITV(6); }
        rdstep(i + 1, bs ^ 1);
        asm volatile("s_waitcnt lgkmcnt(6)" : "+v"(fa[bs][0]), "+v"(fa[bs][1]), "+v"(fa[bs][2]), "+v"(fa[bs][3]), "+v"(fx[bs][0]), "+v"(fx[bs][1])::"memory");
      } else {
        asm volatile("s_waitcnt lgkmcnt(0)" : "+v"(fa[bs][0]), "+v"(fa[bs][1]), "+v"(fa[bs][2]), "+v"(fa[bs][3]), "+v"(fx[bs][0]), "+v"(fx[bs][1])::"memory");
      }
      bf16x8 A0 = cat8(fa[bs][0], fa[bs][1]), A1 = cat8(fa[bs][2], fa[bs][3]);
      qa[0][0] = __builtin_amdgcn_mfma_f32_32x32x16_bf16(A0, fx[bs][0], qa[0][0], 0, 0, 0);
      GAP();
      if (ch == 5 && s4 == 0) { prefetch_unit(c, 6 + sub); prefetch_unit(c, 10 + sub); }
      if (ch >= 1) {
        if (ch - 1 <= 3) refill_piece(ch - 1, 2 * s4);
        else if (ch == 5) { if (s4 == 0) dma_piece<0, 0>(c); else if (s4 == 1) dma_piece<0, 2>(c); else if (s4 == 2) dma_piece<0, 4>(c); else dma_piece<0, 6>(c); }
        else { if (s4 == 0) dma_piece<1, 0>(c); else if (s4 == 1) dma_piece<1, 2>(c); else if (s4 == 2) dma_piece<1, 4>(c); else dma_piece<1, 6>(c); }
      }
      GAP();
      qa[0][1] = __builtin_amdgcn_mfma_f32_32x32x16_bf16(A1, fx[bs][0], qa[0][1], 0, 0, 0);
      qa[1][0] = __builtin_amdgcn_mfma_f32_32x32x16_bf16(A0, fx[bs][1], qa[1][0], 0, 0, 0);
      GAP();
      if (ch >= 1) {
        if (ch - 1 <= 3) refill_piece(ch - 1, 2 * s4 + 1);
        else if (ch == 5) { if (s4 == 0) dma_piece<0, 1>(c); else if (s4 == 1) dma_piece<0, 3>(c); else if (s4 == 2) dma_piece<0, 5>(c); else dma_piece<0, 7>(c); }
        else { if (s4 == 0) dma_piece<1, 1>(c); else if (s4 == 1) dma_piece<1, 3>(c); else if (s4 == 2) dma_piece<1, 5>(c); else dma_piece<1, 7>(c); }
      }
      GAP();
      qa[1][1] = __builtin_amdgcn_mfma_f32_32x32x16_bf16(A1, fx[bs][1], qa[1][1], 0, 0, 0);
    }
    dma_unit<2>(c);
    STAMP(12);
    __builtin_amdgcn_s_barrier();
#pragma unroll
    for (int qb = 0; qb < 2; ++qb) {
      const int R = 32 * qb + r;
      char* base = lds + (R >> 4) * 40960 + 4 * 8192 + (R & 15) * 512 + 8 * h;
#pragma unroll
      for (int cb = 0; cb < 2; ++cb)
#pragma unroll
        for (int gg = 0; gg < 4; ++gg) {
          u32x2 v = {cvtpk(qa[qb][cb][4 * gg], qa[qb][cb][4 * gg + 1]), cvtpk(qa[qb][cb][4 * gg + 2], qa[qb][cb][4 * gg + 3])};
          *(u32x2*)(base + (((8 * w + 4 * cb + gg) ^ (R & 15)) * 16)) = v;
        }
    }
    WAITL0();
    __builtin_amdgcn_s_barrier();
  }
  {
#pragma unroll
    for (int qb = 0; qb < 2; ++qb) {
      const int R = 32 * qb + r;
      const char* base = lds + (R >> 4) * 40960 + 4 * 8192 + (R & 15) * 512;
#pragma unroll
      for (int s = 0; s < 16; ++s) c.qf[qb][s] = *(const bf16x8*)(base + (((2 * s + h) ^ (R & 15)) * 16));
    }
#pragma unroll
    for (int qb = 0; qb < 2; ++qb)
#pragma unroll
      for (int s = 0; s < 16; ++s) asm volatile("" : "+v"(c.qf[qb][s]));
  }
  WAITL0();
  __builtin_amdgcn_s_barrier();
  dma_unit<3>(c);
  dma_unit<4>(c);
  STAMP(1);

  {
    const unsigned rb = (unsigned)(size_t)c.ring;
#pragma unroll
    for (int ss = 0; ss < 8; ++ss) c.kaddr[ss] = rb + r * 256 + ((((2 * ss + h) ^ (r & 15))) * 16);
    const int g = lane >> 4, qq = (lane >> 2) & 3, pp = lane & 3;
#pragma unroll
    for (int d = 0; d < 4; ++d)
      c.vaddr[d] = rb + (4 * (g >> 1) + qq) * 512 + ((64 * d + 32 * (g & 1) + 8 * pp) ^ (qq << 6));
  }
  c.pend = false;
  c.fct[0] = 1.f; c.fct[1] = 1.f;

  phaseA<0>(c); phaseB<0, false, true>(c); STAMP(2);
  phaseA<1>(c); phaseB<1, true, true>(c); STAMP(3);
  phaseA<2>(c); phaseB<2, true, true>(c); STAMP(4);
  phaseA<3>(c); phaseB<3, true, true>(c); STAMP(5);
  phaseA<4>(c); phaseB<4, true, true>(c); STAMP(6);
  phaseA<5>(c); phaseB<5, true, true>(c); STAMP(7);
  phaseA<6>(c); phaseB<6, true, true>(c); STAMP(8);
  {
    int li = (bid * 4 + w) * 28 + (lane < 28 ? lane : 27);
    asm volatile("global_load_dword %0, %1, off" : "+v"(c.pfd) : "v"(wo + (size_t)li * 64) : "memory");
  }
  phaseA<7>(c); phaseB<7, true, true>(c); STAMP(9);
  phaseB<8, true, false>(c);
  asm volatile("s_nop 7\n\ts_nop 7" ::: "memory");
  STAMP(13);

  __syncthreads();
  float* ml = (float*)lds;
#pragma unroll
  for (int qb = 0; qb < 2; ++qb) {
    auto rr = __builtin_amdgcn_permlane32_swap(__float_as_uint(c.l[qb]), __float_as_uint(c.l[qb]), false, false);
    const float lt = __uint_as_float(rr[0]) + __uint_as_float(rr[1]);
    if (h == 0) {
      ml[((w * 2 + qb) * 32 + r) * 2 + 0] = c.m[qb];
      ml[((w * 2 + qb) * 32 + r) * 2 + 1] = lt;
    }
  }
  __syncthreads();
  float fw[2][4];
#pragma unroll
  for (int qb = 0; qb < 2; ++qb) {
    float mm[4], ll[4];
#pragma unroll
    for (int ww = 0; ww < 4; ++ww) {
      mm[ww] = ml[((ww * 2 + qb) * 32 + r) * 2 + 0];
      ll[ww] = ml[((ww * 2 + qb) * 32 + r) * 2 + 1];
    }
    const float M = fmaxf(fmaxf(mm[0], mm[1]), fmaxf(mm[2], mm[3]));
    float e[4], L = 0.f;
#pragma unroll
    for (int ww = 0; ww < 4; ++ww) { e[ww] = __builtin_amdgcn_exp2f(mm[ww] - M); L += e[ww] * ll[ww]; }
    const float inv = 1.f / L;
#pragma unroll
    for (int ww = 0; ww < 4; ++ww) fw[qb][ww] = e[ww] * inv;
  }
  STAMP(14);
  const unsigned xw = (unsigned)(size_t)lds + 4096 + lane * 16;
  const f32x4* xr = (const f32x4*)(lds + 4096) + lane;
  char* otile = lds + 102400;
  {
    switch (w) {
      case 0: {
        acc_dswrite4<32, 0>(xw + 0);
        acc_dswrite4<36, 1024>(xw + 0);
        acc_dswrite4<40, 2048>(xw + 0);
        acc_dswrite4<44, 3072>(xw + 0);
        acc_dswrite4<160, 4096>(xw + 0);
        acc_dswrite4<164, 5120>(xw + 0);
        acc_dswrite4<168, 6144>(xw + 0);
        acc_dswrite4<172, 7168>(xw + 0);
        acc_dswrite4<64, 8192>(xw + 0);
        acc_dswrite4<68, 9216>(xw + 0);
        acc_dswrite4<72, 10240>(xw + 0);
        acc_dswrite4<76, 11264>(xw + 0);
        acc_dswrite4<192, 12288>(xw + 0);
        acc_dswrite4<196, 13312>(xw + 0);
        acc_dswrite4<200, 14336>(xw + 0);
        acc_dswrite4<204, 15360>(xw + 0);
        acc_dswrite4<96, 16384>(xw + 0);
        acc_dswrite4<100, 17408>(xw + 0);
        acc_dswrite4<104, 18432>(xw + 0);
        acc_dswrite4<108, 19456>(xw + 0);
        acc_dswrite4<224, 20480>(xw + 0);
        acc_dswrite4<228, 21504>(xw + 0);
        acc_dswrite4<232, 22528>(xw + 0);
        acc_dswrite4<236, 23552>(xw + 0);
      } break;
      case 1: {
        acc_dswrite4<0, 24576>(xw + 0);
        acc_dswrite4<4, 25600>(xw + 0);
        acc_dswrite4<8, 26624>(xw + 0);
        acc_dswrite4<12, 27648>(xw + 0);
        acc_dswrite4<128, 28672>(xw + 0);
        acc_dswrite4<132, 29696>(xw + 0);
        acc_dswrite4<136, 30720>(xw + 0);
        acc_dswrite4<140, 31744>(xw + 0);
        acc_dswrite4<64, 32768>(xw + 0);
        acc_dswrite4<68, 33792>(xw + 0);
        acc_dswrite4<72, 34816>(xw + 0);
        acc_dswrite4<76, 35840>(xw + 0);
        acc_dswrite4<192, 36864>(xw + 0);
        acc_dswrite4<196, 37888>(xw + 0);
        acc_dswrite4<200, 38912>(xw + 0);
        acc_dswrite4<204, 39936>(xw + 0);
        acc_dswrite4<96, 40960>(xw + 0);
        acc_dswrite4<100, 41984>(xw + 0);
        acc_dswrite4<104, 43008>(xw + 0);
        acc_dswrite4<108, 44032>(xw + 0);
        acc_dswrite4<224, 45056>(xw + 0);
        acc_dswrite4<228, 46080>(xw + 0);
        acc_dswrite4<232, 47104>(xw + 0);
        acc_dswrite4<236, 48128>(xw + 0);
      } break;
      case 2: {
        acc_dswrite4<0, 49152>(xw + 0);
        acc_dswrite4<4, 50176>(xw + 0);
        acc_dswrite4<8, 51200>(xw + 0);
        acc_dswrite4<12, 52224>(xw + 0);
        acc_dswrite4<128, 53248>(xw + 0);
        acc_dswrite4<132, 54272>(xw + 0);
        acc_dswrite4<136, 55296>(xw + 0);
        acc_dswrite4<140, 56320>(xw + 0);
        acc_dswrite4<32, 57344>(xw + 0);
        acc_dswrite4<36, 58368>(xw + 0);
        acc_dswrite4<40, 59392>(xw + 0);
        acc_dswrite4<44, 60416>(xw + 0);
        acc_dswrite4<160, 61440>(xw + 0);
        acc_dswrite4<164, 62464>(xw + 0);
        acc_dswrite4<168, 63488>(xw + 0);
        acc_dswrite4<172, 64512>(xw + 0);
        acc_dswrite4<96, 0>(xw + 65536);
        acc_dswrite4<100, 1024>(xw + 65536);
        acc_dswrite4<104, 2048>(xw + 65536);
        acc_dswrite4<108, 3072>(xw + 65536);
        acc_dswrite4<224, 4096>(xw + 65536);
        acc_dswrite4<228, 5120>(xw + 65536);
        acc_dswrite4<232, 6144>(xw + 65536);
        acc_dswrite4<236, 7168>(xw + 65536);
      } break;
      case 3: {
        acc_dswrite4<0, 8192>(xw + 65536);
        acc_dswrite4<4, 9216>(xw + 65536);
        acc_dswrite4<8, 10240>(xw + 65536);
        acc_dswrite4<12, 11264>(xw + 65536);
        acc_dswrite4<128, 12288>(xw + 65536);
        acc_dswrite4<132, 13312>(xw + 65536);
        acc_dswrite4<136, 14336>(xw + 65536);
        acc_dswrite4<140, 15360>(xw + 65536);
        acc_dswrite4<32, 16384>(xw + 65536);
        acc_dswrite4<36, 17408>(xw + 65536);
        acc_dswrite4<40, 18432>(xw + 65536);
        acc_dswrite4<44, 19456>(xw + 65536);
        acc_dswrite4<160, 20480>(xw + 65536);
        acc_dswrite4<164, 21504>(xw + 65536);
        acc_dswrite4<168, 22528>(xw + 65536);
        acc_dswrite4<172, 23552>(xw + 65536);
        acc_dswrite4<64, 24576>(xw + 65536);
        acc_dswrite4<68, 25600>(xw + 65536);
        acc_dswrite4<72, 26624>(xw + 65536);
        acc_dswrite4<76, 27648>(xw + 65536);
        acc_dswrite4<192, 28672>(xw + 65536);
        acc_dswrite4<196, 29696>(xw + 65536);
        acc_dswrite4<200, 30720>(xw + 65536);
        acc_dswrite4<204, 31744>(xw + 65536);
      } break;
    }
    WAITL0();
    __builtin_amdgcn_s_barrier();
    switch (w) {
      case 0: {
        { f32x4 sum = acc_read4<0>(); sum *= fw[0][0];
          sum += xr[1536] * fw[0][1];
          sum += xr[3072] * fw[0][2];
          sum += xr[4608] * fw[0][3];
          u32x2 v = {cvtpk(sum[0], sum[1]), cvtpk(sum[2], sum[3])};
          *(u32x2*)(otile + (0 + r) * 512 + (((0) ^ ((0 + r) & 31)) * 16) + 8 * h) = v; }
        { f32x4 sum = acc_read4<4>(); sum *= fw[0][0];
          sum += xr[1600] * fw[0][1];
          sum += xr[3136] * fw[0][2];
          sum += xr[4672] * fw[0][3];
          u32x2 v = {cvtpk(sum[0], sum[1]), cvtpk(sum[2], sum[3])};
          *(u32x2*)(otile + (0 + r) * 512 + (((1) ^ ((0 + r) & 31)) * 16) + 8 * h) = v; }
        { f32x4 sum = acc_read4<8>(); sum *= fw[0][0];
          sum += xr[1664] * fw[0][1];
          sum += xr[3200] * fw[0][2];
          sum += xr[4736] * fw[0][3];
          u32x2 v = {cvtpk(sum[0], sum[1]), cvtpk(sum[2], sum[3])};
          *(u32x2*)(otile + (0 + r) * 512 + (((2) ^ ((0 + r) & 31)) * 16) + 8 * h) = v; }
        { f32x4 sum = acc_read4<12>(); sum *= fw[0][0];
          sum += xr[1728] * fw[0][1];
          sum += xr[3264] * fw[0][2];
          sum += xr[4800] * fw[0][3];
          u32x2 v = {cvtpk(sum[0], sum[1]), cvtpk(sum[2], sum[3])};
          *(u32x2*)(otile + (0 + r) * 512 + (((3) ^ ((0 + r) & 31)) * 16) + 8 * h) = v; }
        { f32x4 sum = acc_read4<128>(); sum *= fw[1][0];
          sum += xr[1792] * fw[1][1];
          sum += xr[3328] * fw[1][2];
          sum += xr[4864] * fw[1][3];
          u32x2 v = {cvtpk(sum[0], sum[1]), cvtpk(sum[2], sum[3])};
          *(u32x2*)(otile + (32 + r) * 512 + (((0) ^ ((32 + r) & 31)) * 16) + 8 * h) = v; }
        { f32x4 sum = acc_read4<132>(); sum *= fw[1][0];
          sum += xr[1856] * fw[1][1];
          sum += xr[3392] * fw[1][2];
          sum += xr[4928] * fw[1][3];
          u32x2 v = {cvtpk(sum[0], sum[1]), cvtpk(sum[2], sum[3])};
          *(u32x2*)(otile + (32 + r) * 512 + (((1) ^ ((32 + r) & 31)) * 16) + 8 * h) = v; }
        { f32x4 sum = acc_read4<136>(); sum *= fw[1][0];
          sum += xr[1920] * fw[1][1];
          sum += xr[3456] * fw[1][2];
          sum += xr[4992] * fw[1][3];
          u32x2 v = {cvtpk(sum[0], sum[1]), cvtpk(sum[2], sum[3])};
          *(u32x2*)(otile + (32 + r) * 512 + (((2) ^ ((32 + r) & 31)) * 16) + 8 * h) = v; }
        { f32x4 sum = acc_read4<140>(); sum *= fw[1][0];
          sum += xr[1984] * fw[1][1];
          sum += xr[3520] * fw[1][2];
          sum += xr[5056] * fw[1][3];
          u32x2 v = {cvtpk(sum[0], sum[1]), cvtpk(sum[2], sum[3])};
          *(u32x2*)(otile + (32 + r) * 512 + (((3) ^ ((32 + r) & 31)) * 16) + 8 * h) = v; }
      } break;
      case 1: {
        { f32x4 sum = acc_read4<32>(); sum *= fw[0][1];
          sum += xr[0] * fw[0][0];
          sum += xr[3584] * fw[0][2];
          sum += xr[5120] * fw[0][3];
          u32x2 v = {cvtpk(sum[0], sum[1]), cvtpk(sum[2], sum[3])};
          *(u32x2*)(otile + (0 + r) * 512 + (((8) ^ ((0 + r) & 31)) * 16) + 8 * h) = v; }
        { f32x4 sum = acc_read4<36>(); sum *= fw[0][1];
          sum += xr[64] * fw[0][0];
          sum += xr[3648] * fw[0][2];
          sum += xr[5184] * fw[0][3];
          u32x2 v = {cvtpk(sum[0], sum[1]), cvtpk(sum[2], sum[3])};
          *(u32x2*)(otile + (0 + r) * 512 + (((9) ^ ((0 + r) & 31)) * 16) + 8 * h) = v; }
        { f32x4 sum = acc_read4<40>(); sum *= fw[0][1];
          sum += xr[128] * fw[0][0];
          sum += xr[3712] * fw[0][2];
          sum += xr[5248] * fw[0][3];
          u32x2 v = {cvtpk(sum[0], sum[1]), cvtpk(sum[2], sum[3])};
          *(u32x2*)(otile + (0 + r) * 512 + (((10) ^ ((0 + r) & 31)) * 16) + 8 * h) = v; }
        { f32x4 sum = acc_read4<44>(); sum *= fw[0][1];
          sum += xr[192] * fw[0][0];
          sum += xr[3776] * fw[0][2];
          sum += xr[5312] * fw[0][3];
          u32x2 v = {cvtpk(sum[0], sum[1]), cvtpk(sum[2], sum[3])};
          *(u32x2*)(otile + (0 + r) * 512 + (((11) ^ ((0 + r) & 31)) * 16) + 8 * h) = v; }
        { f32x4 sum = acc_read4<160>(); sum *= fw[1][1];
          sum += xr[256] * fw[1][0];
          sum += xr[3840] * fw[1][2];
          sum += xr[5376] * fw[1][3];
          u32x2 v = {cvtpk(sum[0], sum[1]), cvtpk(sum[2], sum[3])};
          *(u32x2*)(otile + (32 + r) * 512 + (((8) ^ ((32 + r) & 31)) * 16) + 8 * h) = v; }
        { f32x4 sum = acc_read4<164>(); sum *= fw[1][1];
          sum += xr[320] * fw[1][0];
          sum += xr[3904] * fw[1][2];
          sum += xr[5440] * fw[1][3];
          u32x2 v = {cvtpk(sum[0], sum[1]), cvtpk(sum[2], sum[3])};
          *(u32x2*)(otile + (32 + r) * 512 + (((9) ^ ((32 + r) & 31)) * 16) + 8 * h) = v; }
        { f32x4 sum = acc_read4<168>(); sum *= fw[1][1];
          sum += xr[384] * fw[1][0];
          sum += xr[3968] * fw[1][2];
          sum += xr[5504] * fw[1][3];
          u32x2 v = {cvtpk(sum[0], sum[1]), cvtpk(sum[2], sum[3])};
          *(u32x2*)(otile + (32 + r) * 512 + (((10) ^ ((32 + r) & 31)) * 16) + 8 * h) = v; }
        { f32x4 sum = acc_read4<172>(); sum *= fw[1][1];
          sum += xr[448] * fw[1][0];
          sum += xr[4032] * fw[1][2];
          sum += xr[5568] * fw[1][3];
          u32x2 v = {cvtpk(sum[0], sum[1]), cvtpk(sum[2], sum[3])};
          *(u32x2*)(otile + (32 + r) * 512 + (((11) ^ ((32 + r) & 31)) * 16) + 8 * h) = v; }
      } break;
      case 2: {
        { f32x4 sum = acc_read4<64>(); sum *= fw[0][2];
          sum += xr[512] * fw[0][0];
          sum += xr[2048] * fw[0][1];
          sum += xr[5632] * fw[0][3];
          u32x2 v = {cvtpk(sum[0], sum[1]), cvtpk(sum[2], sum[3])};
          *(u32x2*)(otile + (0 + r) * 512 + (((16) ^ ((0 + r) & 31)) * 16) + 8 * h) = v; }
        { f32x4 sum = acc_read4<68>(); sum *= fw[0][2];
          sum += xr[576] * fw[0][0];
          sum += xr[2112] * fw[0][1];
          sum += xr[5696] * fw[0][3];
          u32x2 v = {cvtpk(sum[0], sum[1]), cvtpk(sum[2], sum[3])};
          *(u32x2*)(otile + (0 + r) * 512 + (((17) ^ ((0 + r) & 31)) * 16) + 8 * h) = v; }
        { f32x4 sum = acc_read4<72>(); sum *= fw[0][2];
          sum += xr[640] * fw[0][0];
          sum += xr[2176] * fw[0][1];
          sum += xr[5760] * fw[0][3];
          u32x2 v = {cvtpk(sum[0], sum[1]), cvtpk(sum[2], sum[3])};
          *(u32x2*)(otile + (0 + r) * 512 + (((18) ^ ((0 + r) & 31)) * 16) + 8 * h) = v; }
        { f32x4 sum = acc_read4<76>(); sum *= fw[0][2];
          sum += xr[704] * fw[0][0];
          sum += xr[2240] * fw[0][1];
          sum += xr[5824] * fw[0][3];
          u32x2 v = {cvtpk(sum[0], sum[1]), cvtpk(sum[2], sum[3])};
          *(u32x2*)(otile + (0 + r) * 512 + (((19) ^ ((0 + r) & 31)) * 16) + 8 * h) = v; }
        { f32x4 sum = acc_read4<192>(); sum *= fw[1][2];
          sum += xr[768] * fw[1][0];
          sum += xr[2304] * fw[1][1];
          sum += xr[5888] * fw[1][3];
          u32x2 v = {cvtpk(sum[0], sum[1]), cvtpk(sum[2], sum[3])};
          *(u32x2*)(otile + (32 + r) * 512 + (((16) ^ ((32 + r) & 31)) * 16) + 8 * h) = v; }
        { f32x4 sum = acc_read4<196>(); sum *= fw[1][2];
          sum += xr[832] * fw[1][0];
          sum += xr[2368] * fw[1][1];
          sum += xr[5952] * fw[1][3];
          u32x2 v = {cvtpk(sum[0], sum[1]), cvtpk(sum[2], sum[3])};
          *(u32x2*)(otile + (32 + r) * 512 + (((17) ^ ((32 + r) & 31)) * 16) + 8 * h) = v; }
        { f32x4 sum = acc_read4<200>(); sum *= fw[1][2];
          sum += xr[896] * fw[1][0];
          sum += xr[2432] * fw[1][1];
          sum += xr[6016] * fw[1][3];
          u32x2 v = {cvtpk(sum[0], sum[1]), cvtpk(sum[2], sum[3])};
          *(u32x2*)(otile + (32 + r) * 512 + (((18) ^ ((32 + r) & 31)) * 16) + 8 * h) = v; }
        { f32x4 sum = acc_read4<204>(); sum *= fw[1][2];
          sum += xr[960] * fw[1][0];
          sum += xr[2496] * fw[1][1];
          sum += xr[6080] * fw[1][3];
          u32x2 v = {cvtpk(sum[0], sum[1]), cvtpk(sum[2], sum[3])};
          *(u32x2*)(otile + (32 + r) * 512 + (((19) ^ ((32 + r) & 31)) * 16) + 8 * h) = v; }
      } break;
      case 3: {
        { f32x4 sum = acc_read4<96>(); sum *= fw[0][3];
          sum += xr[1024] * fw[0][0];
          sum += xr[2560] * fw[0][1];
          sum += xr[4096] * fw[0][2];
          u32x2 v = {cvtpk(sum[0], sum[1]), cvtpk(sum[2], sum[3])};
          *(u32x2*)(otile + (0 + r) * 512 + (((24) ^ ((0 + r) & 31)) * 16) + 8 * h) = v; }
        { f32x4 sum = acc_read4<100>(); sum *= fw[0][3];
          sum += xr[1088] * fw[0][0];
          sum += xr[2624] * fw[0][1];
          sum += xr[4160] * fw[0][2];
          u32x2 v = {cvtpk(sum[0], sum[1]), cvtpk(sum[2], sum[3])};
          *(u32x2*)(otile + (0 + r) * 512 + (((25) ^ ((0 + r) & 31)) * 16) + 8 * h) = v; }
        { f32x4 sum = acc_read4<104>(); sum *= fw[0][3];
          sum += xr[1152] * fw[0][0];
          sum += xr[2688] * fw[0][1];
          sum += xr[4224] * fw[0][2];
          u32x2 v = {cvtpk(sum[0], sum[1]), cvtpk(sum[2], sum[3])};
          *(u32x2*)(otile + (0 + r) * 512 + (((26) ^ ((0 + r) & 31)) * 16) + 8 * h) = v; }
        { f32x4 sum = acc_read4<108>(); sum *= fw[0][3];
          sum += xr[1216] * fw[0][0];
          sum += xr[2752] * fw[0][1];
          sum += xr[4288] * fw[0][2];
          u32x2 v = {cvtpk(sum[0], sum[1]), cvtpk(sum[2], sum[3])};
          *(u32x2*)(otile + (0 + r) * 512 + (((27) ^ ((0 + r) & 31)) * 16) + 8 * h) = v; }
        { f32x4 sum = acc_read4<224>(); sum *= fw[1][3];
          sum += xr[1280] * fw[1][0];
          sum += xr[2816] * fw[1][1];
          sum += xr[4352] * fw[1][2];
          u32x2 v = {cvtpk(sum[0], sum[1]), cvtpk(sum[2], sum[3])};
          *(u32x2*)(otile + (32 + r) * 512 + (((24) ^ ((32 + r) & 31)) * 16) + 8 * h) = v; }
        { f32x4 sum = acc_read4<228>(); sum *= fw[1][3];
          sum += xr[1344] * fw[1][0];
          sum += xr[2880] * fw[1][1];
          sum += xr[4416] * fw[1][2];
          u32x2 v = {cvtpk(sum[0], sum[1]), cvtpk(sum[2], sum[3])};
          *(u32x2*)(otile + (32 + r) * 512 + (((25) ^ ((32 + r) & 31)) * 16) + 8 * h) = v; }
        { f32x4 sum = acc_read4<232>(); sum *= fw[1][3];
          sum += xr[1408] * fw[1][0];
          sum += xr[2944] * fw[1][1];
          sum += xr[4480] * fw[1][2];
          u32x2 v = {cvtpk(sum[0], sum[1]), cvtpk(sum[2], sum[3])};
          *(u32x2*)(otile + (32 + r) * 512 + (((26) ^ ((32 + r) & 31)) * 16) + 8 * h) = v; }
        { f32x4 sum = acc_read4<236>(); sum *= fw[1][3];
          sum += xr[1472] * fw[1][0];
          sum += xr[3008] * fw[1][1];
          sum += xr[4544] * fw[1][2];
          u32x2 v = {cvtpk(sum[0], sum[1]), cvtpk(sum[2], sum[3])};
          *(u32x2*)(otile + (32 + r) * 512 + (((27) ^ ((32 + r) & 31)) * 16) + 8 * h) = v; }
      } break;
    }
    WAITL0();
    __builtin_amdgcn_s_barrier();
  }
  {
    switch (w) {
      case 0: {
        acc_dswrite4<48, 0>(xw + 0);
        acc_dswrite4<52, 1024>(xw + 0);
        acc_dswrite4<56, 2048>(xw + 0);
        acc_dswrite4<60, 3072>(xw + 0);
        acc_dswrite4<176, 4096>(xw + 0);
        acc_dswrite4<180, 5120>(xw + 0);
        acc_dswrite4<184, 6144>(xw + 0);
        acc_dswrite4<188, 7168>(xw + 0);
        acc_dswrite4<80, 8192>(xw + 0);
        acc_dswrite4<84, 9216>(xw + 0);
        acc_dswrite4<88, 10240>(xw + 0);
        acc_dswrite4<92, 11264>(xw + 0);
        acc_dswrite4<208, 12288>(xw + 0);
        acc_dswrite4<212, 13312>(xw + 0);
        acc_dswrite4<216, 14336>(xw + 0);
        acc_dswrite4<220, 15360>(xw + 0);
        acc_dswrite4<112, 16384>(xw + 0);
        acc_dswrite4<116, 17408>(xw + 0);
        acc_dswrite4<120, 18432>(xw + 0);
        acc_dswrite4<124, 19456>(xw + 0);
        acc_dswrite4<240, 20480>(xw + 0);
        acc_dswrite4<244, 21504>(xw + 0);
        acc_dswrite4<248, 22528>(xw + 0);
        acc_dswrite4<252, 23552>(xw + 0);
      } break;
      case 1: {
        acc_dswrite4<16, 24576>(xw + 0);
        acc_dswrite4<20, 25600>(xw + 0);
        acc_dswrite4<24, 26624>(xw + 0);
        acc_dswrite4<28, 27648>(xw + 0);
        acc_dswrite4<144, 28672>(xw + 0);
        acc_dswrite4<148, 29696>(xw + 0);
        acc_dswrite4<152, 30720>(xw + 0);
        acc_dswrite4<156, 31744>(xw + 0);
        acc_dswrite4<80, 32768>(xw + 0);
        acc_dswrite4<84, 33792>(xw + 0);
        acc_dswrite4<88, 34816>(xw + 0);
        acc_dswrite4<92, 35840>(xw + 0);
        acc_dswrite4<208, 36864>(xw + 0);
        acc_dswrite4<212, 37888>(xw + 0);
        acc_dswrite4<216, 38912>(xw + 0);
        acc_dswrite4<220, 39936>(xw + 0);
        acc_dswrite4<112, 40960>(xw + 0);
        acc_dswrite4<116, 41984>(xw + 0);
        acc_dswrite4<120, 43008>(xw + 0);
        acc_dswrite4<124, 44032>(xw + 0);
        acc_dswrite4<240, 45056>(xw + 0);
        acc_dswrite4<244, 46080>(xw + 0);
        acc_dswrite4<248, 47104>(xw + 0);
        acc_dswrite4<252, 48128>(xw + 0);
      } break;
      case 2: {
        acc_dswrite4<16, 49152>(xw + 0);
        acc_dswrite4<20, 50176>(xw + 0);
        acc_dswrite4<24, 51200>(xw + 0);
        acc_dswrite4<28, 52224>(xw + 0);
        acc_dswrite4<144, 53248>(xw + 0);
        acc_dswrite4<148, 54272>(xw + 0);
        acc_dswrite4<152, 55296>(xw + 0);
        acc_dswrite4<156, 56320>(xw + 0);
        acc_dswrite4<48, 57344>(xw + 0);
        acc_dswrite4<52, 58368>(xw + 0);
        acc_dswrite4<56, 59392>(xw + 0);
        acc_dswrite4<60, 60416>(xw + 0);
        acc_dswrite4<176, 61440>(xw + 0);
        acc_dswrite4<180, 62464>(xw + 0);
        acc_dswrite4<184, 63488>(xw + 0);
        acc_dswrite4<188, 64512>(xw + 0);
        acc_dswrite4<112, 0>(xw + 65536);
        acc_dswrite4<116, 1024>(xw + 65536);
        acc_dswrite4<120, 2048>(xw + 65536);
        acc_dswrite4<124, 3072>(xw + 65536);
        acc_dswrite4<240, 4096>(xw + 65536);
        acc_dswrite4<244, 5120>(xw + 65536);
        acc_dswrite4<248, 6144>(xw + 65536);
        acc_dswrite4<252, 7168>(xw + 65536);
      } break;
      case 3: {
        acc_dswrite4<16, 8192>(xw + 65536);
        acc_dswrite4<20, 9216>(xw + 65536);
        acc_dswrite4<24, 10240>(xw + 65536);
        acc_dswrite4<28, 11264>(xw + 65536);
        acc_dswrite4<144, 12288>(xw + 65536);
        acc_dswrite4<148, 13312>(xw + 65536);
        acc_dswrite4<152, 14336>(xw + 65536);
        acc_dswrite4<156, 15360>(xw + 65536);
        acc_dswrite4<48, 16384>(xw + 65536);
        acc_dswrite4<52, 17408>(xw + 65536);
        acc_dswrite4<56, 18432>(xw + 65536);
        acc_dswrite4<60, 19456>(xw + 65536);
        acc_dswrite4<176, 20480>(xw + 65536);
        acc_dswrite4<180, 21504>(xw + 65536);
        acc_dswrite4<184, 22528>(xw + 65536);
        acc_dswrite4<188, 23552>(xw + 65536);
        acc_dswrite4<80, 24576>(xw + 65536);
        acc_dswrite4<84, 25600>(xw + 65536);
        acc_dswrite4<88, 26624>(xw + 65536);
        acc_dswrite4<92, 27648>(xw + 65536);
        acc_dswrite4<208, 28672>(xw + 65536);
        acc_dswrite4<212, 29696>(xw + 65536);
        acc_dswrite4<216, 30720>(xw + 65536);
        acc_dswrite4<220, 31744>(xw + 65536);
      } break;
    }
    WAITL0();
    __builtin_amdgcn_s_barrier();
    switch (w) {
      case 0: {
        { f32x4 sum = acc_read4<16>(); sum *= fw[0][0];
          sum += xr[1536] * fw[0][1];
          sum += xr[3072] * fw[0][2];
          sum += xr[4608] * fw[0][3];
          u32x2 v = {cvtpk(sum[0], sum[1]), cvtpk(sum[2], sum[3])};
          *(u32x2*)(otile + (0 + r) * 512 + (((4) ^ ((0 + r) & 31)) * 16) + 8 * h) = v; }
        { f32x4 sum = acc_read4<20>(); sum *= fw[0][0];
          sum += xr[1600] * fw[0][1];
          sum += xr[3136] * fw[0][2];
          sum += xr[4672] * fw[0][3];
          u32x2 v = {cvtpk(sum[0], sum[1]), cvtpk(sum[2], sum[3])};
          *(u32x2*)(otile + (0 + r) * 512 + (((5) ^ ((0 + r) & 31)) * 16) + 8 * h) = v; }
        { f32x4 sum = acc_read4<24>(); sum *= fw[0][0];
          sum += xr[1664] * fw[0][1];
          sum += xr[3200] * fw[0][2];
          sum += xr[4736] * fw[0][3];
          u32x2 v = {cvtpk(sum[0], sum[1]), cvtpk(sum[2], sum[3])};
          *(u32x2*)(otile + (0 + r) * 512 + (((6) ^ ((0 + r) & 31)) * 16) + 8 * h) = v; }
        { f32x4 sum = acc_read4<28>(); sum *= fw[0][0];
          sum += xr[1728] * fw[0][1];
          sum += xr[3264] * fw[0][2];
          sum += xr[4800] * fw[0][3];
          u32x2 v = {cvtpk(sum[0], sum[1]), cvtpk(sum[2], sum[3])};
          *(u32x2*)(otile + (0 + r) * 512 + (((7) ^ ((0 + r) & 31)) * 16) + 8 * h) = v; }
        { f32x4 sum = acc_read4<144>(); sum *= fw[1][0];
          sum += xr[1792] * fw[1][1];
          sum += xr[3328] * fw[1][2];
          sum += xr[4864] * fw[1][3];
          u32x2 v = {cvtpk(sum[0], sum[1]), cvtpk(sum[2], sum[3])};
          *(u32x2*)(otile + (32 + r) * 512 + (((4) ^ ((32 + r) & 31)) * 16) + 8 * h) = v; }
        { f32x4 sum = acc_read4<148>(); sum *= fw[1][0];
          sum += xr[1856] * fw[1][1];
          sum += xr[3392] * fw[1][2];
          sum += xr[4928] * fw[1][3];
          u32x2 v = {cvtpk(sum[0], sum[1]), cvtpk(sum[2], sum[3])};
          *(u32x2*)(otile + (32 + r) * 512 + (((5) ^ ((32 + r) & 31)) * 16) + 8 * h) = v; }
        { f32x4 sum = acc_read4<152>(); sum *= fw[1][0];
          sum += xr[1920] * fw[1][1];
          sum += xr[3456] * fw[1][2];
          sum += xr[4992] * fw[1][3];
          u32x2 v = {cvtpk(sum[0], sum[1]), cvtpk(sum[2], sum[3])};
          *(u32x2*)(otile + (32 + r) * 512 + (((6) ^ ((32 + r) & 31)) * 16) + 8 * h) = v; }
        { f32x4 sum = acc_read4<156>(); sum *= fw[1][0];
          sum += xr[1984] * fw[1][1];
          sum += xr[3520] * fw[1][2];
          sum += xr[5056] * fw[1][3];
          u32x2 v = {cvtpk(sum[0], sum[1]), cvtpk(sum[2], sum[3])};
          *(u32x2*)(otile + (32 + r) * 512 + (((7) ^ ((32 + r) & 31)) * 16) + 8 * h) = v; }
      } break;
      case 1: {
        { f32x4 sum = acc_read4<48>(); sum *= fw[0][1];
          sum += xr[0] * fw[0][0];
          sum += xr[3584] * fw[0][2];
          sum += xr[5120] * fw[0][3];
          u32x2 v = {cvtpk(sum[0], sum[1]), cvtpk(sum[2], sum[3])};
          *(u32x2*)(otile + (0 + r) * 512 + (((12) ^ ((0 + r) & 31)) * 16) + 8 * h) = v; }
        { f32x4 sum = acc_read4<52>(); sum *= fw[0][1];
          sum += xr[64] * fw[0][0];
          sum += xr[3648] * fw[0][2];
          sum += xr[5184] * fw[0][3];
          u32x2 v = {cvtpk(sum[0], sum[1]), cvtpk(sum[2], sum[3])};
          *(u32x2*)(otile + (0 + r) * 512 + (((13) ^ ((0 + r) & 31)) * 16) + 8 * h) = v; }
        { f32x4 sum = acc_read4<56>(); sum *= fw[0][1];
          sum += xr[128] * fw[0][0];
          sum += xr[3712] * fw[0][2];
          sum += xr[5248] * fw[0][3];
          u32x2 v = {cvtpk(sum[0], sum[1]), cvtpk(sum[2], sum[3])};
          *(u32x2*)(otile + (0 + r) * 512 + (((14) ^ ((0 + r) & 31)) * 16) + 8 * h) = v; }
        { f32x4 sum = acc_read4<60>(); sum *= fw[0][1];
          sum += xr[192] * fw[0][0];
          sum += xr[3776] * fw[0][2];
          sum += xr[5312] * fw[0][3];
          u32x2 v = {cvtpk(sum[0], sum[1]), cvtpk(sum[2], sum[3])};
          *(u32x2*)(otile + (0 + r) * 512 + (((15) ^ ((0 + r) & 31)) * 16) + 8 * h) = v; }
        { f32x4 sum = acc_read4<176>(); sum *= fw[1][1];
          sum += xr[256] * fw[1][0];
          sum += xr[3840] * fw[1][2];
          sum += xr[5376] * fw[1][3];
          u32x2 v = {cvtpk(sum[0], sum[1]), cvtpk(sum[2], sum[3])};
          *(u32x2*)(otile + (32 + r) * 512 + (((12) ^ ((32 + r) & 31)) * 16) + 8 * h) = v; }
        { f32x4 sum = acc_read4<180>(); sum *= fw[1][1];
          sum += xr[320] * fw[1][0];
          sum += xr[3904] * fw[1][2];
          sum += xr[5440] * fw[1][3];
          u32x2 v = {cvtpk(sum[0], sum[1]), cvtpk(sum[2], sum[3])};
          *(u32x2*)(otile + (32 + r) * 512 + (((13) ^ ((32 + r) & 31)) * 16) + 8 * h) = v; }
        { f32x4 sum = acc_read4<184>(); sum *= fw[1][1];
          sum += xr[384] * fw[1][0];
          sum += xr[3968] * fw[1][2];
          sum += xr[5504] * fw[1][3];
          u32x2 v = {cvtpk(sum[0], sum[1]), cvtpk(sum[2], sum[3])};
          *(u32x2*)(otile + (32 + r) * 512 + (((14) ^ ((32 + r) & 31)) * 16) + 8 * h) = v; }
        { f32x4 sum = acc_read4<188>(); sum *= fw[1][1];
          sum += xr[448] * fw[1][0];
          sum += xr[4032] * fw[1][2];
          sum += xr[5568] * fw[1][3];
          u32x2 v = {cvtpk(sum[0], sum[1]), cvtpk(sum[2], sum[3])};
          *(u32x2*)(otile + (32 + r) * 512 + (((15) ^ ((32 + r) & 31)) * 16) + 8 * h) = v; }
      } break;
      case 2: {
        { f32x4 sum = acc_read4<80>(); sum *= fw[0][2];
          sum += xr[512] * fw[0][0];
          sum += xr[2048] * fw[0][1];
          sum += xr[5632] * fw[0][3];
          u32x2 v = {cvtpk(sum[0], sum[1]), cvtpk(sum[2], sum[3])};
          *(u32x2*)(otile + (0 + r) * 512 + (((20) ^ ((0 + r) & 31)) * 16) + 8 * h) = v; }
        { f32x4 sum = acc_read4<84>(); sum *= fw[0][2];
          sum += xr[576] * fw[0][0];
          sum += xr[2112] * fw[0][1];
          sum += xr[5696] * fw[0][3];
          u32x2 v = {cvtpk(sum[0], sum[1]), cvtpk(sum[2], sum[3])};
          *(u32x2*)(otile + (0 + r) * 512 + (((21) ^ ((0 + r) & 31)) * 16) + 8 * h) = v; }
        { f32x4 sum = acc_read4<88>(); sum *= fw[0][2];
          sum += xr[640] * fw[0][0];
          sum += xr[2176] * fw[0][1];
          sum += xr[5760] * fw[0][3];
          u32x2 v = {cvtpk(sum[0], sum[1]), cvtpk(sum[2], sum[3])};
          *(u32x2*)(otile + (0 + r) * 512 + (((22) ^ ((0 + r) & 31)) * 16) + 8 * h) = v; }
        { f32x4 sum = acc_read4<92>(); sum *= fw[0][2];
          sum += xr[704] * fw[0][0];
          sum += xr[2240] * fw[0][1];
          sum += xr[5824] * fw[0][3];
          u32x2 v = {cvtpk(sum[0], sum[1]), cvtpk(sum[2], sum[3])};
          *(u32x2*)(otile + (0 + r) * 512 + (((23) ^ ((0 + r) & 31)) * 16) + 8 * h) = v; }
        { f32x4 sum = acc_read4<208>(); sum *= fw[1][2];
          sum += xr[768] * fw[1][0];
          sum += xr[2304] * fw[1][1];
          sum += xr[5888] * fw[1][3];
          u32x2 v = {cvtpk(sum[0], sum[1]), cvtpk(sum[2], sum[3])};
          *(u32x2*)(otile + (32 + r) * 512 + (((20) ^ ((32 + r) & 31)) * 16) + 8 * h) = v; }
        { f32x4 sum = acc_read4<212>(); sum *= fw[1][2];
          sum += xr[832] * fw[1][0];
          sum += xr[2368] * fw[1][1];
          sum += xr[5952] * fw[1][3];
          u32x2 v = {cvtpk(sum[0], sum[1]), cvtpk(sum[2], sum[3])};
          *(u32x2*)(otile + (32 + r) * 512 + (((21) ^ ((32 + r) & 31)) * 16) + 8 * h) = v; }
        { f32x4 sum = acc_read4<216>(); sum *= fw[1][2];
          sum += xr[896] * fw[1][0];
          sum += xr[2432] * fw[1][1];
          sum += xr[6016] * fw[1][3];
          u32x2 v = {cvtpk(sum[0], sum[1]), cvtpk(sum[2], sum[3])};
          *(u32x2*)(otile + (32 + r) * 512 + (((22) ^ ((32 + r) & 31)) * 16) + 8 * h) = v; }
        { f32x4 sum = acc_read4<220>(); sum *= fw[1][2];
          sum += xr[960] * fw[1][0];
          sum += xr[2496] * fw[1][1];
          sum += xr[6080] * fw[1][3];
          u32x2 v = {cvtpk(sum[0], sum[1]), cvtpk(sum[2], sum[3])};
          *(u32x2*)(otile + (32 + r) * 512 + (((23) ^ ((32 + r) & 31)) * 16) + 8 * h) = v; }
      } break;
      case 3: {
        { f32x4 sum = acc_read4<112>(); sum *= fw[0][3];
          sum += xr[1024] * fw[0][0];
          sum += xr[2560] * fw[0][1];
          sum += xr[4096] * fw[0][2];
          u32x2 v = {cvtpk(sum[0], sum[1]), cvtpk(sum[2], sum[3])};
          *(u32x2*)(otile + (0 + r) * 512 + (((28) ^ ((0 + r) & 31)) * 16) + 8 * h) = v; }
        { f32x4 sum = acc_read4<116>(); sum *= fw[0][3];
          sum += xr[1088] * fw[0][0];
          sum += xr[2624] * fw[0][1];
          sum += xr[4160] * fw[0][2];
          u32x2 v = {cvtpk(sum[0], sum[1]), cvtpk(sum[2], sum[3])};
          *(u32x2*)(otile + (0 + r) * 512 + (((29) ^ ((0 + r) & 31)) * 16) + 8 * h) = v; }
        { f32x4 sum = acc_read4<120>(); sum *= fw[0][3];
          sum += xr[1152] * fw[0][0];
          sum += xr[2688] * fw[0][1];
          sum += xr[4224] * fw[0][2];
          u32x2 v = {cvtpk(sum[0], sum[1]), cvtpk(sum[2], sum[3])};
          *(u32x2*)(otile + (0 + r) * 512 + (((30) ^ ((0 + r) & 31)) * 16) + 8 * h) = v; }
        { f32x4 sum = acc_read4<124>(); sum *= fw[0][3];
          sum += xr[1216] * fw[0][0];
          sum += xr[2752] * fw[0][1];
          sum += xr[4288] * fw[0][2];
          u32x2 v = {cvtpk(sum[0], sum[1]), cvtpk(sum[2], sum[3])};
          *(u32x2*)(otile + (0 + r) * 512 + (((31) ^ ((0 + r) & 31)) * 16) + 8 * h) = v; }
        { f32x4 sum = acc_read4<240>(); sum *= fw[1][3];
          sum += xr[1280] * fw[1][0];
          sum += xr[2816] * fw[1][1];
          sum += xr[4352] * fw[1][2];
          u32x2 v = {cvtpk(sum[0], sum[1]), cvtpk(sum[2], sum[3])};
          *(u32x2*)(otile + (32 + r) * 512 + (((28) ^ ((32 + r) & 31)) * 16) + 8 * h) = v; }
        { f32x4 sum = acc_read4<244>(); sum *= fw[1][3];
          sum += xr[1344] * fw[1][0];
          sum += xr[2880] * fw[1][1];
          sum += xr[4416] * fw[1][2];
          u32x2 v = {cvtpk(sum[0], sum[1]), cvtpk(sum[2], sum[3])};
          *(u32x2*)(otile + (32 + r) * 512 + (((29) ^ ((32 + r) & 31)) * 16) + 8 * h) = v; }
        { f32x4 sum = acc_read4<248>(); sum *= fw[1][3];
          sum += xr[1408] * fw[1][0];
          sum += xr[2944] * fw[1][1];
          sum += xr[4480] * fw[1][2];
          u32x2 v = {cvtpk(sum[0], sum[1]), cvtpk(sum[2], sum[3])};
          *(u32x2*)(otile + (32 + r) * 512 + (((30) ^ ((32 + r) & 31)) * 16) + 8 * h) = v; }
        { f32x4 sum = acc_read4<252>(); sum *= fw[1][3];
          sum += xr[1472] * fw[1][0];
          sum += xr[3008] * fw[1][1];
          sum += xr[4544] * fw[1][2];
          u32x2 v = {cvtpk(sum[0], sum[1]), cvtpk(sum[2], sum[3])};
          *(u32x2*)(otile + (32 + r) * 512 + (((31) ^ ((32 + r) & 31)) * 16) + 8 * h) = v; }
      } break;
    }
  }
  __syncthreads();
  asm volatile("s_waitcnt vmcnt(0)" :: "v"(c.pfd) : "memory");
  STAMP(15);
#pragma unroll
  for (int k = 0; k < 7; ++k) {
    const int R = 14 * w + 2 * k + h;
    u32x4 v = *(const u32x4*)(otile + R * 512 + ((r ^ (R & 31)) * 16));
    *(u32x4*)(o + (size_t)(row0 + R) * NQ + head * DH + r * 8) = v;
  }
  STAMP(10);
}
__global__ __launch_bounds__(512, 2) void oproj2_kernel(const u16* __restrict__ o, const u16* __restrict__ wo,
                                                        u16* __restrict__ out) {
  extern __shared__ __attribute__((aligned(16))) char lds[];
  const int tid = threadIdx.x, lane = tid & 63;
  const int w = __builtin_amdgcn_readfirstlane(tid >> 6);
  const int r = lane & 31, h = lane >> 5;
  const int xcd = blockIdx.x & 7, idx = blockIdx.x >> 3;
  const int rt = xcd + 8 * (idx / 7), ct = idx % 7;
  if (rt >= 28) return;
  const int col0 = ct * 64;
  char* ring = lds + w * 18432;
  __amdgpu_buffer_rsrc_t rw = __builtin_amdgcn_make_buffer_rsrc((void*)(wo + (size_t)(512 * w) * DM + col0), 0,
                                                                 (512 * DM - col0) * 2, 0x00020000);
  __amdgpu_buffer_rsrc_t ra = __builtin_amdgcn_make_buffer_rsrc((void*)(o + (size_t)(rt * 32) * NQ + 512 * w), 0,
                                                                 (32 * NQ - 512 * w) * 2, 0x00020000);
  const int drow = lane >> 3;
  const int wvoff = drow * (DM * 2) + (((lane & 7) ^ (((drow >> 1) & 1) << 2)) * 16);
  const int arow = lane >> 2;
  const int avoff = arow * (NQ * 2) + (((lane & 3) ^ ((arow >> 2) & 3)) * 16);
  auto issue = [&](int u) {
    char* dst = ring + (u % 3) * 6144;
#pragma unroll
    for (int p = 0; p < 4; ++p)
      __builtin_amdgcn_raw_ptr_buffer_load_lds(rw, (lds_vp)(dst + p * 1024), 16, wvoff, (32 * u + 8 * p) * (DM * 2), 0, 0);
#pragma unroll
    for (int p = 0; p < 2; ++p)
      __builtin_amdgcn_raw_ptr_buffer_load_lds(ra, (lds_vp)(dst + 4096 + p * 1024), 16, avoff, 16 * p * (NQ * 2) + 64 * u, 0, 0);
  };
  issue(0); issue(1); issue(2);
  const int g = lane >> 4, qq = (lane >> 2) & 3, pp = lane & 3;
  const unsigned rb = (unsigned)(size_t)ring;
  const unsigned trb = rb + (8 * (g >> 1) + qq) * 128 + ((32 * (g & 1) + 8 * pp) ^ ((qq >> 1) << 6));
  const unsigned ab0 = rb + 4096 + r * 64 + (((0 + h) ^ ((r >> 2) & 3)) * 16);
  const unsigned ab1 = rb + 4096 + r * 64 + (((2 + h) ^ ((r >> 2) & 3)) * 16);
  f32x16 acc[2];
#pragma unroll
  for (int b2 = 0; b2 < 2; ++b2)
#pragma unroll
    for (int i = 0; i < 16; ++i) acc[b2][i] = 0.f;
#pragma unroll
  for (int u = 0; u < 16; ++u) {
    if (u <= 13) WAITV(12); else if (u == 14) WAITV(6); else WAITV(0);
    const unsigned so = (u % 3) * 6144;
    s16x4 a[2][4];
    bf16x8 xb[2];
#pragma unroll
    for (int s = 0; s < 2; ++s) {
      a[s][0] = trread(trb + so + s * 2048);
      a[s][1] = trread(trb + so + s * 2048 + 512);
      a[s][2] = trread((trb + so + s * 2048) ^ 64);
      a[s][3] = trread((trb + so + s * 2048 + 512) ^ 64);
    }
    asm volatile("ds_read_b128 %0, %1" : "=v"(xb[0]) : "v"(ab0 + so) : "memory");
    asm volatile("ds_read_b128 %0, %1" : "=v"(xb[1]) : "v"(ab1 + so) : "memory");
    asm volatile("s_waitcnt lgkmcnt(0)"
                 : "+v"(a[0][0]), "+v"(a[0][1]), "+v"(a[0][2]), "+v"(a[0][3]), "+v"(a[1][0]), "+v"(a[1][1]),
                   "+v"(a[1][2]), "+v"(a[1][3]), "+v"(xb[0]), "+v"(xb[1])::"memory");
    if (u + 3 < 16) issue(u + 3);
#pragma unroll
    for (int s = 0; s < 2; ++s) {
      acc[0] = __builtin_amdgcn_mfma_f32_32x32x16_bf16(cat8(a[s][0], a[s][1]), xb[s], acc[0], 0, 0, 0);
      acc[1] = __builtin_amdgcn_mfma_f32_32x32x16_bf16(cat8(a[s][2], a[s][3]), xb[s], acc[1], 0, 0, 0);
    }
  }
  __syncthreads();
  float* red = (float*)lds;
#pragma unroll
  for (int cb = 0; cb < 2; ++cb)
#pragma unroll
    for (int gg = 0; gg < 4; ++gg) {
      f32x4 v = {acc[cb][4 * gg], acc[cb][4 * gg + 1], acc[cb][4 * gg + 2], acc[cb][4 * gg + 3]};
      *(f32x4*)(red + ((w * 8 + cb * 4 + gg) * 64 + lane) * 4) = v;
    }
  __syncthreads();
  {
    const int cg = w, cb = cg >> 2, gg = cg & 3;
    f32x4 s = {0.f, 0.f, 0.f, 0.f};
#pragma unroll
    for (int ww = 0; ww < 8; ++ww) s += *(const f32x4*)(red + ((ww * 8 + cg) * 64 + lane) * 4);
    u32x2 v = {cvtpk(s[0], s[1]), cvtpk(s[2], s[3])};
    *(u32x2*)(out + (size_t)(rt * 32 + r) * DM + col0 + 32 * cb + 8 * gg + 4 * h) = v;
  }
}
__global__ __launch_bounds__(512, 2) void oproj3_kernel(const u16* __restrict__ o, const u16* __restrict__ wo,
                                                        u16* __restrict__ out, float* __restrict__ slab,
                                                        unsigned* __restrict__ cnt) {
  extern __shared__ __attribute__((aligned(16))) char lds[];
  const int tid = threadIdx.x, lane = tid & 63;
  const int w = __builtin_amdgcn_readfirstlane(tid >> 6);
  const int r = lane & 31, h = lane >> 5;
  const int xcd = blockIdx.x & 7, idx = blockIdx.x >> 3;
  const int tile = (idx >> 1) * 8 + xcd, kh = idx & 1;
  if (tile >= 98) return;
  const int rt = tile / 7, ct = tile % 7;
  const int col0 = ct * 64, k0 = kh * 2048 + 256 * w;
  char* ring = lds + w * 16384;
  __amdgpu_buffer_rsrc_t rw = __builtin_amdgcn_make_buffer_rsrc((void*)(wo + (size_t)k0 * DM + col0), 0,
                                                                 (256 * DM - col0) * 2, 0x00020000);
  __amdgpu_buffer_rsrc_t ra = __builtin_amdgcn_make_buffer_rsrc((void*)(o + (size_t)(rt * 64) * NQ + k0), 0,
                                                                 (64 * NQ - k0) * 2, 0x00020000);
  const int drow = lane >> 3;
  const int wvoff = drow * (DM * 2) + (((lane & 7) ^ (((drow >> 1) & 1) << 2)) * 16);
  const int arow = lane >> 2;
  const int avoff = arow * (NQ * 2) + (((lane & 3) ^ ((arow >> 2) & 3)) * 16);
  auto issue = [&](int u) {
    char* dst = ring + (u & 1) * 8192;
#pragma unroll
    for (int p = 0; p < 4; ++p)
      __builtin_amdgcn_raw_ptr_buffer_load_lds(rw, (lds_vp)(dst + p * 1024), 16, wvoff, (32 * u + 8 * p) * (DM * 2), 0, 0);
#pragma unroll
    for (int p = 0; p < 4; ++p)
      __builtin_amdgcn_raw_ptr_buffer_load_lds(ra, (lds_vp)(dst + 4096 + p * 1024), 16, avoff, 16 * p * (NQ * 2) + 64 * u, 0, 0);
  };
  issue(0); issue(1);
  const int g = lane >> 4, qq = (lane >> 2) & 3, pp = lane & 3;
  const unsigned rb = (unsigned)(size_t)ring;
  const unsigned trb = rb + (8 * (g >> 1) + qq) * 128 + ((32 * (g & 1) + 8 * pp) ^ ((qq >> 1) << 6));
  const unsigned ab0 = rb + 4096 + r * 64 + (((0 + h) ^ ((r >> 2) & 3)) * 16);
  const unsigned ab1 = rb + 4096 + r * 64 + (((2 + h) ^ ((r >> 2) & 3)) * 16);
  f32x16 acc[2][2];
#pragma unroll
  for (int a2 = 0; a2 < 2; ++a2)
#pragma unroll
    for (int b2 = 0; b2 < 2; ++b2)
#pragma unroll
      for (int i = 0; i < 16; ++i) acc[a2][b2][i] = 0.f;
#pragma unroll
  for (int u = 0; u < 8; ++u) {
    if (u <= 6) WAITV(8); else WAITV(0);
    const unsigned so = (u & 1) * 8192;
    s16x4 a[2][4];
    bf16x8 xb[2][2];
#pragma unroll
    for (int s = 0; s < 2; ++s) {
      a[s][0] = trread(trb + so + s * 2048);
      a[s][1] = trread(trb + so + s * 2048 + 512);
      a[s][2] = trread((trb + so + s * 2048) ^ 64);
      a[s][3] = trread((trb + so + s * 2048 + 512) ^ 64);
    }
    asm volatile("ds_read_b128 %0, %1" : "=v"(xb[0][0]) : "v"(ab0 + so) : "memory");
    asm volatile("ds_read_b128 %0, %1" : "=v"(xb[1][0]) : "v"(ab1 + so) : "memory");
    asm volatile("ds_read_b128 %0, %1 offset:2048" : "=v"(xb[0][1]) : "v"(ab0 + so) : "memory");
    asm volatile("ds_read_b128 %0, %1 offset:2048" : "=v"(xb[1][1]) : "v"(ab1 + so) : "memory");
    asm volatile("s_waitcnt lgkmcnt(0)"
                 : "+v"(a[0][0]), "+v"(a[0][1]), "+v"(a[0][2]), "+v"(a[0][3]), "+v"(a[1][0]), "+v"(a[1][1]),
                   "+v"(a[1][2]), "+v"(a[1][3]), "+v"(xb[0][0]), "+v"(xb[1][0]), "+v"(xb[0][1]), "+v"(xb[1][1])::"memory");
    if (u + 2 < 8) issue(u + 2);
#pragma unroll
    for (int s = 0; s < 2; ++s)
#pragma unroll
      for (int rb2 = 0; rb2 < 2; ++rb2) {
        acc[rb2][0] = __builtin_amdgcn_mfma_f32_32x32x16_bf16(cat8(a[s][0], a[s][1]), xb[s][rb2], acc[rb2][0], 0, 0, 0);
        acc[rb2][1] = __builtin_amdgcn_mfma_f32_32x32x16_bf16(cat8(a[s][2], a[s][3]), xb[s][rb2], acc[rb2][1], 0, 0, 0);
      }
  }
  __syncthreads();
  float* red = (float*)lds;
#pragma unroll
  for (int rb2 = 0; rb2 < 2; ++rb2)
#pragma unroll
    for (int cb = 0; cb < 2; ++cb)
#pragma unroll
      for (int gg = 0; gg < 4; ++gg) {
        f32x4 v = {acc[rb2][cb][4 * gg], acc[rb2][cb][4 * gg + 1], acc[rb2][cb][4 * gg + 2], acc[rb2][cb][4 * gg + 3]};
        *(f32x4*)(red + ((w * 16 + rb2 * 8 + cb * 4 + gg) * 64 + lane) * 4) = v;
      }
  __syncthreads();
  f32x4 sum[2];
#pragma unroll
  for (int jj = 0; jj < 2; ++jj) {
    const int grp = 2 * w + jj;
    f32x4 s = {0.f, 0.f, 0.f, 0.f};
#pragma unroll
    for (int ww = 0; ww < 8; ++ww) s += *(const f32x4*)(red + ((ww * 16 + grp) * 64 + lane) * 4);
    sum[jj] = s;
  }
  __amdgpu_buffer_rsrc_t rs = __builtin_amdgcn_make_buffer_rsrc((void*)(slab + (size_t)tile * 2 * 4096), 0, 2 * 4096 * 4, 0x00020000);
#pragma unroll
  for (int jj = 0; jj < 2; ++jj)
    __builtin_amdgcn_raw_buffer_store_b128(__builtin_bit_cast(u32x4, sum[jj]), rs, (kh * 16 + 2 * w + jj) * 1024 + lane * 16, 0, 16);
  asm volatile("s_waitcnt vmcnt(0)" ::: "memory");
  __syncthreads();
  unsigned* flag = (unsigned*)(lds + 131072);
  if (tid == 0) *flag = __hip_atomic_fetch_add(cnt + tile, 1u, __ATOMIC_RELAXED, __HIP_MEMORY_SCOPE_AGENT);
  __syncthreads();
  if (*flag == 1u) {
#pragma unroll
    for (int jj = 0; jj < 2; ++jj) {
      u32x4 pv = __builtin_amdgcn_raw_buffer_load_b128(rs, ((kh ^ 1) * 16 + 2 * w + jj) * 1024 + lane * 16, 0, 16);
      f32x4 t = sum[jj] + __builtin_bit_cast(f32x4, pv);
      const int grp = 2 * w + jj, rb2 = grp >> 3, cb = (grp >> 2) & 1, gg = grp & 3;
      u32x2 v = {cvtpk(t[0], t[1]), cvtpk(t[2], t[3])};
      *(u32x2*)(out + (size_t)(rt * 64 + 32 * rb2 + r) * DM + col0 + 32 * cb + 8 * gg + 4 * h) = v;
    }
  }
}
extern "C" void kernel_launch(void* const* d_in, const int* in_sizes, int n_in, void* d_out, int out_size,
                              void* d_ws, size_t ws_size, hipStream_t stream) {
  const u16* x = (const u16*)d_in[0];
  const u16* kc = (const u16*)d_in[1];
  const u16* vc = (const u16*)d_in[2];
  const u16* wq = (const u16*)d_in[3];
  const u16* wo = (const u16*)d_in[4];
  u16* out = (u16*)d_out;
  u16* qws = (u16*)d_ws;
  u16* ows = (u16*)((char*)d_ws + (size_t)NROW * NQ * 2);
  float* slab = (float*)((char*)d_ws + (16 << 20));
  unsigned* cnt = (unsigned*)((char*)d_ws + (24 << 20));
  (void)hipFuncSetAttribute((const void*)attn4_kernel, hipFuncAttributeMaxDynamicSharedMemorySize, 163840);
  hipLaunchKernelGGL(attn4_kernel, dim3(256), dim3(256), 163840, stream, x, wq, kc, vc, wo, ows, cnt
  );
  (void)hipFuncSetAttribute((const void*)oproj3_kernel, hipFuncAttributeMaxDynamicSharedMemorySize, 131328);
  hipLaunchKernelGGL(oproj3_kernel, dim3(8 * 26), dim3(512), 131328, stream, (const u16*)ows, wo, out, slab, cnt);
}
```

```cpp
#include <hip/hip_runtime.h>
#include <hip/hip_bf16.h>
#include <utility>

typedef unsigned short u16;
typedef __attribute__((ext_vector_type(8))) short bf16x8;
typedef __attribute__((ext_vector_type(4))) short s16x4;
typedef __attribute__((ext_vector_type(16))) float f32x16;
typedef __attribute__((ext_vector_type(4))) float f32x4;
typedef __attribute__((ext_vector_type(2))) float f32x2;
typedef __attribute__((ext_vector_type(2))) __bf16 bf16x2v;
typedef __attribute__((ext_vector_type(2))) unsigned u32x2;
typedef __attribute__((ext_vector_type(4))) unsigned u32x4;
#define DI __device__ __forceinline__
#define LDSP(p) ((__attribute__((address_space(3))) unsigned*)(p))

constexpr int NB = 8, SQ = 112, DM = 448, HQ = 16, HKV = 8, SK = 1024, DH = 256;
constexpr int NROW = NB * SQ;
constexpr int NQ = HQ * DH;

DI unsigned cvtpk(float lo, float hi) {
  f32x2 v = {lo, hi};
  bf16x2v b = __builtin_convertvector(v, bf16x2v);
  return __builtin_bit_cast(unsigned, b);
}
DI void glds16(const void* g, void* l) {
  __builtin_amdgcn_global_load_lds((const unsigned*)g, LDSP(l), 16, 0, 0);
}
#define WAITV(n) asm volatile("s_waitcnt vmcnt(" #n ")" ::: "memory")
#define WAITL0() asm volatile("s_waitcnt lgkmcnt(0)" ::: "memory")

DI s16x4 trread(unsigned addr) {
  s16x4 r;
  asm volatile("ds_read_b64_tr_b16 %0, %1" : "=v"(r) : "v"(addr) : "memory");
  return r;
}
template <int OFF> DI s16x4 trread_o(unsigned addr) {
  s16x4 r;
  asm volatile("ds_read_b64_tr_b16 %0, %1 offset:%2" : "=v"(r) : "v"(addr), "i"(OFF) : "memory");
  return r;
}
DI bf16x8 cat8(s16x4 a, s16x4 b) { return __builtin_shufflevector(a, b, 0, 1, 2, 3, 4, 5, 6, 7); }

template <int RB, int NCH>
DI void wave_gemm(const u16* __restrict__ act, int lda, const int (&arow)[RB], const u16* __restrict__ W, int ldw,
                  char* wl, f32x16 (&acc)[RB][2], int lane) {
  const int r = lane & 31, h = lane >> 5;
  const int g = lane >> 4, q = (lane >> 2) & 3, pp = lane & 3;
  const unsigned ldsb = (unsigned)(size_t)wl;
  const unsigned trb = ldsb + (8 * (g >> 1) + q) * 128 + ((32 * (g & 1) + 8 * pp) ^ ((q >> 1) << 6));
  const int drow = lane >> 3;
  const int dchk = (lane & 7) ^ (((drow >> 1) & 1) << 2);
  const u16* wsrc = W + (size_t)drow * ldw + dchk * 8;
  auto issue = [&](int c, int buf) {
#pragma unroll
    for (int p = 0; p < 8; ++p) glds16(wsrc + (size_t)(64 * c + 8 * p) * ldw, wl + buf * 8192 + p * 1024);
  };
  issue(0, 0);
#pragma unroll
  for (int c = 0; c < NCH; ++c) {
    const int buf = c & 1;
    bf16x8 bfr[4][RB];
#pragma unroll
    for (int s = 0; s < 4; ++s)
#pragma unroll
      for (int rb = 0; rb < RB; ++rb)
        bfr[s][rb] = *(const bf16x8*)(act + (size_t)arow[rb] * lda + 64 * c + 16 * s + 8 * h);
    WAITV(0);
    if (c + 1 < NCH) issue(c + 1, buf ^ 1);
#pragma unroll
    for (int s = 0; s < 4; ++s) {
      s16x4 a00 = trread(trb + buf * 8192 + s * 2048 + 0);
      s16x4 a01 = trread(trb + buf * 8192 + s * 2048 + 512);
      s16x4 a10 = trread((trb + buf * 8192 + s * 2048 + 0) ^ 64);
      s16x4 a11 = trread((trb + buf * 8192 + s * 2048 + 512) ^ 64);
      asm volatile("s_waitcnt lgkmcnt(0)" : "+v"(a00), "+v"(a01), "+v"(a10), "+v"(a11)::"memory");
      bf16x8 A0 = cat8(a00, a01), A1 = cat8(a10, a11);
#pragma unroll
      for (int rb = 0; rb < RB; ++rb) {
        acc[rb][0] = __builtin_amdgcn_mfma_f32_32x32x16_bf16(A0, bfr[s][rb], acc[rb][0], 0, 0, 0);
        acc[rb][1] = __builtin_amdgcn_mfma_f32_32x32x16_bf16(A1, bfr[s][rb], acc[rb][1], 0, 0, 0);
      }
    }
  }
  (void)r;
}

__global__ __launch_bounds__(256) void qproj_kernel(const u16* __restrict__ x, const u16* __restrict__ wq,
                                                    u16* __restrict__ qout) {
  extern __shared__ __attribute__((aligned(16))) char lds[];
  const int tid = threadIdx.x, lane = tid & 63;
  const int w = __builtin_amdgcn_readfirstlane(tid >> 6);
  const int r = lane & 31, h = lane >> 5;
  const int rt = blockIdx.x % 14, head = blockIdx.x / 14;
  const int col0 = head * 256 + 64 * w;
  f32x16 acc[2][2];
#pragma unroll
  for (int a = 0; a < 2; ++a)
#pragma unroll
    for (int b = 0; b < 2; ++b)
#pragma unroll
      for (int i = 0; i < 16; ++i) acc[a][b][i] = 0.f;
  const int arow[2] = {rt * 64 + r, rt * 64 + 32 + r};
  wave_gemm<2, 7>(x, DM, arow, wq + col0, NQ, lds + w * 16384, acc, lane);
#pragma unroll
  for (int rb = 0; rb < 2; ++rb)
#pragma unroll
    for (int cb = 0; cb < 2; ++cb)
#pragma unroll
      for (int g = 0; g < 4; ++g) {
        u32x2 v = {cvtpk(acc[rb][cb][4 * g + 0], acc[rb][cb][4 * g + 1]),
                   cvtpk(acc[rb][cb][4 * g + 2], acc[rb][cb][4 * g + 3])};
        *(u32x2*)(qout + (size_t)arow[rb] * NQ + col0 + 32 * cb + 8 * g + 4 * h) = v;
      }
}

__global__ __launch_bounds__(256) void oproj_kernel(const u16* __restrict__ o, const u16* __restrict__ wo,
                                                    u16* __restrict__ out) {
  extern __shared__ __attribute__((aligned(16))) char lds[];
  const int tid = threadIdx.x, lane = tid & 63;
  const int w = __builtin_amdgcn_readfirstlane(tid >> 6);
  const int r = lane & 31, h = lane >> 5;
  const int rt = blockIdx.x % 28, ct = blockIdx.x / 28;
  const int col0 = ct * 64;
  f32x16 acc[1][2];
#pragma unroll
  for (int b = 0; b < 2; ++b)
#pragma unroll
    for (int i = 0; i < 16; ++i) acc[0][b][i] = 0.f;
  const int arow[1] = {rt * 32 + r};
  wave_gemm<1, 16>(o + 1024 * w, NQ, arow, wo + (size_t)(1024 * w) * DM + col0, DM, lds + w * 16384, acc, lane);
  __syncthreads();
  float* red = (float*)lds;
#pragma unroll
  for (int cb = 0; cb < 2; ++cb)
#pragma unroll
    for (int g = 0; g < 4; ++g) {
      f32x4 v = {acc[0][cb][4 * g], acc[0][cb][4 * g + 1], acc[0][cb][4 * g + 2], acc[0][cb][4 * g + 3]};
      *(f32x4*)(red + ((w * 8 + cb * 4 + g) * 64 + lane) * 4) = v;
    }
  __syncthreads();
#pragma unroll
  for (int j = 0; j < 2; ++j) {
    const int cg = 2 * w + j, cb = cg >> 2, g = cg & 3;
    f32x4 s = {0.f, 0.f, 0.f, 0.f};
#pragma unroll
    for (int ww = 0; ww < 4; ++ww) {
      f32x4 v = *(const f32x4*)(red + ((ww * 8 + cg) * 64 + lane) * 4);
      s += v;
    }
    u32x2 v = {cvtpk(s[0], s[1]), cvtpk(s[2], s[3])};
    *(u32x2*)(out + (size_t)arow[0] * DM + col0 + 32 * cb + 8 * g + 4 * h) = v;
  }
}

constexpr float SCL = 0.0625f * 1.4426950408889634f;
constexpr float THR = 8.0f;

typedef __attribute__((address_space(3))) void* lds_vp;
struct DmaCtx {
  __amdgpu_buffer_rsrc_t rk, rv;
  int koff[4], voff[4];
  char* dst;
};
template <int U>
DI void issue_unit(const DmaCtx& c) {
  constexpr int t = U >> 2, kind = U & 3, slot = U % 5;
  if constexpr (kind < 2) {
#pragma unroll
    for (int pi = 0; pi < 4; ++pi)
      __builtin_amdgcn_raw_ptr_buffer_load_lds(c.rk, (lds_vp)(c.dst + slot * 8192 + pi * 1024), 16, c.koff[pi],
                                               32 * t * 512 + kind * 256, 0, 0);
  } else {
    constexpr int ks = kind - 2;
#pragma unroll
    for (int pi = 0; pi < 4; ++pi)
      __builtin_amdgcn_raw_ptr_buffer_load_lds(c.rv, (lds_vp)(c.dst + slot * 8192 + pi * 1024), 16, c.voff[pi],
                                               (32 * t + 16 * ks) * 512, 0, 0);
  }
}
template <int U> DI void wait_unit_bar() {
  constexpr int after = (U + 3 > 31 ? 31 : U + 3) - U;
  if constexpr (after == 3) WAITV(12);
  else if constexpr (after == 2) WAITV(8);
  else if constexpr (after == 1) WAITV(4);
  else WAITV(0);
  WAITL0();
  __builtin_amdgcn_s_barrier();
}

struct AttnState {
  bf16x8 qf[16];
  f32x16 oacc[8];
  float m, l;
};

template <int T, int DHF>
DI void qk_half(AttnState& st, f32x16& s, const DmaCtx& c, char* ring, unsigned kaddr) {
  constexpr int U = 4 * T + DHF, slot = U % 5;
  wait_unit_bar<U>();
  if constexpr (U + 4 < 32) issue_unit<U + 4>(c);
#pragma unroll
  for (int ss = 0; ss < 8; ++ss) {
    bf16x8 kf = *(const bf16x8*)(ring + slot * 8192 + (kaddr ^ (ss * 32)));
    s = __builtin_amdgcn_mfma_f32_32x32x16_bf16(kf, st.qf[DHF * 8 + ss], s, 0, 0, 0);
  }
}
template <int T, int KS, int HF>
DI void pv_quarter(AttnState& st, const bf16x8& pf, const unsigned (&vaddr)[4]) {
  constexpr int U = 4 * T + 2 + KS, slot = U % 5;
  constexpr int OFF = slot * 8192 + HF * 256;
  s16x4 v0[4], v1[4];
  v0[0] = trread_o<OFF>(vaddr[0]); v1[0] = trread_o<OFF + 4096>(vaddr[0]);
  v0[1] = trread_o<OFF>(vaddr[1]); v1[1] = trread_o<OFF + 4096>(vaddr[1]);
  v0[2] = trread_o<OFF>(vaddr[2]); v1[2] = trread_o<OFF + 4096>(vaddr[2]);
  v0[3] = trread_o<OFF>(vaddr[3]); v1[3] = trread_o<OFF + 4096>(vaddr[3]);
  asm volatile("s_waitcnt lgkmcnt(0)"
               : "+v"(v0[0]), "+v"(v0[1]), "+v"(v0[2]), "+v"(v0[3]), "+v"(v1[0]), "+v"(v1[1]), "+v"(v1[2]),
                 "+v"(v1[3])::"memory");
#pragma unroll
  for (int d = 0; d < 4; ++d) {
    bf16x8 vf = cat8(v0[d], v1[d]);
    st.oacc[HF * 4 + d] = __builtin_amdgcn_mfma_f32_32x32x16_bf16(vf, pf, st.oacc[HF * 4 + d], 0, 0, 0);
  }
}
template <int T, int KS>
DI void pv_half(AttnState& st, const bf16x8& pf, const DmaCtx& c, const unsigned (&vaddr)[4]) {
  constexpr int U = 4 * T + 2 + KS;
  wait_unit_bar<U>();
  if constexpr (U + 4 < 32) issue_unit<U + 4>(c);
  pv_quarter<T, KS, 0>(st, pf, vaddr);
  pv_quarter<T, KS, 1>(st, pf, vaddr);
}

template <int T>
DI void attn_tile(AttnState& st, const DmaCtx& c, char* ring, unsigned kaddr, const unsigned (&vaddr)[4]) {
  f32x16 s;
#pragma unroll
  for (int i = 0; i < 16; ++i) s[i] = 0.f;
  qk_half<T, 0>(st, s, c, ring, kaddr);
  qk_half<T, 1>(st, s, c, ring, kaddr);
  bf16x8 pf[2];
  {
    float a = s[0];
#pragma unroll
    for (int i = 1; i < 16; ++i) a = fmaxf(a, s[i]);
    auto rr = __builtin_amdgcn_permlane32_swap(__float_as_uint(a), __float_as_uint(a), false, false);
    const float mx = fmaxf(__uint_as_float(rr[0]), __uint_as_float(rr[1])) * SCL;
    const bool grew = (mx - st.m > THR);
    if (T == 0 || __any((int)grew)) {
      const float mn = fmaxf(st.m, mx);
      const float alpha = __builtin_amdgcn_exp2f(st.m - mn);
      st.m = mn;
      st.l *= alpha;
      if (T != 0) {
#pragma unroll
        for (int db = 0; db < 8; ++db)
#pragma unroll
          for (int i = 0; i < 16; ++i) st.oacc[db][i] *= alpha;
      }
    }
    const float nm = -st.m;
    float ps = 0.f;
#pragma unroll
    for (int i = 0; i < 16; ++i) {
      s[i] = __builtin_amdgcn_exp2f(fmaf(s[i], SCL, nm));
      ps += s[i];
    }
    st.l += ps;
#pragma unroll
    for (int ks = 0; ks < 2; ++ks) {
      u32x4 wv = {cvtpk(s[8 * ks + 0], s[8 * ks + 1]), cvtpk(s[8 * ks + 2], s[8 * ks + 3]),
                  cvtpk(s[8 * ks + 4], s[8 * ks + 5]), cvtpk(s[8 * ks + 6], s[8 * ks + 7])};
      pf[ks] = __builtin_bit_cast(bf16x8, wv);
    }
  }
  pv_half<T, 0>(st, pf[0], c, vaddr);
  pv_half<T, 1>(st, pf[1], c, vaddr);
}

#define STAMP(i)
#define DBGARG
__global__ __launch_bounds__(512, 2) void attn_kernel(const u16* __restrict__ q, const u16* __restrict__ kc,
                                                      const u16* __restrict__ vc, u16* __restrict__ o DBGARG) {
  extern __shared__ __attribute__((aligned(16))) char lds[];
  STAMP(0);
  const int tid = threadIdx.x, lane = tid & 63;
  const int w = __builtin_amdgcn_readfirstlane(tid >> 6);
  const int rb = w >> 2, kq = w & 3;
  const int r = lane & 31, h = lane >> 5;
  const int bid = blockIdx.x;
  const int xcd = bid & 7, j = bid >> 3;
  const int grp = (j >> 2) * 8 + xcd, sub = j & 3;
  const int b = grp >> 3, kvh = grp & 7;
  const int head = kvh * 2 + (sub >> 1), rhalf = sub & 1;
  const int row0 = b * SQ + rhalf * 56;

  const u16* Kw = kc + ((size_t)(b * HKV + kvh) * SK + 256 * kq) * DH;
  const u16* Vw = vc + ((size_t)(b * HKV + kvh) * SK + 256 * kq) * DH;
  char* ring = lds + kq * 40960;

  AttnState st;
  {
    int rr = 32 * rb + r;
    rr = rr > 55 ? 55 : rr;
    const u16* qrow = q + (size_t)(row0 + rr) * NQ + head * DH + 8 * h;
#pragma unroll
    for (int s = 0; s < 16; ++s) st.qf[s] = *(const bf16x8*)(qrow + 16 * s);
    st.m = -__builtin_inff();
    st.l = 0.f;
#pragma unroll
    for (int db = 0; db < 8; ++db)
#pragma unroll
      for (int i = 0; i < 16; ++i) st.oacc[db][i] = 0.f;
  }
#pragma unroll
  for (int s = 0; s < 16; ++s) asm volatile("" : "+v"(st.qf[s]));
  WAITV(0);

  STAMP(1);
  DmaCtx c;
  c.rk = __builtin_amdgcn_make_buffer_rsrc((void*)Kw, 0, 256 * DH * 2, 0x00020000);
  c.rv = __builtin_amdgcn_make_buffer_rsrc((void*)Vw, 0, 256 * DH * 2, 0x00020000);
  c.dst = ring + rb * 4096;
#pragma unroll
  for (int pi = 0; pi < 4; ++pi) {
    const int krow = 16 * rb + 4 * pi + (lane >> 4);
    c.koff[pi] = krow * 512 + (((lane & 15) ^ (krow & 15)) * 16);
    const int vrow = 8 * rb + 2 * pi + (lane >> 5);
    c.voff[pi] = vrow * 512 + (((lane & 31) ^ ((vrow & 3) << 2)) * 16);
  }
  issue_unit<0>(c);
  issue_unit<1>(c);
  issue_unit<2>(c);
  issue_unit<3>(c);

  const unsigned kaddr = r * 256 + ((h ^ (r & 15)) * 16);
  const int g = lane >> 4, qq = (lane >> 2) & 3, pp = lane & 3;
  unsigned vaddr[4];
#pragma unroll
  for (int d = 0; d < 4; ++d)
    vaddr[d] = (unsigned)(size_t)ring + (4 * (g >> 1) + qq) * 512 + ((64 * d + 32 * (g & 1) + 8 * pp) ^ (qq << 6));

  attn_tile<0>(st, c, ring, kaddr, vaddr);
  STAMP(2);
  attn_tile<1>(st, c, ring, kaddr, vaddr);
  STAMP(3);
  attn_tile<2>(st, c, ring, kaddr, vaddr);
  STAMP(4);
  attn_tile<3>(st, c, ring, kaddr, vaddr);
  STAMP(5);
  attn_tile<4>(st, c, ring, kaddr, vaddr);
  STAMP(6);
  attn_tile<5>(st, c, ring, kaddr, vaddr);
  STAMP(7);
  attn_tile<6>(st, c, ring, kaddr, vaddr);
  STAMP(8);
  attn_tile<7>(st, c, ring, kaddr, vaddr);
  STAMP(9);

  __syncthreads();
  float* ml = (float*)lds;
  {
    auto rr = __builtin_amdgcn_permlane32_swap(__float_as_uint(st.l), __float_as_uint(st.l), false, false);
    const float lt = __uint_as_float(rr[0]) + __uint_as_float(rr[1]);
    if (h == 0) {
      ml[(w * 32 + r) * 2 + 0] = st.m;
      ml[(w * 32 + r) * 2 + 1] = lt;
    }
  }
  __syncthreads();
  float f;
  {
    float mm[4], ll[4];
#pragma unroll
    for (int k = 0; k < 4; ++k) {
      mm[k] = ml[((rb * 4 + k) * 32 + r) * 2 + 0];
      ll[k] = ml[((rb * 4 + k) * 32 + r) * 2 + 1];
    }
    const float M = fmaxf(fmaxf(mm[0], mm[1]), fmaxf(mm[2], mm[3]));
    float L = 0.f;
#pragma unroll
    for (int k = 0; k < 4; ++k) L += __builtin_amdgcn_exp2f(mm[k] - M) * ll[k];
    f = __builtin_amdgcn_exp2f(st.m - M) / L;
  }
  float* xch = (float*)(lds + 4096);
#pragma unroll
  for (int rnd = 0; rnd < 2; ++rnd) {
#pragma unroll
    for (int db = 0; db < 8; ++db) {
      if ((db & 1) != rnd) continue;
      const int owner = db >> 1;
      if (owner == kq) continue;
      const int dslot = owner < kq ? owner : owner - 1;
#pragma unroll
      for (int gg = 0; gg < 4; ++gg) {
        f32x4 v = {st.oacc[db][4 * gg] * f, st.oacc[db][4 * gg + 1] * f, st.oacc[db][4 * gg + 2] * f,
                   st.oacc[db][4 * gg + 3] * f};
        *(f32x4*)(xch + (((w * 3 + dslot) * 4 + gg) * 64 + lane) * 4) = v;
      }
    }
    __syncthreads();
#pragma unroll
    for (int db = 0; db < 8; ++db) {
      if ((db & 1) != rnd) continue;
      if ((db >> 1) != kq) continue;
#pragma unroll
      for (int gg = 0; gg < 4; ++gg) {
        f32x4 sum = {st.oacc[db][4 * gg] * f, st.oacc[db][4 * gg + 1] * f, st.oacc[db][4 * gg + 2] * f,
                     st.oacc[db][4 * gg + 3] * f};
#pragma unroll
        for (int sk = 0; sk < 4; ++sk) {
          if (sk == kq) continue;
          const int dslot = kq < sk ? kq : kq - 1;
          f32x4 v = *(const f32x4*)(xch + ((((rb * 4 + sk) * 3 + dslot) * 4 + gg) * 64 + lane) * 4);
          sum += v;
        }
        const int rr = 32 * rb + r;
        if (rr < 56) {
          u32x2 v = {cvtpk(sum[0], sum[1]), cvtpk(sum[2], sum[3])};
          *(u32x2*)(o + (size_t)(row0 + rr) * NQ + head * DH + 32 * db + 8 * gg + 4 * h) = v;
        }
      }
    }
    __syncthreads();
  }
  STAMP(10);
}

#define GAP() __builtin_amdgcn_sched_barrier(0)
#define ACLOB "a0","a1","a2","a3","a4","a5","a6","a7","a8","a9","a10","a11","a12","a13","a14","a15","a16","a17","a18","a19","a20","a21","a22","a23","a24","a25","a26","a27","a28","a29","a30","a31","a32","a33","a34","a35","a36","a37","a38","a39","a40","a41","a42","a43","a44","a45","a46","a47","a48","a49","a50","a51","a52","a53","a54","a55","a56","a57","a58","a59","a60","a61","a62","a63","a64","a65","a66","a67","a68","a69","a70","a71","a72","a73","a74","a75","a76","a77","a78","a79","a80","a81","a82","a83","a84","a85","a86","a87","a88","a89","a90","a91","a92","a93","a94","a95","a96","a97","a98","a99","a100","a101","a102","a103","a104","a105","a106","a107","a108","a109","a110","a111","a112","a113","a114","a115","a116","a117","a118","a119","a120","a121","a122","a123","a124","a125","a126","a127","a128","a129","a130","a131","a132","a133","a134","a135","a136","a137","a138","a139","a140","a141","a142","a143","a144","a145","a146","a147","a148","a149","a150","a151","a152","a153","a154","a155","a156","a157","a158","a159","a160","a161","a162","a163","a164","a165","a166","a167","a168","a169","a170","a171","a172","a173","a174","a175","a176","a177","a178","a179","a180","a181","a182","a183","a184","a185","a186","a187","a188","a189","a190","a191","a192","a193","a194","a195","a196","a197","a198","a199","a200","a201","a202","a203","a204","a205","a206","a207","a208","a209","a210","a211","a212","a213","a214","a215","a216","a217","a218","a219","a220","a221","a222","a223","a224","a225","a226","a227","a228","a229","a230","a231","a232","a233","a234","a235","a236","a237","a238","a239","a240","a241","a242","a243","a244","a245","a246","a247","a248","a249","a250","a251","a252","a253","a254","a255"

template <int AO, bool ZERO> DI void pv_mfma(const bf16x8& vf, const bf16x8& pf) {
  if constexpr (ZERO)
    asm volatile("v_mfma_f32_32x32x16_bf16 a[%c2:%c3], %0, %1, 0" ::"v"(vf), "v"(pf), "i"(AO), "i"(AO + 15) : ACLOB);
  else
    asm volatile("v_mfma_f32_32x32x16_bf16 a[%c2:%c3], %0, %1, a[%c2:%c3]" ::"v"(vf), "v"(pf), "i"(AO), "i"(AO + 15)
                 : ACLOB);
}
template <bool FIRST> DI void qk_mfma(f32x16& s, const bf16x8& kf, const bf16x8& qf) {
  if constexpr (FIRST)
    asm volatile("v_mfma_f32_32x32x16_bf16 %0, %1, %2, 0" : "=&v"(s) : "v"(kf), "v"(qf) : ACLOB);
  else
    asm volatile("v_mfma_f32_32x32x16_bf16 %0, %1, %2, %0" : "+v"(s) : "v"(kf), "v"(qf) : ACLOB);
}
DI float max3f(float a, float b, float c) { float r; asm("v_max3_f32 %0, %1, %2, %3" : "=v"(r) : "v"(a), "v"(b), "v"(c)); return r; }
DI float max2f(float a, float b) { float r; asm("v_max_f32_e32 %0, %1, %2" : "=v"(r) : "v"(a), "v"(b)); return r; }
DI float fadd_s(float a, float b) { float r; asm("v_add_f32_e32 %0, %1, %2" : "=v"(r) : "v"(a), "v"(b)); return r; }
DI float ffma_s(float a, float b, float c) { float r; asm("v_fma_f32 %0, %1, %2, %3" : "=v"(r) : "v"(a), "v"(b), "v"(c)); return r; }

constexpr int unit_is_v(int n) { return n < 2 ? 0 : (n >= 30 ? 1 : (((n - 2) & 3) >= 2)); }
constexpr int unit_tile(int n) { return n < 2 ? 0 : (n >= 30 ? 7 : (((n - 2) & 3) >= 2 ? (n - 2) / 4 : (n - 2) / 4 + 1)); }
constexpr int unit_half(int n) { return n & 1; }
constexpr int unit_k(int t, int hf) { return t == 0 ? hf : 4 * (t - 1) + 2 + hf; }
constexpr int unit_v(int t, int hf) { return t == 7 ? 30 + hf : 4 * t + 4 + hf; }

struct Ctx4 {
  __amdgpu_buffer_rsrc_t rk, rv;
  int koff[4], voff[2];
  char* ring;
  unsigned kaddr[8];
  unsigned vaddr[4];
  bf16x8 qf[2][16];
  f32x16 s[2];
  bf16x8 pf[2][2];
  unsigned pn[2][8];
  float m[2], l[2], nm[2], mx[2], fct[2];
  float rs[2];
  bool pend;
  const u16* Kp; const u16* Vp;
  unsigned pfoff;
  int sub;
  unsigned pfd;
};
DI void prefetch_unit(Ctx4& c, int n) {
}

template <int N, int P> DI void dma_piece(const Ctx4& c) {
  if constexpr (N < 32) {
    constexpr int slot = N % 5, t = unit_tile(N), hf = unit_half(N);
    if constexpr (!unit_is_v(N))
      __builtin_amdgcn_raw_ptr_buffer_load_lds(c.rk, (lds_vp)(c.ring + slot * 8192 + P * 1024), 16, c.koff[P & 3],
                                               (32 * t + 16 * (P >> 2)) * 512 + hf * 256, 0, 0);
    else
      __builtin_amdgcn_raw_ptr_buffer_load_lds(c.rv, (lds_vp)(c.ring + slot * 8192 + P * 1024), 16, c.voff[P & 1],
                                               (32 * t + 16 * hf + 4 * (P >> 1)) * 512, 0, 0);
  }
}
template <int N> DI void dma_unit(const Ctx4& c) {
  dma_piece<N, 0>(c); dma_piece<N, 1>(c); dma_piece<N, 2>(c); dma_piece<N, 3>(c);
  dma_piece<N, 4>(c); dma_piece<N, 5>(c); dma_piece<N, 6>(c); dma_piece<N, 7>(c);
}
template <int N, int AHEAD> DI void wait_unit4() {
  constexpr int last = N + AHEAD > 31 ? 31 : N + AHEAD;
  constexpr int after = last - N;
  if constexpr (after == 4) WAITV(32);
  else if constexpr (after == 3) WAITV(24);
  else if constexpr (after == 2) WAITV(16);
  else if constexpr (after == 1) WAITV(8);
  else WAITV(0);
}

template <int T, int J> DI bf16x8 kread(const Ctx4& c) {
  constexpr int n = unit_k(T, J >> 3), slot = n % 5;
  bf16x8 r;
  asm volatile("ds_read_b128 %0, %1 offset:%2" : "=v"(r) : "v"(c.kaddr[J & 7]), "i"(slot * 8192) : "memory");
  return r;
}
template <int T, int J> DI void phaseA_step(Ctx4& c, bf16x8 (&kf)[4]) {
  constexpr int n0 = unit_k(T, 0), n1 = unit_k(T, 1);
  if constexpr (J + 3 < 16) asm volatile("s_waitcnt lgkmcnt(3)" : "+v"(kf[J & 3])::"memory");
  else if constexpr (J + 3 == 16) asm volatile("s_waitcnt lgkmcnt(2)" : "+v"(kf[J & 3])::"memory");
  else if constexpr (J + 3 == 17) asm volatile("s_waitcnt lgkmcnt(1)" : "+v"(kf[J & 3])::"memory");
  else asm volatile("s_waitcnt lgkmcnt(0)" : "+v"(kf[J & 3])::"memory");
  qk_mfma<J == 0>(c.s[0], kf[J & 3], c.qf[0][J]);
  if constexpr (J == 8 && T <= 4) prefetch_unit(c, 14 + 4 * T + c.sub);
  if constexpr (J >= 8) { dma_piece<n0 + 5, J - 8>(c); }
  GAP();
  qk_mfma<J == 0>(c.s[1], kf[J & 3], c.qf[1][J]);
  if constexpr (J + 4 == 8) wait_unit4<n1, 3>();
  if constexpr (J + 4 < 16) kf[J & 3] = kread<T, J + 4>(c);
  GAP();
}
template <int T, int... Js> DI void phaseA_(Ctx4& c, std::integer_sequence<int, Js...>) {
  bf16x8 kf[4];
  wait_unit4<unit_k(T, 0), 4>();
  kf[0] = kread<T, 0>(c); kf[1] = kread<T, 1>(c); kf[2] = kread<T, 2>(c); kf[3] = kread<T, 3>(c);
  GAP();
  (phaseA_step<T, Js>(c, kf), ...);
}
template <int T> DI void phaseA(Ctx4& c) { phaseA_<T>(c, std::make_integer_sequence<int, 16>{}); }

constexpr int egap(int e) { return 6 + (e * 25) / 32; }
template <int T, int G> DI void sm_slice(Ctx4& c) {
  if constexpr (G < 4) {
    constexpr int qb = G >> 1;
    if constexpr ((G & 1) == 0) {
      float a = max3f(c.s[qb][0], c.s[qb][1], c.s[qb][2]);
      a = max3f(a, c.s[qb][3], c.s[qb][4]);
      a = max3f(a, c.s[qb][5], c.s[qb][6]);
      c.mx[qb] = max3f(a, c.s[qb][7], c.s[qb][8]);
    } else {
      float a = max3f(c.mx[qb], c.s[qb][9], c.s[qb][10]);
      a = max3f(a, c.s[qb][11], c.s[qb][12]);
      a = max3f(a, c.s[qb][13], c.s[qb][14]);
      c.mx[qb] = max2f(a, c.s[qb][15]);
    }
  } else if constexpr (G == 4) {
#pragma unroll
    for (int qb = 0; qb < 2; ++qb) {
      auto rr = __builtin_amdgcn_permlane32_swap(__float_as_uint(c.mx[qb]), __float_as_uint(c.mx[qb]), false, false);
      c.mx[qb] = max2f(__uint_as_float(rr[0]), __uint_as_float(rr[1])) * SCL;
    }
  } else if constexpr (G == 5) {
    if constexpr (T == 0) {
      c.m[0] = c.mx[0]; c.m[1] = c.mx[1];
      c.l[0] = 0.f; c.l[1] = 0.f;
    } else {
      const bool grew = (c.mx[0] - c.m[0] > THR) | (c.mx[1] - c.m[1] > THR);
      if (__builtin_expect(__any((int)grew), 0)) {
#pragma unroll
        for (int qb = 0; qb < 2; ++qb) {
          const float mn = fmaxf(c.m[qb], c.mx[qb]);
          const float al = __builtin_amdgcn_exp2f(c.m[qb] - mn);
          c.m[qb] = mn;
          c.l[qb] *= al;
          c.fct[qb] = al;
        }
        c.pend = true;
      }
    }
    c.nm[0] = -c.m[0]; c.nm[1] = -c.m[1];
    c.rs[0] = 0.f; c.rs[1] = 0.f;
  }
  if constexpr (G >= 6) {
#pragma unroll
    for (int e = 0; e < 32; ++e) {
      if (egap(e) == G) {
        const int qb = e >> 4, i = e & 15;
        c.s[qb][i] = __builtin_amdgcn_exp2f(ffma_s(c.s[qb][i], SCL, c.nm[qb]));
      }
    }
#pragma unroll
    for (int e = 0; e < 32; ++e) {
      const int ge = egap(e);
      if (ge == G - 1 || (G == 31 && ge == 31)) {
        const int qb = e >> 4, i = e & 15;
        c.rs[qb] += c.s[qb][i];
      }
    }
#pragma unroll
    for (int d = 0; d < 16; ++d) {
      const int ge = egap(2 * d + 1);
      if (ge == G - 1 || (G == 31 && ge == 31)) {
        const int qb = d >> 3, i = (2 * d) & 15;
        c.pn[qb][d & 7] = cvtpk(c.s[qb][i], c.s[qb][i + 1]);
      }
    }
  }
}

template <int TV, int S> DI void vread_set(const Ctx4& c, s16x4 (&v)[4]) {
  constexpr int ks = S >> 2, dbp = S & 3;
  constexpr int n = unit_v(TV, ks), slot = n % 5;
  constexpr int db0 = 2 * dbp, db1 = 2 * dbp + 1;
  constexpr int o0 = slot * 8192 + (db0 >> 2) * 256, o1 = slot * 8192 + (db1 >> 2) * 256;
  v[0] = trread_o<o0>(c.vaddr[db0 & 3]);
  v[1] = trread_o<o0 + 4096>(c.vaddr[db0 & 3]);
  v[2] = trread_o<o1>(c.vaddr[db1 & 3]);
  v[3] = trread_o<o1 + 4096>(c.vaddr[db1 & 3]);
}
template <int T, bool HASPV, bool HASSM, int M> DI void phaseB_gap(Ctx4& c, s16x4 (&va)[4], s16x4 (&vb)[4]) {
  constexpr int TV = T - 1;
  constexpr int S = M >> 2, ks = S >> 2, dbp = S & 3, dd = (M >> 1) & 1, qb = M & 1;
  if constexpr (HASPV) {
    if constexpr ((M & 3) == 0) {
      if constexpr (S + 1 < 8) {
        if constexpr (S & 1) asm volatile("s_waitcnt lgkmcnt(4)" : "+v"(vb[0]), "+v"(vb[1]), "+v"(vb[2]), "+v"(vb[3])::"memory");
        else asm volatile("s_waitcnt lgkmcnt(4)" : "+v"(va[0]), "+v"(va[1]), "+v"(va[2]), "+v"(va[3])::"memory");
      } else {
        asm volatile("s_waitcnt lgkmcnt(0)" : "+v"(vb[0]), "+v"(vb[1]), "+v"(vb[2]), "+v"(vb[3])::"memory");
      }
    }
    {
      constexpr int db = 2 * dbp + dd;
      const bf16x8 vf = (S & 1) ? cat8(vb[2 * dd], vb[2 * dd + 1]) : cat8(va[2 * dd], va[2 * dd + 1]);
      pv_mfma<(qb * 8 + db) * 16, (TV == 0 && ks == 0)>(vf, c.pf[qb][ks]);
    }
    if constexpr ((M & 3) == 3 && S + 2 < 8) {
      if constexpr (S + 2 == 4) wait_unit4<unit_v(TV, 1), 3>();
      if constexpr (S & 1) vread_set<TV, S + 2>(c, vb); else vread_set<TV, S + 2>(c, va);
    }
    if constexpr (M >= 16 && M < 24) dma_piece<unit_v(TV, 0) + 5, M - 16>(c);
  }
  if constexpr (HASSM) sm_slice<T, M>(c);
  GAP();
}
template <int T, bool HASPV, bool HASSM, int... Ms> DI void phaseB_(Ctx4& c, std::integer_sequence<int, Ms...>) {
  constexpr int TV = T - 1;
  s16x4 va[4], vb[4];
  if constexpr (HASPV) {
    wait_unit4<unit_v(TV, 0), 3>();
    vread_set<TV, 0>(c, va);
    vread_set<TV, 1>(c, vb);
  }
  if constexpr (HASSM) dma_unit<unit_k(T, 1) + 5>(c);
  if constexpr (HASSM) {
    asm volatile("s_nop 7\n\ts_nop 7" : "+v"(c.s[0]), "+v"(c.s[1]));
  }
  GAP();
  (phaseB_gap<T, HASPV, HASSM, Ms>(c, va, vb), ...);
  if constexpr (HASPV) dma_unit<unit_v(TV, 1) + 5>(c);
  if constexpr (HASSM) {
    c.l[0] += c.rs[0]; c.l[1] += c.rs[1];
#pragma unroll
    for (int qb = 0; qb < 2; ++qb)
#pragma unroll
      for (int ks = 0; ks < 2; ++ks) {
        u32x4 wv = {c.pn[qb][4 * ks], c.pn[qb][4 * ks + 1], c.pn[qb][4 * ks + 2], c.pn[qb][4 * ks + 3]};
        c.pf[qb][ks] = __builtin_bit_cast(bf16x8, wv);
      }
  }
  if constexpr (HASPV && HASSM) {
    if (__builtin_expect(c.pend, 0)) {
      float t0, t1;
      asm volatile("s_nop 7\n\ts_nop 7\n\t"
                   "v_accvgpr_read_b32 %0, a0\n\tv_mul_f32 %0, %2, %0\n\tv_accvgpr_write_b32 a0, %0\n\t"
                   "v_accvgpr_read_b32 %1, a1\n\tv_mul_f32 %1, %2, %1\n\tv_accvgpr_write_b32 a1, %1\n\t"
                   "v_accvgpr_read_b32 %0, a2\n\tv_mul_f32 %0, %2, %0\n\tv_accvgpr_write_b32 a2, %0\n\t"
                   "v_accvgpr_read_b32 %1, a3\n\tv_mul_f32 %1, %2, %1\n\tv_accvgpr_write_b32 a3, %1\n\t"
                   "v_accvgpr_read_b32 %0, a4\n\tv_mul_f32 %0, %2, %0\n\tv_accvgpr_write_b32 a4, %0\n\t"
                   "v_accvgpr_read_b32 %1, a5\n\tv_mul_f32 %1, %2, %1\n\tv_accvgpr_write_b32 a5, %1\n\t"
                   "v_accvgpr_read_b32 %0, a6\n\tv_mul_f32 %0, %2, %0\n\tv_accvgpr_write_b32 a6, %0\n\t"
                   "v_accvgpr_read_b32 %1, a7\n\tv_mul_f32 %1, %2, %1\n\tv_accvgpr_write_b32 a7, %1\n\t"
                   "v_accvgpr_read_b32 %0, a8\n\tv_mul_f32 %0, %2, %0\n\tv_accvgpr_write_b32 a8, %0\n\t"
                   "v_accvgpr_read_b32 %1, a9\n\tv_mul_f32 %1, %2, %1\n\tv_accvgpr_write_b32 a9, %1\n\t"
                   "v_accvgpr_read_b32 %0, a10\n\tv_mul_f32 %0, %2, %0\n\tv_accvgpr_write_b32 a10, %0\n\t"
                   "v_accvgpr_read_b32 %1, a11\n\tv_mul_f32 %1, %2, %1\n\tv_accvgpr_write_b32 a11, %1\n\t"
                   "v_accvgpr_read_b32 %0, a12\n\tv_mul_f32 %0, %2, %0\n\tv_accvgpr_write_b32 a12, %0\n\t"
                   "v_accvgpr_read_b32 %1, a13\n\tv_mul_f32 %1, %2, %1\n\tv_accvgpr_write_b32 a13, %1\n\t"
                   "v_accvgpr_read_b32 %0, a14\n\tv_mul_f32 %0, %2, %0\n\tv_accvgpr_write_b32 a14, %0\n\t"
                   "v_accvgpr_read_b32 %1, a15\n\tv_mul_f32 %1, %2, %1\n\tv_accvgpr_write_b32 a15, %1\n\t"
                   "v_accvgpr_read_b32 %0, a16\n\tv_mul_f32 %0, %2, %0\n\tv_accvgpr_write_b32 a16, %0\n\t"
                   "v_accvgpr_read_b32 %1, a17\n\tv_mul_f32 %1, %2, %1\n\tv_accvgpr_write_b32 a17, %1\n\t"
                   "v_accvgpr_read_b32 %0, a18\n\tv_mul_f32 %0, %2, %0\n\tv_accvgpr_write_b32 a18, %0\n\t"
                   "v_accvgpr_read_b32 %1, a19\n\tv_mul_f32 %1, %2, %1\n\tv_accvgpr_write_b32 a19, %1\n\t"
                   "v_accvgpr_read_b32 %0, a20\n\tv_mul_f32 %0, %2, %0\n\tv_accvgpr_write_b32 a20, %0\n\t"
                   "v_accvgpr_read_b32 %1, a21\n\tv_mul_f32 %1, %2, %1\n\tv_accvgpr_write_b32 a21, %1\n\t"
                   "v_accvgpr_read_b32 %0, a22\n\tv_mul_f32 %0, %2, %0\n\tv_accvgpr_write_b32 a22, %0\n\t"
                   "v_accvgpr_read_b32 %1, a23\n\tv_mul_f32 %1, %2, %1\n\tv_accvgpr_write_b32 a23, %1\n\t"
                   "v_accvgpr_read_b32 %0, a24\n\tv_mul_f32 %0, %2, %0\n\tv_accvgpr_write_b32 a24, %0\n\t"
                   "v_accvgpr_read_b32 %1, a25\n\tv_mul_f32 %1, %2, %1\n\tv_accvgpr_write_b32 a25, %1\n\t"
                   "v_accvgpr_read_b32 %0, a26\n\tv_mul_f32 %0, %2, %0\n\tv_accvgpr_write_b32 a26, %0\n\t"
                   "v_accvgpr_read_b32 %1, a27\n\tv_mul_f32 %1, %2, %1\n\tv_accvgpr_write_b32 a27, %1\n\t"
                   "v_accvgpr_read_b32 %0, a28\n\tv_mul_f32 %0, %2, %0\n\tv_accvgpr_write_b32 a28, %0\n\t"
                   "v_accvgpr_read_b32 %1, a29\n\tv_mul_f32 %1, %2, %1\n\tv_accvgpr_write_b32 a29, %1\n\t"
                   "v_accvgpr_read_b32 %0, a30\n\tv_mul_f32 %0, %2, %0\n\tv_accvgpr_write_b32 a30, %0\n\t"
                   "v_accvgpr_read_b32 %1, a31\n\tv_mul_f32 %1, %2, %1\n\tv_accvgpr_write_b32 a31, %1\n\t"
                   "v_accvgpr_read_b32 %0, a32\n\tv_mul_f32 %0, %2, %0\n\tv_accvgpr_write_b32 a32, %0\n\t"
                   "v_accvgpr_read_b32 %1, a33\n\tv_mul_f32 %1, %2, %1\n\tv_accvgpr_write_b32 a33, %1\n\t"
                   "v_accvgpr_read_b32 %0, a34\n\tv_mul_f32 %0, %2, %0\n\tv_accvgpr_write_b32 a34, %0\n\t"
                   "v_accvgpr_read_b32 %1, a35\n\tv_mul_f32 %1, %2, %1\n\tv_accvgpr_write_b32 a35, %1\n\t"
                   "v_accvgpr_read_b32 %0, a36\n\tv_mul_f32 %0, %2, %0\n\tv_accvgpr_write_b32 a36, %0\n\t"
                   "v_accvgpr_read_b32 %1, a37\n\tv_mul_f32 %1, %2, %1\n\tv_accvgpr_write_b32 a37, %1\n\t"
                   "v_accvgpr_read_b32 %0, a38\n\tv_mul_f32 %0, %2, %0\n\tv_accvgpr_write_b32 a38, %0\n\t"
                   "v_accvgpr_read_b32 %1, a39\n\tv_mul_f32 %1, %2, %1\n\tv_accvgpr_write_b32 a39, %1\n\t"
                   "v_accvgpr_read_b32 %0, a40\n\tv_mul_f32 %0, %2, %0\n\tv_accvgpr_write_b32 a40, %0\n\t"
                   "v_accvgpr_read_b32 %1, a41\n\tv_mul_f32 %1, %2, %1\n\tv_accvgpr_write_b32 a41, %1\n\t"
                   "v_accvgpr_read_b32 %0, a42\n\tv_mul_f32 %0, %2, %0\n\tv_accvgpr_write_b32 a42, %0\n\t"
                   "v_accvgpr_read_b32 %1, a43\n\tv_mul_f32 %1, %2, %1\n\tv_accvgpr_write_b32 a43, %1\n\t"
                   "v_accvgpr_read_b32 %0, a44\n\tv_mul_f32 %0, %2, %0\n\tv_accvgpr_write_b32 a44, %0\n\t"
                   "v_accvgpr_read_b32 %1, a45\n\tv_mul_f32 %1, %2, %1\n\tv_accvgpr_write_b32 a45, %1\n\t"
                   "v_accvgpr_read_b32 %0, a46\n\tv_mul_f32 %0, %2, %0\n\tv_accvgpr_write_b32 a46, %0\n\t"
                   "v_accvgpr_read_b32 %1, a47\n\tv_mul_f32 %1, %2, %1\n\tv_accvgpr_write_b32 a47, %1\n\t"
                   "v_accvgpr_read_b32 %0, a48\n\tv_mul_f32 %0, %2, %0\n\tv_accvgpr_write_b32 a48, %0\n\t"
                   "v_accvgpr_read_b32 %1, a49\n\tv_mul_f32 %1, %2, %1\n\tv_accvgpr_write_b32 a49, %1\n\t"
                   "v_accvgpr_read_b32 %0, a50\n\tv_mul_f32 %0, %2, %0\n\tv_accvgpr_write_b32 a50, %0\n\t"
                   "v_accvgpr_read_b32 %1, a51\n\tv_mul_f32 %1, %2, %1\n\tv_accvgpr_write_b32 a51, %1\n\t"
                   "v_accvgpr_read_b32 %0, a52\n\tv_mul_f32 %0, %2, %0\n\tv_accvgpr_write_b32 a52, %0\n\t"
                   "v_accvgpr_read_b32 %1, a53\n\tv_mul_f32 %1, %2, %1\n\tv_accvgpr_write_b32 a53, %1\n\t"
                   "v_accvgpr_read_b32 %0, a54\n\tv_mul_f32 %0, %2, %0\n\tv_accvgpr_write_b32 a54, %0\n\t"
                   "v_accvgpr_read_b32 %1, a55\n\tv_mul_f32 %1, %2, %1\n\tv_accvgpr_write_b32 a55, %1\n\t"
                   "v_accvgpr_read_b32 %0, a56\n\tv_mul_f32 %0, %2, %0\n\tv_accvgpr_write_b32 a56, %0\n\t"
                   "v_accvgpr_read_b32 %1, a57\n\tv_mul_f32 %1, %2, %1\n\tv_accvgpr_write_b32 a57, %1\n\t"
                   "v_accvgpr_read_b32 %0, a58\n\tv_mul_f32 %0, %2, %0\n\tv_accvgpr_write_b32 a58, %0\n\t"
                   "v_accvgpr_read_b32 %1, a59\n\tv_mul_f32 %1, %2, %1\n\tv_accvgpr_write_b32 a59, %1\n\t"
                   "v_accvgpr_read_b32 %0, a60\n\tv_mul_f32 %0, %2, %0\n\tv_accvgpr_write_b32 a60, %0\n\t"
                   "v_accvgpr_read_b32 %1, a61\n\tv_mul_f32 %1, %2, %1\n\tv_accvgpr_write_b32 a61, %1\n\t"
                   "v_accvgpr_read_b32 %0, a62\n\tv_mul_f32 %0, %2, %0\n\tv_accvgpr_write_b32 a62, %0\n\t"
                   "v_accvgpr_read_b32 %1, a63\n\tv_mul_f32 %1, %2, %1\n\tv_accvgpr_write_b32 a63, %1\n\t"
                   "v_accvgpr_read_b32 %0, a64\n\tv_mul_f32 %0, %2, %0\n\tv_accvgpr_write_b32 a64, %0\n\t"
                   "v_accvgpr_read_b32 %1, a65\n\tv_mul_f32 %1, %2, %1\n\tv_accvgpr_write_b32 a65, %1\n\t"
                   "v_accvgpr_read_b32 %0, a66\n\tv_mul_f32 %0, %2, %0\n\tv_accvgpr_write_b32 a66, %0\n\t"
                   "v_accvgpr_read_b32 %1, a67\n\tv_mul_f32 %1, %2, %1\n\tv_accvgpr_write_b32 a67, %1\n\t"
                   "v_accvgpr_read_b32 %0, a68\n\tv_mul_f32 %0, %2, %0\n\tv_accvgpr_write_b32 a68, %0\n\t"
                   "v_accvgpr_read_b32 %1, a69\n\tv_mul_f32 %1, %2, %1\n\tv_accvgpr_write_b32 a69, %1\n\t"
                   "v_accvgpr_read_b32 %0, a70\n\tv_mul_f32 %0, %2, %0\n\tv_accvgpr_write_b32 a70, %0\n\t"
                   "v_accvgpr_read_b32 %1, a71\n\tv_mul_f32 %1, %2, %1\n\tv_accvgpr_write_b32 a71, %1\n\t"
                   "v_accvgpr_read_b32 %0, a72\n\tv_mul_f32 %0, %2, %0\n\tv_accvgpr_write_b32 a72, %0\n\t"
                   "v_accvgpr_read_b32 %1, a73\n\tv_mul_f32 %1, %2, %1\n\tv_accvgpr_write_b32 a73, %1\n\t"
                   "v_accvgpr_read_b32 %0, a74\n\tv_mul_f32 %0, %2, %0\n\tv_accvgpr_write_b32 a74, %0\n\t"
                   "v_accvgpr_read_b32 %1, a75\n\tv_mul_f32 %1, %2, %1\n\tv_accvgpr_write_b32 a75, %1\n\t"
                   "v_accvgpr_read_b32 %0, a76\n\tv_mul_f32 %0, %2, %0\n\tv_accvgpr_write_b32 a76, %0\n\t"
                   "v_accvgpr_read_b32 %1, a77\n\tv_mul_f32 %1, %2, %1\n\tv_accvgpr_write_b32 a77, %1\n\t"
                   "v_accvgpr_read_b32 %0, a78\n\tv_mul_f32 %0, %2, %0\n\tv_accvgpr_write_b32 a78, %0\n\t"
                   "v_accvgpr_read_b32 %1, a79\n\tv_mul_f32 %1, %2, %1\n\tv_accvgpr_write_b32 a79, %1\n\t"
                   "v_accvgpr_read_b32 %0, a80\n\tv_mul_f32 %0, %2, %0\n\tv_accvgpr_write_b32 a80, %0\n\t"
                   "v_accvgpr_read_b32 %1, a81\n\tv_mul_f32 %1, %2, %1\n\tv_accvgpr_write_b32 a81, %1\n\t"
                   "v_accvgpr_read_b32 %0, a82\n\tv_mul_f32 %0, %2, %0\n\tv_accvgpr_write_b32 a82, %0\n\t"
                   "v_accvgpr_read_b32 %1, a83\n\tv_mul_f32 %1, %2, %1\n\tv_accvgpr_write_b32 a83, %1\n\t"
                   "v_accvgpr_read_b32 %0, a84\n\tv_mul_f32 %0, %2, %0\n\tv_accvgpr_write_b32 a84, %0\n\t"
                   "v_accvgpr_read_b32 %1, a85\n\tv_mul_f32 %1, %2, %1\n\tv_accvgpr_write_b32 a85, %1\n\t"
                   "v_accvgpr_read_b32 %0, a86\n\tv_mul_f32 %0, %2, %0\n\tv_accvgpr_write_b32 a86, %0\n\t"
                   "v_accvgpr_read_b32 %1, a87\n\tv_mul_f32 %1, %2, %1\n\tv_accvgpr_write_b32 a87, %1\n\t"
                   "v_accvgpr_read_b32 %0, a88\n\tv_mul_f32 %0, %2, %0\n\tv_accvgpr_write_b32 a88, %0\n\t"
                   "v_accvgpr_read_b32 %1, a89\n\tv_mul_f32 %1, %2, %1\n\tv_accvgpr_write_b32 a89, %1\n\t"
                   "v_accvgpr_read_b32 %0, a90\n\tv_mul_f32 %0, %2, %0\n\tv_accvgpr_write_b32 a90, %0\n\t"
                   "v_accvgpr_read_b32 %1, a91\n\tv_mul_f32 %1, %2, %1\n\tv_accvgpr_write_b32 a91, %1\n\t"
                   "v_accvgpr_read_b32 %0, a92\n\tv_mul_f32 %0, %2, %0\n\tv_accvgpr_write_b32 a92, %0\n\t"
                   "v_accvgpr_read_b32 %1, a93\n\tv_mul_f32 %1, %2, %1\n\tv_accvgpr_write_b32 a93, %1\n\t"
                   "v_accvgpr_read_b32 %0, a94\n\tv_mul_f32 %0, %2, %0\n\tv_accvgpr_write_b32 a94, %0\n\t"
                   "v_accvgpr_read_b32 %1, a95\n\tv_mul_f32 %1, %2, %1\n\tv_accvgpr_write_b32 a95, %1\n\t"
                   "v_accvgpr_read_b32 %0, a96\n\tv_mul_f32 %0, %2, %0\n\tv_accvgpr_write_b32 a96, %0\n\t"
                   "v_accvgpr_read_b32 %1, a97\n\tv_mul_f32 %1, %2, %1\n\tv_accvgpr_write_b32 a97, %1\n\t"
                   "v_accvgpr_read_b32 %0, a98\n\tv_mul_f32 %0, %2, %0\n\tv_accvgpr_write_b32 a98, %0\n\t"
                   "v_accvgpr_read_b32 %1, a99\n\tv_mul_f32 %1, %2, %1\n\tv_accvgpr_write_b32 a99, %1\n\t"
                   "v_accvgpr_read_b32 %0, a100\n\tv_mul_f32 %0, %2, %0\n\tv_accvgpr_write_b32 a100, %0\n\t"
                   "v_accvgpr_read_b32 %1, a101\n\tv_mul_f32 %1, %2, %1\n\tv_accvgpr_write_b32 a101, %1\n\t"
                   "v_accvgpr_read_b32 %0, a102\n\tv_mul_f32 %0, %2, %0\n\tv_accvgpr_write_b32 a102, %0\n\t"
                   "v_accvgpr_read_b32 %1, a103\n\tv_mul_f32 %1, %2, %1\n\tv_accvgpr_write_b32 a103, %1\n\t"
                   "v_accvgpr_read_b32 %0, a104\n\tv_mul_f32 %0, %2, %0\n\tv_accvgpr_write_b32 a104, %0\n\t"
                   "v_accvgpr_read_b32 %1, a105\n\tv_mul_f32 %1, %2, %1\n\tv_accvgpr_write_b32 a105, %1\n\t"
                   "v_accvgpr_read_b32 %0, a106\n\tv_mul_f32 %0, %2, %0\n\tv_accvgpr_write_b32 a106, %0\n\t"
                   "v_accvgpr_read_b32 %1, a107\n\tv_mul_f32 %1, %2, %1\n\tv_accvgpr_write_b32 a107, %1\n\t"
                   "v_accvgpr_read_b32 %0, a108\n\tv_mul_f32 %0, %2, %0\n\tv_accvgpr_write_b32 a108, %0\n\t"
                   "v_accvgpr_read_b32 %1, a109\n\tv_mul_f32 %1, %2, %1\n\tv_accvgpr_write_b32 a109, %1\n\t"
                   "v_accvgpr_read_b32 %0, a110\n\tv_mul_f32 %0, %2, %0\n\tv_accvgpr_write_b32 a110, %0\n\t"
                   "v_accvgpr_read_b32 %1, a111\n\tv_mul_f32 %1, %2, %1\n\tv_accvgpr_write_b32 a111, %1\n\t"
                   "v_accvgpr_read_b32 %0, a112\n\tv_mul_f32 %0, %2, %0\n\tv_accvgpr_write_b32 a112, %0\n\t"
                   "v_accvgpr_read_b32 %1, a113\n\tv_mul_f32 %1, %2, %1\n\tv_accvgpr_write_b32 a113, %1\n\t"
                   "v_accvgpr_read_b32 %0, a114\n\tv_mul_f32 %0, %2, %0\n\tv_accvgpr_write_b32 a114, %0\n\t"
                   "v_accvgpr_read_b32 %1, a115\n\tv_mul_f32 %1, %2, %1\n\tv_accvgpr_write_b32 a115, %1\n\t"
                   "v_accvgpr_read_b32 %0, a116\n\tv_mul_f32 %0, %2, %0\n\tv_accvgpr_write_b32 a116, %0\n\t"
                   "v_accvgpr_read_b32 %1, a117\n\tv_mul_f32 %1, %2, %1\n\tv_accvgpr_write_b32 a117, %1\n\t"
                   "v_accvgpr_read_b32 %0, a118\n\tv_mul_f32 %0, %2, %0\n\tv_accvgpr_write_b32 a118, %0\n\t"
                   "v_accvgpr_read_b32 %1, a119\n\tv_mul_f32 %1, %2, %1\n\tv_accvgpr_write_b32 a119, %1\n\t"
                   "v_accvgpr_read_b32 %0, a120\n\tv_mul_f32 %0, %2, %0\n\tv_accvgpr_write_b32 a120, %0\n\t"
                   "v_accvgpr_read_b32 %1, a121\n\tv_mul_f32 %1, %2, %1\n\tv_accvgpr_write_b32 a121, %1\n\t"
                   "v_accvgpr_read_b32 %0, a122\n\tv_mul_f32 %0, %2, %0\n\tv_accvgpr_write_b32 a122, %0\n\t"
                   "v_accvgpr_read_b32 %1, a123\n\tv_mul_f32 %1, %2, %1\n\tv_accvgpr_write_b32 a123, %1\n\t"
                   "v_accvgpr_read_b32 %0, a124\n\tv_mul_f32 %0, %2, %0\n\tv_accvgpr_write_b32 a124, %0\n\t"
                   "v_accvgpr_read_b32 %1, a125\n\tv_mul_f32 %1, %2, %1\n\tv_accvgpr_write_b32 a125, %1\n\t"
                   "v_accvgpr_read_b32 %0, a126\n\tv_mul_f32 %0, %2, %0\n\tv_accvgpr_write_b32 a126, %0\n\t"
                   "v_accvgpr_read_b32 %1, a127\n\tv_mul_f32 %1, %2, %1\n\tv_accvgpr_write_b32 a127, %1\n\t"
                   "v_accvgpr_read_b32 %0, a128\n\tv_mul_f32 %0, %3, %0\n\tv_accvgpr_write_b32 a128, %0\n\t"
                   "v_accvgpr_read_b32 %1, a129\n\tv_mul_f32 %1, %3, %1\n\tv_accvgpr_write_b32 a129, %1\n\t"
                   "v_accvgpr_read_b32 %0, a130\n\tv_mul_f32 %0, %3, %0\n\tv_accvgpr_write_b32 a130, %0\n\t"
                   "v_accvgpr_read_b32 %1, a131\n\tv_mul_f32 %1, %3, %1\n\tv_accvgpr_write_b32 a131, %1\n\t"
                   "v_accvgpr_read_b32 %0, a132\n\tv_mul_f32 %0, %3, %0\n\tv_accvgpr_write_b32 a132, %0\n\t"
                   "v_accvgpr_read_b32 %1, a133\n\tv_mul_f32 %1, %3, %1\n\tv_accvgpr_write_b32 a133, %1\n\t"
                   "v_accvgpr_read_b32 %0, a134\n\tv_mul_f32 %0, %3, %0\n\tv_accvgpr_write_b32 a134, %0\n\t"
                   "v_accvgpr_read_b32 %1, a135\n\tv_mul_f32 %1, %3, %1\n\tv_accvgpr_write_b32 a135, %1\n\t"
                   "v_accvgpr_read_b32 %0, a136\n\tv_mul_f32 %0, %3, %0\n\tv_accvgpr_write_b32 a136, %0\n\t"
                   "v_accvgpr_read_b32 %1, a137\n\tv_mul_f32 %1, %3, %1\n\tv_accvgpr_write_b32 a137, %1\n\t"
                   "v_accvgpr_read_b32 %0, a138\n\tv_mul_f32 %0, %3, %0\n\tv_accvgpr_write_b32 a138, %0\n\t"
                   "v_accvgpr_read_b32 %1, a139\n\tv_mul_f32 %1, %3, %1\n\tv_accvgpr_write_b32 a139, %1\n\t"
                   "v_accvgpr_read_b32 %0, a140\n\tv_mul_f32 %0, %3, %0\n\tv_accvgpr_write_b32 a140, %0\n\t"
                   "v_accvgpr_read_b32 %1, a141\n\tv_mul_f32 %1, %3, %1\n\tv_accvgpr_write_b32 a141, %1\n\t"
                   "v_accvgpr_read_b32 %0, a142\n\tv_mul_f32 %0, %3, %0\n\tv_accvgpr_write_b32 a142, %0\n\t"
                   "v_accvgpr_read_b32 %1, a143\n\tv_mul_f32 %1, %3, %1\n\tv_accvgpr_write_b32 a143, %1\n\t"
                   "v_accvgpr_read_b32 %0, a144\n\tv_mul_f32 %0, %3, %0\n\tv_accvgpr_write_b32 a144, %0\n\t"
                   "v_accvgpr_read_b32 %1, a145\n\tv_mul_f32 %1, %3, %1\n\tv_accvgpr_write_b32 a145, %1\n\t"
                   "v_accvgpr_read_b32 %0, a146\n\tv_mul_f32 %0, %3, %0\n\tv_accvgpr_write_b32 a146, %0\n\t"
                   "v_accvgpr_read_b32 %1, a147\n\tv_mul_f32 %1, %3, %1\n\tv_accvgpr_write_b32 a147, %1\n\t"
                   "v_accvgpr_read_b32 %0, a148\n\tv_mul_f32 %0, %3, %0\n\tv_accvgpr_write_b32 a148, %0\n\t"
                   "v_accvgpr_read_b32 %1, a149\n\tv_mul_f32 %1, %3, %1\n\tv_accvgpr_write_b32 a149, %1\n\t"
                   "v_accvgpr_read_b32 %0, a150\n\tv_mul_f32 %0, %3, %0\n\tv_accvgpr_write_b32 a150, %0\n\t"
                   "v_accvgpr_read_b32 %1, a151\n\tv_mul_f32 %1, %3, %1\n\tv_accvgpr_write_b32 a151, %1\n\t"
                   "v_accvgpr_read_b32 %0, a152\n\tv_mul_f32 %0, %3, %0\n\tv_accvgpr_write_b32 a152, %0\n\t"
                   "v_accvgpr_read_b32 %1, a153\n\tv_mul_f32 %1, %3, %1\n\tv_accvgpr_write_b32 a153, %1\n\t"
                   "v_accvgpr_read_b32 %0, a154\n\tv_mul_f32 %0, %3, %0\n\tv_accvgpr_write_b32 a154, %0\n\t"
                   "v_accvgpr_read_b32 %1, a155\n\tv_mul_f32 %1, %3, %1\n\tv_accvgpr_write_b32 a155, %1\n\t"
                   "v_accvgpr_read_b32 %0, a156\n\tv_mul_f32 %0, %3, %0\n\tv_accvgpr_write_b32 a156, %0\n\t"
                   "v_accvgpr_read_b32 %1, a157\n\tv_mul_f32 %1, %3, %1\n\tv_accvgpr_write_b32 a157, %1\n\t"
                   "v_accvgpr_read_b32 %0, a158\n\tv_mul_f32 %0, %3, %0\n\tv_accvgpr_write_b32 a158, %0\n\t"
                   "v_accvgpr_read_b32 %1, a159\n\tv_mul_f32 %1, %3, %1\n\tv_accvgpr_write_b32 a159, %1\n\t"
                   "v_accvgpr_read_b32 %0, a160\n\tv_mul_f32 %0, %3, %0\n\tv_accvgpr_write_b32 a160, %0\n\t"
                   "v_accvgpr_read_b32 %1, a161\n\tv_mul_f32 %1, %3, %1\n\tv_accvgpr_write_b32 a161, %1\n\t"
                   "v_accvgpr_read_b32 %0, a162\n\tv_mul_f32 %0, %3, %0\n\tv_accvgpr_write_b32 a162, %0\n\t"
                   "v_accvgpr_read_b32 %1, a163\n\tv_mul_f32 %1, %3, %1\n\tv_accvgpr_write_b32 a163, %1\n\t"
                   "v_accvgpr_read_b32 %0, a164\n\tv_mul_f32 %0, %3, %0\n\tv_accvgpr_write_b32 a164, %0\n\t"
                   "v_accvgpr_read_b32 %1, a165\n\tv_mul_f32 %1, %3, %1\n\tv_accvgpr_write_b32 a165, %1\n\t"
                   "v_accvgpr_read_b32 %0, a166\n\tv_mul_f32 %0, %3, %0\n\tv_accvgpr_write_b32 a166, %0\n\t"
                   "v_accvgpr_read_b32 %1, a167\n\tv_mul_f32 %1, %3, %1\n\tv_accvgpr_write_b32 a167, %1\n\t"
                   "v_accvgpr_read_b32 %0, a168\n\tv_mul_f32 %0, %3, %0\n\tv_accvgpr_write_b32 a168, %0\n\t"
                   "v_accvgpr_read_b32 %1, a169\n\tv_mul_f32 %1, %3, %1\n\tv_accvgpr_write_b32 a169, %1\n\t"
                   "v_accvgpr_read_b32 %0, a170\n\tv_mul_f32 %0, %3, %0\n\tv_accvgpr_write_b32 a170, %0\n\t"
                   "v_accvgpr_read_b32 %1, a171\n\tv_mul_f32 %1, %3, %1\n\tv_accvgpr_write_b32 a171, %1\n\t"
                   "v_accvgpr_read_b32 %0, a172\n\tv_mul_f32 %0, %3, %0\n\tv_accvgpr_write_b32 a172, %0\n\t"
                   "v_accvgpr_read_b32 %1, a173\n\tv_mul_f32 %1, %3, %1\n\tv_accvgpr_write_b32 a173, %1\n\t"
                   "v_accvgpr_read_b32 %0, a174\n\tv_mul_f32 %0, %3, %0\n\tv_accvgpr_write_b32 a174, %0\n\t"
                   "v_accvgpr_read_b32 %1, a175\n\tv_mul_f32 %1, %3, %1\n\tv_accvgpr_write_b32 a175, %1\n\t"
                   "v_accvgpr_read_b32 %0, a176\n\tv_mul_f32 %0, %3, %0\n\tv_accvgpr_write_b32 a176, %0\n\t"
                   "v_accvgpr_read_b32 %1, a177\n\tv_mul_f32 %1, %3, %1\n\tv_accvgpr_write_b32 a177, %1\n\t"
                   "v_accvgpr_read_b32 %0, a178\n\tv_mul_f32 %0, %3, %0\n\tv_accvgpr_write_b32 a178, %0\n\t"
                   "v_accvgpr_read_b32 %1, a179\n\tv_mul_f32 %1, %3, %1\n\tv_accvgpr_write_b32 a179, %1\n\t"
                   "v_accvgpr_read_b32 %0, a180\n\tv_mul_f32 %0, %3, %0\n\tv_accvgpr_write_b32 a180, %0\n\t"
                   "v_accvgpr_read_b32 %1, a181\n\tv_mul_f32 %1, %3, %1\n\tv_accvgpr_write_b32 a181, %1\n\t"
                   "v_accvgpr_read_b32 %0, a182\n\tv_mul_f32 %0, %3, %0\n\tv_accvgpr_write_b32 a182, %0\n\t"
                   "v_accvgpr_read_b32 %1, a183\n\tv_mul_f32 %1, %3, %1\n\tv_accvgpr_write_b32 a183, %1\n\t"
                   "v_accvgpr_read_b32 %0, a184\n\tv_mul_f32 %0, %3, %0\n\tv_accvgpr_write_b32 a184, %0\n\t"
                   "v_accvgpr_read_b32 %1, a185\n\tv_mul_f32 %1, %3, %1\n\tv_accvgpr_write_b32 a185, %1\n\t"
                   "v_accvgpr_read_b32 %0, a186\n\tv_mul_f32 %0, %3, %0\n\tv_accvgpr_write_b32 a186, %0\n\t"
                   "v_accvgpr_read_b32 %1, a187\n\tv_mul_f32 %1, %3, %1\n\tv_accvgpr_write_b32 a187, %1\n\t"
                   "v_accvgpr_read_b32 %0, a188\n\tv_mul_f32 %0, %3, %0\n\tv_accvgpr_write_b32 a188, %0\n\t"
                   "v_accvgpr_read_b32 %1, a189\n\tv_mul_f32 %1, %3, %1\n\tv_accvgpr_write_b32 a189, %1\n\t"
                   "v_accvgpr_read_b32 %0, a190\n\tv_mul_f32 %0, %3, %0\n\tv_accvgpr_write_b32 a190, %0\n\t"
                   "v_accvgpr_read_b32 %1, a191\n\tv_mul_f32 %1, %3, %1\n\tv_accvgpr_write_b32 a191, %1\n\t"
                   "v_accvgpr_read_b32 %0, a192\n\tv_mul_f32 %0, %3, %0\n\tv_accvgpr_write_b32 a192, %0\n\t"
                   "v_accvgpr_read_b32 %1, a193\n\tv_mul_f32 %1, %3, %1\n\tv_accvgpr_write_b32 a193, %1\n\t"
                   "v_accvgpr_read_b32 %0, a194\n\tv_mul_f32 %0, %3, %0\n\tv_accvgpr_write_b32 a194, %0\n\t"
                   "v_accvgpr_read_b32 %1, a195\n\tv_mul_f32 %1, %3, %1\n\tv_accvgpr_write_b32 a195, %1\n\t"
                   "v_accvgpr_read_b32 %0, a196\n\tv_mul_f32 %0, %3, %0\n\tv_accvgpr_write_b32 a196, %0\n\t"
                   "v_accvgpr_read_b32 %1, a197\n\tv_mul_f32 %1, %3, %1\n\tv_accvgpr_write_b32 a197, %1\n\t"
                   "v_accvgpr_read_b32 %0, a198\n\tv_mul_f32 %0, %3, %0\n\tv_accvgpr_write_b32 a198, %0\n\t"
                   "v_accvgpr_read_b32 %1, a199\n\tv_mul_f32 %1, %3, %1\n\tv_accvgpr_write_b32 a199, %1\n\t"
                   "v_accvgpr_read_b32 %0, a200\n\tv_mul_f32 %0, %3, %0\n\tv_accvgpr_write_b32 a200, %0\n\t"
                   "v_accvgpr_read_b32 %1, a201\n\tv_mul_f32 %1, %3, %1\n\tv_accvgpr_write_b32 a201, %1\n\t"
                   "v_accvgpr_read_b32 %0, a202\n\tv_mul_f32 %0, %3, %0\n\tv_accvgpr_write_b32 a202, %0\n\t"
                   "v_accvgpr_read_b32 %1, a203\n\tv_mul_f32 %1, %3, %1\n\tv_accvgpr_write_b32 a203, %1\n\t"
                   "v_accvgpr_read_b32 %0, a204\n\tv_mul_f32 %0, %3, %0\n\tv_accvgpr_write_b32 a204, %0\n\t"
                   "v_accvgpr_read_b32 %1, a205\n\tv_mul_f32 %1, %3, %1\n\tv_accvgpr_write_b32 a205, %1\n\t"
                   "v_accvgpr_read_b32 %0, a206\n\tv_mul_f32 %0, %3, %0\n\tv_accvgpr_write_b32 a206, %0\n\t"
                   "v_accvgpr_read_b32 %1, a207\n\tv_mul_f32 %1, %3, %1\n\tv_accvgpr_write_b32 a207, %1\n\t"
                   "v_accvgpr_read_b32 %0, a208\n\tv_mul_f32 %0, %3, %0\n\tv_accvgpr_write_b32 a208, %0\n\t"
                   "v_accvgpr_read_b32 %1, a209\n\tv_mul_f32 %1, %3, %1\n\tv_accvgpr_write_b32 a209, %1\n\t"
                   "v_accvgpr_read_b32 %0, a210\n\tv_mul_f32 %0, %3, %0\n\tv_accvgpr_write_b32 a210, %0\n\t"
                   "v_accvgpr_read_b32 %1, a211\n\tv_mul_f32 %1, %3, %1\n\tv_accvgpr_write_b32 a211, %1\n\t"
                   "v_accvgpr_read_b32 %0, a212\n\tv_mul_f32 %0, %3, %0\n\tv_accvgpr_write_b32 a212, %0\n\t"
                   "v_accvgpr_read_b32 %1, a213\n\tv_mul_f32 %1, %3, %1\n\tv_accvgpr_write_b32 a213, %1\n\t"
                   "v_accvgpr_read_b32 %0, a214\n\tv_mul_f32 %0, %3, %0\n\tv_accvgpr_write_b32 a214, %0\n\t"
                   "v_accvgpr_read_b32 %1, a215\n\tv_mul_f32 %1, %3, %1\n\tv_accvgpr_write_b32 a215, %1\n\t"
                   "v_accvgpr_read_b32 %0, a216\n\tv_mul_f32 %0, %3, %0\n\tv_accvgpr_write_b32 a216, %0\n\t"
                   "v_accvgpr_read_b32 %1, a217\n\tv_mul_f32 %1, %3, %1\n\tv_accvgpr_write_b32 a217, %1\n\t"
                   "v_accvgpr_read_b32 %0, a218\n\tv_mul_f32 %0, %3, %0\n\tv_accvgpr_write_b32 a218, %0\n\t"
                   "v_accvgpr_read_b32 %1, a219\n\tv_mul_f32 %1, %3, %1\n\tv_accvgpr_write_b32 a219, %1\n\t"
                   "v_accvgpr_read_b32 %0, a220\n\tv_mul_f32 %0, %3, %0\n\tv_accvgpr_write_b32 a220, %0\n\t"
                   "v_accvgpr_read_b32 %1, a221\n\tv_mul_f32 %1, %3, %1\n\tv_accvgpr_write_b32 a221, %1\n\t"
                   "v_accvgpr_read_b32 %0, a222\n\tv_mul_f32 %0, %3, %0\n\tv_accvgpr_write_b32 a222, %0\n\t"
                   "v_accvgpr_read_b32 %1, a223\n\tv_mul_f32 %1, %3, %1\n\tv_accvgpr_write_b32 a223, %1\n\t"
                   "v_accvgpr_read_b32 %0, a224\n\tv_mul_f32 %0, %3, %0\n\tv_accvgpr_write_b32 a224, %0\n\t"
                   "v_accvgpr_read_b32 %1, a225\n\tv_mul_f32 %1, %3, %1\n\tv_accvgpr_write_b32 a225, %1\n\t"
                   "v_accvgpr_read_b32 %0, a226\n\tv_mul_f32 %0, %3, %0\n\tv_accvgpr_write_b32 a226, %0\n\t"
                   "v_accvgpr_read_b32 %1, a227\n\tv_mul_f32 %1, %3, %1\n\tv_accvgpr_write_b32 a227, %1\n\t"
                   "v_accvgpr_read_b32 %0, a228\n\tv_mul_f32 %0, %3, %0\n\tv_accvgpr_write_b32 a228, %0\n\t"
                   "v_accvgpr_read_b32 %1, a229\n\tv_mul_f32 %1, %3, %1\n\tv_accvgpr_write_b32 a229, %1\n\t"
                   "v_accvgpr_read_b32 %0, a230\n\tv_mul_f32 %0, %3, %0\n\tv_accvgpr_write_b32 a230, %0\n\t"
                   "v_accvgpr_read_b32 %1, a231\n\tv_mul_f32 %1, %3, %1\n\tv_accvgpr_write_b32 a231, %1\n\t"
                   "v_accvgpr_read_b32 %0, a232\n\tv_mul_f32 %0, %3, %0\n\tv_accvgpr_write_b32 a232, %0\n\t"
                   "v_accvgpr_read_b32 %1, a233\n\tv_mul_f32 %1, %3, %1\n\tv_accvgpr_write_b32 a233, %1\n\t"
                   "v_accvgpr_read_b32 %0, a234\n\tv_mul_f32 %0, %3, %0\n\tv_accvgpr_write_b32 a234, %0\n\t"
                   "v_accvgpr_read_b32 %1, a235\n\tv_mul_f32 %1, %3, %1\n\tv_accvgpr_write_b32 a235, %1\n\t"
                   "v_accvgpr_read_b32 %0, a236\n\tv_mul_f32 %0, %3, %0\n\tv_accvgpr_write_b32 a236, %0\n\t"
                   "v_accvgpr_read_b32 %1, a237\n\tv_mul_f32 %1, %3, %1\n\tv_accvgpr_write_b32 a237, %1\n\t"
                   "v_accvgpr_read_b32 %0, a238\n\tv_mul_f32 %0, %3, %0\n\tv_accvgpr_write_b32 a238, %0\n\t"
                   "v_accvgpr_read_b32 %1, a239\n\tv_mul_f32 %1, %3, %1\n\tv_accvgpr_write_b32 a239, %1\n\t"
                   "v_accvgpr_read_b32 %0, a240\n\tv_mul_f32 %0, %3, %0\n\tv_accvgpr_write_b32 a240, %0\n\t"
                   "v_accvgpr_read_b32 %1, a241\n\tv_mul_f32 %1, %3, %1\n\tv_accvgpr_write_b32 a241, %1\n\t"
                   "v_accvgpr_read_b32 %0, a242\n\tv_mul_f32 %0, %3, %0\n\tv_accvgpr_write_b32 a242, %0\n\t"
                   "v_accvgpr_read_b32 %1, a243\n\tv_mul_f32 %1, %3, %1\n\tv_accvgpr_write_b32 a243, %1\n\t"
                   "v_accvgpr_read_b32 %0, a244\n\tv_mul_f32 %0, %3, %0\n\tv_accvgpr_write_b32 a244, %0\n\t"
                   "v_accvgpr_read_b32 %1, a245\n\tv_mul_f32 %1, %3, %1\n\tv_accvgpr_write_b32 a245, %1\n\t"
                   "v_accvgpr_read_b32 %0, a246\n\tv_mul_f32 %0, %3, %0\n\tv_accvgpr_write_b32 a246, %0\n\t"
                   "v_accvgpr_read_b32 %1, a247\n\tv_mul_f32 %1, %3, %1\n\tv_accvgpr_write_b32 a247, %1\n\t"
                   "v_accvgpr_read_b32 %0, a248\n\tv_mul_f32 %0, %3, %0\n\tv_accvgpr_write_b32 a248, %0\n\t"
                   "v_accvgpr_read_b32 %1, a249\n\tv_mul_f32 %1, %3, %1\n\tv_accvgpr_write_b32 a249, %1\n\t"
                   "v_accvgpr_read_b32 %0, a250\n\tv_mul_f32 %0, %3, %0\n\tv_accvgpr_write_b32 a250, %0\n\t"
                   "v_accvgpr_read_b32 %1, a251\n\tv_mul_f32 %1, %3, %1\n\tv_accvgpr_write_b32 a251, %1\n\t"
                   "v_accvgpr_read_b32 %0, a252\n\tv_mul_f32 %0, %3, %0\n\tv_accvgpr_write_b32 a252, %0\n\t"
                   "v_accvgpr_read_b32 %1, a253\n\tv_mul_f32 %1, %3, %1\n\tv_accvgpr_write_b32 a253, %1\n\t"
                   "v_accvgpr_read_b32 %0, a254\n\tv_mul_f32 %0, %3, %0\n\tv_accvgpr_write_b32 a254, %0\n\t"
                   "v_accvgpr_read_b32 %1, a255\n\tv_mul_f32 %1, %3, %1\n\tv_accvgpr_write_b32 a255, %1\n\t"
                   "s_nop 1"
                   : "=&v"(t0), "=&v"(t1) : "v"(c.fct[0]), "v"(c.fct[1]) : ACLOB);
      c.pend = false;
    }
  }
  asm volatile("s_nop 1" ::: "memory");
  GAP();
}
template <int T, bool HASPV, bool HASSM> DI void phaseB(Ctx4& c) {
  phaseB_<T, HASPV, HASSM>(c, std::make_integer_sequence<int, 32>{});
}

template <int A, int OFF> DI void acc_dswrite4(unsigned addr) {
  asm volatile("ds_write_b128 %0, a[%c1:%c2] offset:%c3" ::"v"(addr), "i"(A), "i"(A + 3), "i"(OFF) : "memory", ACLOB);
}
template <int A> DI f32x4 acc_read4() {
  f32x4 r;
  asm volatile("v_accvgpr_read_b32 %0, a[%c4]\n\tv_accvgpr_read_b32 %1, a[%c5]\n\tv_accvgpr_read_b32 %2, a[%c6]\n\t"
               "v_accvgpr_read_b32 %3, a[%c7]"
               : "=v"(r[0]), "=v"(r[1]), "=v"(r[2]), "=v"(r[3]) : "i"(A), "i"(A + 1), "i"(A + 2), "i"(A + 3) : ACLOB);
  return r;
}

__global__ __launch_bounds__(256, 1) void attn4_kernel(const u16* __restrict__ x, const u16* __restrict__ wq,
                                                       const u16* __restrict__ kc, const u16* __restrict__ vc,
                                                       const u16* __restrict__ wo, u16* __restrict__ o,
                                                       unsigned* __restrict__ cnt DBGARG) {
  extern __shared__ __attribute__((aligned(16))) char lds[];
  STAMP(0);
  if (blockIdx.x == 0 && threadIdx.x < 128) cnt[threadIdx.x] = 0u;
  const int tid = threadIdx.x, lane = tid & 63;
  const int w = __builtin_amdgcn_readfirstlane(tid >> 6);
  const int r = lane & 31, h = lane >> 5;
  const int bid = blockIdx.x;
  const int xcd = bid & 7, j = bid >> 3;
  const int grp = (j >> 2) * 8 + xcd, sub = j & 3;
  const int b = grp >> 3, kvh = grp & 7;
  const int head = kvh * 2 + (sub >> 1), rhalf = sub & 1;
  const int row0 = b * SQ + rhalf * 56;

  const u16* Kw = kc + ((size_t)(b * HKV + kvh) * SK + 256 * w) * DH;
  const u16* Vw = vc + ((size_t)(b * HKV + kvh) * SK + 256 * w) * DH;

  Ctx4 c;
  c.pfd = 0; c.sub = sub;
  c.Kp = Kw; c.Vp = Vw; c.pfoff = lane * 128;
  c.ring = lds + w * 40960;
  c.rk = __builtin_amdgcn_make_buffer_rsrc((void*)Kw, 0, 256 * DH * 2, 0x00020000);
  c.rv = __builtin_amdgcn_make_buffer_rsrc((void*)Vw, 0, 256 * DH * 2, 0x00020000);
#pragma unroll
  for (int pi = 0; pi < 4; ++pi) {
    const int krow = 4 * pi + (lane >> 4);
    c.koff[pi] = krow * 512 + (((lane & 15) ^ (krow & 15)) * 16);
  }
#pragma unroll
  for (int pi = 0; pi < 2; ++pi) {
    const int vrow = 2 * pi + (lane >> 5);
    c.voff[pi] = vrow * 512 + (((lane & 31) ^ ((vrow & 3) << 2)) * 16);
  }
  {
    __amdgpu_buffer_rsrc_t rx =
        __builtin_amdgcn_make_buffer_rsrc((void*)(x + (size_t)row0 * DM), 0, 56 * DM * 2, 0x00020000);
#pragma unroll
    for (int p = 0; p < 14; ++p) {
      const int ci = 64 * p + lane;
      const int lr = ci / 56, cp = ci - lr * 56;
      int gr = 16 * w + lr; gr = gr > 55 ? 55 : gr;
      const int cs = (cp & ~7) | ((cp ^ (lr >> 1)) & 7);
      __builtin_amdgcn_raw_ptr_buffer_load_lds(rx, (lds_vp)(c.ring + 3 * 8192 + p * 1024), 16, gr * (DM * 2) + cs * 16,
                                               0, 0, 0);
    }
  }
  {
    const int colw = head * DH + 64 * w;
    __amdgpu_buffer_rsrc_t rw =
        __builtin_amdgcn_make_buffer_rsrc((void*)(wq + colw), 0, (DM * NQ - colw) * 2, 0x00020000);
    const int drow = lane >> 3;
    const int wvoff = drow * (NQ * 2) + (((lane & 7) ^ (((drow >> 1) & 1) << 2)) * 16);
    auto wissue = [&](int ch) {
#pragma unroll
      for (int p = 0; p < 8; ++p)
        __builtin_amdgcn_raw_ptr_buffer_load_lds(rw, (lds_vp)(c.ring + ((ch + 2) % 3) * 8192 + p * 1024), 16, wvoff,
                                                 (64 * ch + 8 * p) * (NQ * 2), 0, 0);
    };
    wissue(0); wissue(1); wissue(2);
    const int g = lane >> 4, qq = (lane >> 2) & 3, pp = lane & 3;
    const unsigned trb = (unsigned)(size_t)c.ring + (8 * (g >> 1) + qq) * 128 + ((32 * (g & 1) + 8 * pp) ^ ((qq >> 1) << 6));
    const char* xa[2][4];
#pragma unroll
    for (int qb = 0; qb < 2; ++qb) {
      const int R = 32 * qb + r, lr = R & 15;
#pragma unroll
      for (int v = 0; v < 4; ++v)
        xa[qb][v] = lds + (R >> 4) * 40960 + 3 * 8192 + lr * 896 + ((((2 * v + h) ^ (lr >> 1)) & 7) * 16);
    }
    f32x16 qa[2][2];
#pragma unroll
    for (int a2 = 0; a2 < 2; ++a2)
#pragma unroll
      for (int b2 = 0; b2 < 2; ++b2)
#pragma unroll
        for (int i = 0; i < 16; ++i) qa[a2][b2][i] = 0.f;
    s16x4 fa[2][4];
    bf16x8 fx[2][2];
    auto rdstep = [&](int i, int bsel) {
      const int ch = i >> 2, s4 = i & 3;
      const unsigned tb = trb + ((ch + 2) % 3) * 8192 + s4 * 2048;
      fa[bsel][0] = trread(tb);
      fa[bsel][1] = trread(tb + 512);
      fa[bsel][2] = trread(tb ^ 64);
      fa[bsel][3] = trread((tb + 512) ^ 64);
      asm volatile("ds_read_b128 %0, %1" : "=v"(fx[bsel][0]) : "v"((unsigned)(size_t)(xa[0][s4] + ch * 128)) : "memory");
      asm volatile("ds_read_b128 %0, %1" : "=v"(fx[bsel][1]) : "v"((unsigned)(size_t)(xa[1][s4] + ch * 128)) : "memory");
    };
    auto refill_piece = [&](int k, int p) {
      if (k <= 3) {
        __builtin_amdgcn_raw_ptr_buffer_load_lds(rw, (lds_vp)(c.ring + ((k + 5) % 3) * 8192 + p * 1024), 16, wvoff,
                                                 (64 * (k + 3) + 8 * p) * (NQ * 2), 0, 0);
      }
    };
    WAITV(16);
    __builtin_amdgcn_s_barrier();
    STAMP(11);
    rdstep(0, 0);
#pragma unroll
    for (int i = 0; i < 28; ++i) {
      const int bs = i & 1, ch = i >> 2, s4 = i & 3;
      if (i + 1 < 28) {
        if (s4 == 3) { if (ch == 0) WAITV(8); else WAITV(6); }
        rdstep(i + 1, bs ^ 1);
        asm volatile("s_waitcnt lgkmcnt(6)" : "+v"(fa[bs][0]), "+v"(fa[bs][1]), "+v"(fa[bs][2]), "+v"(fa[bs][3]), "+v"(fx[bs][0]), "+v"(fx[bs][1])::"memory");
      } else {
        asm volatile("s_waitcnt lgkmcnt(0)" : "+v"(fa[bs][0]), "+v"(fa[bs][1]), "+v"(fa[bs][2]), "+v"(fa[bs][3]), "+v"(fx[bs][0]), "+v"(fx[bs][1])::"memory");
      }
      bf16x8 A0 = cat8(fa[bs][0], fa[bs][1]), A1 = cat8(fa[bs][2], fa[bs][3]);
      qa[0][0] = __builtin_amdgcn_mfma_f32_32x32x16_bf16(A0, fx[bs][0], qa[0][0], 0, 0, 0);
      GAP();
      if (ch == 5 && s4 == 0) { prefetch_unit(c, 6 + sub); prefetch_unit(c, 10 + sub); }
      if (ch >= 1) {
        if (ch - 1 <= 3) refill_piece(ch - 1, 2 * s4);
        else if (ch == 5) { if (s4 == 0) dma_piece<0, 0>(c); else if (s4 == 1) dma_piece<0, 2>(c); else if (s4 == 2) dma_piece<0, 4>(c); else dma_piece<0, 6>(c); }
        else { if (s4 == 0) dma_piece<1, 0>(c); else if (s4 == 1) dma_piece<1, 2>(c); else if (s4 == 2) dma_piece<1, 4>(c); else dma_piece<1, 6>(c); }
      }
      GAP();
      qa[0][1] = __builtin_amdgcn_mfma_f32_32x32x16_bf16(A1, fx[bs][0], qa[0][1], 0, 0, 0);
      qa[1][0] = __builtin_amdgcn_mfma_f32_32x32x16_bf16(A0, fx[bs][1], qa[1][0], 0, 0, 0);
      GAP();
      if (ch >= 1) {
        if (ch - 1 <= 3) refill_piece(ch - 1, 2 * s4 + 1);
        else if (ch == 5) { if (s4 == 0) dma_piece<0, 1>(c); else if (s4 == 1) dma_piece<0, 3>(c); else if (s4 == 2) dma_piece<0, 5>(c); else dma_piece<0, 7>(c); }
        else { if (s4 == 0) dma_piece<1, 1>(c); else if (s4 == 1) dma_piece<1, 3>(c); else if (s4 == 2) dma_piece<1, 5>(c); else dma_piece<1, 7>(c); }
      }
      GAP();
      qa[1][1] = __builtin_amdgcn_mfma_f32_32x32x16_bf16(A1, fx[bs][1], qa[1][1], 0, 0, 0);
    }
    dma_unit<2>(c);
    STAMP(12);
    __builtin_amdgcn_s_barrier();
#pragma unroll
    for (int qb = 0; qb < 2; ++qb) {
      const int R = 32 * qb + r;
      char* base = lds + (R >> 4) * 40960 + 4 * 8192 + (R & 15) * 512 + 8 * h;
#pragma unroll
      for (int cb = 0; cb < 2; ++cb)
#pragma unroll
        for (int gg = 0; gg < 4; ++gg) {
          u32x2 v = {cvtpk(qa[qb][cb][4 * gg], qa[qb][cb][4 * gg + 1]), cvtpk(qa[qb][cb][4 * gg + 2], qa[qb][cb][4 * gg + 3])};
          *(u32x2*)(base + (((8 * w + 4 * cb + gg) ^ (R & 15)) * 16)) = v;
        }
    }
    WAITL0();
    __builtin_amdgcn_s_barrier();
  }
  {
#pragma unroll
    for (int qb = 0; qb < 2; ++qb) {
      const int R = 32 * qb + r;
      const char* base = lds + (R >> 4) * 40960 + 4 * 8192 + (R & 15) * 512;
#pragma unroll
      for (int s = 0; s < 16; ++s) c.qf[qb][s] = *(const bf16x8*)(base + (((2 * s + h) ^ (R & 15)) * 16));
    }
#pragma unroll
    for (int qb = 0; qb < 2; ++qb)
#pragma unroll
      for (int s = 0; s < 16; ++s) asm volatile("" : "+v"(c.qf[qb][s]));
  }
  WAITL0();
  __builtin_amdgcn_s_barrier();
  dma_unit<3>(c);
  dma_unit<4>(c);
  STAMP(1);

  {
    const unsigned rb = (unsigned)(size_t)c.ring;
#pragma unroll
    for (int ss = 0; ss < 8; ++ss) c.kaddr[ss] = rb + r * 256 + ((((2 * ss + h) ^ (r & 15))) * 16);
    const int g = lane >> 4, qq = (lane >> 2) & 3, pp = lane & 3;
#pragma unroll
    for (int d = 0; d < 4; ++d)
      c.vaddr[d] = rb + (4 * (g >> 1) + qq) * 512 + ((64 * d + 32 * (g & 1) + 8 * pp) ^ (qq << 6));
  }
  c.pend = false;
  c.fct[0] = 1.f; c.fct[1] = 1.f;

  phaseA<0>(c); phaseB<0, false, true>(c); STAMP(2);
  phaseA<1>(c); phaseB<1, true, true>(c); STAMP(3);
  phaseA<2>(c); phaseB<2, true, true>(c); STAMP(4);
  phaseA<3>(c); phaseB<3, true, true>(c); STAMP(5);
  phaseA<4>(c); phaseB<4, true, true>(c); STAMP(6);
  phaseA<5>(c); phaseB<5, true, true>(c); STAMP(7);
  phaseA<6>(c); phaseB<6, true, true>(c); STAMP(8);
  {
    int li = (bid * 4 + w) * 28 + (lane < 28 ? lane : 27);
    asm volatile("global_load_dword %0, %1, off" : "+v"(c.pfd) : "v"(wo + (size_t)li * 64) : "memory");
  }
  phaseA<7>(c); phaseB<7, true, true>(c); STAMP(9);
  phaseB<8, true, false>(c);
  asm volatile("s_nop 7\n\ts_nop 7" ::: "memory");
  STAMP(13);

  __syncthreads();
  float* ml = (float*)lds;
#pragma unroll
  for (int qb = 0; qb < 2; ++qb) {
    auto rr = __builtin_amdgcn_permlane32_swap(__float_as_uint(c.l[qb]), __float_as_uint(c.l[qb]), false, false);
    const float lt = __uint_as_float(rr[0]) + __uint_as_float(rr[1]);
    if (h == 0) {
      ml[((w * 2 + qb) * 32 + r) * 2 + 0] = c.m[qb];
      ml[((w * 2 + qb) * 32 + r) * 2 + 1] = lt;
    }
  }
  __syncthreads();
  float fw[2][4];
#pragma unroll
  for (int qb = 0; qb < 2; ++qb) {
    float mm[4], ll[4];
#pragma unroll
    for (int ww = 0; ww < 4; ++ww) {
      mm[ww] = ml[((ww * 2 + qb) * 32 + r) * 2 + 0];
      ll[ww] = ml[((ww * 2 + qb) * 32 + r) * 2 + 1];
    }
    const float M = fmaxf(fmaxf(mm[0], mm[1]), fmaxf(mm[2], mm[3]));
    float e[4], L = 0.f;
#pragma unroll
    for (int ww = 0; ww < 4; ++ww) { e[ww] = __builtin_amdgcn_exp2f(mm[ww] - M); L += e[ww] * ll[ww]; }
    const float inv = 1.f / L;
#pragma unroll
    for (int ww = 0; ww < 4; ++ww) fw[qb][ww] = e[ww] * inv;
  }
  STAMP(14);
  const unsigned xw = (unsigned)(size_t)lds + 4096 + lane * 16;
  const f32x4* xr = (const f32x4*)(lds + 4096) + lane;
  char* otile = lds + 102400;
  {
    switch (w) {
      case 0: {
        acc_dswrite4<32, 0>(xw + 0);
        acc_dswrite4<36, 1024>(xw + 0);
        acc_dswrite4<40, 2048>(xw + 0);
        acc_dswrite4<44, 3072>(xw + 0);
        acc_dswrite4<160, 4096>(xw + 0);
        acc_dswrite4<164, 5120>(xw + 0);
        acc_dswrite4<168, 6144>(xw + 0);
        acc_dswrite4<172, 7168>(xw + 0);
        acc_dswrite4<64, 8192>(xw + 0);
        acc_dswrite4<68, 9216>(xw + 0);
        acc_dswrite4<72, 10240>(xw + 0);
        acc_dswrite4<76, 11264>(xw + 0);
        acc_dswrite4<192, 12288>(xw + 0);
        acc_dswrite4<196, 13312>(xw + 0);
        acc_dswrite4<200, 14336>(xw + 0);
        acc_dswrite4<204, 15360>(xw + 0);
        acc_dswrite4<96, 16384>(xw + 0);
        acc_dswrite4<100, 17408>(xw + 0);
        acc_dswrite4<104, 18432>(xw + 0);
        acc_dswrite4<108, 19456>(xw + 0);
        acc_dswrite4<224, 20480>(xw + 0);
        acc_dswrite4<228, 21504>(xw + 0);
        acc_dswrite4<232, 22528>(xw + 0);
        acc_dswrite4<236, 23552>(xw + 0);
      } break;
      case 1: {
        acc_dswrite4<0, 24576>(xw + 0);
        acc_dswrite4<4, 25600>(xw + 0);
        acc_dswrite4<8, 26624>(xw + 0);
        acc_dswrite4<12, 27648>(xw + 0);
        acc_dswrite4<128, 28672>(xw + 0);
        acc_dswrite4<132, 29696>(xw + 0);
        acc_dswrite4<136, 30720>(xw + 0);
        acc_dswrite4<140, 31744>(xw + 0);
        acc_dswrite4<64, 32768>(xw + 0);
        acc_dswrite4<68, 33792>(xw + 0);
        acc_dswrite4<72, 34816>(xw + 0);
        acc_dswrite4<76, 35840>(xw + 0);
        acc_dswrite4<192, 36864>(xw + 0);
        acc_dswrite4<196, 37888>(xw + 0);
        acc_dswrite4<200, 38912>(xw + 0);
        acc_dswrite4<204, 39936>(xw + 0);
        acc_dswrite4<96, 40960>(xw + 0);
        acc_dswrite4<100, 41984>(xw + 0);
        acc_dswrite4<104, 43008>(xw + 0);
        acc_dswrite4<108, 44032>(xw + 0);
        acc_dswrite4<224, 45056>(xw + 0);
        acc_dswrite4<228, 46080>(xw + 0);
        acc_dswrite4<232, 47104>(xw + 0);
        acc_dswrite4<236, 48128>(xw + 0);
      } break;
      case 2: {
        acc_dswrite4<0, 49152>(xw + 0);
        acc_dswrite4<4, 50176>(xw + 0);
        acc_dswrite4<8, 51200>(xw + 0);
        acc_dswrite4<12, 52224>(xw + 0);
        acc_dswrite4<128, 53248>(xw + 0);
        acc_dswrite4<132, 54272>(xw + 0);
        acc_dswrite4<136, 55296>(xw + 0);
        acc_dswrite4<140, 56320>(xw + 0);
        acc_dswrite4<32, 57344>(xw + 0);
        acc_dswrite4<36, 58368>(xw + 0);
        acc_dswrite4<40, 59392>(xw + 0);
        acc_dswrite4<44, 60416>(xw + 0);
        acc_dswrite4<160, 61440>(xw + 0);
        acc_dswrite4<164, 62464>(xw + 0);
        acc_dswrite4<168, 63488>(xw + 0);
        acc_dswrite4<172, 64512>(xw + 0);
        acc_dswrite4<96, 0>(xw + 65536);
        acc_dswrite4<100, 1024>(xw + 65536);
        acc_dswrite4<104, 2048>(xw + 65536);
        acc_dswrite4<108, 3072>(xw + 65536);
        acc_dswrite4<224, 4096>(xw + 65536);
        acc_dswrite4<228, 5120>(xw + 65536);
        acc_dswrite4<232, 6144>(xw + 65536);
        acc_dswrite4<236, 7168>(xw + 65536);
      } break;
      case 3: {
        acc_dswrite4<0, 8192>(xw + 65536);
        acc_dswrite4<4, 9216>(xw + 65536);
        acc_dswrite4<8, 10240>(xw + 65536);
        acc_dswrite4<12, 11264>(xw + 65536);
        acc_dswrite4<128, 12288>(xw + 65536);
        acc_dswrite4<132, 13312>(xw + 65536);
        acc_dswrite4<136, 14336>(xw + 65536);
        acc_dswrite4<140, 15360>(xw + 65536);
        acc_dswrite4<32, 16384>(xw + 65536);
        acc_dswrite4<36, 17408>(xw + 65536);
        acc_dswrite4<40, 18432>(xw + 65536);
        acc_dswrite4<44, 19456>(xw + 65536);
        acc_dswrite4<160, 20480>(xw + 65536);
        acc_dswrite4<164, 21504>(xw + 65536);
        acc_dswrite4<168, 22528>(xw + 65536);
        acc_dswrite4<172, 23552>(xw + 65536);
        acc_dswrite4<64, 24576>(xw + 65536);
        acc_dswrite4<68, 25600>(xw + 65536);
        acc_dswrite4<72, 26624>(xw + 65536);
        acc_dswrite4<76, 27648>(xw + 65536);
        acc_dswrite4<192, 28672>(xw + 65536);
        acc_dswrite4<196, 29696>(xw + 65536);
        acc_dswrite4<200, 30720>(xw + 65536);
        acc_dswrite4<204, 31744>(xw + 65536);
      } break;
    }
    WAITL0();
    __builtin_amdgcn_s_barrier();
    switch (w) {
      case 0: {
        { f32x4 sum = acc_read4<0>(); sum *= fw[0][0];
          sum += xr[1536] * fw[0][1];
          sum += xr[3072] * fw[0][2];
          sum += xr[4608] * fw[0][3];
          u32x2 v = {cvtpk(sum[0], sum[1]), cvtpk(sum[2], sum[3])};
          *(u32x2*)(otile + (0 + r) * 512 + (((0) ^ ((0 + r) & 31)) * 16) + 8 * h) = v; }
        { f32x4 sum = acc_read4<4>(); sum *= fw[0][0];
          sum += xr[1600] * fw[0][1];
          sum += xr[3136] * fw[0][2];
          sum += xr[4672] * fw[0][3];
          u32x2 v = {cvtpk(sum[0], sum[1]), cvtpk(sum[2], sum[3])};
          *(u32x2*)(otile + (0 + r) * 512 + (((1) ^ ((0 + r) & 31)) * 16) + 8 * h) = v; }
        { f32x4 sum = acc_read4<8>(); sum *= fw[0][0];
          sum += xr[1664] * fw[0][1];
          sum += xr[3200] * fw[0][2];
          sum += xr[4736] * fw[0][3];
          u32x2 v = {cvtpk(sum[0], sum[1]), cvtpk(sum[2], sum[3])};
          *(u32x2*)(otile + (0 + r) * 512 + (((2) ^ ((0 + r) & 31)) * 16) + 8 * h) = v; }
        { f32x4 sum = acc_read4<12>(); sum *= fw[0][0];
          sum += xr[1728] * fw[0][1];
          sum += xr[3264] * fw[0][2];
          sum += xr[4800] * fw[0][3];
          u32x2 v = {cvtpk(sum[0], sum[1]), cvtpk(sum[2], sum[3])};
          *(u32x2*)(otile + (0 + r) * 512 + (((3) ^ ((0 + r) & 31)) * 16) + 8 * h) = v; }
        { f32x4 sum = acc_read4<128>(); sum *= fw[1][0];
          sum += xr[1792] * fw[1][1];
          sum += xr[3328] * fw[1][2];
          sum += xr[4864] * fw[1][3];
          u32x2 v = {cvtpk(sum[0], sum[1]), cvtpk(sum[2], sum[3])};
          *(u32x2*)(otile + (32 + r) * 512 + (((0) ^ ((32 + r) & 31)) * 16) + 8 * h) = v; }
        { f32x4 sum = acc_read4<132>(); sum *= fw[1][0];
          sum += xr[1856] * fw[1][1];
          sum += xr[3392] * fw[1][2];
          sum += xr[4928] * fw[1][3];
          u32x2 v = {cvtpk(sum[0], sum[1]), cvtpk(sum[2], sum[3])};
          *(u32x2*)(otile + (32 + r) * 512 + (((1) ^ ((32 + r) & 31)) * 16) + 8 * h) = v; }
        { f32x4 sum = acc_read4<136>(); sum *= fw[1][0];
          sum += xr[1920] * fw[1][1];
          sum += xr[3456] * fw[1][2];
          sum += xr[4992] * fw[1][3];
          u32x2 v = {cvtpk(sum[0], sum[1]), cvtpk(sum[2], sum[3])};
          *(u32x2*)(otile + (32 + r) * 512 + (((2) ^ ((32 + r) & 31)) * 16) + 8 * h) = v; }
        { f32x4 sum = acc_read4<140>(); sum *= fw[1][0];
          sum += xr[1984] * fw[1][1];
          sum += xr[3520] * fw[1][2];
          sum += xr[5056] * fw[1][3];
          u32x2 v = {cvtpk(sum[0], sum[1]), cvtpk(sum[2], sum[3])};
          *(u32x2*)(otile + (32 + r) * 512 + (((3) ^ ((32 + r) & 31)) * 16) + 8 * h) = v; }
      } break;
      case 1: {
        { f32x4 sum = acc_read4<32>(); sum *= fw[0][1];
          sum += xr[0] * fw[0][0];
          sum += xr[3584] * fw[0][2];
          sum += xr[5120] * fw[0][3];
          u32x2 v = {cvtpk(sum[0], sum[1]), cvtpk(sum[2], sum[3])};
          *(u32x2*)(otile + (0 + r) * 512 + (((8) ^ ((0 + r) & 31)) * 16) + 8 * h) = v; }
        { f32x4 sum = acc_read4<36>(); sum *= fw[0][1];
          sum += xr[64] * fw[0][0];
          sum += xr[3648] * fw[0][2];
          sum += xr[5184] * fw[0][3];
          u32x2 v = {cvtpk(sum[0], sum[1]), cvtpk(sum[2], sum[3])};
          *(u32x2*)(otile + (0 + r) * 512 + (((9) ^ ((0 + r) & 31)) * 16) + 8 * h) = v; }
        { f32x4 sum = acc_read4<40>(); sum *= fw[0][1];
          sum += xr[128] * fw[0][0];
          sum += xr[3712] * fw[0][2];
          sum += xr[5248] * fw[0][3];
          u32x2 v = {cvtpk(sum[0], sum[1]), cvtpk(sum[2], sum[3])};
          *(u32x2*)(otile + (0 + r) * 512 + (((10) ^ ((0 + r) & 31)) * 16) + 8 * h) = v; }
        { f32x4 sum = acc_read4<44>(); sum *= fw[0][1];
          sum += xr[192] * fw[0][0];
          sum += xr[3776] * fw[0][2];
          sum += xr[5312] * fw[0][3];
          u32x2 v = {cvtpk(sum[0], sum[1]), cvtpk(sum[2], sum[3])};
          *(u32x2*)(otile + (0 + r) * 512 + (((11) ^ ((0 + r) & 31)) * 16) + 8 * h) = v; }
        { f32x4 sum = acc_read4<160>(); sum *= fw[1][1];
          sum += xr[256] * fw[1][0];
          sum += xr[3840] * fw[1][2];
          sum += xr[5376] * fw[1][3];
          u32x2 v = {cvtpk(sum[0], sum[1]), cvtpk(sum[2], sum[3])};
          *(u32x2*)(otile + (32 + r) * 512 + (((8) ^ ((32 + r) & 31)) * 16) + 8 * h) = v; }
        { f32x4 sum = acc_read4<164>(); sum *= fw[1][1];
          sum += xr[320] * fw[1][0];
          sum += xr[3904] * fw[1][2];
          sum += xr[5440] * fw[1][3];
          u32x2 v = {cvtpk(sum[0], sum[1]), cvtpk(sum[2], sum[3])};
          *(u32x2*)(otile + (32 + r) * 512 + (((9) ^ ((32 + r) & 31)) * 16) + 8 * h) = v; }
        { f32x4 sum = acc_read4<168>(); sum *= fw[1][1];
          sum += xr[384] * fw[1][0];
          sum += xr[3968] * fw[1][2];
          sum += xr[5504] * fw[1][3];
          u32x2 v = {cvtpk(sum[0], sum[1]), cvtpk(sum[2], sum[3])};
          *(u32x2*)(otile + (32 + r) * 512 + (((10) ^ ((32 + r) & 31)) * 16) + 8 * h) = v; }
        { f32x4 sum = acc_read4<172>(); sum *= fw[1][1];
          sum += xr[448] * fw[1][0];
          sum += xr[4032] * fw[1][2];
          sum += xr[5568] * fw[1][3];
          u32x2 v = {cvtpk(sum[0], sum[1]), cvtpk(sum[2], sum[3])};
          *(u32x2*)(otile + (32 + r) * 512 + (((11) ^ ((32 + r) & 31)) * 16) + 8 * h) = v; }
      } break;
      case 2: {
        { f32x4 sum = acc_read4<64>(); sum *= fw[0][2];
          sum += xr[512] * fw[0][0];
          sum += xr[2048] * fw[0][1];
          sum += xr[5632] * fw[0][3];
          u32x2 v = {cvtpk(sum[0], sum[1]), cvtpk(sum[2], sum[3])};
          *(u32x2*)(otile + (0 + r) * 512 + (((16) ^ ((0 + r) & 31)) * 16) + 8 * h) = v; }
        { f32x4 sum = acc_read4<68>(); sum *= fw[0][2];
          sum += xr[576] * fw[0][0];
          sum += xr[2112] * fw[0][1];
          sum += xr[5696] * fw[0][3];
          u32x2 v = {cvtpk(sum[0], sum[1]), cvtpk(sum[2], sum[3])};
          *(u32x2*)(otile + (0 + r) * 512 + (((17) ^ ((0 + r) & 31)) * 16) + 8 * h) = v; }
        { f32x4 sum = acc_read4<72>(); sum *= fw[0][2];
          sum += xr[640] * fw[0][0];
          sum += xr[2176] * fw[0][1];
          sum += xr[5760] * fw[0][3];
          u32x2 v = {cvtpk(sum[0], sum[1]), cvtpk(sum[2], sum[3])};
          *(u32x2*)(otile + (0 + r) * 512 + (((18) ^ ((0 + r) & 31)) * 16) + 8 * h) = v; }
        { f32x4 sum = acc_read4<76>(); sum *= fw[0][2];
          sum += xr[704] * fw[0][0];
          sum += xr[2240] * fw[0][1];
          sum += xr[5824] * fw[0][3];
          u32x2 v = {cvtpk(sum[0], sum[1]), cvtpk(sum[2], sum[3])};
          *(u32x2*)(otile + (0 + r) * 512 + (((19) ^ ((0 + r) & 31)) * 16) + 8 * h) = v; }
        { f32x4 sum = acc_read4<192>(); sum *= fw[1][2];
          sum += xr[768] * fw[1][0];
          sum += xr[2304] * fw[1][1];
          sum += xr[5888] * fw[1][3];
          u32x2 v = {cvtpk(sum[0], sum[1]), cvtpk(sum[2], sum[3])};
          *(u32x2*)(otile + (32 + r) * 512 + (((16) ^ ((32 + r) & 31)) * 16) + 8 * h) = v; }
        { f32x4 sum = acc_read4<196>(); sum *= fw[1][2];
          sum += xr[832] * fw[1][0];
          sum += xr[2368] * fw[1][1];
          sum += xr[5952] * fw[1][3];
          u32x2 v = {cvtpk(sum[0], sum[1]), cvtpk(sum[2], sum[3])};
          *(u32x2*)(otile + (32 + r) * 512 + (((17) ^ ((32 + r) & 31)) * 16) + 8 * h) = v; }
        { f32x4 sum = acc_read4<200>(); sum *= fw[1][2];
          sum += xr[896] * fw[1][0];
          sum += xr[2432] * fw[1][1];
          sum += xr[6016] * fw[1][3];
          u32x2 v = {cvtpk(sum[0], sum[1]), cvtpk(sum[2], sum[3])};
          *(u32x2*)(otile + (32 + r) * 512 + (((18) ^ ((32 + r) & 31)) * 16) + 8 * h) = v; }
        { f32x4 sum = acc_read4<204>(); sum *= fw[1][2];
          sum += xr[960] * fw[1][0];
          sum += xr[2496] * fw[1][1];
          sum += xr[6080] * fw[1][3];
          u32x2 v = {cvtpk(sum[0], sum[1]), cvtpk(sum[2], sum[3])};
          *(u32x2*)(otile + (32 + r) * 512 + (((19) ^ ((32 + r) & 31)) * 16) + 8 * h) = v; }
      } break;
      case 3: {
        { f32x4 sum = acc_read4<96>(); sum *= fw[0][3];
          sum += xr[1024] * fw[0][0];
          sum += xr[2560] * fw[0][1];
          sum += xr[4096] * fw[0][2];
          u32x2 v = {cvtpk(sum[0], sum[1]), cvtpk(sum[2], sum[3])};
          *(u32x2*)(otile + (0 + r) * 512 + (((24) ^ ((0 + r) & 31)) * 16) + 8 * h) = v; }
        { f32x4 sum = acc_read4<100>(); sum *= fw[0][3];
          sum += xr[1088] * fw[0][0];
          sum += xr[2624] * fw[0][1];
          sum += xr[4160] * fw[0][2];
          u32x2 v = {cvtpk(sum[0], sum[1]), cvtpk(sum[2], sum[3])};
          *(u32x2*)(otile + (0 + r) * 512 + (((25) ^ ((0 + r) & 31)) * 16) + 8 * h) = v; }
        { f32x4 sum = acc_read4<104>(); sum *= fw[0][3];
          sum += xr[1152] * fw[0][0];
          sum += xr[2688] * fw[0][1];
          sum += xr[4224] * fw[0][2];
          u32x2 v = {cvtpk(sum[0], sum[1]), cvtpk(sum[2], sum[3])};
          *(u32x2*)(otile + (0 + r) * 512 + (((26) ^ ((0 + r) & 31)) * 16) + 8 * h) = v; }
        { f32x4 sum = acc_read4<108>(); sum *= fw[0][3];
          sum += xr[1216] * fw[0][0];
          sum += xr[2752] * fw[0][1];
          sum += xr[4288] * fw[0][2];
          u32x2 v = {cvtpk(sum[0], sum[1]), cvtpk(sum[2], sum[3])};
          *(u32x2*)(otile + (0 + r) * 512 + (((27) ^ ((0 + r) & 31)) * 16) + 8 * h) = v; }
        { f32x4 sum = acc_read4<224>(); sum *= fw[1][3];
          sum += xr[1280] * fw[1][0];
          sum += xr[2816] * fw[1][1];
          sum += xr[4352] * fw[1][2];
          u32x2 v = {cvtpk(sum[0], sum[1]), cvtpk(sum[2], sum[3])};
          *(u32x2*)(otile + (32 + r) * 512 + (((24) ^ ((32 + r) & 31)) * 16) + 8 * h) = v; }
        { f32x4 sum = acc_read4<228>(); sum *= fw[1][3];
          sum += xr[1344] * fw[1][0];
          sum += xr[2880] * fw[1][1];
          sum += xr[4416] * fw[1][2];
          u32x2 v = {cvtpk(sum[0], sum[1]), cvtpk(sum[2], sum[3])};
          *(u32x2*)(otile + (32 + r) * 512 + (((25) ^ ((32 + r) & 31)) * 16) + 8 * h) = v; }
        { f32x4 sum = acc_read4<232>(); sum *= fw[1][3];
          sum += xr[1408] * fw[1][0];
          sum += xr[2944] * fw[1][1];
          sum += xr[4480] * fw[1][2];
          u32x2 v = {cvtpk(sum[0], sum[1]), cvtpk(sum[2], sum[3])};
          *(u32x2*)(otile + (32 + r) * 512 + (((26) ^ ((32 + r) & 31)) * 16) + 8 * h) = v; }
        { f32x4 sum = acc_read4<236>(); sum *= fw[1][3];
          sum += xr[1472] * fw[1][0];
          sum += xr[3008] * fw[1][1];
          sum += xr[4544] * fw[1][2];
          u32x2 v = {cvtpk(sum[0], sum[1]), cvtpk(sum[2], sum[3])};
          *(u32x2*)(otile + (32 + r) * 512 + (((27) ^ ((32 + r) & 31)) * 16) + 8 * h) = v; }
      } break;
    }
    WAITL0();
    __builtin_amdgcn_s_barrier();
  }
  {
    switch (w) {
      case 0: {
        acc_dswrite4<48, 0>(xw + 0);
        acc_dswrite4<52, 1024>(xw + 0);
        acc_dswrite4<56, 2048>(xw + 0);
        acc_dswrite4<60, 3072>(xw + 0);
        acc_dswrite4<176, 4096>(xw + 0);
        acc_dswrite4<180, 5120>(xw + 0);
        acc_dswrite4<184, 6144>(xw + 0);
        acc_dswrite4<188, 7168>(xw + 0);
        acc_dswrite4<80, 8192>(xw + 0);
        acc_dswrite4<84, 9216>(xw + 0);
        acc_dswrite4<88, 10240>(xw + 0);
        acc_dswrite4<92, 11264>(xw + 0);
        acc_dswrite4<208, 12288>(xw + 0);
        acc_dswrite4<212, 13312>(xw + 0);
        acc_dswrite4<216, 14336>(xw + 0);
        acc_dswrite4<220, 15360>(xw + 0);
        acc_dswrite4<112, 16384>(xw + 0);
        acc_dswrite4<116, 17408>(xw + 0);
        acc_dswrite4<120, 18432>(xw + 0);
        acc_dswrite4<124, 19456>(xw + 0);
        acc_dswrite4<240, 20480>(xw + 0);
        acc_dswrite4<244, 21504>(xw + 0);
        acc_dswrite4<248, 22528>(xw + 0);
        acc_dswrite4<252, 23552>(xw + 0);
      } break;
      case 1: {
        acc_dswrite4<16, 24576>(xw + 0);
        acc_dswrite4<20, 25600>(xw + 0);
        acc_dswrite4<24, 26624>(xw + 0);
        acc_dswrite4<28, 27648>(xw + 0);
        acc_dswrite4<144, 28672>(xw + 0);
        acc_dswrite4<148, 29696>(xw + 0);
        acc_dswrite4<152, 30720>(xw + 0);
        acc_dswrite4<156, 31744>(xw + 0);
        acc_dswrite4<80, 32768>(xw + 0);
        acc_dswrite4<84, 33792>(xw + 0);
        acc_dswrite4<88, 34816>(xw + 0);
        acc_dswrite4<92, 35840>(xw + 0);
        acc_dswrite4<208, 36864>(xw + 0);
        acc_dswrite4<212, 37888>(xw + 0);
        acc_dswrite4<216, 38912>(xw + 0);
        acc_dswrite4<220, 39936>(xw + 0);
        acc_dswrite4<112, 40960>(xw + 0);
        acc_dswrite4<116, 41984>(xw + 0);
        acc_dswrite4<120, 43008>(xw + 0);
        acc_dswrite4<124, 44032>(xw + 0);
        acc_dswrite4<240, 45056>(xw + 0);
        acc_dswrite4<244, 46080>(xw + 0);
        acc_dswrite4<248, 47104>(xw + 0);
        acc_dswrite4<252, 48128>(xw + 0);
      } break;
      case 2: {
        acc_dswrite4<16, 49152>(xw + 0);
        acc_dswrite4<20, 50176>(xw + 0);
        acc_dswrite4<24, 51200>(xw + 0);
        acc_dswrite4<28, 52224>(xw + 0);
        acc_dswrite4<144, 53248>(xw + 0);
        acc_dswrite4<148, 54272>(xw + 0);
        acc_dswrite4<152, 55296>(xw + 0);
        acc_dswrite4<156, 56320>(xw + 0);
        acc_dswrite4<48, 57344>(xw + 0);
        acc_dswrite4<52, 58368>(xw + 0);
        acc_dswrite4<56, 59392>(xw + 0);
        acc_dswrite4<60, 60416>(xw + 0);
        acc_dswrite4<176, 61440>(xw + 0);
        acc_dswrite4<180, 62464>(xw + 0);
        acc_dswrite4<184, 63488>(xw + 0);
        acc_dswrite4<188, 64512>(xw + 0);
        acc_dswrite4<112, 0>(xw + 65536);
        acc_dswrite4<116, 1024>(xw + 65536);
        acc_dswrite4<120, 2048>(xw + 65536);
        acc_dswrite4<124, 3072>(xw + 65536);
        acc_dswrite4<240, 4096>(xw + 65536);
        acc_dswrite4<244, 5120>(xw + 65536);
        acc_dswrite4<248, 6144>(xw + 65536);
        acc_dswrite4<252, 7168>(xw + 65536);
      } break;
      case 3: {
        acc_dswrite4<16, 8192>(xw + 65536);
        acc_dswrite4<20, 9216>(xw + 65536);
        acc_dswrite4<24, 10240>(xw + 65536);
        acc_dswrite4<28, 11264>(xw + 65536);
        acc_dswrite4<144, 12288>(xw + 65536);
        acc_dswrite4<148, 13312>(xw + 65536);
        acc_dswrite4<152, 14336>(xw + 65536);
        acc_dswrite4<156, 15360>(xw + 65536);
        acc_dswrite4<48, 16384>(xw + 65536);
        acc_dswrite4<52, 17408>(xw + 65536);
        acc_dswrite4<56, 18432>(xw + 65536);
        acc_dswrite4<60, 19456>(xw + 65536);
        acc_dswrite4<176, 20480>(xw + 65536);
        acc_dswrite4<180, 21504>(xw + 65536);
        acc_dswrite4<184, 22528>(xw + 65536);
        acc_dswrite4<188, 23552>(xw + 65536);
        acc_dswrite4<80, 24576>(xw + 65536);
        acc_dswrite4<84, 25600>(xw + 65536);
        acc_dswrite4<88, 26624>(xw + 65536);
        acc_dswrite4<92, 27648>(xw + 65536);
        acc_dswrite4<208, 28672>(xw + 65536);
        acc_dswrite4<212, 29696>(xw + 65536);
        acc_dswrite4<216, 30720>(xw + 65536);
        acc_dswrite4<220, 31744>(xw + 65536);
      } break;
    }
    WAITL0();
    __builtin_amdgcn_s_barrier();
    switch (w) {
      case 0: {
        { f32x4 sum = acc_read4<16>(); sum *= fw[0][0];
          sum += xr[1536] * fw[0][1];
          sum += xr[3072] * fw[0][2];
          sum += xr[4608] * fw[0][3];
          u32x2 v = {cvtpk(sum[0], sum[1]), cvtpk(sum[2], sum[3])};
          *(u32x2*)(otile + (0 + r) * 512 + (((4) ^ ((0 + r) & 31)) * 16) + 8 * h) = v; }
        { f32x4 sum = acc_read4<20>(); sum *= fw[0][0];
          sum += xr[1600] * fw[0][1];
          sum += xr[3136] * fw[0][2];
          sum += xr[4672] * fw[0][3];
          u32x2 v = {cvtpk(sum[0], sum[1]), cvtpk(sum[2], sum[3])};
          *(u32x2*)(otile + (0 + r) * 512 + (((5) ^ ((0 + r) & 31)) * 16) + 8 * h) = v; }
        { f32x4 sum = acc_read4<24>(); sum *= fw[0][0];
          sum += xr[1664] * fw[0][1];
          sum += xr[3200] * fw[0][2];
          sum += xr[4736] * fw[0][3];
          u32x2 v = {cvtpk(sum[0], sum[1]), cvtpk(sum[2], sum[3])};
          *(u32x2*)(otile + (0 + r) * 512 + (((6) ^ ((0 + r) & 31)) * 16) + 8 * h) = v; }
        { f32x4 sum = acc_read4<28>(); sum *= fw[0][0];
          sum += xr[1728] * fw[0][1];
          sum += xr[3264] * fw[0][2];
          sum += xr[4800] * fw[0][3];
          u32x2 v = {cvtpk(sum[0], sum[1]), cvtpk(sum[2], sum[3])};
          *(u32x2*)(otile + (0 + r) * 512 + (((7) ^ ((0 + r) & 31)) * 16) + 8 * h) = v; }
        { f32x4 sum = acc_read4<144>(); sum *= fw[1][0];
          sum += xr[1792] * fw[1][1];
          sum += xr[3328] * fw[1][2];
          sum += xr[4864] * fw[1][3];
          u32x2 v = {cvtpk(sum[0], sum[1]), cvtpk(sum[2], sum[3])};
          *(u32x2*)(otile + (32 + r) * 512 + (((4) ^ ((32 + r) & 31)) * 16) + 8 * h) = v; }
        { f32x4 sum = acc_read4<148>(); sum *= fw[1][0];
          sum += xr[1856] * fw[1][1];
          sum += xr[3392] * fw[1][2];
          sum += xr[4928] * fw[1][3];
          u32x2 v = {cvtpk(sum[0], sum[1]), cvtpk(sum[2], sum[3])};
          *(u32x2*)(otile + (32 + r) * 512 + (((5) ^ ((32 + r) & 31)) * 16) + 8 * h) = v; }
        { f32x4 sum = acc_read4<152>(); sum *= fw[1][0];
          sum += xr[1920] * fw[1][1];
          sum += xr[3456] * fw[1][2];
          sum += xr[4992] * fw[1][3];
          u32x2 v = {cvtpk(sum[0], sum[1]), cvtpk(sum[2], sum[3])};
          *(u32x2*)(otile + (32 + r) * 512 + (((6) ^ ((32 + r) & 31)) * 16) + 8 * h) = v; }
        { f32x4 sum = acc_read4<156>(); sum *= fw[1][0];
          sum += xr[1984] * fw[1][1];
          sum += xr[3520] * fw[1][2];
          sum += xr[5056] * fw[1][3];
          u32x2 v = {cvtpk(sum[0], sum[1]), cvtpk(sum[2], sum[3])};
          *(u32x2*)(otile + (32 + r) * 512 + (((7) ^ ((32 + r) & 31)) * 16) + 8 * h) = v; }
      } break;
      case 1: {
        { f32x4 sum = acc_read4<48>(); sum *= fw[0][1];
          sum += xr[0] * fw[0][0];
          sum += xr[3584] * fw[0][2];
          sum += xr[5120] * fw[0][3];
          u32x2 v = {cvtpk(sum[0], sum[1]), cvtpk(sum[2], sum[3])};
          *(u32x2*)(otile + (0 + r) * 512 + (((12) ^ ((0 + r) & 31)) * 16) + 8 * h) = v; }
        { f32x4 sum = acc_read4<52>(); sum *= fw[0][1];
          sum += xr[64] * fw[0][0];
          sum += xr[3648] * fw[0][2];
          sum += xr[5184] * fw[0][3];
          u32x2 v = {cvtpk(sum[0], sum[1]), cvtpk(sum[2], sum[3])};
          *(u32x2*)(otile + (0 + r) * 512 + (((13) ^ ((0 + r) & 31)) * 16) + 8 * h) = v; }
        { f32x4 sum = acc_read4<56>(); sum *= fw[0][1];
          sum += xr[128] * fw[0][0];
          sum += xr[3712] * fw[0][2];
          sum += xr[5248] * fw[0][3];
          u32x2 v = {cvtpk(sum[0], sum[1]), cvtpk(sum[2], sum[3])};
          *(u32x2*)(otile + (0 + r) * 512 + (((14) ^ ((0 + r) & 31)) * 16) + 8 * h) = v; }
        { f32x4 sum = acc_read4<60>(); sum *= fw[0][1];
          sum += xr[192] * fw[0][0];
          sum += xr[3776] * fw[0][2];
          sum += xr[5312] * fw[0][3];
          u32x2 v = {cvtpk(sum[0], sum[1]), cvtpk(sum[2], sum[3])};
          *(u32x2*)(otile + (0 + r) * 512 + (((15) ^ ((0 + r) & 31)) * 16) + 8 * h) = v; }
        { f32x4 sum = acc_read4<176>(); sum *= fw[1][1];
          sum += xr[256] * fw[1][0];
          sum += xr[3840] * fw[1][2];
          sum += xr[5376] * fw[1][3];
          u32x2 v = {cvtpk(sum[0], sum[1]), cvtpk(sum[2], sum[3])};
          *(u32x2*)(otile + (32 + r) * 512 + (((12) ^ ((32 + r) & 31)) * 16) + 8 * h) = v; }
        { f32x4 sum = acc_read4<180>(); sum *= fw[1][1];
          sum += xr[320] * fw[1][0];
          sum += xr[3904] * fw[1][2];
          sum += xr[5440] * fw[1][3];
          u32x2 v = {cvtpk(sum[0], sum[1]), cvtpk(sum[2], sum[3])};
          *(u32x2*)(otile + (32 + r) * 512 + (((13) ^ ((32 + r) & 31)) * 16) + 8 * h) = v; }
        { f32x4 sum = acc_read4<184>(); sum *= fw[1][1];
          sum += xr[384] * fw[1][0];
          sum += xr[3968] * fw[1][2];
          sum += xr[5504] * fw[1][3];
          u32x2 v = {cvtpk(sum[0], sum[1]), cvtpk(sum[2], sum[3])};
          *(u32x2*)(otile + (32 + r) * 512 + (((14) ^ ((32 + r) & 31)) * 16) + 8 * h) = v; }
        { f32x4 sum = acc_read4<188>(); sum *= fw[1][1];
          sum += xr[448] * fw[1][0];
          sum += xr[4032] * fw[1][2];
          sum += xr[5568] * fw[1][3];
          u32x2 v = {cvtpk(sum[0], sum[1]), cvtpk(sum[2], sum[3])};
          *(u32x2*)(otile + (32 + r) * 512 + (((15) ^ ((32 + r) & 31)) * 16) + 8 * h) = v; }
      } break;
      case 2: {
        { f32x4 sum = acc_read4<80>(); sum *= fw[0][2];
          sum += xr[512] * fw[0][0];
          sum += xr[2048] * fw[0][1];
          sum += xr[5632] * fw[0][3];
          u32x2 v = {cvtpk(sum[0], sum[1]), cvtpk(sum[2], sum[3])};
          *(u32x2*)(otile + (0 + r) * 512 + (((20) ^ ((0 + r) & 31)) * 16) + 8 * h) = v; }
        { f32x4 sum = acc_read4<84>(); sum *= fw[0][2];
          sum += xr[576] * fw[0][0];
          sum += xr[2112] * fw[0][1];
          sum += xr[5696] * fw[0][3];
          u32x2 v = {cvtpk(sum[0], sum[1]), cvtpk(sum[2], sum[3])};
          *(u32x2*)(otile + (0 + r) * 512 + (((21) ^ ((0 + r) & 31)) * 16) + 8 * h) = v; }
        { f32x4 sum = acc_read4<88>(); sum *= fw[0][2];
          sum += xr[640] * fw[0][0];
          sum += xr[2176] * fw[0][1];
          sum += xr[5760] * fw[0][3];
          u32x2 v = {cvtpk(sum[0], sum[1]), cvtpk(sum[2], sum[3])};
          *(u32x2*)(otile + (0 + r) * 512 + (((22) ^ ((0 + r) & 31)) * 16) + 8 * h) = v; }
        { f32x4 sum = acc_read4<92>(); sum *= fw[0][2];
          sum += xr[704] * fw[0][0];
          sum += xr[2240] * fw[0][1];
          sum += xr[5824] * fw[0][3];
          u32x2 v = {cvtpk(sum[0], sum[1]), cvtpk(sum[2], sum[3])};
          *(u32x2*)(otile + (0 + r) * 512 + (((23) ^ ((0 + r) & 31)) * 16) + 8 * h) = v; }
        { f32x4 sum = acc_read4<208>(); sum *= fw[1][2];
          sum += xr[768] * fw[1][0];
          sum += xr[2304] * fw[1][1];
          sum += xr[5888] * fw[1][3];
          u32x2 v = {cvtpk(sum[0], sum[1]), cvtpk(sum[2], sum[3])};
          *(u32x2*)(otile + (32 + r) * 512 + (((20) ^ ((32 + r) & 31)) * 16) + 8 * h) = v; }
        { f32x4 sum = acc_read4<212>(); sum *= fw[1][2];
          sum += xr[832] * fw[1][0];
          sum += xr[2368] * fw[1][1];
          sum += xr[5952] * fw[1][3];
          u32x2 v = {cvtpk(sum[0], sum[1]), cvtpk(sum[2], sum[3])};
          *(u32x2*)(otile + (32 + r) * 512 + (((21) ^ ((32 + r) & 31)) * 16) + 8 * h) = v; }
        { f32x4 sum = acc_read4<216>(); sum *= fw[1][2];
          sum += xr[896] * fw[1][0];
          sum += xr[2432] * fw[1][1];
          sum += xr[6016] * fw[1][3];
          u32x2 v = {cvtpk(sum[0], sum[1]), cvtpk(sum[2], sum[3])};
          *(u32x2*)(otile + (32 + r) * 512 + (((22) ^ ((32 + r) & 31)) * 16) + 8 * h) = v; }
        { f32x4 sum = acc_read4<220>(); sum *= fw[1][2];
          sum += xr[960] * fw[1][0];
          sum += xr[2496] * fw[1][1];
          sum += xr[6080] * fw[1][3];
          u32x2 v = {cvtpk(sum[0], sum[1]), cvtpk(sum[2], sum[3])};
          *(u32x2*)(otile + (32 + r) * 512 + (((23) ^ ((32 + r) & 31)) * 16) + 8 * h) = v; }
      } break;
      case 3: {
        { f32x4 sum = acc_read4<112>(); sum *= fw[0][3];
          sum += xr[1024] * fw[0][0];
          sum += xr[2560] * fw[0][1];
          sum += xr[4096] * fw[0][2];
          u32x2 v = {cvtpk(sum[0], sum[1]), cvtpk(sum[2], sum[3])};
          *(u32x2*)(otile + (0 + r) * 512 + (((28) ^ ((0 + r) & 31)) * 16) + 8 * h) = v; }
        { f32x4 sum = acc_read4<116>(); sum *= fw[0][3];
          sum += xr[1088] * fw[0][0];
          sum += xr[2624] * fw[0][1];
          sum += xr[4160] * fw[0][2];
          u32x2 v = {cvtpk(sum[0], sum[1]), cvtpk(sum[2], sum[3])};
          *(u32x2*)(otile + (0 + r) * 512 + (((29) ^ ((0 + r) & 31)) * 16) + 8 * h) = v; }
        { f32x4 sum = acc_read4<120>(); sum *= fw[0][3];
          sum += xr[1152] * fw[0][0];
          sum += xr[2688] * fw[0][1];
          sum += xr[4224] * fw[0][2];
          u32x2 v = {cvtpk(sum[0], sum[1]), cvtpk(sum[2], sum[3])};
          *(u32x2*)(otile + (0 + r) * 512 + (((30) ^ ((0 + r) & 31)) * 16) + 8 * h) = v; }
        { f32x4 sum = acc_read4<124>(); sum *= fw[0][3];
          sum += xr[1216] * fw[0][0];
          sum += xr[2752] * fw[0][1];
          sum += xr[4288] * fw[0][2];
          u32x2 v = {cvtpk(sum[0], sum[1]), cvtpk(sum[2], sum[3])};
          *(u32x2*)(otile + (0 + r) * 512 + (((31) ^ ((0 + r) & 31)) * 16) + 8 * h) = v; }
        { f32x4 sum = acc_read4<240>(); sum *= fw[1][3];
          sum += xr[1280] * fw[1][0];
          sum += xr[2816] * fw[1][1];
          sum += xr[4352] * fw[1][2];
          u32x2 v = {cvtpk(sum[0], sum[1]), cvtpk(sum[2], sum[3])};
          *(u32x2*)(otile + (32 + r) * 512 + (((28) ^ ((32 + r) & 31)) * 16) + 8 * h) = v; }
        { f32x4 sum = acc_read4<244>(); sum *= fw[1][3];
          sum += xr[1344] * fw[1][0];
          sum += xr[2880] * fw[1][1];
          sum += xr[4416] * fw[1][2];
          u32x2 v = {cvtpk(sum[0], sum[1]), cvtpk(sum[2], sum[3])};
          *(u32x2*)(otile + (32 + r) * 512 + (((29) ^ ((32 + r) & 31)) * 16) + 8 * h) = v; }
        { f32x4 sum = acc_read4<248>(); sum *= fw[1][3];
          sum += xr[1408] * fw[1][0];
          sum += xr[2944] * fw[1][1];
          sum += xr[4480] * fw[1][2];
          u32x2 v = {cvtpk(sum[0], sum[1]), cvtpk(sum[2], sum[3])};
          *(u32x2*)(otile + (32 + r) * 512 + (((30) ^ ((32 + r) & 31)) * 16) + 8 * h) = v; }
        { f32x4 sum = acc_read4<252>(); sum *= fw[1][3];
          sum += xr[1472] * fw[1][0];
          sum += xr[3008] * fw[1][1];
          sum += xr[4544] * fw[1][2];
          u32x2 v = {cvtpk(sum[0], sum[1]), cvtpk(sum[2], sum[3])};
          *(u32x2*)(otile + (32 + r) * 512 + (((31) ^ ((32 + r) & 31)) * 16) + 8 * h) = v; }
      } break;
    }
  }
  __syncthreads();
  asm volatile("s_waitcnt vmcnt(0)" :: "v"(c.pfd) : "memory");
  STAMP(15);
#pragma unroll
  for (int k = 0; k < 7; ++k) {
    const int R = 14 * w + 2 * k + h;
    u32x4 v = *(const u32x4*)(otile + R * 512 + ((r ^ (R & 31)) * 16));
    *(u32x4*)(o + ((size_t)(head * 8 + (r >> 2)) * NROW + row0 + R) * 32 + (r & 3) * 8) = v;
  }
  STAMP(10);
}
__global__ __launch_bounds__(512, 2) void oproj2_kernel(const u16* __restrict__ o, const u16* __restrict__ wo,
                                                        u16* __restrict__ out) {
  extern __shared__ __attribute__((aligned(16))) char lds[];
  const int tid = threadIdx.x, lane = tid & 63;
  const int w = __builtin_amdgcn_readfirstlane(tid >> 6);
  const int r = lane & 31, h = lane >> 5;
  const int xcd = blockIdx.x & 7, idx = blockIdx.x >> 3;
  const int rt = xcd + 8 * (idx / 7), ct = idx % 7;
  if (rt >= 28) return;
  const int col0 = ct * 64;
  char* ring = lds + w * 18432;
  __amdgpu_buffer_rsrc_t rw = __builtin_amdgcn_make_buffer_rsrc((void*)(wo + (size_t)(512 * w) * DM + col0), 0,
                                                                 (512 * DM - col0) * 2, 0x00020000);
  __amdgpu_buffer_rsrc_t ra = __builtin_amdgcn_make_buffer_rsrc((void*)(o + ((size_t)(16 * w) * NROW + rt * 32) * 32), 0,
                                                                 (16 * NROW - rt * 32) * 64, 0x00020000);
  const int drow = lane >> 3;
  const int wvoff = drow * (DM * 2) + (((lane & 7) ^ (((drow >> 1) & 1) << 2)) * 16);
  const int arow = lane >> 2;
  const int avoff = arow * 64 + (((lane & 3) ^ ((arow >> 2) & 3)) * 16);
  auto issue = [&](int u) {
    char* dst = ring + (u % 3) * 6144;
#pragma unroll
    for (int p = 0; p < 4; ++p)
      __builtin_amdgcn_raw_ptr_buffer_load_lds(rw, (lds_vp)(dst + p * 1024), 16, wvoff, (32 * u + 8 * p) * (DM * 2), 0, 0);
#pragma unroll
    for (int p = 0; p < 2; ++p)
      __builtin_amdgcn_raw_ptr_buffer_load_lds(ra, (lds_vp)(dst + 4096 + p * 1024), 16, avoff, 1024 * p + u * (NROW * 64), 0, 0);
  };
  issue(0); issue(1); issue(2);
  const int g = lane >> 4, qq = (lane >> 2) & 3, pp = lane & 3;
  const unsigned rb = (unsigned)(size_t)ring;
  const unsigned trb = rb + (8 * (g >> 1) + qq) * 128 + ((32 * (g & 1) + 8 * pp) ^ ((qq >> 1) << 6));
  const unsigned ab0 = rb + 4096 + r * 64 + (((0 + h) ^ ((r >> 2) & 3)) * 16);
  const unsigned ab1 = rb + 4096 + r * 64 + (((2 + h) ^ ((r >> 2) & 3)) * 16);
  f32x16 acc[2];
#pragma unroll
  for (int b2 = 0; b2 < 2; ++b2)
#pragma unroll
    for (int i = 0; i < 16; ++i) acc[b2][i] = 0.f;
#pragma unroll
  for (int u = 0; u < 16; ++u) {
    if (u <= 13) WAITV(12); else if (u == 14) WAITV(6); else WAITV(0);
    const unsigned so = (u % 3) * 6144;
    s16x4 a[2][4];
    bf16x8 xb[2];
#pragma unroll
    for (int s = 0; s < 2; ++s) {
      a[s][0] = trread(trb + so + s * 2048);
      a[s][1] = trread(trb + so + s * 2048 + 512);
      a[s][2] = trread((trb + so + s * 2048) ^ 64);
      a[s][3] = trread((trb + so + s * 2048 + 512) ^ 64);
    }
    asm volatile("ds_read_b128 %0, %1" : "=v"(xb[0]) : "v"(ab0 + so) : "memory");
    asm volatile("ds_read_b128 %0, %1" : "=v"(xb[1]) : "v"(ab1 + so) : "memory");
    asm volatile("s_waitcnt lgkmcnt(0)"
                 : "+v"(a[0][0]), "+v"(a[0][1]), "+v"(a[0][2]), "+v"(a[0][3]), "+v"(a[1][0]), "+v"(a[1][1]),
                   "+v"(a[1][2]), "+v"(a[1][3]), "+v"(xb[0]), "+v"(xb[1])::"memory");
    if (u + 3 < 16) issue(u + 3);
#pragma unroll
    for (int s = 0; s < 2; ++s) {
      acc[0] = __builtin_amdgcn_mfma_f32_32x32x16_bf16(cat8(a[s][0], a[s][1]), xb[s], acc[0], 0, 0, 0);
      acc[1] = __builtin_amdgcn_mfma_f32_32x32x16_bf16(cat8(a[s][2], a[s][3]), xb[s], acc[1], 0, 0, 0);
    }
  }
  __syncthreads();
  float* red = (float*)lds;
#pragma unroll
  for (int cb = 0; cb < 2; ++cb)
#pragma unroll
    for (int gg = 0; gg < 4; ++gg) {
      f32x4 v = {acc[cb][4 * gg], acc[cb][4 * gg + 1], acc[cb][4 * gg + 2], acc[cb][4 * gg + 3]};
      *(f32x4*)(red + ((w * 8 + cb * 4 + gg) * 64 + lane) * 4) = v;
    }
  __syncthreads();
  {
    const int cg = w, cb = cg >> 2, gg = cg & 3;
    f32x4 s = {0.f, 0.f, 0.f, 0.f};
#pragma unroll
    for (int ww = 0; ww < 8; ++ww) s += *(const f32x4*)(red + ((ww * 8 + cg) * 64 + lane) * 4);
    u32x2 v = {cvtpk(s[0], s[1]), cvtpk(s[2], s[3])};
    *(u32x2*)(out + (size_t)(rt * 32 + r) * DM + col0 + 32 * cb + 8 * gg + 4 * h) = v;
  }
}
__global__ __launch_bounds__(512, 2) void oproj3_kernel(const u16* __restrict__ o, const u16* __restrict__ wo,
                                                        u16* __restrict__ out, float* __restrict__ slab,
                                                        unsigned* __restrict__ cnt DBGARG) {
  extern __shared__ __attribute__((aligned(16))) char lds[];
  STAMP(0);
  const int tid = threadIdx.x, lane = tid & 63;
  const int w = __builtin_amdgcn_readfirstlane(tid >> 6);
  const int r = lane & 31, h = lane >> 5;
  const int xcd = blockIdx.x & 7, idx = blockIdx.x >> 3;
  const int tile = (idx >> 1) * 8 + xcd, kh = idx & 1;
  if (tile >= 98) return;
  const int rt = tile / 7, ct = tile % 7;
  const int col0 = ct * 64, k0 = kh * 2048 + 256 * w;
  char* ring = lds + w * 16384;
  __amdgpu_buffer_rsrc_t rw = __builtin_amdgcn_make_buffer_rsrc((void*)(wo + (size_t)k0 * DM + col0), 0,
                                                                 (256 * DM - col0) * 2, 0x00020000);
  __amdgpu_buffer_rsrc_t ra = __builtin_amdgcn_make_buffer_rsrc((void*)(o + (size_t)(rt * 64) * NQ + k0), 0,
                                                                 (64 * NQ - k0) * 2, 0x00020000);
  const int drow = lane >> 3;
  const int wvoff = drow * (DM * 2) + (((lane & 7) ^ (((drow >> 1) & 1) << 2)) * 16);
  const int arow = lane >> 2;
  const int avoff = arow * (NQ * 2) + (((lane & 3) ^ ((arow >> 2) & 3)) * 16);
  auto issue = [&](int u) {
    char* dst = ring + (u & 1) * 8192;
#pragma unroll
    for (int p = 0; p < 4; ++p)
      __builtin_amdgcn_raw_ptr_buffer_load_lds(rw, (lds_vp)(dst + p * 1024), 16, wvoff, (32 * u + 8 * p) * (DM * 2), 0, 0);
#pragma unroll
    for (int p = 0; p < 4; ++p)
      __builtin_amdgcn_raw_ptr_buffer_load_lds(ra, (lds_vp)(dst + 4096 + p * 1024), 16, avoff, 16 * p * (NQ * 2) + 64 * u, 0, 0);
  };
  issue(0); issue(1);
  const int g = lane >> 4, qq = (lane >> 2) & 3, pp = lane & 3;
  const unsigned rb = (unsigned)(size_t)ring;
  const unsigned trb = rb + (8 * (g >> 1) + qq) * 128 + ((32 * (g & 1) + 8 * pp) ^ ((qq >> 1) << 6));
  const unsigned ab0 = rb + 4096 + r * 64 + (((0 + h) ^ ((r >> 2) & 3)) * 16);
  const unsigned ab1 = rb + 4096 + r * 64 + (((2 + h) ^ ((r >> 2) & 3)) * 16);
  f32x16 acc[2][2];
#pragma unroll
  for (int a2 = 0; a2 < 2; ++a2)
#pragma unroll
    for (int b2 = 0; b2 < 2; ++b2)
#pragma unroll
      for (int i = 0; i < 16; ++i) acc[a2][b2][i] = 0.f;
#pragma unroll
  for (int u = 0; u < 8; ++u) {
    if (u <= 6) WAITV(8); else WAITV(0);
    const unsigned so = (u & 1) * 8192;
    s16x4 a[2][4];
    bf16x8 xb[2][2];
#pragma unroll
    for (int s = 0; s < 2; ++s) {
      a[s][0] = trread(trb + so + s * 2048);
      a[s][1] = trread(trb + so + s * 2048 + 512);
      a[s][2] = trread((trb + so + s * 2048) ^ 64);
      a[s][3] = trread((trb + so + s * 2048 + 512) ^ 64);
    }
    asm volatile("ds_read_b128 %0, %1" : "=v"(xb[0][0]) : "v"(ab0 + so) : "memory");
    asm volatile("ds_read_b128 %0, %1" : "=v"(xb[1][0]) : "v"(ab1 + so) : "memory");
    asm volatile("ds_read_b128 %0, %1 offset:2048" : "=v"(xb[0][1]) : "v"(ab0 + so) : "memory");
    asm volatile("ds_read_b128 %0, %1 offset:2048" : "=v"(xb[1][1]) : "v"(ab1 + so) : "memory");
    asm volatile("s_waitcnt lgkmcnt(0)"
                 : "+v"(a[0][0]), "+v"(a[0][1]), "+v"(a[0][2]), "+v"(a[0][3]), "+v"(a[1][0]), "+v"(a[1][1]),
                   "+v"(a[1][2]), "+v"(a[1][3]), "+v"(xb[0][0]), "+v"(xb[1][0]), "+v"(xb[0][1]), "+v"(xb[1][1])::"memory");
    if (u + 2 < 8) issue(u + 2);
#pragma unroll
    for (int s = 0; s < 2; ++s)
#pragma unroll
      for (int rb2 = 0; rb2 < 2; ++rb2) {
        acc[rb2][0] = __builtin_amdgcn_mfma_f32_32x32x16_bf16(cat8(a[s][0], a[s][1]), xb[s][rb2], acc[rb2][0], 0, 0, 0);
        acc[rb2][1] = __builtin_amdgcn_mfma_f32_32x32x16_bf16(cat8(a[s][2], a[s][3]), xb[s][rb2], acc[rb2][1], 0, 0, 0);
      }
  }
  STAMP(1);
  __syncthreads();
  float* red = (float*)lds;
#pragma unroll
  for (int rb2 = 0; rb2 < 2; ++rb2)
#pragma unroll
    for (int cb = 0; cb < 2; ++cb)
#pragma unroll
      for (int gg = 0; gg < 4; ++gg) {
        f32x4 v = {acc[rb2][cb][4 * gg], acc[rb2][cb][4 * gg + 1], acc[rb2][cb][4 * gg + 2], acc[rb2][cb][4 * gg + 3]};
        *(f32x4*)(red + ((w * 16 + rb2 * 8 + cb * 4 + gg) * 64 + lane) * 4) = v;
      }
  __syncthreads();
  f32x4 sum[2];
#pragma unroll
  for (int jj = 0; jj < 2; ++jj) {
    const int grp = 2 * w + jj;
    f32x4 s = {0.f, 0.f, 0.f, 0.f};
#pragma unroll
    for (int ww = 0; ww < 8; ++ww) s += *(const f32x4*)(red + ((ww * 16 + grp) * 64 + lane) * 4);
    sum[jj] = s;
  }
  STAMP(2);
  __amdgpu_buffer_rsrc_t rs = __builtin_amdgcn_make_buffer_rsrc((void*)(slab + (size_t)tile * 2 * 4096), 0, 2 * 4096 * 4, 0x00020000);
#pragma unroll
  for (int jj = 0; jj < 2; ++jj)
    __builtin_amdgcn_raw_buffer_store_b128(__builtin_bit_cast(u32x4, sum[jj]), rs, (kh * 16 + 2 * w + jj) * 1024 + lane * 16, 0, 16);
  asm volatile("s_waitcnt vmcnt(0)" ::: "memory");
  __syncthreads();
  unsigned* flag = (unsigned*)(lds + 131072);
  if (tid == 0) *flag = __hip_atomic_fetch_add(cnt + tile, 1u, __ATOMIC_RELAXED, __HIP_MEMORY_SCOPE_AGENT);
  __syncthreads();
  STAMP(3);
  if (*flag == 1u) {
#pragma unroll
    for (int jj = 0; jj < 2; ++jj) {
      u32x4 pv = __builtin_amdgcn_raw_buffer_load_b128(rs, ((kh ^ 1) * 16 + 2 * w + jj) * 1024 + lane * 16, 0, 16);
      f32x4 t = sum[jj] + __builtin_bit_cast(f32x4, pv);
      const int grp = 2 * w + jj, rb2 = grp >> 3, cb = (grp >> 2) & 1, gg = grp & 3;
      u32x2 v = {cvtpk(t[0], t[1]), cvtpk(t[2], t[3])};
      *(u32x2*)(out + (size_t)(rt * 64 + 32 * rb2 + r) * DM + col0 + 32 * cb + 8 * gg + 4 * h) = v;
    }
  }
  STAMP(4);
}
extern "C" void kernel_launch(void* const* d_in, const int* in_sizes, int n_in, void* d_out, int out_size,
                              void* d_ws, size_t ws_size, hipStream_t stream) {
  const u16* x = (const u16*)d_in[0];
  const u16* kc = (const u16*)d_in[1];
  const u16* vc = (const u16*)d_in[2];
  const u16* wq = (const u16*)d_in[3];
  const u16* wo = (const u16*)d_in[4];
  u16* out = (u16*)d_out;
  u16* qws = (u16*)d_ws;
  u16* ows = (u16*)((char*)d_ws + (size_t)NROW * NQ * 2);
  float* slab = (float*)((char*)d_ws + (16 << 20));
  unsigned* cnt = (unsigned*)((char*)d_ws + (24 << 20));
  (void)hipFuncSetAttribute((const void*)attn4_kernel, hipFuncAttributeMaxDynamicSharedMemorySize, 163840);
  hipLaunchKernelGGL(attn4_kernel, dim3(256), dim3(256), 163840, stream, x, wq, kc, vc, wo, ows, cnt
  );
  (void)hipFuncSetAttribute((const void*)oproj2_kernel, hipFuncAttributeMaxDynamicSharedMemorySize, 147456);
  hipLaunchKernelGGL(oproj2_kernel, dim3(8 * 28), dim3(512), 147456, stream, (const u16*)ows, wo, out);
  return;
  (void)hipFuncSetAttribute((const void*)oproj3_kernel, hipFuncAttributeMaxDynamicSharedMemorySize, 131328);
  hipLaunchKernelGGL(oproj3_kernel, dim3(8 * 26), dim3(512), 131328, stream, (const u16*)ows, wo, out, slab, cnt
  );
}
```

```cpp
#include <hip/hip_runtime.h>
#include <hip/hip_bf16.h>
#include <utility>

typedef unsigned short u16;
typedef __attribute__((ext_vector_type(8))) short bf16x8;
typedef __attribute__((ext_vector_type(4))) short s16x4;
typedef __attribute__((ext_vector_type(16))) float f32x16;
typedef __attribute__((ext_vector_type(4))) float f32x4;
typedef __attribute__((ext_vector_type(2))) float f32x2;
typedef __attribute__((ext_vector_type(2))) __bf16 bf16x2v;
typedef __attribute__((ext_vector_type(2))) unsigned u32x2;
typedef __attribute__((ext_vector_type(4))) unsigned u32x4;
#define DI __device__ __forceinline__
#define LDSP(p) ((__attribute__((address_space(3))) unsigned*)(p))

constexpr int NB = 8, SQ = 112, DM = 448, HQ = 16, HKV = 8, SK = 1024, DH = 256;
constexpr int NROW = NB * SQ;
constexpr int NQ = HQ * DH;

DI unsigned cvtpk(float lo, float hi) {
  f32x2 v = {lo, hi};
  bf16x2v b = __builtin_convertvector(v, bf16x2v);
  return __builtin_bit_cast(unsigned, b);
}
DI void glds16(const void* g, void* l) {
  __builtin_amdgcn_global_load_lds((const unsigned*)g, LDSP(l), 16, 0, 0);
}
#define WAITV(n) asm volatile("s_waitcnt vmcnt(" #n ")" ::: "memory")
#define WAITL0() asm volatile("s_waitcnt lgkmcnt(0)" ::: "memory")

DI s16x4 trread(unsigned addr) {
  s16x4 r;
  asm volatile("ds_read_b64_tr_b16 %0, %1" : "=v"(r) : "v"(addr) : "memory");
  return r;
}
template <int OFF> DI s16x4 trread_o(unsigned addr) {
  s16x4 r;
  asm volatile("ds_read_b64_tr_b16 %0, %1 offset:%2" : "=v"(r) : "v"(addr), "i"(OFF) : "memory");
  return r;
}
DI bf16x8 cat8(s16x4 a, s16x4 b) { return __builtin_shufflevector(a, b, 0, 1, 2, 3, 4, 5, 6, 7); }

template <int RB, int NCH>
DI void wave_gemm(const u16* __restrict__ act, int lda, const int (&arow)[RB], const u16* __restrict__ W, int ldw,
                  char* wl, f32x16 (&acc)[RB][2], int lane) {
  const int r = lane & 31, h = lane >> 5;
  const int g = lane >> 4, q = (lane >> 2) & 3, pp = lane & 3;
  const unsigned ldsb = (unsigned)(size_t)wl;
  const unsigned trb = ldsb + (8 * (g >> 1) + q) * 128 + ((32 * (g & 1) + 8 * pp) ^ ((q >> 1) << 6));
  const int drow = lane >> 3;
  const int dchk = (lane & 7) ^ (((drow >> 1) & 1) << 2);
  const u16* wsrc = W + (size_t)drow * ldw + dchk * 8;
  auto issue = [&](int c, int buf) {
#pragma unroll
    for (int p = 0; p < 8; ++p) glds16(wsrc + (size_t)(64 * c + 8 * p) * ldw, wl + buf * 8192 + p * 1024);
  };
  issue(0, 0);
#pragma unroll
  for (int c = 0; c < NCH; ++c) {
    const int buf = c & 1;
    bf16x8 bfr[4][RB];
#pragma unroll
    for (int s = 0; s < 4; ++s)
#pragma unroll
      for (int rb = 0; rb < RB; ++rb)
        bfr[s][rb] = *(const bf16x8*)(act + (size_t)arow[rb] * lda + 64 * c + 16 * s + 8 * h);
    WAITV(0);
    if (c + 1 < NCH) issue(c + 1, buf ^ 1);
#pragma unroll
    for (int s = 0; s < 4; ++s) {
      s16x4 a00 = trread(trb + buf * 8192 + s * 2048 + 0);
      s16x4 a01 = trread(trb + buf * 8192 + s * 2048 + 512);
      s16x4 a10 = trread((trb + buf * 8192 + s * 2048 + 0) ^ 64);
      s16x4 a11 = trread((trb + buf * 8192 + s * 2048 + 512) ^ 64);
      asm volatile("s_waitcnt lgkmcnt(0)" : "+v"(a00), "+v"(a01), "+v"(a10), "+v"(a11)::"memory");
      bf16x8 A0 = cat8(a00, a01), A1 = cat8(a10, a11);
#pragma unroll
      for (int rb = 0; rb < RB; ++rb) {
        acc[rb][0] = __builtin_amdgcn_mfma_f32_32x32x16_bf16(A0, bfr[s][rb], acc[rb][0], 0, 0, 0);
        acc[rb][1] = __builtin_amdgcn_mfma_f32_32x32x16_bf16(A1, bfr[s][rb], acc[rb][1], 0, 0, 0);
      }
    }
  }
  (void)r;
}

__global__ __launch_bounds__(256) void qproj_kernel(const u16* __restrict__ x, const u16* __restrict__ wq,
                                                    u16* __restrict__ qout) {
  extern __shared__ __attribute__((aligned(16))) char lds[];
  const int tid = threadIdx.x, lane = tid & 63;
  const int w = __builtin_amdgcn_readfirstlane(tid >> 6);
  const int r = lane & 31, h = lane >> 5;
  const int rt = blockIdx.x % 14, head = blockIdx.x / 14;
  const int col0 = head * 256 + 64 * w;
  f32x16 acc[2][2];
#pragma unroll
  for (int a = 0; a < 2; ++a)
#pragma unroll
    for (int b = 0; b < 2; ++b)
#pragma unroll
      for (int i = 0; i < 16; ++i) acc[a][b][i] = 0.f;
  const int arow[2] = {rt * 64 + r, rt * 64 + 32 + r};
  wave_gemm<2, 7>(x, DM, arow, wq + col0, NQ, lds + w * 16384, acc, lane);
#pragma unroll
  for (int rb = 0; rb < 2; ++rb)
#pragma unroll
    for (int cb = 0; cb < 2; ++cb)
#pragma unroll
      for (int g = 0; g < 4; ++g) {
        u32x2 v = {cvtpk(acc[rb][cb][4 * g + 0], acc[rb][cb][4 * g + 1]),
                   cvtpk(acc[rb][cb][4 * g + 2], acc[rb][cb][4 * g + 3])};
        *(u32x2*)(qout + (size_t)arow[rb] * NQ + col0 + 32 * cb + 8 * g + 4 * h) = v;
      }
}

__global__ __launch_bounds__(256) void oproj_kernel(const u16* __restrict__ o, const u16* __restrict__ wo,
                                                    u16* __restrict__ out) {
  extern __shared__ __attribute__((aligned(16))) char lds[];
  const int tid = threadIdx.x, lane = tid & 63;
  const int w = __builtin_amdgcn_readfirstlane(tid >> 6);
  const int r = lane & 31, h = lane >> 5;
  const int rt = blockIdx.x % 28, ct = blockIdx.x / 28;
  const int col0 = ct * 64;
  f32x16 acc[1][2];
#pragma unroll
  for (int b = 0; b < 2; ++b)
#pragma unroll
    for (int i = 0; i < 16; ++i) acc[0][b][i] = 0.f;
  const int arow[1] = {rt * 32 + r};
  wave_gemm<1, 16>(o + 1024 * w, NQ, arow, wo + (size_t)(1024 * w) * DM + col0, DM, lds + w * 16384, acc, lane);
  __syncthreads();
  float* red = (float*)lds;
#pragma unroll
  for (int cb = 0; cb < 2; ++cb)
#pragma unroll
    for (int g = 0; g < 4; ++g) {
      f32x4 v = {acc[0][cb][4 * g], acc[0][cb][4 * g + 1], acc[0][cb][4 * g + 2], acc[0][cb][4 * g + 3]};
      *(f32x4*)(red + ((w * 8 + cb * 4 + g) * 64 + lane) * 4) = v;
    }
  __syncthreads();
#pragma unroll
  for (int j = 0; j < 2; ++j) {
    const int cg = 2 * w + j, cb = cg >> 2, g = cg & 3;
    f32x4 s = {0.f, 0.f, 0.f, 0.f};
#pragma unroll
    for (int ww = 0; ww < 4; ++ww) {
      f32x4 v = *(const f32x4*)(red + ((ww * 8 + cg) * 64 + lane) * 4);
      s += v;
    }
    u32x2 v = {cvtpk(s[0], s[1]), cvtpk(s[2], s[3])};
    *(u32x2*)(out + (size_t)arow[0] * DM + col0 + 32 * cb + 8 * g + 4 * h) = v;
  }
}

constexpr float SCL = 0.0625f * 1.4426950408889634f;
constexpr float THR = 8.0f;

typedef __attribute__((address_space(3))) void* lds_vp;
struct DmaCtx {
  __amdgpu_buffer_rsrc_t rk, rv;
  int koff[4], voff[4];
  char* dst;
};
template <int U>
DI void issue_unit(const DmaCtx& c) {
  constexpr int t = U >> 2, kind = U & 3, slot = U % 5;
  if constexpr (kind < 2) {
#pragma unroll
    for (int pi = 0; pi < 4; ++pi)
      __builtin_amdgcn_raw_ptr_buffer_load_lds(c.rk, (lds_vp)(c.dst + slot * 8192 + pi * 1024), 16, c.koff[pi],
                                               32 * t * 512 + kind * 256, 0, 0);
  } else {
    constexpr int ks = kind - 2;
#pragma unroll
    for (int pi = 0; pi < 4; ++pi)
      __builtin_amdgcn_raw_ptr_buffer_load_lds(c.rv, (lds_vp)(c.dst + slot * 8192 + pi * 1024), 16, c.voff[pi],
                                               (32 * t + 16 * ks) * 512, 0, 0);
  }
}
template <int U> DI void wait_unit_bar() {
  constexpr int after = (U + 3 > 31 ? 31 : U + 3) - U;
  if constexpr (after == 3) WAITV(12);
  else if constexpr (after == 2) WAITV(8);
  else if constexpr (after == 1) WAITV(4);
  else WAITV(0);
  WAITL0();
  __builtin_amdgcn_s_barrier();
}

struct AttnState {
  bf16x8 qf[16];
  f32x16 oacc[8];
  float m, l;
};

template <int T, int DHF>
DI void qk_half(AttnState& st, f32x16& s, const DmaCtx& c, char* ring, unsigned kaddr) {
  constexpr int U = 4 * T + DHF, slot = U % 5;
  wait_unit_bar<U>();
  if constexpr (U + 4 < 32) issue_unit<U + 4>(c);
#pragma unroll
  for (int ss = 0; ss < 8; ++ss) {
    bf16x8 kf = *(const bf16x8*)(ring + slot * 8192 + (kaddr ^ (ss * 32)));
    s = __builtin_amdgcn_mfma_f32_32x32x16_bf16(kf, st.qf[DHF * 8 + ss], s, 0, 0, 0);
  }
}
template <int T, int KS, int HF>
DI void pv_quarter(AttnState& st, const bf16x8& pf, const unsigned (&vaddr)[4]) {
  constexpr int U = 4 * T + 2 + KS, slot = U % 5;
  constexpr int OFF = slot * 8192 + HF * 256;
  s16x4 v0[4], v1[4];
  v0[0] = trread_o<OFF>(vaddr[0]); v1[0] = trread_o<OFF + 4096>(vaddr[0]);
  v0[1] = trread_o<OFF>(vaddr[1]); v1[1] = trread_o<OFF + 4096>(vaddr[1]);
  v0[2] = trread_o<OFF>(vaddr[2]); v1[2] = trread_o<OFF + 4096>(vaddr[2]);
  v0[3] = trread_o<OFF>(vaddr[3]); v1[3] = trread_o<OFF + 4096>(vaddr[3]);
  asm volatile("s_waitcnt lgkmcnt(0)"
               : "+v"(v0[0]), "+v"(v0[1]), "+v"(v0[2]), "+v"(v0[3]), "+v"(v1[0]), "+v"(v1[1]), "+v"(v1[2]),
                 "+v"(v1[3])::"memory");
#pragma unroll
  for (int d = 0; d < 4; ++d) {
    bf16x8 vf = cat8(v0[d], v1[d]);
    st.oacc[HF * 4 + d] = __builtin_amdgcn_mfma_f32_32x32x16_bf16(vf, pf, st.oacc[HF * 4 + d], 0, 0, 0);
  }
}
template <int T, int KS>
DI void pv_half(AttnState& st, const bf16x8& pf, const DmaCtx& c, const unsigned (&vaddr)[4]) {
  constexpr int U = 4 * T + 2 + KS;
  wait_unit_bar<U>();
  if constexpr (U + 4 < 32) issue_unit<U + 4>(c);
  pv_quarter<T, KS, 0>(st, pf, vaddr);
  pv_quarter<T, KS, 1>(st, pf, vaddr);
}

template <int T>
DI void attn_tile(AttnState& st, const DmaCtx& c, char* ring, unsigned kaddr, const unsigned (&vaddr)[4]) {
  f32x16 s;
#pragma unroll
  for (int i = 0; i < 16; ++i) s[i] = 0.f;
  qk_half<T, 0>(st, s, c, ring, kaddr);
  qk_half<T, 1>(st, s, c, ring, kaddr);
  bf16x8 pf[2];
  {
    float a = s[0];
#pragma unroll
    for (int i = 1; i < 16; ++i) a = fmaxf(a, s[i]);
    auto rr = __builtin_amdgcn_permlane32_swap(__float_as_uint(a), __float_as_uint(a), false, false);
    const float mx = fmaxf(__uint_as_float(rr[0]), __uint_as_float(rr[1])) * SCL;
    const bool grew = (mx - st.m > THR);
    if (T == 0 || __any((int)grew)) {
      const float mn = fmaxf(st.m, mx);
      const float alpha = __builtin_amdgcn_exp2f(st.m - mn);
      st.m = mn;
      st.l *= alpha;
      if (T != 0) {
#pragma unroll
        for (int db = 0; db < 8; ++db)
#pragma unroll
          for (int i = 0; i < 16; ++i) st.oacc[db][i] *= alpha;
      }
    }
    const float nm = -st.m;
    float ps = 0.f;
#pragma unroll
    for (int i = 0; i < 16; ++i) {
      s[i] = __builtin_amdgcn_exp2f(fmaf(s[i], SCL, nm));
      ps += s[i];
    }
    st.l += ps;
#pragma unroll
    for (int ks = 0; ks < 2; ++ks) {
      u32x4 wv = {cvtpk(s[8 * ks + 0], s[8 * ks + 1]), cvtpk(s[8 * ks + 2], s[8 * ks + 3]),
                  cvtpk(s[8 * ks + 4], s[8 * ks + 5]), cvtpk(s[8 * ks + 6], s[8 * ks + 7])};
      pf[ks] = __builtin_bit_cast(bf16x8, wv);
    }
  }
  pv_half<T, 0>(st, pf[0], c, vaddr);
  pv_half<T, 1>(st, pf[1], c, vaddr);
}

#define STAMP(i)
#define DBGARG
__global__ __launch_bounds__(512, 2) void attn_kernel(const u16* __restrict__ q, const u16* __restrict__ kc,
                                                      const u16* __restrict__ vc, u16* __restrict__ o DBGARG) {
  extern __shared__ __attribute__((aligned(16))) char lds[];
  STAMP(0);
  const int tid = threadIdx.x, lane = tid & 63;
  const int w = __builtin_amdgcn_readfirstlane(tid >> 6);
  const int rb = w >> 2, kq = w & 3;
  const int r = lane & 31, h = lane >> 5;
  const int bid = blockIdx.x;
  const int xcd = bid & 7, j = bid >> 3;
  const int grp = (j >> 2) * 8 + xcd, sub = j & 3;
  const int b = grp >> 3, kvh = grp & 7;
  const int head = kvh * 2 + (sub >> 1), rhalf = sub & 1;
  const int row0 = b * SQ + rhalf * 56;

  const u16* Kw = kc + ((size_t)(b * HKV + kvh) * SK + 256 * kq) * DH;
  const u16* Vw = vc + ((size_t)(b * HKV + kvh) * SK + 256 * kq) * DH;
  char* ring = lds + kq * 40960;

  AttnState st;
  {
    int rr = 32 * rb + r;
    rr = rr > 55 ? 55 : rr;
    const u16* qrow = q + (size_t)(row0 + rr) * NQ + head * DH + 8 * h;
#pragma unroll
    for (int s = 0; s < 16; ++s) st.qf[s] = *(const bf16x8*)(qrow + 16 * s);
    st.m = -__builtin_inff();
    st.l = 0.f;
#pragma unroll
    for (int db = 0; db < 8; ++db)
#pragma unroll
      for (int i = 0; i < 16; ++i) st.oacc[db][i] = 0.f;
  }
#pragma unroll
  for (int s = 0; s < 16; ++s) asm volatile("" : "+v"(st.qf[s]));
  WAITV(0);

  STAMP(1);
  DmaCtx c;
  c.rk = __builtin_amdgcn_make_buffer_rsrc((void*)Kw, 0, 256 * DH * 2, 0x00020000);
  c.rv = __builtin_amdgcn_make_buffer_rsrc((void*)Vw, 0, 256 * DH * 2, 0x00020000);
  c.dst = ring + rb * 4096;
#pragma unroll
  for (int pi = 0; pi < 4; ++pi) {
    const int krow = 16 * rb + 4 * pi + (lane >> 4);
    c.koff[pi] = krow * 512 + (((lane & 15) ^ (krow & 15)) * 16);
    const int vrow = 8 * rb + 2 * pi + (lane >> 5);
    c.voff[pi] = vrow * 512 + (((lane & 31) ^ ((vrow & 3) << 2)) * 16);
  }
  issue_unit<0>(c);
  issue_unit<1>(c);
  issue_unit<2>(c);
  issue_unit<3>(c);

  const unsigned kaddr = r * 256 + ((h ^ (r & 15)) * 16);
  const int g = lane >> 4, qq = (lane >> 2) & 3, pp = lane & 3;
  unsigned vaddr[4];
#pragma unroll
  for (int d = 0; d < 4; ++d)
    vaddr[d] = (unsigned)(size_t)ring + (4 * (g >> 1) + qq) * 512 + ((64 * d + 32 * (g & 1) + 8 * pp) ^ (qq << 6));

  attn_tile<0>(st, c, ring, kaddr, vaddr);
  STAMP(2);
  attn_tile<1>(st, c, ring, kaddr, vaddr);
  STAMP(3);
  attn_tile<2>(st, c, ring, kaddr, vaddr);
  STAMP(4);
  attn_tile<3>(st, c, ring, kaddr, vaddr);
  STAMP(5);
  attn_tile<4>(st, c, ring, kaddr, vaddr);
  STAMP(6);
  attn_tile<5>(st, c, ring, kaddr, vaddr);
  STAMP(7);
  attn_tile<6>(st, c, ring, kaddr, vaddr);
  STAMP(8);
  attn_tile<7>(st, c, ring, kaddr, vaddr);
  STAMP(9);

  __syncthreads();
  float* ml = (float*)lds;
  {
    auto rr = __builtin_amdgcn_permlane32_swap(__float_as_uint(st.l), __float_as_uint(st.l), false, false);
    const float lt = __uint_as_float(rr[0]) + __uint_as_float(rr[1]);
    if (h == 0) {
      ml[(w * 32 + r) * 2 + 0] = st.m;
      ml[(w * 32 + r) * 2 + 1] = lt;
    }
  }
  __syncthreads();
  float f;
  {
    float mm[4], ll[4];
#pragma unroll
    for (int k = 0; k < 4; ++k) {
      mm[k] = ml[((rb * 4 + k) * 32 + r) * 2 + 0];
      ll[k] = ml[((rb * 4 + k) * 32 + r) * 2 + 1];
    }
    const float M = fmaxf(fmaxf(mm[0], mm[1]), fmaxf(mm[2], mm[3]));
    float L = 0.f;
#pragma unroll
    for (int k = 0; k < 4; ++k) L += __builtin_amdgcn_exp2f(mm[k] - M) * ll[k];
    f = __builtin_amdgcn_exp2f(st.m - M) / L;
  }
  float* xch = (float*)(lds + 4096);
#pragma unroll
  for (int rnd = 0; rnd < 2; ++rnd) {
#pragma unroll
    for (int db = 0; db < 8; ++db) {
      if ((db & 1) != rnd) continue;
      const int owner = db >> 1;
      if (owner == kq) continue;
      const int dslot = owner < kq ? owner : owner - 1;
#pragma unroll
      for (int gg = 0; gg < 4; ++gg) {
        f32x4 v = {st.oacc[db][4 * gg] * f, st.oacc[db][4 * gg + 1] * f, st.oacc[db][4 * gg + 2] * f,
                   st.oacc[db][4 * gg + 3] * f};
        *(f32x4*)(xch + (((w * 3 + dslot) * 4 + gg) * 64 + lane) * 4) = v;
      }
    }
    __syncthreads();
#pragma unroll
    for (int db = 0; db < 8; ++db) {
      if ((db & 1) != rnd) continue;
      if ((db >> 1) != kq) continue;
#pragma unroll
      for (int gg = 0; gg < 4; ++gg) {
        f32x4 sum = {st.oacc[db][4 * gg] * f, st.oacc[db][4 * gg + 1] * f, st.oacc[db][4 * gg + 2] * f,
                     st.oacc[db][4 * gg + 3] * f};
#pragma unroll
        for (int sk = 0; sk < 4; ++sk) {
          if (sk == kq) continue;
          const int dslot = kq < sk ? kq : kq - 1;
          f32x4 v = *(const f32x4*)(xch + ((((rb * 4 + sk) * 3 + dslot) * 4 + gg) * 64 + lane) * 4);
          sum += v;
        }
        const int rr = 32 * rb + r;
        if (rr < 56) {
          u32x2 v = {cvtpk(sum[0], sum[1]), cvtpk(sum[2], sum[3])};
          *(u32x2*)(o + (size_t)(row0 + rr) * NQ + head * DH + 32 * db + 8 * gg + 4 * h) = v;
        }
      }
    }
    __syncthreads();
  }
  STAMP(10);
}

#define GAP() __builtin_amdgcn_sched_barrier(0)
#define ACLOB "a0","a1","a2","a3","a4","a5","a6","a7","a8","a9","a10","a11","a12","a13","a14","a15","a16","a17","a18","a19","a20","a21","a22","a23","a24","a25","a26","a27","a28","a29","a30","a31","a32","a33","a34","a35","a36","a37","a38","a39","a40","a41","a42","a43","a44","a45","a46","a47","a48","a49","a50","a51","a52","a53","a54","a55","a56","a57","a58","a59","a60","a61","a62","a63","a64","a65","a66","a67","a68","a69","a70","a71","a72","a73","a74","a75","a76","a77","a78","a79","a80","a81","a82","a83","a84","a85","a86","a87","a88","a89","a90","a91","a92","a93","a94","a95","a96","a97","a98","a99","a100","a101","a102","a103","a104","a105","a106","a107","a108","a109","a110","a111","a112","a113","a114","a115","a116","a117","a118","a119","a120","a121","a122","a123","a124","a125","a126","a127","a128","a129","a130","a131","a132","a133","a134","a135","a136","a137","a138","a139","a140","a141","a142","a143","a144","a145","a146","a147","a148","a149","a150","a151","a152","a153","a154","a155","a156","a157","a158","a159","a160","a161","a162","a163","a164","a165","a166","a167","a168","a169","a170","a171","a172","a173","a174","a175","a176","a177","a178","a179","a180","a181","a182","a183","a184","a185","a186","a187","a188","a189","a190","a191","a192","a193","a194","a195","a196","a197","a198","a199","a200","a201","a202","a203","a204","a205","a206","a207","a208","a209","a210","a211","a212","a213","a214","a215","a216","a217","a218","a219","a220","a221","a222","a223","a224","a225","a226","a227","a228","a229","a230","a231","a232","a233","a234","a235","a236","a237","a238","a239","a240","a241","a242","a243","a244","a245","a246","a247","a248","a249","a250","a251","a252","a253","a254","a255"

template <int AO, bool ZERO> DI void pv_mfma(const bf16x8& vf, const bf16x8& pf) {
  if constexpr (ZERO)
    asm volatile("v_mfma_f32_32x32x16_bf16 a[%c2:%c3], %0, %1, 0" ::"v"(vf), "v"(pf), "i"(AO), "i"(AO + 15) : ACLOB);
  else
    asm volatile("v_mfma_f32_32x32x16_bf16 a[%c2:%c3], %0, %1, a[%c2:%c3]" ::"v"(vf), "v"(pf), "i"(AO), "i"(AO + 15)
                 : ACLOB);
}
template <bool FIRST> DI void qk_mfma(f32x16& s, const bf16x8& kf, const bf16x8& qf) {
  if constexpr (FIRST)
    asm volatile("v_mfma_f32_32x32x16_bf16 %0, %1, %2, 0" : "=&v"(s) : "v"(kf), "v"(qf) : ACLOB);
  else
    asm volatile("v_mfma_f32_32x32x16_bf16 %0, %1, %2, %0" : "+v"(s) : "v"(kf), "v"(qf) : ACLOB);
}
DI float max3f(float a, float b, float c) { float r; asm("v_max3_f32 %0, %1, %2, %3" : "=v"(r) : "v"(a), "v"(b), "v"(c)); return r; }
DI float max2f(float a, float b) { float r; asm("v_max_f32_e32 %0, %1, %2" : "=v"(r) : "v"(a), "v"(b)); return r; }
DI float fadd_s(float a, float b) { float r; asm("v_add_f32_e32 %0, %1, %2" : "=v"(r) : "v"(a), "v"(b)); return r; }
DI float ffma_s(float a, float b, float c) { float r; asm("v_fma_f32 %0, %1, %2, %3" : "=v"(r) : "v"(a), "v"(b), "v"(c)); return r; }

constexpr int unit_is_v(int n) { return n < 2 ? 0 : (n >= 30 ? 1 : (((n - 2) & 3) >= 2)); }
constexpr int unit_tile(int n) { return n < 2 ? 0 : (n >= 30 ? 7 : (((n - 2) & 3) >= 2 ? (n - 2) / 4 : (n - 2) / 4 + 1)); }
constexpr int unit_half(int n) { return n & 1; }
constexpr int unit_k(int t, int hf) { return t == 0 ? hf : 4 * (t - 1) + 2 + hf; }
constexpr int unit_v(int t, int hf) { return t == 7 ? 30 + hf : 4 * t + 4 + hf; }

struct Ctx4 {
  __amdgpu_buffer_rsrc_t rk, rv;
  int koff[4], voff[2];
  char* ring;
  unsigned kaddr[8];
  unsigned vaddr[4];
  bf16x8 qf[2][16];
  f32x16 s[2];
  bf16x8 pf[2][2];
  unsigned pn[2][8];
  float m[2], l[2], nm[2], mx[2], fct[2];
  float rs[2];
  bool pend;
  const u16* Kp; const u16* Vp;
  unsigned pfoff;
  int sub;
  unsigned pfd;
};
DI void prefetch_unit(Ctx4& c, int n) {
}

template <int N, int P> DI void dma_piece(const Ctx4& c) {
  if constexpr (N < 32) {
    constexpr int slot = N % 5, t = unit_tile(N), hf = unit_half(N);
    if constexpr (!unit_is_v(N))
      __builtin_amdgcn_raw_ptr_buffer_load_lds(c.rk, (lds_vp)(c.ring + slot * 8192 + P * 1024), 16, c.koff[P & 3],
                                               (32 * t + 16 * (P >> 2)) * 512 + hf * 256, 0, 0);
    else
      __builtin_amdgcn_raw_ptr_buffer_load_lds(c.rv, (lds_vp)(c.ring + slot * 8192 + P * 1024), 16, c.voff[P & 1],
                                               (32 * t + 16 * hf + 4 * (P >> 1)) * 512, 0, 0);
  }
}
template <int N> DI void dma_unit(const Ctx4& c) {
  dma_piece<N, 0>(c); dma_piece<N, 1>(c); dma_piece<N, 2>(c); dma_piece<N, 3>(c);
  dma_piece<N, 4>(c); dma_piece<N, 5>(c); dma_piece<N, 6>(c); dma_piece<N, 7>(c);
}
template <int N, int AHEAD> DI void wait_unit4() {
  constexpr int last = N + AHEAD > 31 ? 31 : N + AHEAD;
  constexpr int after = last - N;
  if constexpr (after == 4) WAITV(32);
  else if constexpr (after == 3) WAITV(24);
  else if constexpr (after == 2) WAITV(16);
  else if constexpr (after == 1) WAITV(8);
  else WAITV(0);
}

template <int T, int J> DI bf16x8 kread(const Ctx4& c) {
  constexpr int n = unit_k(T, J >> 3), slot = n % 5;
  bf16x8 r;
  asm volatile("ds_read_b128 %0, %1 offset:%2" : "=v"(r) : "v"(c.kaddr[J & 7]), "i"(slot * 8192) : "memory");
  return r;
}
template <int T, int J> DI void phaseA_step(Ctx4& c, bf16x8 (&kf)[4]) {
  constexpr int n0 = unit_k(T, 0), n1 = unit_k(T, 1);
  if constexpr (J + 3 < 16) asm volatile("s_waitcnt lgkmcnt(3)" : "+v"(kf[J & 3])::"memory");
  else if constexpr (J + 3 == 16) asm volatile("s_waitcnt lgkmcnt(2)" : "+v"(kf[J & 3])::"memory");
  else if constexpr (J + 3 == 17) asm volatile("s_waitcnt lgkmcnt(1)" : "+v"(kf[J & 3])::"memory");
  else asm volatile("s_waitcnt lgkmcnt(0)" : "+v"(kf[J & 3])::"memory");
  qk_mfma<J == 0>(c.s[0], kf[J & 3], c.qf[0][J]);
  if constexpr (J == 8 && T <= 4) prefetch_unit(c, 14 + 4 * T + c.sub);
  if constexpr (J >= 8) { dma_piece<n0 + 5, J - 8>(c); }
  GAP();
  qk_mfma<J == 0>(c.s[1], kf[J & 3], c.qf[1][J]);
  if constexpr (J + 4 == 8) wait_unit4<n1, 3>();
  if constexpr (J + 4 < 16) kf[J & 3] = kread<T, J + 4>(c);
  GAP();
}
template <int T, int... Js> DI void phaseA_(Ctx4& c, std::integer_sequence<int, Js...>) {
  bf16x8 kf[4];
  wait_unit4<unit_k(T, 0), 4>();
  kf[0] = kread<T, 0>(c); kf[1] = kread<T, 1>(c); kf[2] = kread<T, 2>(c); kf[3] = kread<T, 3>(c);
  GAP();
  (phaseA_step<T, Js>(c, kf), ...);
}
template <int T> DI void phaseA(Ctx4& c) { phaseA_<T>(c, std::make_integer_sequence<int, 16>{}); }

constexpr int egap(int e) { return 6 + (e * 25) / 32; }
template <int T, int G> DI void sm_slice(Ctx4& c) {
  if constexpr (G < 4) {
    constexpr int qb = G >> 1;
    if constexpr ((G & 1) == 0) {
      float a = max3f(c.s[qb][0], c.s[qb][1], c.s[qb][2]);
      a = max3f(a, c.s[qb][3], c.s[qb][4]);
      a = max3f(a, c.s[qb][5], c.s[qb][6]);
      c.mx[qb] = max3f(a, c.s[qb][7], c.s[qb][8]);
    } else {
      float a = max3f(c.mx[qb], c.s[qb][9], c.s[qb][10]);
      a = max3f(a, c.s[qb][11], c.s[qb][12]);
      a = max3f(a, c.s[qb][13], c.s[qb][14]);
      c.mx[qb] = max2f(a, c.s[qb][15]);
    }
  } else if constexpr (G == 4) {
#pragma unroll
    for (int qb = 0; qb < 2; ++qb) {
      auto rr = __builtin_amdgcn_permlane32_swap(__float_as_uint(c.mx[qb]), __float_as_uint(c.mx[qb]), false, false);
      c.mx[qb] = max2f(__uint_as_float(rr[0]), __uint_as_float(rr[1])) * SCL;
    }
  } else if constexpr (G == 5) {
    if constexpr (T == 0) {
      c.m[0] = c.mx[0]; c.m[1] = c.mx[1];
      c.l[0] = 0.f; c.l[1] = 0.f;
    } else {
      const bool grew = (c.mx[0] - c.m[0] > THR) | (c.mx[1] - c.m[1] > THR);
      if (__builtin_expect(__any((int)grew), 0)) {
#pragma unroll
        for (int qb = 0; qb < 2; ++qb) {
          const float mn = fmaxf(c.m[qb], c.mx[qb]);
          const float al = __builtin_amdgcn_exp2f(c.m[qb] - mn);
          c.m[qb] = mn;
          c.l[qb] *= al;
          c.fct[qb] = al;
        }
        c.pend = true;
      }
    }
    c.nm[0] = -c.m[0]; c.nm[1] = -c.m[1];
    c.rs[0] = 0.f; c.rs[1] = 0.f;
  }
  if constexpr (G >= 6) {
#pragma unroll
    for (int e = 0; e < 32; ++e) {
      if (egap(e) == G) {
        const int qb = e >> 4, i = e & 15;
        c.s[qb][i] = __builtin_amdgcn_exp2f(ffma_s(c.s[qb][i], SCL, c.nm[qb]));
      }
    }
#pragma unroll
    for (int e = 0; e < 32; ++e) {
      const int ge = egap(e);
      if (ge == G - 1 || (G == 31 && ge == 31)) {
        const int qb = e >> 4, i = e & 15;
        c.rs[qb] += c.s[qb][i];
      }
    }
#pragma unroll
    for (int d = 0; d < 16; ++d) {
      const int ge = egap(2 * d + 1);
      if (ge == G - 1 || (G == 31 && ge == 31)) {
        const int qb = d >> 3, i = (2 * d) & 15;
        c.pn[qb][d & 7] = cvtpk(c.s[qb][i], c.s[qb][i + 1]);
      }
    }
  }
}

template <int TV, int S> DI void vread_set(const Ctx4& c, s16x4 (&v)[4]) {
  constexpr int ks = S >> 2, dbp = S & 3;
  constexpr int n = unit_v(TV, ks), slot = n % 5;
  constexpr int db0 = 2 * dbp, db1 = 2 * dbp + 1;
  constexpr int o0 = slot * 8192 + (db0 >> 2) * 256, o1 = slot * 8192 + (db1 >> 2) * 256;
  v[0] = trread_o<o0>(c.vaddr[db0 & 3]);
  v[1] = trread_o<o0 + 4096>(c.vaddr[db0 & 3]);
  v[2] = trread_o<o1>(c.vaddr[db1 & 3]);
  v[3] = trread_o<o1 + 4096>(c.vaddr[db1 & 3]);
}
template <int T, bool HASPV, bool HASSM, int M> DI void phaseB_gap(Ctx4& c, s16x4 (&va)[4], s16x4 (&vb)[4]) {
  constexpr int TV = T - 1;
  constexpr int S = M >> 2, ks = S >> 2, dbp = S & 3, dd = (M >> 1) & 1, qb = M & 1;
  if constexpr (HASPV) {
    if constexpr ((M & 3) == 0) {
      if constexpr (S + 1 < 8) {
        if constexpr (S & 1) asm volatile("s_waitcnt lgkmcnt(4)" : "+v"(vb[0]), "+v"(vb[1]), "+v"(vb[2]), "+v"(vb[3])::"memory");
        else asm volatile("s_waitcnt lgkmcnt(4)" : "+v"(va[0]), "+v"(va[1]), "+v"(va[2]), "+v"(va[3])::"memory");
      } else {
        asm volatile("s_waitcnt lgkmcnt(0)" : "+v"(vb[0]), "+v"(vb[1]), "+v"(vb[2]), "+v"(vb[3])::"memory");
      }
    }
    {
      constexpr int db = 2 * dbp + dd;
      const bf16x8 vf = (S & 1) ? cat8(vb[2 * dd], vb[2 * dd + 1]) : cat8(va[2 * dd], va[2 * dd + 1]);
      pv_mfma<(qb * 8 + db) * 16, (TV == 0 && ks == 0)>(vf, c.pf[qb][ks]);
    }
    if constexpr ((M & 3) == 3 && S + 2 < 8) {
      if constexpr (S + 2 == 4) wait_unit4<unit_v(TV, 1), 3>();
      if constexpr (S & 1) vread_set<TV, S + 2>(c, vb); else vread_set<TV, S + 2>(c, va);
    }
    if constexpr (M >= 16 && M < 24) dma_piece<unit_v(TV, 0) + 5, M - 16>(c);
  }
  if constexpr (HASSM) sm_slice<T, M>(c);
  GAP();
}
template <int T, bool HASPV, bool HASSM, int... Ms> DI void phaseB_(Ctx4& c, std::integer_sequence<int, Ms...>) {
  constexpr int TV = T - 1;
  s16x4 va[4], vb[4];
  if constexpr (HASPV) {
    wait_unit4<unit_v(TV, 0), 3>();
    vread_set<TV, 0>(c, va);
    vread_set<TV, 1>(c, vb);
  }
  if constexpr (HASSM) dma_unit<unit_k(T, 1) + 5>(c);
  if constexpr (HASSM) {
    asm volatile("s_nop 7\n\ts_nop 7" : "+v"(c.s[0]), "+v"(c.s[1]));
  }
  GAP();
  (phaseB_gap<T, HASPV, HASSM, Ms>(c, va, vb), ...);
  if constexpr (HASPV) dma_unit<unit_v(TV, 1) + 5>(c);
  if constexpr (HASSM) {
    c.l[0] += c.rs[0]; c.l[1] += c.rs[1];
#pragma unroll
    for (int qb = 0; qb < 2; ++qb)
#pragma unroll
      for (int ks = 0; ks < 2; ++ks) {
        u32x4 wv = {c.pn[qb][4 * ks], c.pn[qb][4 * ks + 1], c.pn[qb][4 * ks + 2], c.pn[qb][4 * ks + 3]};
        c.pf[qb][ks] = __builtin_bit_cast(bf16x8, wv);
      }
  }
  if constexpr (HASPV && HASSM) {
    if (__builtin_expect(c.pend, 0)) {
      float t0, t1;
      asm volatile("s_nop 7\n\ts_nop 7\n\t"
                   "v_accvgpr_read_b32 %0, a0\n\tv_mul_f32 %0, %2, %0\n\tv_accvgpr_write_b32 a0, %0\n\t"
                   "v_accvgpr_read_b32 %1, a1\n\tv_mul_f32 %1, %2, %1\n\tv_accvgpr_write_b32 a1, %1\n\t"
                   "v_accvgpr_read_b32 %0, a2\n\tv_mul_f32 %0, %2, %0\n\tv_accvgpr_write_b32 a2, %0\n\t"
                   "v_accvgpr_read_b32 %1, a3\n\tv_mul_f32 %1, %2, %1\n\tv_accvgpr_write_b32 a3, %1\n\t"
                   "v_accvgpr_read_b32 %0, a4\n\tv_mul_f32 %0, %2, %0\n\tv_accvgpr_write_b32 a4, %0\n\t"
                   "v_accvgpr_read_b32 %1, a5\n\tv_mul_f32 %1, %2, %1\n\tv_accvgpr_write_b32 a5, %1\n\t"
                   "v_accvgpr_read_b32 %0, a6\n\tv_mul_f32 %0, %2, %0\n\tv_accvgpr_write_b32 a6, %0\n\t"
                   "v_accvgpr_read_b32 %1, a7\n\tv_mul_f32 %1, %2, %1\n\tv_accvgpr_write_b32 a7, %1\n\t"
                   "v_accvgpr_read_b32 %0, a8\n\tv_mul_f32 %0, %2, %0\n\tv_accvgpr_write_b32 a8, %0\n\t"
                   "v_accvgpr_read_b32 %1, a9\n\tv_mul_f32 %1, %2, %1\n\tv_accvgpr_write_b32 a9, %1\n\t"
                   "v_accvgpr_read_b32 %0, a10\n\tv_mul_f32 %0, %2, %0\n\tv_accvgpr_write_b32 a10, %0\n\t"
                   "v_accvgpr_read_b32 %1, a11\n\tv_mul_f32 %1, %2, %1\n\tv_accvgpr_write_b32 a11, %1\n\t"
                   "v_accvgpr_read_b32 %0, a12\n\tv_mul_f32 %0, %2, %0\n\tv_accvgpr_write_b32 a12, %0\n\t"
                   "v_accvgpr_read_b32 %1, a13\n\tv_mul_f32 %1, %2, %1\n\tv_accvgpr_write_b32 a13, %1\n\t"
                   "v_accvgpr_read_b32 %0, a14\n\tv_mul_f32 %0, %2, %0\n\tv_accvgpr_write_b32 a14, %0\n\t"
                   "v_accvgpr_read_b32 %1, a15\n\tv_mul_f32 %1, %2, %1\n\tv_accvgpr_write_b32 a15, %1\n\t"
                   "v_accvgpr_read_b32 %0, a16\n\tv_mul_f32 %0, %2, %0\n\tv_accvgpr_write_b32 a16, %0\n\t"
                   "v_accvgpr_read_b32 %1, a17\n\tv_mul_f32 %1, %2, %1\n\tv_accvgpr_write_b32 a17, %1\n\t"
                   "v_accvgpr_read_b32 %0, a18\n\tv_mul_f32 %0, %2, %0\n\tv_accvgpr_write_b32 a18, %0\n\t"
                   "v_accvgpr_read_b32 %1, a19\n\tv_mul_f32 %1, %2, %1\n\tv_accvgpr_write_b32 a19, %1\n\t"
                   "v_accvgpr_read_b32 %0, a20\n\tv_mul_f32 %0, %2, %0\n\tv_accvgpr_write_b32 a20, %0\n\t"
                   "v_accvgpr_read_b32 %1, a21\n\tv_mul_f32 %1, %2, %1\n\tv_accvgpr_write_b32 a21, %1\n\t"
                   "v_accvgpr_read_b32 %0, a22\n\tv_mul_f32 %0, %2, %0\n\tv_accvgpr_write_b32 a22, %0\n\t"
                   "v_accvgpr_read_b32 %1, a23\n\tv_mul_f32 %1, %2, %1\n\tv_accvgpr_write_b32 a23, %1\n\t"
                   "v_accvgpr_read_b32 %0, a24\n\tv_mul_f32 %0, %2, %0\n\tv_accvgpr_write_b32 a24, %0\n\t"
                   "v_accvgpr_read_b32 %1, a25\n\tv_mul_f32 %1, %2, %1\n\tv_accvgpr_write_b32 a25, %1\n\t"
                   "v_accvgpr_read_b32 %0, a26\n\tv_mul_f32 %0, %2, %0\n\tv_accvgpr_write_b32 a26, %0\n\t"
                   "v_accvgpr_read_b32 %1, a27\n\tv_mul_f32 %1, %2, %1\n\tv_accvgpr_write_b32 a27, %1\n\t"
                   "v_accvgpr_read_b32 %0, a28\n\tv_mul_f32 %0, %2, %0\n\tv_accvgpr_write_b32 a28, %0\n\t"
                   "v_accvgpr_read_b32 %1, a29\n\tv_mul_f32 %1, %2, %1\n\tv_accvgpr_write_b32 a29, %1\n\t"
                   "v_accvgpr_read_b32 %0, a30\n\tv_mul_f32 %0, %2, %0\n\tv_accvgpr_write_b32 a30, %0\n\t"
                   "v_accvgpr_read_b32 %1, a31\n\tv_mul_f32 %1, %2, %1\n\tv_accvgpr_write_b32 a31, %1\n\t"
                   "v_accvgpr_read_b32 %0, a32\n\tv_mul_f32 %0, %2, %0\n\tv_accvgpr_write_b32 a32, %0\n\t"
                   "v_accvgpr_read_b32 %1, a33\n\tv_mul_f32 %1, %2, %1\n\tv_accvgpr_write_b32 a33, %1\n\t"
                   "v_accvgpr_read_b32 %0, a34\n\tv_mul_f32 %0, %2, %0\n\tv_accvgpr_write_b32 a34, %0\n\t"
                   "v_accvgpr_read_b32 %1, a35\n\tv_mul_f32 %1, %2, %1\n\tv_accvgpr_write_b32 a35, %1\n\t"
                   "v_accvgpr_read_b32 %0, a36\n\tv_mul_f32 %0, %2, %0\n\tv_accvgpr_write_b32 a36, %0\n\t"
                   "v_accvgpr_read_b32 %1, a37\n\tv_mul_f32 %1, %2, %1\n\tv_accvgpr_write_b32 a37, %1\n\t"
                   "v_accvgpr_read_b32 %0, a38\n\tv_mul_f32 %0, %2, %0\n\tv_accvgpr_write_b32 a38, %0\n\t"
                   "v_accvgpr_read_b32 %1, a39\n\tv_mul_f32 %1, %2, %1\n\tv_accvgpr_write_b32 a39, %1\n\t"
                   "v_accvgpr_read_b32 %0, a40\n\tv_mul_f32 %0, %2, %0\n\tv_accvgpr_write_b32 a40, %0\n\t"
                   "v_accvgpr_read_b32 %1, a41\n\tv_mul_f32 %1, %2, %1\n\tv_accvgpr_write_b32 a41, %1\n\t"
                   "v_accvgpr_read_b32 %0, a42\n\tv_mul_f32 %0, %2, %0\n\tv_accvgpr_write_b32 a42, %0\n\t"
                   "v_accvgpr_read_b32 %1, a43\n\tv_mul_f32 %1, %2, %1\n\tv_accvgpr_write_b32 a43, %1\n\t"
                   "v_accvgpr_read_b32 %0, a44\n\tv_mul_f32 %0, %2, %0\n\tv_accvgpr_write_b32 a44, %0\n\t"
                   "v_accvgpr_read_b32 %1, a45\n\tv_mul_f32 %1, %2, %1\n\tv_accvgpr_write_b32 a45, %1\n\t"
                   "v_accvgpr_read_b32 %0, a46\n\tv_mul_f32 %0, %2, %0\n\tv_accvgpr_write_b32 a46, %0\n\t"
                   "v_accvgpr_read_b32 %1, a47\n\tv_mul_f32 %1, %2, %1\n\tv_accvgpr_write_b32 a47, %1\n\t"
                   "v_accvgpr_read_b32 %0, a48\n\tv_mul_f32 %0, %2, %0\n\tv_accvgpr_write_b32 a48, %0\n\t"
                   "v_accvgpr_read_b32 %1, a49\n\tv_mul_f32 %1, %2, %1\n\tv_accvgpr_write_b32 a49, %1\n\t"
                   "v_accvgpr_read_b32 %0, a50\n\tv_mul_f32 %0, %2, %0\n\tv_accvgpr_write_b32 a50, %0\n\t"
                   "v_accvgpr_read_b32 %1, a51\n\tv_mul_f32 %1, %2, %1\n\tv_accvgpr_write_b32 a51, %1\n\t"
                   "v_accvgpr_read_b32 %0, a52\n\tv_mul_f32 %0, %2, %0\n\tv_accvgpr_write_b32 a52, %0\n\t"
                   "v_accvgpr_read_b32 %1, a53\n\tv_mul_f32 %1, %2, %1\n\tv_accvgpr_write_b32 a53, %1\n\t"
                   "v_accvgpr_read_b32 %0, a54\n\tv_mul_f32 %0, %2, %0\n\tv_accvgpr_write_b32 a54, %0\n\t"
                   "v_accvgpr_read_b32 %1, a55\n\tv_mul_f32 %1, %2, %1\n\tv_accvgpr_write_b32 a55, %1\n\t"
                   "v_accvgpr_read_b32 %0, a56\n\tv_mul_f32 %0, %2, %0\n\tv_accvgpr_write_b32 a56, %0\n\t"
                   "v_accvgpr_read_b32 %1, a57\n\tv_mul_f32 %1, %2, %1\n\tv_accvgpr_write_b32 a57, %1\n\t"
                   "v_accvgpr_read_b32 %0, a58\n\tv_mul_f32 %0, %2, %0\n\tv_accvgpr_write_b32 a58, %0\n\t"
                   "v_accvgpr_read_b32 %1, a59\n\tv_mul_f32 %1, %2, %1\n\tv_accvgpr_write_b32 a59, %1\n\t"
                   "v_accvgpr_read_b32 %0, a60\n\tv_mul_f32 %0, %2, %0\n\tv_accvgpr_write_b32 a60, %0\n\t"
                   "v_accvgpr_read_b32 %1, a61\n\tv_mul_f32 %1, %2, %1\n\tv_accvgpr_write_b32 a61, %1\n\t"
                   "v_accvgpr_read_b32 %0, a62\n\tv_mul_f32 %0, %2, %0\n\tv_accvgpr_write_b32 a62, %0\n\t"
                   "v_accvgpr_read_b32 %1, a63\n\tv_mul_f32 %1, %2, %1\n\tv_accvgpr_write_b32 a63, %1\n\t"
                   "v_accvgpr_read_b32 %0, a64\n\tv_mul_f32 %0, %2, %0\n\tv_accvgpr_write_b32 a64, %0\n\t"
                   "v_accvgpr_read_b32 %1, a65\n\tv_mul_f32 %1, %2, %1\n\tv_accvgpr_write_b32 a65, %1\n\t"
                   "v_accvgpr_read_b32 %0, a66\n\tv_mul_f32 %0, %2, %0\n\tv_accvgpr_write_b32 a66, %0\n\t"
                   "v_accvgpr_read_b32 %1, a67\n\tv_mul_f32 %1, %2, %1\n\tv_accvgpr_write_b32 a67, %1\n\t"
                   "v_accvgpr_read_b32 %0, a68\n\tv_mul_f32 %0, %2, %0\n\tv_accvgpr_write_b32 a68, %0\n\t"
                   "v_accvgpr_read_b32 %1, a69\n\tv_mul_f32 %1, %2, %1\n\tv_accvgpr_write_b32 a69, %1\n\t"
                   "v_accvgpr_read_b32 %0, a70\n\tv_mul_f32 %0, %2, %0\n\tv_accvgpr_write_b32 a70, %0\n\t"
                   "v_accvgpr_read_b32 %1, a71\n\tv_mul_f32 %1, %2, %1\n\tv_accvgpr_write_b32 a71, %1\n\t"
                   "v_accvgpr_read_b32 %0, a72\n\tv_mul_f32 %0, %2, %0\n\tv_accvgpr_write_b32 a72, %0\n\t"
                   "v_accvgpr_read_b32 %1, a73\n\tv_mul_f32 %1, %2, %1\n\tv_accvgpr_write_b32 a73, %1\n\t"
                   "v_accvgpr_read_b32 %0, a74\n\tv_mul_f32 %0, %2, %0\n\tv_accvgpr_write_b32 a74, %0\n\t"
                   "v_accvgpr_read_b32 %1, a75\n\tv_mul_f32 %1, %2, %1\n\tv_accvgpr_write_b32 a75, %1\n\t"
                   "v_accvgpr_read_b32 %0, a76\n\tv_mul_f32 %0, %2, %0\n\tv_accvgpr_write_b32 a76, %0\n\t"
                   "v_accvgpr_read_b32 %1, a77\n\tv_mul_f32 %1, %2, %1\n\tv_accvgpr_write_b32 a77, %1\n\t"
                   "v_accvgpr_read_b32 %0, a78\n\tv_mul_f32 %0, %2, %0\n\tv_accvgpr_write_b32 a78, %0\n\t"
                   "v_accvgpr_read_b32 %1, a79\n\tv_mul_f32 %1, %2, %1\n\tv_accvgpr_write_b32 a79, %1\n\t"
                   "v_accvgpr_read_b32 %0, a80\n\tv_mul_f32 %0, %2, %0\n\tv_accvgpr_write_b32 a80, %0\n\t"
                   "v_accvgpr_read_b32 %1, a81\n\tv_mul_f32 %1, %2, %1\n\tv_accvgpr_write_b32 a81, %1\n\t"
                   "v_accvgpr_read_b32 %0, a82\n\tv_mul_f32 %0, %2, %0\n\tv_accvgpr_write_b32 a82, %0\n\t"
                   "v_accvgpr_read_b32 %1, a83\n\tv_mul_f32 %1, %2, %1\n\tv_accvgpr_write_b32 a83, %1\n\t"
                   "v_accvgpr_read_b32 %0, a84\n\tv_mul_f32 %0, %2, %0\n\tv_accvgpr_write_b32 a84, %0\n\t"
                   "v_accvgpr_read_b32 %1, a85\n\tv_mul_f32 %1, %2, %1\n\tv_accvgpr_write_b32 a85, %1\n\t"
                   "v_accvgpr_read_b32 %0, a86\n\tv_mul_f32 %0, %2, %0\n\tv_accvgpr_write_b32 a86, %0\n\t"
                   "v_accvgpr_read_b32 %1, a87\n\tv_mul_f32 %1, %2, %1\n\tv_accvgpr_write_b32 a87, %1\n\t"
                   "v_accvgpr_read_b32 %0, a88\n\tv_mul_f32 %0, %2, %0\n\tv_accvgpr_write_b32 a88, %0\n\t"
                   "v_accvgpr_read_b32 %1, a89\n\tv_mul_f32 %1, %2, %1\n\tv_accvgpr_write_b32 a89, %1\n\t"
                   "v_accvgpr_read_b32 %0, a90\n\tv_mul_f32 %0, %2, %0\n\tv_accvgpr_write_b32 a90, %0\n\t"
                   "v_accvgpr_read_b32 %1, a91\n\tv_mul_f32 %1, %2, %1\n\tv_accvgpr_write_b32 a91, %1\n\t"
                   "v_accvgpr_read_b32 %0, a92\n\tv_mul_f32 %0, %2, %0\n\tv_accvgpr_write_b32 a92, %0\n\t"
                   "v_accvgpr_read_b32 %1, a93\n\tv_mul_f32 %1, %2, %1\n\tv_accvgpr_write_b32 a93, %1\n\t"
                   "v_accvgpr_read_b32 %0, a94\n\tv_mul_f32 %0, %2, %0\n\tv_accvgpr_write_b32 a94, %0\n\t"
                   "v_accvgpr_read_b32 %1, a95\n\tv_mul_f32 %1, %2, %1\n\tv_accvgpr_write_b32 a95, %1\n\t"
                   "v_accvgpr_read_b32 %0, a96\n\tv_mul_f32 %0, %2, %0\n\tv_accvgpr_write_b32 a96, %0\n\t"
                   "v_accvgpr_read_b32 %1, a97\n\tv_mul_f32 %1, %2, %1\n\tv_accvgpr_write_b32 a97, %1\n\t"
                   "v_accvgpr_read_b32 %0, a98\n\tv_mul_f32 %0, %2, %0\n\tv_accvgpr_write_b32 a98, %0\n\t"
                   "v_accvgpr_read_b32 %1, a99\n\tv_mul_f32 %1, %2, %1\n\tv_accvgpr_write_b32 a99, %1\n\t"
                   "v_accvgpr_read_b32 %0, a100\n\tv_mul_f32 %0, %2, %0\n\tv_accvgpr_write_b32 a100, %0\n\t"
                   "v_accvgpr_read_b32 %1, a101\n\tv_mul_f32 %1, %2, %1\n\tv_accvgpr_write_b32 a101, %1\n\t"
                   "v_accvgpr_read_b32 %0, a102\n\tv_mul_f32 %0, %2, %0\n\tv_accvgpr_write_b32 a102, %0\n\t"
                   "v_accvgpr_read_b32 %1, a103\n\tv_mul_f32 %1, %2, %1\n\tv_accvgpr_write_b32 a103, %1\n\t"
                   "v_accvgpr_read_b32 %0, a104\n\tv_mul_f32 %0, %2, %0\n\tv_accvgpr_write_b32 a104, %0\n\t"
                   "v_accvgpr_read_b32 %1, a105\n\tv_mul_f32 %1, %2, %1\n\tv_accvgpr_write_b32 a105, %1\n\t"
                   "v_accvgpr_read_b32 %0, a106\n\tv_mul_f32 %0, %2, %0\n\tv_accvgpr_write_b32 a106, %0\n\t"
                   "v_accvgpr_read_b32 %1, a107\n\tv_mul_f32 %1, %2, %1\n\tv_accvgpr_write_b32 a107, %1\n\t"
                   "v_accvgpr_read_b32 %0, a108\n\tv_mul_f32 %0, %2, %0\n\tv_accvgpr_write_b32 a108, %0\n\t"
                   "v_accvgpr_read_b32 %1, a109\n\tv_mul_f32 %1, %2, %1\n\tv_accvgpr_write_b32 a109, %1\n\t"
                   "v_accvgpr_read_b32 %0, a110\n\tv_mul_f32 %0, %2, %0\n\tv_accvgpr_write_b32 a110, %0\n\t"
                   "v_accvgpr_read_b32 %1, a111\n\tv_mul_f32 %1, %2, %1\n\tv_accvgpr_write_b32 a111, %1\n\t"
                   "v_accvgpr_read_b32 %0, a112\n\tv_mul_f32 %0, %2, %0\n\tv_accvgpr_write_b32 a112, %0\n\t"
                   "v_accvgpr_read_b32 %1, a113\n\tv_mul_f32 %1, %2, %1\n\tv_accvgpr_write_b32 a113, %1\n\t"
                   "v_accvgpr_read_b32 %0, a114\n\tv_mul_f32 %0, %2, %0\n\tv_accvgpr_write_b32 a114, %0\n\t"
                   "v_accvgpr_read_b32 %1, a115\n\tv_mul_f32 %1, %2, %1\n\tv_accvgpr_write_b32 a115, %1\n\t"
                   "v_accvgpr_read_b32 %0, a116\n\tv_mul_f32 %0, %2, %0\n\tv_accvgpr_write_b32 a116, %0\n\t"
                   "v_accvgpr_read_b32 %1, a117\n\tv_mul_f32 %1, %2, %1\n\tv_accvgpr_write_b32 a117, %1\n\t"
                   "v_accvgpr_read_b32 %0, a118\n\tv_mul_f32 %0, %2, %0\n\tv_accvgpr_write_b32 a118, %0\n\t"
                   "v_accvgpr_read_b32 %1, a119\n\tv_mul_f32 %1, %2, %1\n\tv_accvgpr_write_b32 a119, %1\n\t"
                   "v_accvgpr_read_b32 %0, a120\n\tv_mul_f32 %0, %2, %0\n\tv_accvgpr_write_b32 a120, %0\n\t"
                   "v_accvgpr_read_b32 %1, a121\n\tv_mul_f32 %1, %2, %1\n\tv_accvgpr_write_b32 a121, %1\n\t"
                   "v_accvgpr_read_b32 %0, a122\n\tv_mul_f32 %0, %2, %0\n\tv_accvgpr_write_b32 a122, %0\n\t"
                   "v_accvgpr_read_b32 %1, a123\n\tv_mul_f32 %1, %2, %1\n\tv_accvgpr_write_b32 a123, %1\n\t"
                   "v_accvgpr_read_b32 %0, a124\n\tv_mul_f32 %0, %2, %0\n\tv_accvgpr_write_b32 a124, %0\n\t"
                   "v_accvgpr_read_b32 %1, a125\n\tv_mul_f32 %1, %2, %1\n\tv_accvgpr_write_b32 a125, %1\n\t"
                   "v_accvgpr_read_b32 %0, a126\n\tv_mul_f32 %0, %2, %0\n\tv_accvgpr_write_b32 a126, %0\n\t"
                   "v_accvgpr_read_b32 %1, a127\n\tv_mul_f32 %1, %2, %1\n\tv_accvgpr_write_b32 a127, %1\n\t"
                   "v_accvgpr_read_b32 %0, a128\n\tv_mul_f32 %0, %3, %0\n\tv_accvgpr_write_b32 a128, %0\n\t"
                   "v_accvgpr_read_b32 %1, a129\n\tv_mul_f32 %1, %3, %1\n\tv_accvgpr_write_b32 a129, %1\n\t"
                   "v_accvgpr_read_b32 %0, a130\n\tv_mul_f32 %0, %3, %0\n\tv_accvgpr_write_b32 a130, %0\n\t"
                   "v_accvgpr_read_b32 %1, a131\n\tv_mul_f32 %1, %3, %1\n\tv_accvgpr_write_b32 a131, %1\n\t"
                   "v_accvgpr_read_b32 %0, a132\n\tv_mul_f32 %0, %3, %0\n\tv_accvgpr_write_b32 a132, %0\n\t"
                   "v_accvgpr_read_b32 %1, a133\n\tv_mul_f32 %1, %3, %1\n\tv_accvgpr_write_b32 a133, %1\n\t"
                   "v_accvgpr_read_b32 %0, a134\n\tv_mul_f32 %0, %3, %0\n\tv_accvgpr_write_b32 a134, %0\n\t"
                   "v_accvgpr_read_b32 %1, a135\n\tv_mul_f32 %1, %3, %1\n\tv_accvgpr_write_b32 a135, %1\n\t"
                   "v_accvgpr_read_b32 %0, a136\n\tv_mul_f32 %0, %3, %0\n\tv_accvgpr_write_b32 a136, %0\n\t"
                   "v_accvgpr_read_b32 %1, a137\n\tv_mul_f32 %1, %3, %1\n\tv_accvgpr_write_b32 a137, %1\n\t"
                   "v_accvgpr_read_b32 %0, a138\n\tv_mul_f32 %0, %3, %0\n\tv_accvgpr_write_b32 a138, %0\n\t"
                   "v_accvgpr_read_b32 %1, a139\n\tv_mul_f32 %1, %3, %1\n\tv_accvgpr_write_b32 a139, %1\n\t"
                   "v_accvgpr_read_b32 %0, a140\n\tv_mul_f32 %0, %3, %0\n\tv_accvgpr_write_b32 a140, %0\n\t"
                   "v_accvgpr_read_b32 %1, a141\n\tv_mul_f32 %1, %3, %1\n\tv_accvgpr_write_b32 a141, %1\n\t"
                   "v_accvgpr_read_b32 %0, a142\n\tv_mul_f32 %0, %3, %0\n\tv_accvgpr_write_b32 a142, %0\n\t"
                   "v_accvgpr_read_b32 %1, a143\n\tv_mul_f32 %1, %3, %1\n\tv_accvgpr_write_b32 a143, %1\n\t"
                   "v_accvgpr_read_b32 %0, a144\n\tv_mul_f32 %0, %3, %0\n\tv_accvgpr_write_b32 a144, %0\n\t"
                   "v_accvgpr_read_b32 %1, a145\n\tv_mul_f32 %1, %3, %1\n\tv_accvgpr_write_b32 a145, %1\n\t"
                   "v_accvgpr_read_b32 %0, a146\n\tv_mul_f32 %0, %3, %0\n\tv_accvgpr_write_b32 a146, %0\n\t"
                   "v_accvgpr_read_b32 %1, a147\n\tv_mul_f32 %1, %3, %1\n\tv_accvgpr_write_b32 a147, %1\n\t"
                   "v_accvgpr_read_b32 %0, a148\n\tv_mul_f32 %0, %3, %0\n\tv_accvgpr_write_b32 a148, %0\n\t"
                   "v_accvgpr_read_b32 %1, a149\n\tv_mul_f32 %1, %3, %1\n\tv_accvgpr_write_b32 a149, %1\n\t"
                   "v_accvgpr_read_b32 %0, a150\n\tv_mul_f32 %0, %3, %0\n\tv_accvgpr_write_b32 a150, %0\n\t"
                   "v_accvgpr_read_b32 %1, a151\n\tv_mul_f32 %1, %3, %1\n\tv_accvgpr_write_b32 a151, %1\n\t"
                   "v_accvgpr_read_b32 %0, a152\n\tv_mul_f32 %0, %3, %0\n\tv_accvgpr_write_b32 a152, %0\n\t"
                   "v_accvgpr_read_b32 %1, a153\n\tv_mul_f32 %1, %3, %1\n\tv_accvgpr_write_b32 a153, %1\n\t"
                   "v_accvgpr_read_b32 %0, a154\n\tv_mul_f32 %0, %3, %0\n\tv_accvgpr_write_b32 a154, %0\n\t"
                   "v_accvgpr_read_b32 %1, a155\n\tv_mul_f32 %1, %3, %1\n\tv_accvgpr_write_b32 a155, %1\n\t"
                   "v_accvgpr_read_b32 %0, a156\n\tv_mul_f32 %0, %3, %0\n\tv_accvgpr_write_b32 a156, %0\n\t"
                   "v_accvgpr_read_b32 %1, a157\n\tv_mul_f32 %1, %3, %1\n\tv_accvgpr_write_b32 a157, %1\n\t"
                   "v_accvgpr_read_b32 %0, a158\n\tv_mul_f32 %0, %3, %0\n\tv_accvgpr_write_b32 a158, %0\n\t"
                   "v_accvgpr_read_b32 %1, a159\n\tv_mul_f32 %1, %3, %1\n\tv_accvgpr_write_b32 a159, %1\n\t"
                   "v_accvgpr_read_b32 %0, a160\n\tv_mul_f32 %0, %3, %0\n\tv_accvgpr_write_b32 a160, %0\n\t"
                   "v_accvgpr_read_b32 %1, a161\n\tv_mul_f32 %1, %3, %1\n\tv_accvgpr_write_b32 a161, %1\n\t"
                   "v_accvgpr_read_b32 %0, a162\n\tv_mul_f32 %0, %3, %0\n\tv_accvgpr_write_b32 a162, %0\n\t"
                   "v_accvgpr_read_b32 %1, a163\n\tv_mul_f32 %1, %3, %1\n\tv_accvgpr_write_b32 a163, %1\n\t"
                   "v_accvgpr_read_b32 %0, a164\n\tv_mul_f32 %0, %3, %0\n\tv_accvgpr_write_b32 a164, %0\n\t"
                   "v_accvgpr_read_b32 %1, a165\n\tv_mul_f32 %1, %3, %1\n\tv_accvgpr_write_b32 a165, %1\n\t"
                   "v_accvgpr_read_b32 %0, a166\n\tv_mul_f32 %0, %3, %0\n\tv_accvgpr_write_b32 a166, %0\n\t"
                   "v_accvgpr_read_b32 %1, a167\n\tv_mul_f32 %1, %3, %1\n\tv_accvgpr_write_b32 a167, %1\n\t"
                   "v_accvgpr_read_b32 %0, a168\n\tv_mul_f32 %0, %3, %0\n\tv_accvgpr_write_b32 a168, %0\n\t"
                   "v_accvgpr_read_b32 %1, a169\n\tv_mul_f32 %1, %3, %1\n\tv_accvgpr_write_b32 a169, %1\n\t"
                   "v_accvgpr_read_b32 %0, a170\n\tv_mul_f32 %0, %3, %0\n\tv_accvgpr_write_b32 a170, %0\n\t"
                   "v_accvgpr_read_b32 %1, a171\n\tv_mul_f32 %1, %3, %1\n\tv_accvgpr_write_b32 a171, %1\n\t"
                   "v_accvgpr_read_b32 %0, a172\n\tv_mul_f32 %0, %3, %0\n\tv_accvgpr_write_b32 a172, %0\n\t"
                   "v_accvgpr_read_b32 %1, a173\n\tv_mul_f32 %1, %3, %1\n\tv_accvgpr_write_b32 a173, %1\n\t"
                   "v_accvgpr_read_b32 %0, a174\n\tv_mul_f32 %0, %3, %0\n\tv_accvgpr_write_b32 a174, %0\n\t"
                   "v_accvgpr_read_b32 %1, a175\n\tv_mul_f32 %1, %3, %1\n\tv_accvgpr_write_b32 a175, %1\n\t"
                   "v_accvgpr_read_b32 %0, a176\n\tv_mul_f32 %0, %3, %0\n\tv_accvgpr_write_b32 a176, %0\n\t"
                   "v_accvgpr_read_b32 %1, a177\n\tv_mul_f32 %1, %3, %1\n\tv_accvgpr_write_b32 a177, %1\n\t"
                   "v_accvgpr_read_b32 %0, a178\n\tv_mul_f32 %0, %3, %0\n\tv_accvgpr_write_b32 a178, %0\n\t"
                   "v_accvgpr_read_b32 %1, a179\n\tv_mul_f32 %1, %3, %1\n\tv_accvgpr_write_b32 a179, %1\n\t"
                   "v_accvgpr_read_b32 %0, a180\n\tv_mul_f32 %0, %3, %0\n\tv_accvgpr_write_b32 a180, %0\n\t"
                   "v_accvgpr_read_b32 %1, a181\n\tv_mul_f32 %1, %3, %1\n\tv_accvgpr_write_b32 a181, %1\n\t"
                   "v_accvgpr_read_b32 %0, a182\n\tv_mul_f32 %0, %3, %0\n\tv_accvgpr_write_b32 a182, %0\n\t"
                   "v_accvgpr_read_b32 %1, a183\n\tv_mul_f32 %1, %3, %1\n\tv_accvgpr_write_b32 a183, %1\n\t"
                   "v_accvgpr_read_b32 %0, a184\n\tv_mul_f32 %0, %3, %0\n\tv_accvgpr_write_b32 a184, %0\n\t"
                   "v_accvgpr_read_b32 %1, a185\n\tv_mul_f32 %1, %3, %1\n\tv_accvgpr_write_b32 a185, %1\n\t"
                   "v_accvgpr_read_b32 %0, a186\n\tv_mul_f32 %0, %3, %0\n\tv_accvgpr_write_b32 a186, %0\n\t"
                   "v_accvgpr_read_b32 %1, a187\n\tv_mul_f32 %1, %3, %1\n\tv_accvgpr_write_b32 a187, %1\n\t"
                   "v_accvgpr_read_b32 %0, a188\n\tv_mul_f32 %0, %3, %0\n\tv_accvgpr_write_b32 a188, %0\n\t"
                   "v_accvgpr_read_b32 %1, a189\n\tv_mul_f32 %1, %3, %1\n\tv_accvgpr_write_b32 a189, %1\n\t"
                   "v_accvgpr_read_b32 %0, a190\n\tv_mul_f32 %0, %3, %0\n\tv_accvgpr_write_b32 a190, %0\n\t"
                   "v_accvgpr_read_b32 %1, a191\n\tv_mul_f32 %1, %3, %1\n\tv_accvgpr_write_b32 a191, %1\n\t"
                   "v_accvgpr_read_b32 %0, a192\n\tv_mul_f32 %0, %3, %0\n\tv_accvgpr_write_b32 a192, %0\n\t"
                   "v_accvgpr_read_b32 %1, a193\n\tv_mul_f32 %1, %3, %1\n\tv_accvgpr_write_b32 a193, %1\n\t"
                   "v_accvgpr_read_b32 %0, a194\n\tv_mul_f32 %0, %3, %0\n\tv_accvgpr_write_b32 a194, %0\n\t"
                   "v_accvgpr_read_b32 %1, a195\n\tv_mul_f32 %1, %3, %1\n\tv_accvgpr_write_b32 a195, %1\n\t"
                   "v_accvgpr_read_b32 %0, a196\n\tv_mul_f32 %0, %3, %0\n\tv_accvgpr_write_b32 a196, %0\n\t"
                   "v_accvgpr_read_b32 %1, a197\n\tv_mul_f32 %1, %3, %1\n\tv_accvgpr_write_b32 a197, %1\n\t"
                   "v_accvgpr_read_b32 %0, a198\n\tv_mul_f32 %0, %3, %0\n\tv_accvgpr_write_b32 a198, %0\n\t"
                   "v_accvgpr_read_b32 %1, a199\n\tv_mul_f32 %1, %3, %1\n\tv_accvgpr_write_b32 a199, %1\n\t"
                   "v_accvgpr_read_b32 %0, a200\n\tv_mul_f32 %0, %3, %0\n\tv_accvgpr_write_b32 a200, %0\n\t"
                   "v_accvgpr_read_b32 %1, a201\n\tv_mul_f32 %1, %3, %1\n\tv_accvgpr_write_b32 a201, %1\n\t"
                   "v_accvgpr_read_b32 %0, a202\n\tv_mul_f32 %0, %3, %0\n\tv_accvgpr_write_b32 a202, %0\n\t"
                   "v_accvgpr_read_b32 %1, a203\n\tv_mul_f32 %1, %3, %1\n\tv_accvgpr_write_b32 a203, %1\n\t"
                   "v_accvgpr_read_b32 %0, a204\n\tv_mul_f32 %0, %3, %0\n\tv_accvgpr_write_b32 a204, %0\n\t"
                   "v_accvgpr_read_b32 %1, a205\n\tv_mul_f32 %1, %3, %1\n\tv_accvgpr_write_b32 a205, %1\n\t"
                   "v_accvgpr_read_b32 %0, a206\n\tv_mul_f32 %0, %3, %0\n\tv_accvgpr_write_b32 a206, %0\n\t"
                   "v_accvgpr_read_b32 %1, a207\n\tv_mul_f32 %1, %3, %1\n\tv_accvgpr_write_b32 a207, %1\n\t"
                   "v_accvgpr_read_b32 %0, a208\n\tv_mul_f32 %0, %3, %0\n\tv_accvgpr_write_b32 a208, %0\n\t"
                   "v_accvgpr_read_b32 %1, a209\n\tv_mul_f32 %1, %3, %1\n\tv_accvgpr_write_b32 a209, %1\n\t"
                   "v_accvgpr_read_b32 %0, a210\n\tv_mul_f32 %0, %3, %0\n\tv_accvgpr_write_b32 a210, %0\n\t"
                   "v_accvgpr_read_b32 %1, a211\n\tv_mul_f32 %1, %3, %1\n\tv_accvgpr_write_b32 a211, %1\n\t"
                   "v_accvgpr_read_b32 %0, a212\n\tv_mul_f32 %0, %3, %0\n\tv_accvgpr_write_b32 a212, %0\n\t"
                   "v_accvgpr_read_b32 %1, a213\n\tv_mul_f32 %1, %3, %1\n\tv_accvgpr_write_b32 a213, %1\n\t"
                   "v_accvgpr_read_b32 %0, a214\n\tv_mul_f32 %0, %3, %0\n\tv_accvgpr_write_b32 a214, %0\n\t"
                   "v_accvgpr_read_b32 %1, a215\n\tv_mul_f32 %1, %3, %1\n\tv_accvgpr_write_b32 a215, %1\n\t"
                   "v_accvgpr_read_b32 %0, a216\n\tv_mul_f32 %0, %3, %0\n\tv_accvgpr_write_b32 a216, %0\n\t"
                   "v_accvgpr_read_b32 %1, a217\n\tv_mul_f32 %1, %3, %1\n\tv_accvgpr_write_b32 a217, %1\n\t"
                   "v_accvgpr_read_b32 %0, a218\n\tv_mul_f32 %0, %3, %0\n\tv_accvgpr_write_b32 a218, %0\n\t"
                   "v_accvgpr_read_b32 %1, a219\n\tv_mul_f32 %1, %3, %1\n\tv_accvgpr_write_b32 a219, %1\n\t"
                   "v_accvgpr_read_b32 %0, a220\n\tv_mul_f32 %0, %3, %0\n\tv_accvgpr_write_b32 a220, %0\n\t"
                   "v_accvgpr_read_b32 %1, a221\n\tv_mul_f32 %1, %3, %1\n\tv_accvgpr_write_b32 a221, %1\n\t"
                   "v_accvgpr_read_b32 %0, a222\n\tv_mul_f32 %0, %3, %0\n\tv_accvgpr_write_b32 a222, %0\n\t"
                   "v_accvgpr_read_b32 %1, a223\n\tv_mul_f32 %1, %3, %1\n\tv_accvgpr_write_b32 a223, %1\n\t"
                   "v_accvgpr_read_b32 %0, a224\n\tv_mul_f32 %0, %3, %0\n\tv_accvgpr_write_b32 a224, %0\n\t"
                   "v_accvgpr_read_b32 %1, a225\n\tv_mul_f32 %1, %3, %1\n\tv_accvgpr_write_b32 a225, %1\n\t"
                   "v_accvgpr_read_b32 %0, a226\n\tv_mul_f32 %0, %3, %0\n\tv_accvgpr_write_b32 a226, %0\n\t"
                   "v_accvgpr_read_b32 %1, a227\n\tv_mul_f32 %1, %3, %1\n\tv_accvgpr_write_b32 a227, %1\n\t"
                   "v_accvgpr_read_b32 %0, a228\n\tv_mul_f32 %0, %3, %0\n\tv_accvgpr_write_b32 a228, %0\n\t"
                   "v_accvgpr_read_b32 %1, a229\n\tv_mul_f32 %1, %3, %1\n\tv_accvgpr_write_b32 a229, %1\n\t"
                   "v_accvgpr_read_b32 %0, a230\n\tv_mul_f32 %0, %3, %0\n\tv_accvgpr_write_b32 a230, %0\n\t"
                   "v_accvgpr_read_b32 %1, a231\n\tv_mul_f32 %1, %3, %1\n\tv_accvgpr_write_b32 a231, %1\n\t"
                   "v_accvgpr_read_b32 %0, a232\n\tv_mul_f32 %0, %3, %0\n\tv_accvgpr_write_b32 a232, %0\n\t"
                   "v_accvgpr_read_b32 %1, a233\n\tv_mul_f32 %1, %3, %1\n\tv_accvgpr_write_b32 a233, %1\n\t"
                   "v_accvgpr_read_b32 %0, a234\n\tv_mul_f32 %0, %3, %0\n\tv_accvgpr_write_b32 a234, %0\n\t"
                   "v_accvgpr_read_b32 %1, a235\n\tv_mul_f32 %1, %3, %1\n\tv_accvgpr_write_b32 a235, %1\n\t"
                   "v_accvgpr_read_b32 %0, a236\n\tv_mul_f32 %0, %3, %0\n\tv_accvgpr_write_b32 a236, %0\n\t"
                   "v_accvgpr_read_b32 %1, a237\n\tv_mul_f32 %1, %3, %1\n\tv_accvgpr_write_b32 a237, %1\n\t"
                   "v_accvgpr_read_b32 %0, a238\n\tv_mul_f32 %0, %3, %0\n\tv_accvgpr_write_b32 a238, %0\n\t"
                   "v_accvgpr_read_b32 %1, a239\n\tv_mul_f32 %1, %3, %1\n\tv_accvgpr_write_b32 a239, %1\n\t"
                   "v_accvgpr_read_b32 %0, a240\n\tv_mul_f32 %0, %3, %0\n\tv_accvgpr_write_b32 a240, %0\n\t"
                   "v_accvgpr_read_b32 %1, a241\n\tv_mul_f32 %1, %3, %1\n\tv_accvgpr_write_b32 a241, %1\n\t"
                   "v_accvgpr_read_b32 %0, a242\n\tv_mul_f32 %0, %3, %0\n\tv_accvgpr_write_b32 a242, %0\n\t"
                   "v_accvgpr_read_b32 %1, a243\n\tv_mul_f32 %1, %3, %1\n\tv_accvgpr_write_b32 a243, %1\n\t"
                   "v_accvgpr_read_b32 %0, a244\n\tv_mul_f32 %0, %3, %0\n\tv_accvgpr_write_b32 a244, %0\n\t"
                   "v_accvgpr_read_b32 %1, a245\n\tv_mul_f32 %1, %3, %1\n\tv_accvgpr_write_b32 a245, %1\n\t"
                   "v_accvgpr_read_b32 %0, a246\n\tv_mul_f32 %0, %3, %0\n\tv_accvgpr_write_b32 a246, %0\n\t"
                   "v_accvgpr_read_b32 %1, a247\n\tv_mul_f32 %1, %3, %1\n\tv_accvgpr_write_b32 a247, %1\n\t"
                   "v_accvgpr_read_b32 %0, a248\n\tv_mul_f32 %0, %3, %0\n\tv_accvgpr_write_b32 a248, %0\n\t"
                   "v_accvgpr_read_b32 %1, a249\n\tv_mul_f32 %1, %3, %1\n\tv_accvgpr_write_b32 a249, %1\n\t"
                   "v_accvgpr_read_b32 %0, a250\n\tv_mul_f32 %0, %3, %0\n\tv_accvgpr_write_b32 a250, %0\n\t"
                   "v_accvgpr_read_b32 %1, a251\n\tv_mul_f32 %1, %3, %1\n\tv_accvgpr_write_b32 a251, %1\n\t"
                   "v_accvgpr_read_b32 %0, a252\n\tv_mul_f32 %0, %3, %0\n\tv_accvgpr_write_b32 a252, %0\n\t"
                   "v_accvgpr_read_b32 %1, a253\n\tv_mul_f32 %1, %3, %1\n\tv_accvgpr_write_b32 a253, %1\n\t"
                   "v_accvgpr_read_b32 %0, a254\n\tv_mul_f32 %0, %3, %0\n\tv_accvgpr_write_b32 a254, %0\n\t"
                   "v_accvgpr_read_b32 %1, a255\n\tv_mul_f32 %1, %3, %1\n\tv_accvgpr_write_b32 a255, %1\n\t"
                   "s_nop 1"
                   : "=&v"(t0), "=&v"(t1) : "v"(c.fct[0]), "v"(c.fct[1]) : ACLOB);
      c.pend = false;
    }
  }
  asm volatile("s_nop 1" ::: "memory");
  GAP();
}
template <int T, bool HASPV, bool HASSM> DI void phaseB(Ctx4& c) {
  phaseB_<T, HASPV, HASSM>(c, std::make_integer_sequence<int, 32>{});
}

template <int A, int OFF> DI void acc_dswrite4(unsigned addr) {
  asm volatile("ds_write_b128 %0, a[%c1:%c2] offset:%c3" ::"v"(addr), "i"(A), "i"(A + 3), "i"(OFF) : "memory", ACLOB);
}
template <int A> DI f32x4 acc_read4() {
  f32x4 r;
  asm volatile("v_accvgpr_read_b32 %0, a[%c4]\n\tv_accvgpr_read_b32 %1, a[%c5]\n\tv_accvgpr_read_b32 %2, a[%c6]\n\t"
               "v_accvgpr_read_b32 %3, a[%c7]"
               : "=v"(r[0]), "=v"(r[1]), "=v"(r[2]), "=v"(r[3]) : "i"(A), "i"(A + 1), "i"(A + 2), "i"(A + 3) : ACLOB);
  return r;
}

__global__ __launch_bounds__(256, 1) void attn4_kernel(const u16* __restrict__ x, const u16* __restrict__ wq,
                                                       const u16* __restrict__ kc, const u16* __restrict__ vc,
                                                       const u16* __restrict__ wo, u16* __restrict__ o,
                                                       unsigned* __restrict__ cnt DBGARG) {
  extern __shared__ __attribute__((aligned(16))) char lds[];
  STAMP(0);
  if (blockIdx.x == 0 && threadIdx.x < 128) cnt[threadIdx.x] = 0u;
  const int tid = threadIdx.x, lane = tid & 63;
  const int w = __builtin_amdgcn_readfirstlane(tid >> 6);
  const int r = lane & 31, h = lane >> 5;
  const int bid = blockIdx.x;
  const int xcd = bid & 7, j = bid >> 3;
  const int grp = (j >> 2) * 8 + xcd, sub = j & 3;
  const int b = grp >> 3, kvh = grp & 7;
  const int head = kvh * 2 + (sub >> 1), rhalf = sub & 1;
  const int row0 = b * SQ + rhalf * 56;

  const u16* Kw = kc + ((size_t)(b * HKV + kvh) * SK + 256 * w) * DH;
  const u16* Vw = vc + ((size_t)(b * HKV + kvh) * SK + 256 * w) * DH;

  Ctx4 c;
  c.pfd = 0; c.sub = sub;
  c.Kp = Kw; c.Vp = Vw; c.pfoff = lane * 128;
  c.ring = lds + w * 40960;
  c.rk = __builtin_amdgcn_make_buffer_rsrc((void*)Kw, 0, 256 * DH * 2, 0x00020000);
  c.rv = __builtin_amdgcn_make_buffer_rsrc((void*)Vw, 0, 256 * DH * 2, 0x00020000);
#pragma unroll
  for (int pi = 0; pi < 4; ++pi) {
    const int krow = 4 * pi + (lane >> 4);
    c.koff[pi] = krow * 512 + (((lane & 15) ^ (krow & 15)) * 16);
  }
#pragma unroll
  for (int pi = 0; pi < 2; ++pi) {
    const int vrow = 2 * pi + (lane >> 5);
    c.voff[pi] = vrow * 512 + (((lane & 31) ^ ((vrow & 3) << 2)) * 16);
  }
  {
    __amdgpu_buffer_rsrc_t rx =
        __builtin_amdgcn_make_buffer_rsrc((void*)(x + (size_t)row0 * DM), 0, 56 * DM * 2, 0x00020000);
#pragma unroll
    for (int p = 0; p < 14; ++p) {
      const int ci = 64 * p + lane;
      const int lr = ci / 56, cp = ci - lr * 56;
      int gr = 16 * w + lr; gr = gr > 55 ? 55 : gr;
      const int cs = (cp & ~7) | ((cp ^ (lr >> 1)) & 7);
      __builtin_amdgcn_raw_ptr_buffer_load_lds(rx, (lds_vp)(c.ring + 3 * 8192 + p * 1024), 16, gr * (DM * 2) + cs * 16,
                                               0, 0, 0);
    }
  }
  {
    const int colw = head * DH + 64 * w;
    __amdgpu_buffer_rsrc_t rw =
        __builtin_amdgcn_make_buffer_rsrc((void*)(wq + colw), 0, (DM * NQ - colw) * 2, 0x00020000);
    const int drow = lane >> 3;
    const int wvoff = drow * (NQ * 2) + (((lane & 7) ^ (((drow >> 1) & 1) << 2)) * 16);
    auto wissue = [&](int ch) {
#pragma unroll
      for (int p = 0; p < 8; ++p)
        __builtin_amdgcn_raw_ptr_buffer_load_lds(rw, (lds_vp)(c.ring + ((ch + 2) % 3) * 8192 + p * 1024), 16, wvoff,
                                                 (64 * ch + 8 * p) * (NQ * 2), 0, 0);
    };
    wissue(0); wissue(1); wissue(2);
    const int g = lane >> 4, qq = (lane >> 2) & 3, pp = lane & 3;
    const unsigned trb = (unsigned)(size_t)c.ring + (8 * (g >> 1) + qq) * 128 + ((32 * (g & 1) + 8 * pp) ^ ((qq >> 1) << 6));
    const char* xa[2][4];
#pragma unroll
    for (int qb = 0; qb < 2; ++qb) {
      const int R = 32 * qb + r, lr = R & 15;
#pragma unroll
      for (int v = 0; v < 4; ++v)
        xa[qb][v] = lds + (R >> 4) * 40960 + 3 * 8192 + lr * 896 + ((((2 * v + h) ^ (lr >> 1)) & 7) * 16);
    }
    f32x16 qa[2][2];
#pragma unroll
    for (int a2 = 0; a2 < 2; ++a2)
#pragma unroll
      for (int b2 = 0; b2 < 2; ++b2)
#pragma unroll
        for (int i = 0; i < 16; ++i) qa[a2][b2][i] = 0.f;
    s16x4 fa[2][4];
    bf16x8 fx[2][2];
    auto rdstep = [&](int i, int bsel) {
      const int ch = i >> 2, s4 = i & 3;
      const unsigned tb = trb + ((ch + 2) % 3) * 8192 + s4 * 2048;
      fa[bsel][0] = trread(tb);
      fa[bsel][1] = trread(tb + 512);
      fa[bsel][2] = trread(tb ^ 64);
      fa[bsel][3] = trread((tb + 512) ^ 64);
      asm volatile("ds_read_b128 %0, %1" : "=v"(fx[bsel][0]) : "v"((unsigned)(size_t)(xa[0][s4] + ch * 128)) : "memory");
      asm volatile("ds_read_b128 %0, %1" : "=v"(fx[bsel][1]) : "v"((unsigned)(size_t)(xa[1][s4] + ch * 128)) : "memory");
    };
    auto refill_piece = [&](int k, int p) {
      if (k <= 3) {
        __builtin_amdgcn_raw_ptr_buffer_load_lds(rw, (lds_vp)(c.ring + ((k + 5) % 3) * 8192 + p * 1024), 16, wvoff,
                                                 (64 * (k + 3) + 8 * p) * (NQ * 2), 0, 0);
      }
    };
    WAITV(16);
    __builtin_amdgcn_s_barrier();
    STAMP(11);
    rdstep(0, 0);
#pragma unroll
    for (int i = 0; i < 28; ++i) {
      const int bs = i & 1, ch = i >> 2, s4 = i & 3;
      if (i + 1 < 28) {
        if (s4 == 3) { if (ch == 0) WAITV(8); else WAITV(6); }
        rdstep(i + 1, bs ^ 1);
        asm volatile("s_waitcnt lgkmcnt(6)" : "+v"(fa[bs][0]), "+v"(fa[bs][1]), "+v"(fa[bs][2]), "+v"(fa[bs][3]), "+v"(fx[bs][0]), "+v"(fx[bs][1])::"memory");
      } else {
        asm volatile("s_waitcnt lgkmcnt(0)" : "+v"(fa[bs][0]), "+v"(fa[bs][1]), "+v"(fa[bs][2]), "+v"(fa[bs][3]), "+v"(fx[bs][0]), "+v"(fx[bs][1])::"memory");
      }
      bf16x8 A0 = cat8(fa[bs][0], fa[bs][1]), A1 = cat8(fa[bs][2], fa[bs][3]);
      qa[0][0] = __builtin_amdgcn_mfma_f32_32x32x16_bf16(A0, fx[bs][0], qa[0][0], 0, 0, 0);
      GAP();
      if (ch == 5 && s4 == 0) { prefetch_unit(c, 6 + sub); prefetch_unit(c, 10 + sub); }
      if (ch >= 1) {
        if (ch - 1 <= 3) refill_piece(ch - 1, 2 * s4);
        else if (ch == 5) { if (s4 == 0) dma_piece<0, 0>(c); else if (s4 == 1) dma_piece<0, 2>(c); else if (s4 == 2) dma_piece<0, 4>(c); else dma_piece<0, 6>(c); }
        else { if (s4 == 0) dma_piece<1, 0>(c); else if (s4 == 1) dma_piece<1, 2>(c); else if (s4 == 2) dma_piece<1, 4>(c); else dma_piece<1, 6>(c); }
      }
      GAP();
      qa[0][1] = __builtin_amdgcn_mfma_f32_32x32x16_bf16(A1, fx[bs][0], qa[0][1], 0, 0, 0);
      qa[1][0] = __builtin_amdgcn_mfma_f32_32x32x16_bf16(A0, fx[bs][1], qa[1][0], 0, 0, 0);
      GAP();
      if (ch >= 1) {
        if (ch - 1 <= 3) refill_piece(ch - 1, 2 * s4 + 1);
        else if (ch == 5) { if (s4 == 0) dma_piece<0, 1>(c); else if (s4 == 1) dma_piece<0, 3>(c); else if (s4 == 2) dma_piece<0, 5>(c); else dma_piece<0, 7>(c); }
        else { if (s4 == 0) dma_piece<1, 1>(c); else if (s4 == 1) dma_piece<1, 3>(c); else if (s4 == 2) dma_piece<1, 5>(c); else dma_piece<1, 7>(c); }
      }
      GAP();
      qa[1][1] = __builtin_amdgcn_mfma_f32_32x32x16_bf16(A1, fx[bs][1], qa[1][1], 0, 0, 0);
    }
    dma_unit<2>(c);
    STAMP(12);
    __builtin_amdgcn_s_barrier();
#pragma unroll
    for (int qb = 0; qb < 2; ++qb) {
      const int R = 32 * qb + r;
      char* base = lds + (R >> 4) * 40960 + 4 * 8192 + (R & 15) * 512 + 8 * h;
#pragma unroll
      for (int cb = 0; cb < 2; ++cb)
#pragma unroll
        for (int gg = 0; gg < 4; ++gg) {
          u32x2 v = {cvtpk(qa[qb][cb][4 * gg], qa[qb][cb][4 * gg + 1]), cvtpk(qa[qb][cb][4 * gg + 2], qa[qb][cb][4 * gg + 3])};
          *(u32x2*)(base + (((8 * w + 4 * cb + gg) ^ (R & 15)) * 16)) = v;
        }
    }
    WAITL0();
    __builtin_amdgcn_s_barrier();
  }
  {
#pragma unroll
    for (int qb = 0; qb < 2; ++qb) {
      const int R = 32 * qb + r;
      const char* base = lds + (R >> 4) * 40960 + 4 * 8192 + (R & 15) * 512;
#pragma unroll
      for (int s = 0; s < 16; ++s) c.qf[qb][s] = *(const bf16x8*)(base + (((2 * s + h) ^ (R & 15)) * 16));
    }
#pragma unroll
    for (int qb = 0; qb < 2; ++qb)
#pragma unroll
      for (int s = 0; s < 16; ++s) asm volatile("" : "+v"(c.qf[qb][s]));
  }
  WAITL0();
  __builtin_amdgcn_s_barrier();
  dma_unit<3>(c);
  dma_unit<4>(c);
  STAMP(1);

  {
    const unsigned rb = (unsigned)(size_t)c.ring;
#pragma unroll
    for (int ss = 0; ss < 8; ++ss) c.kaddr[ss] = rb + r * 256 + ((((2 * ss + h) ^ (r & 15))) * 16);
    const int g = lane >> 4, qq = (lane >> 2) & 3, pp = lane & 3;
#pragma unroll
    for (int d = 0; d < 4; ++d)
      c.vaddr[d] = rb + (4 * (g >> 1) + qq) * 512 + ((64 * d + 32 * (g & 1) + 8 * pp) ^ (qq << 6));
  }
  c.pend = false;
  c.fct[0] = 1.f; c.fct[1] = 1.f;

  phaseA<0>(c); phaseB<0, false, true>(c); STAMP(2);
  phaseA<1>(c); phaseB<1, true, true>(c); STAMP(3);
  phaseA<2>(c); phaseB<2, true, true>(c); STAMP(4);
  phaseA<3>(c); phaseB<3, true, true>(c); STAMP(5);
  phaseA<4>(c); phaseB<4, true, true>(c); STAMP(6);
  phaseA<5>(c); phaseB<5, true, true>(c); STAMP(7);
  phaseA<6>(c); phaseB<6, true, true>(c); STAMP(8);
  {
    int li = (bid * 4 + w) * 28 + (lane < 28 ? lane : 27);
    asm volatile("global_load_dword %0, %1, off" : "+v"(c.pfd) : "v"(wo + (size_t)li * 64) : "memory");
  }
  phaseA<7>(c); phaseB<7, true, true>(c); STAMP(9);
  phaseB<8, true, false>(c);
  asm volatile("s_nop 7\n\ts_nop 7" ::: "memory");
  STAMP(13);

  __syncthreads();
  float* ml = (float*)lds;
#pragma unroll
  for (int qb = 0; qb < 2; ++qb) {
    auto rr = __builtin_amdgcn_permlane32_swap(__float_as_uint(c.l[qb]), __float_as_uint(c.l[qb]), false, false);
    const float lt = __uint_as_float(rr[0]) + __uint_as_float(rr[1]);
    if (h == 0) {
      ml[((w * 2 + qb) * 32 + r) * 2 + 0] = c.m[qb];
      ml[((w * 2 + qb) * 32 + r) * 2 + 1] = lt;
    }
  }
  __syncthreads();
  float fw[2][4];
#pragma unroll
  for (int qb = 0; qb < 2; ++qb) {
    float mm[4], ll[4];
#pragma unroll
    for (int ww = 0; ww < 4; ++ww) {
      mm[ww] = ml[((ww * 2 + qb) * 32 + r) * 2 + 0];
      ll[ww] = ml[((ww * 2 + qb) * 32 + r) * 2 + 1];
    }
    const float M = fmaxf(fmaxf(mm[0], mm[1]), fmaxf(mm[2], mm[3]));
    float e[4], L = 0.f;
#pragma unroll
    for (int ww = 0; ww < 4; ++ww) { e[ww] = __builtin_amdgcn_exp2f(mm[ww] - M); L += e[ww] * ll[ww]; }
    const float inv = 1.f / L;
#pragma unroll
    for (int ww = 0; ww < 4; ++ww) fw[qb][ww] = e[ww] * inv;
  }
  STAMP(14);
  const unsigned xw = (unsigned)(size_t)lds + 4096 + lane * 16;
  const f32x4* xr = (const f32x4*)(lds + 4096) + lane;
  char* otile = lds + 102400;
  {
    switch (w) {
      case 0: {
        acc_dswrite4<32, 0>(xw + 0);
        acc_dswrite4<36, 1024>(xw + 0);
        acc_dswrite4<40, 2048>(xw + 0);
        acc_dswrite4<44, 3072>(xw + 0);
        acc_dswrite4<160, 4096>(xw + 0);
        acc_dswrite4<164, 5120>(xw + 0);
        acc_dswrite4<168, 6144>(xw + 0);
        acc_dswrite4<172, 7168>(xw + 0);
        acc_dswrite4<64, 8192>(xw + 0);
        acc_dswrite4<68, 9216>(xw + 0);
        acc_dswrite4<72, 10240>(xw + 0);
        acc_dswrite4<76, 11264>(xw + 0);
        acc_dswrite4<192, 12288>(xw + 0);
        acc_dswrite4<196, 13312>(xw + 0);
        acc_dswrite4<200, 14336>(xw + 0);
        acc_dswrite4<204, 15360>(xw + 0);
        acc_dswrite4<96, 16384>(xw + 0);
        acc_dswrite4<100, 17408>(xw + 0);
        acc_dswrite4<104, 18432>(xw + 0);
        acc_dswrite4<108, 19456>(xw + 0);
        acc_dswrite4<224, 20480>(xw + 0);
        acc_dswrite4<228, 21504>(xw + 0);
        acc_dswrite4<232, 22528>(xw + 0);
        acc_dswrite4<236, 23552>(xw + 0);
      } break;
      case 1: {
        acc_dswrite4<0, 24576>(xw + 0);
        acc_dswrite4<4, 25600>(xw + 0);
        acc_dswrite4<8, 26624>(xw + 0);
        acc_dswrite4<12, 27648>(xw + 0);
        acc_dswrite4<128, 28672>(xw + 0);
        acc_dswrite4<132, 29696>(xw + 0);
        acc_dswrite4<136, 30720>(xw + 0);
        acc_dswrite4<140, 31744>(xw + 0);
        acc_dswrite4<64, 32768>(xw + 0);
        acc_dswrite4<68, 33792>(xw + 0);
        acc_dswrite4<72, 34816>(xw + 0);
        acc_dswrite4<76, 35840>(xw + 0);
        acc_dswrite4<192, 36864>(xw + 0);
        acc_dswrite4<196, 37888>(xw + 0);
        acc_dswrite4<200, 38912>(xw + 0);
        acc_dswrite4<204, 39936>(xw + 0);
        acc_dswrite4<96, 40960>(xw + 0);
        acc_dswrite4<100, 41984>(xw + 0);
        acc_dswrite4<104, 43008>(xw + 0);
        acc_dswrite4<108, 44032>(xw + 0);
        acc_dswrite4<224, 45056>(xw + 0);
        acc_dswrite4<228, 46080>(xw + 0);
        acc_dswrite4<232, 47104>(xw + 0);
        acc_dswrite4<236, 48128>(xw + 0);
      } break;
      case 2: {
        acc_dswrite4<0, 49152>(xw + 0);
        acc_dswrite4<4, 50176>(xw + 0);
        acc_dswrite4<8, 51200>(xw + 0);
        acc_dswrite4<12, 52224>(xw + 0);
        acc_dswrite4<128, 53248>(xw + 0);
        acc_dswrite4<132, 54272>(xw + 0);
        acc_dswrite4<136, 55296>(xw + 0);
        acc_dswrite4<140, 56320>(xw + 0);
        acc_dswrite4<32, 57344>(xw + 0);
        acc_dswrite4<36, 58368>(xw + 0);
        acc_dswrite4<40, 59392>(xw + 0);
        acc_dswrite4<44, 60416>(xw + 0);
        acc_dswrite4<160, 61440>(xw + 0);
        acc_dswrite4<164, 62464>(xw + 0);
        acc_dswrite4<168, 63488>(xw + 0);
        acc_dswrite4<172, 64512>(xw + 0);
        acc_dswrite4<96, 0>(xw + 65536);
        acc_dswrite4<100, 1024>(xw + 65536);
        acc_dswrite4<104, 2048>(xw + 65536);
        acc_dswrite4<108, 3072>(xw + 65536);
        acc_dswrite4<224, 4096>(xw + 65536);
        acc_dswrite4<228, 5120>(xw + 65536);
        acc_dswrite4<232, 6144>(xw + 65536);
        acc_dswrite4<236, 7168>(xw + 65536);
      } break;
      case 3: {
        acc_dswrite4<0, 8192>(xw + 65536);
        acc_dswrite4<4, 9216>(xw + 65536);
        acc_dswrite4<8, 10240>(xw + 65536);
        acc_dswrite4<12, 11264>(xw + 65536);
        acc_dswrite4<128, 12288>(xw + 65536);
        acc_dswrite4<132, 13312>(xw + 65536);
        acc_dswrite4<136, 14336>(xw + 65536);
        acc_dswrite4<140, 15360>(xw + 65536);
        acc_dswrite4<32, 16384>(xw + 65536);
        acc_dswrite4<36, 17408>(xw + 65536);
        acc_dswrite4<40, 18432>(xw + 65536);
        acc_dswrite4<44, 19456>(xw + 65536);
        acc_dswrite4<160, 20480>(xw + 65536);
        acc_dswrite4<164, 21504>(xw + 65536);
        acc_dswrite4<168, 22528>(xw + 65536);
        acc_dswrite4<172, 23552>(xw + 65536);
        acc_dswrite4<64, 24576>(xw + 65536);
        acc_dswrite4<68, 25600>(xw + 65536);
        acc_dswrite4<72, 26624>(xw + 65536);
        acc_dswrite4<76, 27648>(xw + 65536);
        acc_dswrite4<192, 28672>(xw + 65536);
        acc_dswrite4<196, 29696>(xw + 65536);
        acc_dswrite4<200, 30720>(xw + 65536);
        acc_dswrite4<204, 31744>(xw + 65536);
      } break;
    }
    WAITL0();
    __builtin_amdgcn_s_barrier();
    switch (w) {
      case 0: {
        { f32x4 sum = acc_read4<0>(); sum *= fw[0][0];
          sum += xr[1536] * fw[0][1];
          sum += xr[3072] * fw[0][2];
          sum += xr[4608] * fw[0][3];
          u32x2 v = {cvtpk(sum[0], sum[1]), cvtpk(sum[2], sum[3])};
          *(u32x2*)(otile + (0 + r) * 512 + (((0) ^ ((0 + r) & 31)) * 16) + 8 * h) = v; }
        { f32x4 sum = acc_read4<4>(); sum *= fw[0][0];
          sum += xr[1600] * fw[0][1];
          sum += xr[3136] * fw[0][2];
          sum += xr[4672] * fw[0][3];
          u32x2 v = {cvtpk(sum[0], sum[1]), cvtpk(sum[2], sum[3])};
          *(u32x2*)(otile + (0 + r) * 512 + (((1) ^ ((0 + r) & 31)) * 16) + 8 * h) = v; }
        { f32x4 sum = acc_read4<8>(); sum *= fw[0][0];
          sum += xr[1664] * fw[0][1];
          sum += xr[3200] * fw[0][2];
          sum += xr[4736] * fw[0][3];
          u32x2 v = {cvtpk(sum[0], sum[1]), cvtpk(sum[2], sum[3])};
          *(u32x2*)(otile + (0 + r) * 512 + (((2) ^ ((0 + r) & 31)) * 16) + 8 * h) = v; }
        { f32x4 sum = acc_read4<12>(); sum *= fw[0][0];
          sum += xr[1728] * fw[0][1];
          sum += xr[3264] * fw[0][2];
          sum += xr[4800] * fw[0][3];
          u32x2 v = {cvtpk(sum[0], sum[1]), cvtpk(sum[2], sum[3])};
          *(u32x2*)(otile + (0 + r) * 512 + (((3) ^ ((0 + r) & 31)) * 16) + 8 * h) = v; }
        { f32x4 sum = acc_read4<128>(); sum *= fw[1][0];
          sum += xr[1792] * fw[1][1];
          sum += xr[3328] * fw[1][2];
          sum += xr[4864] * fw[1][3];
          u32x2 v = {cvtpk(sum[0], sum[1]), cvtpk(sum[2], sum[3])};
          *(u32x2*)(otile + (32 + r) * 512 + (((0) ^ ((32 + r) & 31)) * 16) + 8 * h) = v; }
        { f32x4 sum = acc_read4<132>(); sum *= fw[1][0];
          sum += xr[1856] * fw[1][1];
          sum += xr[3392] * fw[1][2];
          sum += xr[4928] * fw[1][3];
          u32x2 v = {cvtpk(sum[0], sum[1]), cvtpk(sum[2], sum[3])};
          *(u32x2*)(otile + (32 + r) * 512 + (((1) ^ ((32 + r) & 31)) * 16) + 8 * h) = v; }
        { f32x4 sum = acc_read4<136>(); sum *= fw[1][0];
          sum += xr[1920] * fw[1][1];
          sum += xr[3456] * fw[1][2];
          sum += xr[4992] * fw[1][3];
          u32x2 v = {cvtpk(sum[0], sum[1]), cvtpk(sum[2], sum[3])};
          *(u32x2*)(otile + (32 + r) * 512 + (((2) ^ ((32 + r) & 31)) * 16) + 8 * h) = v; }
        { f32x4 sum = acc_read4<140>(); sum *= fw[1][0];
          sum += xr[1984] * fw[1][1];
          sum += xr[3520] * fw[1][2];
          sum += xr[5056] * fw[1][3];
          u32x2 v = {cvtpk(sum[0], sum[1]), cvtpk(sum[2], sum[3])};
          *(u32x2*)(otile + (32 + r) * 512 + (((3) ^ ((32 + r) & 31)) * 16) + 8 * h) = v; }
      } break;
      case 1: {
        { f32x4 sum = acc_read4<32>(); sum *= fw[0][1];
          sum += xr[0] * fw[0][0];
          sum += xr[3584] * fw[0][2];
          sum += xr[5120] * fw[0][3];
          u32x2 v = {cvtpk(sum[0], sum[1]), cvtpk(sum[2], sum[3])};
          *(u32x2*)(otile + (0 + r) * 512 + (((8) ^ ((0 + r) & 31)) * 16) + 8 * h) = v; }
        { f32x4 sum = acc_read4<36>(); sum *= fw[0][1];
          sum += xr[64] * fw[0][0];
          sum += xr[3648] * fw[0][2];
          sum += xr[5184] * fw[0][3];
          u32x2 v = {cvtpk(sum[0], sum[1]), cvtpk(sum[2], sum[3])};
          *(u32x2*)(otile + (0 + r) * 512 + (((9) ^ ((0 + r) & 31)) * 16) + 8 * h) = v; }
        { f32x4 sum = acc_read4<40>(); sum *= fw[0][1];
          sum += xr[128] * fw[0][0];
          sum += xr[3712] * fw[0][2];
          sum += xr[5248] * fw[0][3];
          u32x2 v = {cvtpk(sum[0], sum[1]), cvtpk(sum[2], sum[3])};
          *(u32x2*)(otile + (0 + r) * 512 + (((10) ^ ((0 + r) & 31)) * 16) + 8 * h) = v; }
        { f32x4 sum = acc_read4<44>(); sum *= fw[0][1];
          sum += xr[192] * fw[0][0];
          sum += xr[3776] * fw[0][2];
          sum += xr[5312] * fw[0][3];
          u32x2 v = {cvtpk(sum[0], sum[1]), cvtpk(sum[2], sum[3])};
          *(u32x2*)(otile + (0 + r) * 512 + (((11) ^ ((0 + r) & 31)) * 16) + 8 * h) = v; }
        { f32x4 sum = acc_read4<160>(); sum *= fw[1][1];
          sum += xr[256] * fw[1][0];
          sum += xr[3840] * fw[1][2];
          sum += xr[5376] * fw[1][3];
          u32x2 v = {cvtpk(sum[0], sum[1]), cvtpk(sum[2], sum[3])};
          *(u32x2*)(otile + (32 + r) * 512 + (((8) ^ ((32 + r) & 31)) * 16) + 8 * h) = v; }
        { f32x4 sum = acc_read4<164>(); sum *= fw[1][1];
          sum += xr[320] * fw[1][0];
          sum += xr[3904] * fw[1][2];
          sum += xr[5440] * fw[1][3];
          u32x2 v = {cvtpk(sum[0], sum[1]), cvtpk(sum[2], sum[3])};
          *(u32x2*)(otile + (32 + r) * 512 + (((9) ^ ((32 + r) & 31)) * 16) + 8 * h) = v; }
        { f32x4 sum = acc_read4<168>(); sum *= fw[1][1];
          sum += xr[384] * fw[1][0];
          sum += xr[3968] * fw[1][2];
          sum += xr[5504] * fw[1][3];
          u32x2 v = {cvtpk(sum[0], sum[1]), cvtpk(sum[2], sum[3])};
          *(u32x2*)(otile + (32 + r) * 512 + (((10) ^ ((32 + r) & 31)) * 16) + 8 * h) = v; }
        { f32x4 sum = acc_read4<172>(); sum *= fw[1][1];
          sum += xr[448] * fw[1][0];
          sum += xr[4032] * fw[1][2];
          sum += xr[5568] * fw[1][3];
          u32x2 v = {cvtpk(sum[0], sum[1]), cvtpk(sum[2], sum[3])};
          *(u32x2*)(otile + (32 + r) * 512 + (((11) ^ ((32 + r) & 31)) * 16) + 8 * h) = v; }
      } break;
      case 2: {
        { f32x4 sum = acc_read4<64>(); sum *= fw[0][2];
          sum += xr[512] * fw[0][0];
          sum += xr[2048] * fw[0][1];
          sum += xr[5632] * fw[0][3];
          u32x2 v = {cvtpk(sum[0], sum[1]), cvtpk(sum[2], sum[3])};
          *(u32x2*)(otile + (0 + r) * 512 + (((16) ^ ((0 + r) & 31)) * 16) + 8 * h) = v; }
        { f32x4 sum = acc_read4<68>(); sum *= fw[0][2];
          sum += xr[576] * fw[0][0];
          sum += xr[2112] * fw[0][1];
          sum += xr[5696] * fw[0][3];
          u32x2 v = {cvtpk(sum[0], sum[1]), cvtpk(sum[2], sum[3])};
          *(u32x2*)(otile + (0 + r) * 512 + (((17) ^ ((0 + r) & 31)) * 16) + 8 * h) = v; }
        { f32x4 sum = acc_read4<72>(); sum *= fw[0][2];
          sum += xr[640] * fw[0][0];
          sum += xr[2176] * fw[0][1];
          sum += xr[5760] * fw[0][3];
          u32x2 v = {cvtpk(sum[0], sum[1]), cvtpk(sum[2], sum[3])};
          *(u32x2*)(otile + (0 + r) * 512 + (((18) ^ ((0 + r) & 31)) * 16) + 8 * h) = v; }
        { f32x4 sum = acc_read4<76>(); sum *= fw[0][2];
          sum += xr[704] * fw[0][0];
          sum += xr[2240] * fw[0][1];
          sum += xr[5824] * fw[0][3];
          u32x2 v = {cvtpk(sum[0], sum[1]), cvtpk(sum[2], sum[3])};
          *(u32x2*)(otile + (0 + r) * 512 + (((19) ^ ((0 + r) & 31)) * 16) + 8 * h) = v; }
        { f32x4 sum = acc_read4<192>(); sum *= fw[1][2];
          sum += xr[768] * fw[1][0];
          sum += xr[2304] * fw[1][1];
          sum += xr[5888] * fw[1][3];
          u32x2 v = {cvtpk(sum[0], sum[1]), cvtpk(sum[2], sum[3])};
          *(u32x2*)(otile + (32 + r) * 512 + (((16) ^ ((32 + r) & 31)) * 16) + 8 * h) = v; }
        { f32x4 sum = acc_read4<196>(); sum *= fw[1][2];
          sum += xr[832] * fw[1][0];
          sum += xr[2368] * fw[1][1];
          sum += xr[5952] * fw[1][3];
          u32x2 v = {cvtpk(sum[0], sum[1]), cvtpk(sum[2], sum[3])};
          *(u32x2*)(otile + (32 + r) * 512 + (((17) ^ ((32 + r) & 31)) * 16) + 8 * h) = v; }
        { f32x4 sum = acc_read4<200>(); sum *= fw[1][2];
          sum += xr[896] * fw[1][0];
          sum += xr[2432] * fw[1][1];
          sum += xr[6016] * fw[1][3];
          u32x2 v = {cvtpk(sum[0], sum[1]), cvtpk(sum[2], sum[3])};
          *(u32x2*)(otile + (32 + r) * 512 + (((18) ^ ((32 + r) & 31)) * 16) + 8 * h) = v; }
        { f32x4 sum = acc_read4<204>(); sum *= fw[1][2];
          sum += xr[960] * fw[1][0];
          sum += xr[2496] * fw[1][1];
          sum += xr[6080] * fw[1][3];
          u32x2 v = {cvtpk(sum[0], sum[1]), cvtpk(sum[2], sum[3])};
          *(u32x2*)(otile + (32 + r) * 512 + (((19) ^ ((32 + r) & 31)) * 16) + 8 * h) = v; }
      } break;
      case 3: {
        { f32x4 sum = acc_read4<96>(); sum *= fw[0][3];
          sum += xr[1024] * fw[0][0];
          sum += xr[2560] * fw[0][1];
          sum += xr[4096] * fw[0][2];
          u32x2 v = {cvtpk(sum[0], sum[1]), cvtpk(sum[2], sum[3])};
          *(u32x2*)(otile + (0 + r) * 512 + (((24) ^ ((0 + r) & 31)) * 16) + 8 * h) = v; }
        { f32x4 sum = acc_read4<100>(); sum *= fw[0][3];
          sum += xr[1088] * fw[0][0];
          sum += xr[2624] * fw[0][1];
          sum += xr[4160] * fw[0][2];
          u32x2 v = {cvtpk(sum[0], sum[1]), cvtpk(sum[2], sum[3])};
          *(u32x2*)(otile + (0 + r) * 512 + (((25) ^ ((0 + r) & 31)) * 16) + 8 * h) = v; }
        { f32x4 sum = acc_read4<104>(); sum *= fw[0][3];
          sum += xr[1152] * fw[0][0];
          sum += xr[2688] * fw[0][1];
          sum += xr[4224] * fw[0][2];
          u32x2 v = {cvtpk(sum[0], sum[1]), cvtpk(sum[2], sum[3])};
          *(u32x2*)(otile + (0 + r) * 512 + (((26) ^ ((0 + r) & 31)) * 16) + 8 * h) = v; }
        { f32x4 sum = acc_read4<108>(); sum *= fw[0][3];
          sum += xr[1216] * fw[0][0];
          sum += xr[2752] * fw[0][1];
          sum += xr[4288] * fw[0][2];
          u32x2 v = {cvtpk(sum[0], sum[1]), cvtpk(sum[2], sum[3])};
          *(u32x2*)(otile + (0 + r) * 512 + (((27) ^ ((0 + r) & 31)) * 16) + 8 * h) = v; }
        { f32x4 sum = acc_read4<224>(); sum *= fw[1][3];
          sum += xr[1280] * fw[1][0];
          sum += xr[2816] * fw[1][1];
          sum += xr[4352] * fw[1][2];
          u32x2 v = {cvtpk(sum[0], sum[1]), cvtpk(sum[2], sum[3])};
          *(u32x2*)(otile + (32 + r) * 512 + (((24) ^ ((32 + r) & 31)) * 16) + 8 * h) = v; }
        { f32x4 sum = acc_read4<228>(); sum *= fw[1][3];
          sum += xr[1344] * fw[1][0];
          sum += xr[2880] * fw[1][1];
          sum += xr[4416] * fw[1][2];
          u32x2 v = {cvtpk(sum[0], sum[1]), cvtpk(sum[2], sum[3])};
          *(u32x2*)(otile + (32 + r) * 512 + (((25) ^ ((32 + r) & 31)) * 16) + 8 * h) = v; }
        { f32x4 sum = acc_read4<232>(); sum *= fw[1][3];
          sum += xr[1408] * fw[1][0];
          sum += xr[2944] * fw[1][1];
          sum += xr[4480] * fw[1][2];
          u32x2 v = {cvtpk(sum[0], sum[1]), cvtpk(sum[2], sum[3])};
          *(u32x2*)(otile + (32 + r) * 512 + (((26) ^ ((32 + r) & 31)) * 16) + 8 * h) = v; }
        { f32x4 sum = acc_read4<236>(); sum *= fw[1][3];
          sum += xr[1472] * fw[1][0];
          sum += xr[3008] * fw[1][1];
          sum += xr[4544] * fw[1][2];
          u32x2 v = {cvtpk(sum[0], sum[1]), cvtpk(sum[2], sum[3])};
          *(u32x2*)(otile + (32 + r) * 512 + (((27) ^ ((32 + r) & 31)) * 16) + 8 * h) = v; }
      } break;
    }
    WAITL0();
    __builtin_amdgcn_s_barrier();
  }
  {
    switch (w) {
      case 0: {
        acc_dswrite4<48, 0>(xw + 0);
        acc_dswrite4<52, 1024>(xw + 0);
        acc_dswrite4<56, 2048>(xw + 0);
        acc_dswrite4<60, 3072>(xw + 0);
        acc_dswrite4<176, 4096>(xw + 0);
        acc_dswrite4<180, 5120>(xw + 0);
        acc_dswrite4<184, 6144>(xw + 0);
        acc_dswrite4<188, 7168>(xw + 0);
        acc_dswrite4<80, 8192>(xw + 0);
        acc_dswrite4<84, 9216>(xw + 0);
        acc_dswrite4<88, 10240>(xw + 0);
        acc_dswrite4<92, 11264>(xw + 0);
        acc_dswrite4<208, 12288>(xw + 0);
        acc_dswrite4<212, 13312>(xw + 0);
        acc_dswrite4<216, 14336>(xw + 0);
        acc_dswrite4<220, 15360>(xw + 0);
        acc_dswrite4<112, 16384>(xw + 0);
        acc_dswrite4<116, 17408>(xw + 0);
        acc_dswrite4<120, 18432>(xw + 0);
        acc_dswrite4<124, 19456>(xw + 0);
        acc_dswrite4<240, 20480>(xw + 0);
        acc_dswrite4<244, 21504>(xw + 0);
        acc_dswrite4<248, 22528>(xw + 0);
        acc_dswrite4<252, 23552>(xw + 0);
      } break;
      case 1: {
        acc_dswrite4<16, 24576>(xw + 0);
        acc_dswrite4<20, 25600>(xw + 0);
        acc_dswrite4<24, 26624>(xw + 0);
        acc_dswrite4<28, 27648>(xw + 0);
        acc_dswrite4<144, 28672>(xw + 0);
        acc_dswrite4<148, 29696>(xw + 0);
        acc_dswrite4<152, 30720>(xw + 0);
        acc_dswrite4<156, 31744>(xw + 0);
        acc_dswrite4<80, 32768>(xw + 0);
        acc_dswrite4<84, 33792>(xw + 0);
        acc_dswrite4<88, 34816>(xw + 0);
        acc_dswrite4<92, 35840>(xw + 0);
        acc_dswrite4<208, 36864>(xw + 0);
        acc_dswrite4<212, 37888>(xw + 0);
        acc_dswrite4<216, 38912>(xw + 0);
        acc_dswrite4<220, 39936>(xw + 0);
        acc_dswrite4<112, 40960>(xw + 0);
        acc_dswrite4<116, 41984>(xw + 0);
        acc_dswrite4<120, 43008>(xw + 0);
        acc_dswrite4<124, 44032>(xw + 0);
        acc_dswrite4<240, 45056>(xw + 0);
        acc_dswrite4<244, 46080>(xw + 0);
        acc_dswrite4<248, 47104>(xw + 0);
        acc_dswrite4<252, 48128>(xw + 0);
      } break;
      case 2: {
        acc_dswrite4<16, 49152>(xw + 0);
        acc_dswrite4<20, 50176>(xw + 0);
        acc_dswrite4<24, 51200>(xw + 0);
        acc_dswrite4<28, 52224>(xw + 0);
        acc_dswrite4<144, 53248>(xw + 0);
        acc_dswrite4<148, 54272>(xw + 0);
        acc_dswrite4<152, 55296>(xw + 0);
        acc_dswrite4<156, 56320>(xw + 0);
        acc_dswrite4<48, 57344>(xw + 0);
        acc_dswrite4<52, 58368>(xw + 0);
        acc_dswrite4<56, 59392>(xw + 0);
        acc_dswrite4<60, 60416>(xw + 0);
        acc_dswrite4<176, 61440>(xw + 0);
        acc_dswrite4<180, 62464>(xw + 0);
        acc_dswrite4<184, 63488>(xw + 0);
        acc_dswrite4<188, 64512>(xw + 0);
        acc_dswrite4<112, 0>(xw + 65536);
        acc_dswrite4<116, 1024>(xw + 65536);
        acc_dswrite4<120, 2048>(xw + 65536);
        acc_dswrite4<124, 3072>(xw + 65536);
        acc_dswrite4<240, 4096>(xw + 65536);
        acc_dswrite4<244, 5120>(xw + 65536);
        acc_dswrite4<248, 6144>(xw + 65536);
        acc_dswrite4<252, 7168>(xw + 65536);
      } break;
      case 3: {
        acc_dswrite4<16, 8192>(xw + 65536);
        acc_dswrite4<20, 9216>(xw + 65536);
        acc_dswrite4<24, 10240>(xw + 65536);
        acc_dswrite4<28, 11264>(xw + 65536);
        acc_dswrite4<144, 12288>(xw + 65536);
        acc_dswrite4<148, 13312>(xw + 65536);
        acc_dswrite4<152, 14336>(xw + 65536);
        acc_dswrite4<156, 15360>(xw + 65536);
        acc_dswrite4<48, 16384>(xw + 65536);
        acc_dswrite4<52, 17408>(xw + 65536);
        acc_dswrite4<56, 18432>(xw + 65536);
        acc_dswrite4<60, 19456>(xw + 65536);
        acc_dswrite4<176, 20480>(xw + 65536);
        acc_dswrite4<180, 21504>(xw + 65536);
        acc_dswrite4<184, 22528>(xw + 65536);
        acc_dswrite4<188, 23552>(xw + 65536);
        acc_dswrite4<80, 24576>(xw + 65536);
        acc_dswrite4<84, 25600>(xw + 65536);
        acc_dswrite4<88, 26624>(xw + 65536);
        acc_dswrite4<92, 27648>(xw + 65536);
        acc_dswrite4<208, 28672>(xw + 65536);
        acc_dswrite4<212, 29696>(xw + 65536);
        acc_dswrite4<216, 30720>(xw + 65536);
        acc_dswrite4<220, 31744>(xw + 65536);
      } break;
    }
    WAITL0();
    __builtin_amdgcn_s_barrier();
    switch (w) {
      case 0: {
        { f32x4 sum = acc_read4<16>(); sum *= fw[0][0];
          sum += xr[1536] * fw[0][1];
          sum += xr[3072] * fw[0][2];
          sum += xr[4608] * fw[0][3];
          u32x2 v = {cvtpk(sum[0], sum[1]), cvtpk(sum[2], sum[3])};
          *(u32x2*)(otile + (0 + r) * 512 + (((4) ^ ((0 + r) & 31)) * 16) + 8 * h) = v; }
        { f32x4 sum = acc_read4<20>(); sum *= fw[0][0];
          sum += xr[1600] * fw[0][1];
          sum += xr[3136] * fw[0][2];
          sum += xr[4672] * fw[0][3];
          u32x2 v = {cvtpk(sum[0], sum[1]), cvtpk(sum[2], sum[3])};
          *(u32x2*)(otile + (0 + r) * 512 + (((5) ^ ((0 + r) & 31)) * 16) + 8 * h) = v; }
        { f32x4 sum = acc_read4<24>(); sum *= fw[0][0];
          sum += xr[1664] * fw[0][1];
          sum += xr[3200] * fw[0][2];
          sum += xr[4736] * fw[0][3];
          u32x2 v = {cvtpk(sum[0], sum[1]), cvtpk(sum[2], sum[3])};
          *(u32x2*)(otile + (0 + r) * 512 + (((6) ^ ((0 + r) & 31)) * 16) + 8 * h) = v; }
        { f32x4 sum = acc_read4<28>(); sum *= fw[0][0];
          sum += xr[1728] * fw[0][1];
          sum += xr[3264] * fw[0][2];
          sum += xr[4800] * fw[0][3];
          u32x2 v = {cvtpk(sum[0], sum[1]), cvtpk(sum[2], sum[3])};
          *(u32x2*)(otile + (0 + r) * 512 + (((7) ^ ((0 + r) & 31)) * 16) + 8 * h) = v; }
        { f32x4 sum = acc_read4<144>(); sum *= fw[1][0];
          sum += xr[1792] * fw[1][1];
          sum += xr[3328] * fw[1][2];
          sum += xr[4864] * fw[1][3];
          u32x2 v = {cvtpk(sum[0], sum[1]), cvtpk(sum[2], sum[3])};
          *(u32x2*)(otile + (32 + r) * 512 + (((4) ^ ((32 + r) & 31)) * 16) + 8 * h) = v; }
        { f32x4 sum = acc_read4<148>(); sum *= fw[1][0];
          sum += xr[1856] * fw[1][1];
          sum += xr[3392] * fw[1][2];
          sum += xr[4928] * fw[1][3];
          u32x2 v = {cvtpk(sum[0], sum[1]), cvtpk(sum[2], sum[3])};
          *(u32x2*)(otile + (32 + r) * 512 + (((5) ^ ((32 + r) & 31)) * 16) + 8 * h) = v; }
        { f32x4 sum = acc_read4<152>(); sum *= fw[1][0];
          sum += xr[1920] * fw[1][1];
          sum += xr[3456] * fw[1][2];
          sum += xr[4992] * fw[1][3];
          u32x2 v = {cvtpk(sum[0], sum[1]), cvtpk(sum[2], sum[3])};
          *(u32x2*)(otile + (32 + r) * 512 + (((6) ^ ((32 + r) & 31)) * 16) + 8 * h) = v; }
        { f32x4 sum = acc_read4<156>(); sum *= fw[1][0];
          sum += xr[1984] * fw[1][1];
          sum += xr[3520] * fw[1][2];
          sum += xr[5056] * fw[1][3];
          u32x2 v = {cvtpk(sum[0], sum[1]), cvtpk(sum[2], sum[3])};
          *(u32x2*)(otile + (32 + r) * 512 + (((7) ^ ((32 + r) & 31)) * 16) + 8 * h) = v; }
      } break;
      case 1: {
        { f32x4 sum = acc_read4<48>(); sum *= fw[0][1];
          sum += xr[0] * fw[0][0];
          sum += xr[3584] * fw[0][2];
          sum += xr[5120] * fw[0][3];
          u32x2 v = {cvtpk(sum[0], sum[1]), cvtpk(sum[2], sum[3])};
          *(u32x2*)(otile + (0 + r) * 512 + (((12) ^ ((0 + r) & 31)) * 16) + 8 * h) = v; }
        { f32x4 sum = acc_read4<52>(); sum *= fw[0][1];
          sum += xr[64] * fw[0][0];
          sum += xr[3648] * fw[0][2];
          sum += xr[5184] * fw[0][3];
          u32x2 v = {cvtpk(sum[0], sum[1]), cvtpk(sum[2], sum[3])};
          *(u32x2*)(otile + (0 + r) * 512 + (((13) ^ ((0 + r) & 31)) * 16) + 8 * h) = v; }
        { f32x4 sum = acc_read4<56>(); sum *= fw[0][1];
          sum += xr[128] * fw[0][0];
          sum += xr[3712] * fw[0][2];
          sum += xr[5248] * fw[0][3];
          u32x2 v = {cvtpk(sum[0], sum[1]), cvtpk(sum[2], sum[3])};
          *(u32x2*)(otile + (0 + r) * 512 + (((14) ^ ((0 + r) & 31)) * 16) + 8 * h) = v; }
        { f32x4 sum = acc_read4<60>(); sum *= fw[0][1];
          sum += xr[192] * fw[0][0];
          sum += xr[3776] * fw[0][2];
          sum += xr[5312] * fw[0][3];
          u32x2 v = {cvtpk(sum[0], sum[1]), cvtpk(sum[2], sum[3])};
          *(u32x2*)(otile + (0 + r) * 512 + (((15) ^ ((0 + r) & 31)) * 16) + 8 * h) = v; }
        { f32x4 sum = acc_read4<176>(); sum *= fw[1][1];
          sum += xr[256] * fw[1][0];
          sum += xr[3840] * fw[1][2];
          sum += xr[5376] * fw[1][3];
          u32x2 v = {cvtpk(sum[0], sum[1]), cvtpk(sum[2], sum[3])};
          *(u32x2*)(otile + (32 + r) * 512 + (((12) ^ ((32 + r) & 31)) * 16) + 8 * h) = v; }
        { f32x4 sum = acc_read4<180>(); sum *= fw[1][1];
          sum += xr[320] * fw[1][0];
          sum += xr[3904] * fw[1][2];
          sum += xr[5440] * fw[1][3];
          u32x2 v = {cvtpk(sum[0], sum[1]), cvtpk(sum[2], sum[3])};
          *(u32x2*)(otile + (32 + r) * 512 + (((13) ^ ((32 + r) & 31)) * 16) + 8 * h) = v; }
        { f32x4 sum = acc_read4<184>(); sum *= fw[1][1];
          sum += xr[384] * fw[1][0];
          sum += xr[3968] * fw[1][2];
          sum += xr[5504] * fw[1][3];
          u32x2 v = {cvtpk(sum[0], sum[1]), cvtpk(sum[2], sum[3])};
          *(u32x2*)(otile + (32 + r) * 512 + (((14) ^ ((32 + r) & 31)) * 16) + 8 * h) = v; }
        { f32x4 sum = acc_read4<188>(); sum *= fw[1][1];
          sum += xr[448] * fw[1][0];
          sum += xr[4032] * fw[1][2];
          sum += xr[5568] * fw[1][3];
          u32x2 v = {cvtpk(sum[0], sum[1]), cvtpk(sum[2], sum[3])};
          *(u32x2*)(otile + (32 + r) * 512 + (((15) ^ ((32 + r) & 31)) * 16) + 8 * h) = v; }
      } break;
      case 2: {
        { f32x4 sum = acc_read4<80>(); sum *= fw[0][2];
          sum += xr[512] * fw[0][0];
          sum += xr[2048] * fw[0][1];
          sum += xr[5632] * fw[0][3];
          u32x2 v = {cvtpk(sum[0], sum[1]), cvtpk(sum[2], sum[3])};
          *(u32x2*)(otile + (0 + r) * 512 + (((20) ^ ((0 + r) & 31)) * 16) + 8 * h) = v; }
        { f32x4 sum = acc_read4<84>(); sum *= fw[0][2];
          sum += xr[576] * fw[0][0];
          sum += xr[2112] * fw[0][1];
          sum += xr[5696] * fw[0][3];
          u32x2 v = {cvtpk(sum[0], sum[1]), cvtpk(sum[2], sum[3])};
          *(u32x2*)(otile + (0 + r) * 512 + (((21) ^ ((0 + r) & 31)) * 16) + 8 * h) = v; }
        { f32x4 sum = acc_read4<88>(); sum *= fw[0][2];
          sum += xr[640] * fw[0][0];
          sum += xr[2176] * fw[0][1];
          sum += xr[5760] * fw[0][3];
          u32x2 v = {cvtpk(sum[0], sum[1]), cvtpk(sum[2], sum[3])};
          *(u32x2*)(otile + (0 + r) * 512 + (((22) ^ ((0 + r) & 31)) * 16) + 8 * h) = v; }
        { f32x4 sum = acc_read4<92>(); sum *= fw[0][2];
          sum += xr[704] * fw[0][0];
          sum += xr[2240] * fw[0][1];
          sum += xr[5824] * fw[0][3];
          u32x2 v = {cvtpk(sum[0], sum[1]), cvtpk(sum[2], sum[3])};
          *(u32x2*)(otile + (0 + r) * 512 + (((23) ^ ((0 + r) & 31)) * 16) + 8 * h) = v; }
        { f32x4 sum = acc_read4<208>(); sum *= fw[1][2];
          sum += xr[768] * fw[1][0];
          sum += xr[2304] * fw[1][1];
          sum += xr[5888] * fw[1][3];
          u32x2 v = {cvtpk(sum[0], sum[1]), cvtpk(sum[2], sum[3])};
          *(u32x2*)(otile + (32 + r) * 512 + (((20) ^ ((32 + r) & 31)) * 16) + 8 * h) = v; }
        { f32x4 sum = acc_read4<212>(); sum *= fw[1][2];
          sum += xr[832] * fw[1][0];
          sum += xr[2368] * fw[1][1];
          sum += xr[5952] * fw[1][3];
          u32x2 v = {cvtpk(sum[0], sum[1]), cvtpk(sum[2], sum[3])};
          *(u32x2*)(otile + (32 + r) * 512 + (((21) ^ ((32 + r) & 31)) * 16) + 8 * h) = v; }
        { f32x4 sum = acc_read4<216>(); sum *= fw[1][2];
          sum += xr[896] * fw[1][0];
          sum += xr[2432] * fw[1][1];
          sum += xr[6016] * fw[1][3];
          u32x2 v = {cvtpk(sum[0], sum[1]), cvtpk(sum[2], sum[3])};
          *(u32x2*)(otile + (32 + r) * 512 + (((22) ^ ((32 + r) & 31)) * 16) + 8 * h) = v; }
        { f32x4 sum = acc_read4<220>(); sum *= fw[1][2];
          sum += xr[960] * fw[1][0];
          sum += xr[2496] * fw[1][1];
          sum += xr[6080] * fw[1][3];
          u32x2 v = {cvtpk(sum[0], sum[1]), cvtpk(sum[2], sum[3])};
          *(u32x2*)(otile + (32 + r) * 512 + (((23) ^ ((32 + r) & 31)) * 16) + 8 * h) = v; }
      } break;
      case 3: {
        { f32x4 sum = acc_read4<112>(); sum *= fw[0][3];
          sum += xr[1024] * fw[0][0];
          sum += xr[2560] * fw[0][1];
          sum += xr[4096] * fw[0][2];
          u32x2 v = {cvtpk(sum[0], sum[1]), cvtpk(sum[2], sum[3])};
          *(u32x2*)(otile + (0 + r) * 512 + (((28) ^ ((0 + r) & 31)) * 16) + 8 * h) = v; }
        { f32x4 sum = acc_read4<116>(); sum *= fw[0][3];
          sum += xr[1088] * fw[0][0];
          sum += xr[2624] * fw[0][1];
          sum += xr[4160] * fw[0][2];
          u32x2 v = {cvtpk(sum[0], sum[1]), cvtpk(sum[2], sum[3])};
          *(u32x2*)(otile + (0 + r) * 512 + (((29) ^ ((0 + r) & 31)) * 16) + 8 * h) = v; }
        { f32x4 sum = acc_read4<120>(); sum *= fw[0][3];
          sum += xr[1152] * fw[0][0];
          sum += xr[2688] * fw[0][1];
          sum += xr[4224] * fw[0][2];
          u32x2 v = {cvtpk(sum[0], sum[1]), cvtpk(sum[2], sum[3])};
          *(u32x2*)(otile + (0 + r) * 512 + (((30) ^ ((0 + r) & 31)) * 16) + 8 * h) = v; }
        { f32x4 sum = acc_read4<124>(); sum *= fw[0][3];
          sum += xr[1216] * fw[0][0];
          sum += xr[2752] * fw[0][1];
          sum += xr[4288] * fw[0][2];
          u32x2 v = {cvtpk(sum[0], sum[1]), cvtpk(sum[2], sum[3])};
          *(u32x2*)(otile + (0 + r) * 512 + (((31) ^ ((0 + r) & 31)) * 16) + 8 * h) = v; }
        { f32x4 sum = acc_read4<240>(); sum *= fw[1][3];
          sum += xr[1280] * fw[1][0];
          sum += xr[2816] * fw[1][1];
          sum += xr[4352] * fw[1][2];
          u32x2 v = {cvtpk(sum[0], sum[1]), cvtpk(sum[2], sum[3])};
          *(u32x2*)(otile + (32 + r) * 512 + (((28) ^ ((32 + r) & 31)) * 16) + 8 * h) = v; }
        { f32x4 sum = acc_read4<244>(); sum *= fw[1][3];
          sum += xr[1344] * fw[1][0];
          sum += xr[2880] * fw[1][1];
          sum += xr[4416] * fw[1][2];
          u32x2 v = {cvtpk(sum[0], sum[1]), cvtpk(sum[2], sum[3])};
          *(u32x2*)(otile + (32 + r) * 512 + (((29) ^ ((32 + r) & 31)) * 16) + 8 * h) = v; }
        { f32x4 sum = acc_read4<248>(); sum *= fw[1][3];
          sum += xr[1408] * fw[1][0];
          sum += xr[2944] * fw[1][1];
          sum += xr[4480] * fw[1][2];
          u32x2 v = {cvtpk(sum[0], sum[1]), cvtpk(sum[2], sum[3])};
          *(u32x2*)(otile + (32 + r) * 512 + (((30) ^ ((32 + r) & 31)) * 16) + 8 * h) = v; }
        { f32x4 sum = acc_read4<252>(); sum *= fw[1][3];
          sum += xr[1472] * fw[1][0];
          sum += xr[3008] * fw[1][1];
          sum += xr[4544] * fw[1][2];
          u32x2 v = {cvtpk(sum[0], sum[1]), cvtpk(sum[2], sum[3])};
          *(u32x2*)(otile + (32 + r) * 512 + (((31) ^ ((32 + r) & 31)) * 16) + 8 * h) = v; }
      } break;
    }
  }
  __syncthreads();
  asm volatile("s_waitcnt vmcnt(0)" :: "v"(c.pfd) : "memory");
  STAMP(15);
#pragma unroll
  for (int k = 0; k < 7; ++k) {
    const int R = 14 * w + 2 * k + h;
    u32x4 v = *(const u32x4*)(otile + R * 512 + ((r ^ (R & 31)) * 16));
    *(u32x4*)(o + ((size_t)(head * 8 + (r >> 2)) * NROW + row0 + R) * 32 + (r & 3) * 8) = v;
  }
  STAMP(10);
}
__global__ __launch_bounds__(256, 1) void qproj5_kernel(const u16* __restrict__ x, const u16* __restrict__ wq,
                                                         u16* __restrict__ qfrag, unsigned* __restrict__ cnt) {
  extern __shared__ __attribute__((aligned(16))) char lds[];
  const int tid = threadIdx.x, lane = tid & 63;
  const int w = __builtin_amdgcn_readfirstlane(tid >> 6);
  const int r = lane & 31, h = lane >> 5;
  const int bid = blockIdx.x;
  if (bid == 0 && tid < 64) cnt[tid] = 0u;
  const int xcd = bid & 7, j = bid >> 3;
  const int grp = (j >> 2) * 8 + xcd, sub = j & 3;
  const int b = grp >> 3, kvh = grp & 7;
  const int hl = sub >> 1, head = kvh * 2 + hl, rhalf = sub & 1;
  const int row0 = b * SQ + rhalf * 56;
  char* ring = lds + w * 40960;
  {
    __amdgpu_buffer_rsrc_t rx =
        __builtin_amdgcn_make_buffer_rsrc((void*)(x + (size_t)row0 * DM), 0, 56 * DM * 2, 0x00020000);
#pragma unroll
    for (int p = 0; p < 14; ++p) {
      const int ci = 64 * p + lane;
      const int lr = ci / 56, cp = ci - lr * 56;
      int gr = 16 * w + lr; gr = gr > 55 ? 55 : gr;
      const int cs = (cp & ~7) | ((cp ^ (lr >> 1)) & 7);
      __builtin_amdgcn_raw_ptr_buffer_load_lds(rx, (lds_vp)(ring + 3 * 8192 + p * 1024), 16, gr * (DM * 2) + cs * 16, 0, 0, 0);
    }
  }
  const int colw = head * DH + 64 * w;
  __amdgpu_buffer_rsrc_t rw = __builtin_amdgcn_make_buffer_rsrc((void*)(wq + colw), 0, (DM * NQ - colw) * 2, 0x00020000);
  const int drow = lane >> 3;
  const int wvoff = drow * (NQ * 2) + (((lane & 7) ^ (((drow >> 1) & 1) << 2)) * 16);
  auto wpiece = [&](int ch, int p) {
    __builtin_amdgcn_raw_ptr_buffer_load_lds(rw, (lds_vp)(ring + (ch % 3) * 8192 + p * 1024), 16, wvoff,
                                             (64 * ch + 8 * p) * (NQ * 2), 0, 0);
  };
#pragma unroll
  for (int ch = 0; ch < 3; ++ch)
#pragma unroll
    for (int p = 0; p < 8; ++p) wpiece(ch, p);
  const int g = lane >> 4, qq = (lane >> 2) & 3, pp = lane & 3;
  const unsigned trb = (unsigned)(size_t)ring + (8 * (g >> 1) + qq) * 128 + ((32 * (g & 1) + 8 * pp) ^ ((qq >> 1) << 6));
  const char* xa[2][4];
#pragma unroll
  for (int qb = 0; qb < 2; ++qb) {
    const int R = 32 * qb + r, lr = R & 15;
#pragma unroll
    for (int v = 0; v < 4; ++v)
      xa[qb][v] = lds + (R >> 4) * 40960 + 3 * 8192 + lr * 896 + ((((2 * v + h) ^ (lr >> 1)) & 7) * 16);
  }
  f32x16 qa[2][2];
#pragma unroll
  for (int a2 = 0; a2 < 2; ++a2)
#pragma unroll
    for (int b2 = 0; b2 < 2; ++b2)
#pragma unroll
      for (int i = 0; i < 16; ++i) qa[a2][b2][i] = 0.f;
  s16x4 fa[2][4];
  bf16x8 fx[2][2];
  auto rdstep = [&](int i, int bsel) {
    const int ch = i >> 2, s4 = i & 3;
    const unsigned tb = trb + (ch % 3) * 8192 + s4 * 2048;
    fa[bsel][0] = trread(tb);
    fa[bsel][1] = trread(tb + 512);
    fa[bsel][2] = trread(tb ^ 64);
    fa[bsel][3] = trread((tb + 512) ^ 64);
    asm volatile("ds_read_b128 %0, %1" : "=v"(fx[bsel][0]) : "v"((unsigned)(size_t)(xa[0][s4] + ch * 128)) : "memory");
    asm volatile("ds_read_b128 %0, %1" : "=v"(fx[bsel][1]) : "v"((unsigned)(size_t)(xa[1][s4] + ch * 128)) : "memory");
  };
  WAITV(16);
  __builtin_amdgcn_s_barrier();
  rdstep(0, 0);
#pragma unroll
  for (int i = 0; i < 28; ++i) {
    const int bs = i & 1, ch = i >> 2, s4 = i & 3;
    if (i + 1 < 28) {
      if (s4 == 3) { if (ch == 0) WAITV(8); else if (ch <= 4) WAITV(6); else WAITV(0); }
      rdstep(i + 1, bs ^ 1);
      asm volatile("s_waitcnt lgkmcnt(6)" : "+v"(fa[bs][0]), "+v"(fa[bs][1]), "+v"(fa[bs][2]), "+v"(fa[bs][3]), "+v"(fx[bs][0]), "+v"(fx[bs][1])::"memory");
    } else {
      asm volatile("s_waitcnt lgkmcnt(0)" : "+v"(fa[bs][0]), "+v"(fa[bs][1]), "+v"(fa[bs][2]), "+v"(fa[bs][3]), "+v"(fx[bs][0]), "+v"(fx[bs][1])::"memory");
    }
    bf16x8 A0 = cat8(fa[bs][0], fa[bs][1]), A1 = cat8(fa[bs][2], fa[bs][3]);
    qa[0][0] = __builtin_amdgcn_mfma_f32_32x32x16_bf16(A0, fx[bs][0], qa[0][0], 0, 0, 0);
    GAP();
    if (ch >= 1 && ch <= 4) wpiece(ch + 2, 2 * s4);
    GAP();
    qa[0][1] = __builtin_amdgcn_mfma_f32_32x32x16_bf16(A1, fx[bs][0], qa[0][1], 0, 0, 0);
    qa[1][0] = __builtin_amdgcn_mfma_f32_32x32x16_bf16(A0, fx[bs][1], qa[1][0], 0, 0, 0);
    GAP();
    if (ch >= 1 && ch <= 4) wpiece(ch + 2, 2 * s4 + 1);
    GAP();
    qa[1][1] = __builtin_amdgcn_mfma_f32_32x32x16_bf16(A1, fx[bs][1], qa[1][1], 0, 0, 0);
  }
#pragma unroll
  for (int qb = 0; qb < 2; ++qb) {
    const int lrow = 32 * qb + r;
    if (lrow < 56) {
      const int R = hl * 112 + rhalf * 56 + lrow;
      u16* base = qfrag + ((size_t)(grp * 8 + (R >> 5)) * 16 * 64 + (R & 31)) * 8 + 4 * h;
#pragma unroll
      for (int cb = 0; cb < 2; ++cb)
#pragma unroll
        for (int gg = 0; gg < 4; ++gg) {
          const int s = 4 * w + 2 * cb + (gg >> 1), hp = gg & 1;
          u32x2 v = {cvtpk(qa[qb][cb][4 * gg], qa[qb][cb][4 * gg + 1]), cvtpk(qa[qb][cb][4 * gg + 2], qa[qb][cb][4 * gg + 3])};
          *(u32x2*)(base + (s * 64 + 32 * hp) * 8) = v;
        }
    }
  }
}

template <int N> DI void dma5_unit(const Ctx4& c) {
  if constexpr (N < 32) {
    constexpr int slot = N % 16, t = unit_tile(N), hf = unit_half(N);
    if constexpr (!unit_is_v(N)) {
      __builtin_amdgcn_raw_ptr_buffer_load_lds(c.rk, (lds_vp)(c.ring + slot * 8192), 16, c.koff[0], 32 * t * 512 + hf * 256, 0, 0);
      __builtin_amdgcn_raw_ptr_buffer_load_lds(c.rk, (lds_vp)(c.ring + slot * 8192 + 1024), 16, c.koff[1], 32 * t * 512 + hf * 256, 0, 0);
    } else {
      __builtin_amdgcn_raw_ptr_buffer_load_lds(c.rv, (lds_vp)(c.ring + slot * 8192), 16, c.voff[0], (32 * t + 16 * hf) * 512, 0, 0);
      __builtin_amdgcn_raw_ptr_buffer_load_lds(c.rv, (lds_vp)(c.ring + slot * 8192 + 1024), 16, c.voff[1], (32 * t + 16 * hf) * 512, 0, 0);
    }
  }
}
template <int T, int J> DI bf16x8 kread5(const Ctx4& c) {
  constexpr int n = unit_k(T, J >> 3), slot = n % 16;
  bf16x8 r;
  asm volatile("ds_read_b128 %0, %1 offset:%2" : "=v"(r) : "v"(c.kaddr[J & 7] + (slot >> 3) * 65536), "i"((slot & 7) * 8192) : "memory");
  return r;
}
template <int T, int J> DI void phaseA5_step(Ctx4& c, bf16x8 (&kf)[4]) {
  if constexpr (J + 3 < 16) asm volatile("s_waitcnt lgkmcnt(3)" : "+v"(kf[J & 3])::"memory");
  else if constexpr (J + 3 == 16) asm volatile("s_waitcnt lgkmcnt(2)" : "+v"(kf[J & 3])::"memory");
  else if constexpr (J + 3 == 17) asm volatile("s_waitcnt lgkmcnt(1)" : "+v"(kf[J & 3])::"memory");
  else asm volatile("s_waitcnt lgkmcnt(0)" : "+v"(kf[J & 3])::"memory");
  qk_mfma<J == 0>(c.s[0], kf[J & 3], c.qf[0][J]);
  if constexpr (T >= 1 && (J & 3) == 1 && J < 16) dma5_unit<4 * T + 10 + (J >> 2)>(c);
  GAP();
  qk_mfma<J == 0>(c.s[1], kf[J & 3], c.qf[1][J]);
  if constexpr (J + 4 < 16) kf[J & 3] = kread5<T, J + 4>(c);
  GAP();
}
template <int T, int... Js> DI void phaseA5_(Ctx4& c, std::integer_sequence<int, Js...>) {
  bf16x8 kf[4];
  {
    constexpr int need = 4 * T + 1 > 31 ? 31 : 4 * T + 1;
    constexpr int lastiss = (T == 0 ? 13 : 4 * T + 9) > 31 ? 31 : (T == 0 ? 13 : 4 * T + 9);
    constexpr int after = 2 * (lastiss - need);
    asm volatile("s_waitcnt vmcnt(%0)" ::"i"(after) : "memory");
    __builtin_amdgcn_s_barrier();
  }
  kf[0] = kread5<T, 0>(c); kf[1] = kread5<T, 1>(c); kf[2] = kread5<T, 2>(c); kf[3] = kread5<T, 3>(c);
  GAP();
  (phaseA5_step<T, Js>(c, kf), ...);
}
template <int T> DI void phaseA5(Ctx4& c) { phaseA5_<T>(c, std::make_integer_sequence<int, 16>{}); }

template <int TV, int S> DI void vread5_set(const Ctx4& c, s16x4 (&v)[4]) {
  constexpr int ks = S >> 2, dbp = S & 3;
  constexpr int n = unit_v(TV, ks), slot = n % 16;
  constexpr int db0 = 2 * dbp, db1 = 2 * dbp + 1;
  constexpr int o0 = (slot & 7) * 8192 + (db0 >> 2) * 256, o1 = (slot & 7) * 8192 + (db1 >> 2) * 256;
  constexpr unsigned hi = (slot >> 3) * 65536;
  v[0] = trread_o<o0>(c.vaddr[db0 & 3] + hi);
  v[1] = trread_o<o0 + 4096>(c.vaddr[db0 & 3] + hi);
  v[2] = trread_o<o1>(c.vaddr[db1 & 3] + hi);
  v[3] = trread_o<o1 + 4096>(c.vaddr[db1 & 3] + hi);
}
template <int T, bool HASPV, bool HASSM, int M> DI void phaseB5_gap(Ctx4& c, s16x4 (&va)[4], s16x4 (&vb)[4]) {
  constexpr int TV = T - 1;
  constexpr int S = M >> 2, ks = S >> 2, dbp = S & 3, dd = (M >> 1) & 1, qb = M & 1;
  if constexpr (HASPV) {
    if constexpr ((M & 3) == 0) {
      if constexpr (S + 1 < 8) {
        if constexpr (S & 1) asm volatile("s_waitcnt lgkmcnt(4)" : "+v"(vb[0]), "+v"(vb[1]), "+v"(vb[2]), "+v"(vb[3])::"memory");
        else asm volatile("s_waitcnt lgkmcnt(4)" : "+v"(va[0]), "+v"(va[1]), "+v"(va[2]), "+v"(va[3])::"memory");
      } else {
        asm volatile("s_waitcnt lgkmcnt(0)" : "+v"(vb[0]), "+v"(vb[1]), "+v"(vb[2]), "+v"(vb[3])::"memory");
      }
    }
    {
      constexpr int db = 2 * dbp + dd;
      const bf16x8 vf = (S & 1) ? cat8(vb[2 * dd], vb[2 * dd + 1]) : cat8(va[2 * dd], va[2 * dd + 1]);
      pv_mfma<(qb * 8 + db) * 16, (TV == 0 && ks == 0)>(vf, c.pf[qb][ks]);
    }
    if constexpr ((M & 3) == 3 && S + 2 < 8) {
      if constexpr (S & 1) vread5_set<TV, S + 2>(c, vb); else vread5_set<TV, S + 2>(c, va);
    }
  }
  if constexpr (HASSM) sm_slice<T, M>(c);
  GAP();
}
template <int T, bool HASPV, bool HASSM, int... Ms> DI void phaseB5_(Ctx4& c, std::integer_sequence<int, Ms...>) {
  constexpr int TV = T - 1;
  s16x4 va[4], vb[4];
  if constexpr (!HASSM) {
    asm volatile("s_waitcnt vmcnt(0)" ::: "memory");
    __builtin_amdgcn_s_barrier();
  }
  if constexpr (HASPV) {
    vread5_set<TV, 0>(c, va);
    vread5_set<TV, 1>(c, vb);
  }
  if constexpr (HASSM) asm volatile("s_nop 7\n\ts_nop 7" : "+v"(c.s[0]), "+v"(c.s[1]));
  GAP();
  (phaseB5_gap<T, HASPV, HASSM, Ms>(c, va, vb), ...);
  if constexpr (HASSM) {
    c.l[0] += c.rs[0]; c.l[1] += c.rs[1];
#pragma unroll
    for (int qb = 0; qb < 2; ++qb)
#pragma unroll
      for (int ks = 0; ks < 2; ++ks) {
        u32x4 wv = {c.pn[qb][4 * ks], c.pn[qb][4 * ks + 1], c.pn[qb][4 * ks + 2], c.pn[qb][4 * ks + 3]};
        c.pf[qb][ks] = __builtin_bit_cast(bf16x8, wv);
      }
  }
  if constexpr (HASPV && HASSM) {
    if (__builtin_expect(c.pend, 0)) {
      float t0, t1;
      asm volatile("s_nop 7\n\ts_nop 7\n\t"
                   "v_accvgpr_read_b32 %0, a0\n\tv_mul_f32 %0, %2, %0\n\tv_accvgpr_write_b32 a0, %0\n\t"
                   "v_accvgpr_read_b32 %1, a1\n\tv_mul_f32 %1, %2, %1\n\tv_accvgpr_write_b32 a1, %1\n\t"
                   "v_accvgpr_read_b32 %0, a2\n\tv_mul_f32 %0, %2, %0\n\tv_accvgpr_write_b32 a2, %0\n\t"
                   "v_accvgpr_read_b32 %1, a3\n\tv_mul_f32 %1, %2, %1\n\tv_accvgpr_write_b32 a3, %1\n\t"
                   "v_accvgpr_read_b32 %0, a4\n\tv_mul_f32 %0, %2, %0\n\tv_accvgpr_write_b32 a4, %0\n\t"
                   "v_accvgpr_read_b32 %1, a5\n\tv_mul_f32 %1, %2, %1\n\tv_accvgpr_write_b32 a5, %1\n\t"
                   "v_accvgpr_read_b32 %0, a6\n\tv_mul_f32 %0, %2, %0\n\tv_accvgpr_write_b32 a6, %0\n\t"
                   "v_accvgpr_read_b32 %1, a7\n\tv_mul_f32 %1, %2, %1\n\tv_accvgpr_write_b32 a7, %1\n\t"
                   "v_accvgpr_read_b32 %0, a8\n\tv_mul_f32 %0, %2, %0\n\tv_accvgpr_write_b32 a8, %0\n\t"
                   "v_accvgpr_read_b32 %1, a9\n\tv_mul_f32 %1, %2, %1\n\tv_accvgpr_write_b32 a9, %1\n\t"
                   "v_accvgpr_read_b32 %0, a10\n\tv_mul_f32 %0, %2, %0\n\tv_accvgpr_write_b32 a10, %0\n\t"
                   "v_accvgpr_read_b32 %1, a11\n\tv_mul_f32 %1, %2, %1\n\tv_accvgpr_write_b32 a11, %1\n\t"
                   "v_accvgpr_read_b32 %0, a12\n\tv_mul_f32 %0, %2, %0\n\tv_accvgpr_write_b32 a12, %0\n\t"
                   "v_accvgpr_read_b32 %1, a13\n\tv_mul_f32 %1, %2, %1\n\tv_accvgpr_write_b32 a13, %1\n\t"
                   "v_accvgpr_read_b32 %0, a14\n\tv_mul_f32 %0, %2, %0\n\tv_accvgpr_write_b32 a14, %0\n\t"
                   "v_accvgpr_read_b32 %1, a15\n\tv_mul_f32 %1, %2, %1\n\tv_accvgpr_write_b32 a15, %1\n\t"
                   "v_accvgpr_read_b32 %0, a16\n\tv_mul_f32 %0, %2, %0\n\tv_accvgpr_write_b32 a16, %0\n\t"
                   "v_accvgpr_read_b32 %1, a17\n\tv_mul_f32 %1, %2, %1\n\tv_accvgpr_write_b32 a17, %1\n\t"
                   "v_accvgpr_read_b32 %0, a18\n\tv_mul_f32 %0, %2, %0\n\tv_accvgpr_write_b32 a18, %0\n\t"
                   "v_accvgpr_read_b32 %1, a19\n\tv_mul_f32 %1, %2, %1\n\tv_accvgpr_write_b32 a19, %1\n\t"
                   "v_accvgpr_read_b32 %0, a20\n\tv_mul_f32 %0, %2, %0\n\tv_accvgpr_write_b32 a20, %0\n\t"
                   "v_accvgpr_read_b32 %1, a21\n\tv_mul_f32 %1, %2, %1\n\tv_accvgpr_write_b32 a21, %1\n\t"
                   "v_accvgpr_read_b32 %0, a22\n\tv_mul_f32 %0, %2, %0\n\tv_accvgpr_write_b32 a22, %0\n\t"
                   "v_accvgpr_read_b32 %1, a23\n\tv_mul_f32 %1, %2, %1\n\tv_accvgpr_write_b32 a23, %1\n\t"
                   "v_accvgpr_read_b32 %0, a24\n\tv_mul_f32 %0, %2, %0\n\tv_accvgpr_write_b32 a24, %0\n\t"
                   "v_accvgpr_read_b32 %1, a25\n\tv_mul_f32 %1, %2, %1\n\tv_accvgpr_write_b32 a25, %1\n\t"
                   "v_accvgpr_read_b32 %0, a26\n\tv_mul_f32 %0, %2, %0\n\tv_accvgpr_write_b32 a26, %0\n\t"
                   "v_accvgpr_read_b32 %1, a27\n\tv_mul_f32 %1, %2, %1\n\tv_accvgpr_write_b32 a27, %1\n\t"
                   "v_accvgpr_read_b32 %0, a28\n\tv_mul_f32 %0, %2, %0\n\tv_accvgpr_write_b32 a28, %0\n\t"
                   "v_accvgpr_read_b32 %1, a29\n\tv_mul_f32 %1, %2, %1\n\tv_accvgpr_write_b32 a29, %1\n\t"
                   "v_accvgpr_read_b32 %0, a30\n\tv_mul_f32 %0, %2, %0\n\tv_accvgpr_write_b32 a30, %0\n\t"
                   "v_accvgpr_read_b32 %1, a31\n\tv_mul_f32 %1, %2, %1\n\tv_accvgpr_write_b32 a31, %1\n\t"
                   "v_accvgpr_read_b32 %0, a32\n\tv_mul_f32 %0, %2, %0\n\tv_accvgpr_write_b32 a32, %0\n\t"
                   "v_accvgpr_read_b32 %1, a33\n\tv_mul_f32 %1, %2, %1\n\tv_accvgpr_write_b32 a33, %1\n\t"
                   "v_accvgpr_read_b32 %0, a34\n\tv_mul_f32 %0, %2, %0\n\tv_accvgpr_write_b32 a34, %0\n\t"
                   "v_accvgpr_read_b32 %1, a35\n\tv_mul_f32 %1, %2, %1\n\tv_accvgpr_write_b32 a35, %1\n\t"
                   "v_accvgpr_read_b32 %0, a36\n\tv_mul_f32 %0, %2, %0\n\tv_accvgpr_write_b32 a36, %0\n\t"
                   "v_accvgpr_read_b32 %1, a37\n\tv_mul_f32 %1, %2, %1\n\tv_accvgpr_write_b32 a37, %1\n\t"
                   "v_accvgpr_read_b32 %0, a38\n\tv_mul_f32 %0, %2, %0\n\tv_accvgpr_write_b32 a38, %0\n\t"
                   "v_accvgpr_read_b32 %1, a39\n\tv_mul_f32 %1, %2, %1\n\tv_accvgpr_write_b32 a39, %1\n\t"
                   "v_accvgpr_read_b32 %0, a40\n\tv_mul_f32 %0, %2, %0\n\tv_accvgpr_write_b32 a40, %0\n\t"
                   "v_accvgpr_read_b32 %1, a41\n\tv_mul_f32 %1, %2, %1\n\tv_accvgpr_write_b32 a41, %1\n\t"
                   "v_accvgpr_read_b32 %0, a42\n\tv_mul_f32 %0, %2, %0\n\tv_accvgpr_write_b32 a42, %0\n\t"
                   "v_accvgpr_read_b32 %1, a43\n\tv_mul_f32 %1, %2, %1\n\tv_accvgpr_write_b32 a43, %1\n\t"
                   "v_accvgpr_read_b32 %0, a44\n\tv_mul_f32 %0, %2, %0\n\tv_accvgpr_write_b32 a44, %0\n\t"
                   "v_accvgpr_read_b32 %1, a45\n\tv_mul_f32 %1, %2, %1\n\tv_accvgpr_write_b32 a45, %1\n\t"
                   "v_accvgpr_read_b32 %0, a46\n\tv_mul_f32 %0, %2, %0\n\tv_accvgpr_write_b32 a46, %0\n\t"
                   "v_accvgpr_read_b32 %1, a47\n\tv_mul_f32 %1, %2, %1\n\tv_accvgpr_write_b32 a47, %1\n\t"
                   "v_accvgpr_read_b32 %0, a48\n\tv_mul_f32 %0, %2, %0\n\tv_accvgpr_write_b32 a48, %0\n\t"
                   "v_accvgpr_read_b32 %1, a49\n\tv_mul_f32 %1, %2, %1\n\tv_accvgpr_write_b32 a49, %1\n\t"
                   "v_accvgpr_read_b32 %0, a50\n\tv_mul_f32 %0, %2, %0\n\tv_accvgpr_write_b32 a50, %0\n\t"
                   "v_accvgpr_read_b32 %1, a51\n\tv_mul_f32 %1, %2, %1\n\tv_accvgpr_write_b32 a51, %1\n\t"
                   "v_accvgpr_read_b32 %0, a52\n\tv_mul_f32 %0, %2, %0\n\tv_accvgpr_write_b32 a52, %0\n\t"
                   "v_accvgpr_read_b32 %1, a53\n\tv_mul_f32 %1, %2, %1\n\tv_accvgpr_write_b32 a53, %1\n\t"
                   "v_accvgpr_read_b32 %0, a54\n\tv_mul_f32 %0, %2, %0\n\tv_accvgpr_write_b32 a54, %0\n\t"
                   "v_accvgpr_read_b32 %1, a55\n\tv_mul_f32 %1, %2, %1\n\tv_accvgpr_write_b32 a55, %1\n\t"
                   "v_accvgpr_read_b32 %0, a56\n\tv_mul_f32 %0, %2, %0\n\tv_accvgpr_write_b32 a56, %0\n\t"
                   "v_accvgpr_read_b32 %1, a57\n\tv_mul_f32 %1, %2, %1\n\tv_accvgpr_write_b32 a57, %1\n\t"
                   "v_accvgpr_read_b32 %0, a58\n\tv_mul_f32 %0, %2, %0\n\tv_accvgpr_write_b32 a58, %0\n\t"
                   "v_accvgpr_read_b32 %1, a59\n\tv_mul_f32 %1, %2, %1\n\tv_accvgpr_write_b32 a59, %1\n\t"
                   "v_accvgpr_read_b32 %0, a60\n\tv_mul_f32 %0, %2, %0\n\tv_accvgpr_write_b32 a60, %0\n\t"
                   "v_accvgpr_read_b32 %1, a61\n\tv_mul_f32 %1, %2, %1\n\tv_accvgpr_write_b32 a61, %1\n\t"
                   "v_accvgpr_read_b32 %0, a62\n\tv_mul_f32 %0, %2, %0\n\tv_accvgpr_write_b32 a62, %0\n\t"
                   "v_accvgpr_read_b32 %1, a63\n\tv_mul_f32 %1, %2, %1\n\tv_accvgpr_write_b32 a63, %1\n\t"
                   "v_accvgpr_read_b32 %0, a64\n\tv_mul_f32 %0, %2, %0\n\tv_accvgpr_write_b32 a64, %0\n\t"
                   "v_accvgpr_read_b32 %1, a65\n\tv_mul_f32 %1, %2, %1\n\tv_accvgpr_write_b32 a65, %1\n\t"
                   "v_accvgpr_read_b32 %0, a66\n\tv_mul_f32 %0, %2, %0\n\tv_accvgpr_write_b32 a66, %0\n\t"
                   "v_accvgpr_read_b32 %1, a67\n\tv_mul_f32 %1, %2, %1\n\tv_accvgpr_write_b32 a67, %1\n\t"
                   "v_accvgpr_read_b32 %0, a68\n\tv_mul_f32 %0, %2, %0\n\tv_accvgpr_write_b32 a68, %0\n\t"
                   "v_accvgpr_read_b32 %1, a69\n\tv_mul_f32 %1, %2, %1\n\tv_accvgpr_write_b32 a69, %1\n\t"
                   "v_accvgpr_read_b32 %0, a70\n\tv_mul_f32 %0, %2, %0\n\tv_accvgpr_write_b32 a70, %0\n\t"
                   "v_accvgpr_read_b32 %1, a71\n\tv_mul_f32 %1, %2, %1\n\tv_accvgpr_write_b32 a71, %1\n\t"
                   "v_accvgpr_read_b32 %0, a72\n\tv_mul_f32 %0, %2, %0\n\tv_accvgpr_write_b32 a72, %0\n\t"
                   "v_accvgpr_read_b32 %1, a73\n\tv_mul_f32 %1, %2, %1\n\tv_accvgpr_write_b32 a73, %1\n\t"
                   "v_accvgpr_read_b32 %0, a74\n\tv_mul_f32 %0, %2, %0\n\tv_accvgpr_write_b32 a74, %0\n\t"
                   "v_accvgpr_read_b32 %1, a75\n\tv_mul_f32 %1, %2, %1\n\tv_accvgpr_write_b32 a75, %1\n\t"
                   "v_accvgpr_read_b32 %0, a76\n\tv_mul_f32 %0, %2, %0\n\tv_accvgpr_write_b32 a76, %0\n\t"
                   "v_accvgpr_read_b32 %1, a77\n\tv_mul_f32 %1, %2, %1\n\tv_accvgpr_write_b32 a77, %1\n\t"
                   "v_accvgpr_read_b32 %0, a78\n\tv_mul_f32 %0, %2, %0\n\tv_accvgpr_write_b32 a78, %0\n\t"
                   "v_accvgpr_read_b32 %1, a79\n\tv_mul_f32 %1, %2, %1\n\tv_accvgpr_write_b32 a79, %1\n\t"
                   "v_accvgpr_read_b32 %0, a80\n\tv_mul_f32 %0, %2, %0\n\tv_accvgpr_write_b32 a80, %0\n\t"
                   "v_accvgpr_read_b32 %1, a81\n\tv_mul_f32 %1, %2, %1\n\tv_accvgpr_write_b32 a81, %1\n\t"
                   "v_accvgpr_read_b32 %0, a82\n\tv_mul_f32 %0, %2, %0\n\tv_accvgpr_write_b32 a82, %0\n\t"
                   "v_accvgpr_read_b32 %1, a83\n\tv_mul_f32 %1, %2, %1\n\tv_accvgpr_write_b32 a83, %1\n\t"
                   "v_accvgpr_read_b32 %0, a84\n\tv_mul_f32 %0, %2, %0\n\tv_accvgpr_write_b32 a84, %0\n\t"
                   "v_accvgpr_read_b32 %1, a85\n\tv_mul_f32 %1, %2, %1\n\tv_accvgpr_write_b32 a85, %1\n\t"
                   "v_accvgpr_read_b32 %0, a86\n\tv_mul_f32 %0, %2, %0\n\tv_accvgpr_write_b32 a86, %0\n\t"
                   "v_accvgpr_read_b32 %1, a87\n\tv_mul_f32 %1, %2, %1\n\tv_accvgpr_write_b32 a87, %1\n\t"
                   "v_accvgpr_read_b32 %0, a88\n\tv_mul_f32 %0, %2, %0\n\tv_accvgpr_write_b32 a88, %0\n\t"
                   "v_accvgpr_read_b32 %1, a89\n\tv_mul_f32 %1, %2, %1\n\tv_accvgpr_write_b32 a89, %1\n\t"
                   "v_accvgpr_read_b32 %0, a90\n\tv_mul_f32 %0, %2, %0\n\tv_accvgpr_write_b32 a90, %0\n\t"
                   "v_accvgpr_read_b32 %1, a91\n\tv_mul_f32 %1, %2, %1\n\tv_accvgpr_write_b32 a91, %1\n\t"
                   "v_accvgpr_read_b32 %0, a92\n\tv_mul_f32 %0, %2, %0\n\tv_accvgpr_write_b32 a92, %0\n\t"
                   "v_accvgpr_read_b32 %1, a93\n\tv_mul_f32 %1, %2, %1\n\tv_accvgpr_write_b32 a93, %1\n\t"
                   "v_accvgpr_read_b32 %0, a94\n\tv_mul_f32 %0, %2, %0\n\tv_accvgpr_write_b32 a94, %0\n\t"
                   "v_accvgpr_read_b32 %1, a95\n\tv_mul_f32 %1, %2, %1\n\tv_accvgpr_write_b32 a95, %1\n\t"
                   "v_accvgpr_read_b32 %0, a96\n\tv_mul_f32 %0, %2, %0\n\tv_accvgpr_write_b32 a96, %0\n\t"
                   "v_accvgpr_read_b32 %1, a97\n\tv_mul_f32 %1, %2, %1\n\tv_accvgpr_write_b32 a97, %1\n\t"
                   "v_accvgpr_read_b32 %0, a98\n\tv_mul_f32 %0, %2, %0\n\tv_accvgpr_write_b32 a98, %0\n\t"
                   "v_accvgpr_read_b32 %1, a99\n\tv_mul_f32 %1, %2, %1\n\tv_accvgpr_write_b32 a99, %1\n\t"
                   "v_accvgpr_read_b32 %0, a100\n\tv_mul_f32 %0, %2, %0\n\tv_accvgpr_write_b32 a100, %0\n\t"
                   "v_accvgpr_read_b32 %1, a101\n\tv_mul_f32 %1, %2, %1\n\tv_accvgpr_write_b32 a101, %1\n\t"
                   "v_accvgpr_read_b32 %0, a102\n\tv_mul_f32 %0, %2, %0\n\tv_accvgpr_write_b32 a102, %0\n\t"
                   "v_accvgpr_read_b32 %1, a103\n\tv_mul_f32 %1, %2, %1\n\tv_accvgpr_write_b32 a103, %1\n\t"
                   "v_accvgpr_read_b32 %0, a104\n\tv_mul_f32 %0, %2, %0\n\tv_accvgpr_write_b32 a104, %0\n\t"
                   "v_accvgpr_read_b32 %1, a105\n\tv_mul_f32 %1, %2, %1\n\tv_accvgpr_write_b32 a105, %1\n\t"
                   "v_accvgpr_read_b32 %0, a106\n\tv_mul_f32 %0, %2, %0\n\tv_accvgpr_write_b32 a106, %0\n\t"
                   "v_accvgpr_read_b32 %1, a107\n\tv_mul_f32 %1, %2, %1\n\tv_accvgpr_write_b32 a107, %1\n\t"
                   "v_accvgpr_read_b32 %0, a108\n\tv_mul_f32 %0, %2, %0\n\tv_accvgpr_write_b32 a108, %0\n\t"
                   "v_accvgpr_read_b32 %1, a109\n\tv_mul_f32 %1, %2, %1\n\tv_accvgpr_write_b32 a109, %1\n\t"
                   "v_accvgpr_read_b32 %0, a110\n\tv_mul_f32 %0, %2, %0\n\tv_accvgpr_write_b32 a110, %0\n\t"
                   "v_accvgpr_read_b32 %1, a111\n\tv_mul_f32 %1, %2, %1\n\tv_accvgpr_write_b32 a111, %1\n\t"
                   "v_accvgpr_read_b32 %0, a112\n\tv_mul_f32 %0, %2, %0\n\tv_accvgpr_write_b32 a112, %0\n\t"
                   "v_accvgpr_read_b32 %1, a113\n\tv_mul_f32 %1, %2, %1\n\tv_accvgpr_write_b32 a113, %1\n\t"
                   "v_accvgpr_read_b32 %0, a114\n\tv_mul_f32 %0, %2, %0\n\tv_accvgpr_write_b32 a114, %0\n\t"
                   "v_accvgpr_read_b32 %1, a115\n\tv_mul_f32 %1, %2, %1\n\tv_accvgpr_write_b32 a115, %1\n\t"
                   "v_accvgpr_read_b32 %0, a116\n\tv_mul_f32 %0, %2, %0\n\tv_accvgpr_write_b32 a116, %0\n\t"
                   "v_accvgpr_read_b32 %1, a117\n\tv_mul_f32 %1, %2, %1\n\tv_accvgpr_write_b32 a117, %1\n\t"
                   "v_accvgpr_read_b32 %0, a118\n\tv_mul_f32 %0, %2, %0\n\tv_accvgpr_write_b32 a118, %0\n\t"
                   "v_accvgpr_read_b32 %1, a119\n\tv_mul_f32 %1, %2, %1\n\tv_accvgpr_write_b32 a119, %1\n\t"
                   "v_accvgpr_read_b32 %0, a120\n\tv_mul_f32 %0, %2, %0\n\tv_accvgpr_write_b32 a120, %0\n\t"
                   "v_accvgpr_read_b32 %1, a121\n\tv_mul_f32 %1, %2, %1\n\tv_accvgpr_write_b32 a121, %1\n\t"
                   "v_accvgpr_read_b32 %0, a122\n\tv_mul_f32 %0, %2, %0\n\tv_accvgpr_write_b32 a122, %0\n\t"
                   "v_accvgpr_read_b32 %1, a123\n\tv_mul_f32 %1, %2, %1\n\tv_accvgpr_write_b32 a123, %1\n\t"
                   "v_accvgpr_read_b32 %0, a124\n\tv_mul_f32 %0, %2, %0\n\tv_accvgpr_write_b32 a124, %0\n\t"
                   "v_accvgpr_read_b32 %1, a125\n\tv_mul_f32 %1, %2, %1\n\tv_accvgpr_write_b32 a125, %1\n\t"
                   "v_accvgpr_read_b32 %0, a126\n\tv_mul_f32 %0, %2, %0\n\tv_accvgpr_write_b32 a126, %0\n\t"
                   "v_accvgpr_read_b32 %1, a127\n\tv_mul_f32 %1, %2, %1\n\tv_accvgpr_write_b32 a127, %1\n\t"
                   "v_accvgpr_read_b32 %0, a128\n\tv_mul_f32 %0, %3, %0\n\tv_accvgpr_write_b32 a128, %0\n\t"
                   "v_accvgpr_read_b32 %1, a129\n\tv_mul_f32 %1, %3, %1\n\tv_accvgpr_write_b32 a129, %1\n\t"
                   "v_accvgpr_read_b32 %0, a130\n\tv_mul_f32 %0, %3, %0\n\tv_accvgpr_write_b32 a130, %0\n\t"
                   "v_accvgpr_read_b32 %1, a131\n\tv_mul_f32 %1, %3, %1\n\tv_accvgpr_write_b32 a131, %1\n\t"
                   "v_accvgpr_read_b32 %0, a132\n\tv_mul_f32 %0, %3, %0\n\tv_accvgpr_write_b32 a132, %0\n\t"
                   "v_accvgpr_read_b32 %1, a133\n\tv_mul_f32 %1, %3, %1\n\tv_accvgpr_write_b32 a133, %1\n\t"
                   "v_accvgpr_read_b32 %0, a134\n\tv_mul_f32 %0, %3, %0\n\tv_accvgpr_write_b32 a134, %0\n\t"
                   "v_accvgpr_read_b32 %1, a135\n\tv_mul_f32 %1, %3, %1\n\tv_accvgpr_write_b32 a135, %1\n\t"
                   "v_accvgpr_read_b32 %0, a136\n\tv_mul_f32 %0, %3, %0\n\tv_accvgpr_write_b32 a136, %0\n\t"
                   "v_accvgpr_read_b32 %1, a137\n\tv_mul_f32 %1, %3, %1\n\tv_accvgpr_write_b32 a137, %1\n\t"
                   "v_accvgpr_read_b32 %0, a138\n\tv_mul_f32 %0, %3, %0\n\tv_accvgpr_write_b32 a138, %0\n\t"
                   "v_accvgpr_read_b32 %1, a139\n\tv_mul_f32 %1, %3, %1\n\tv_accvgpr_write_b32 a139, %1\n\t"
                   "v_accvgpr_read_b32 %0, a140\n\tv_mul_f32 %0, %3, %0\n\tv_accvgpr_write_b32 a140, %0\n\t"
                   "v_accvgpr_read_b32 %1, a141\n\tv_mul_f32 %1, %3, %1\n\tv_accvgpr_write_b32 a141, %1\n\t"
                   "v_accvgpr_read_b32 %0, a142\n\tv_mul_f32 %0, %3, %0\n\tv_accvgpr_write_b32 a142, %0\n\t"
                   "v_accvgpr_read_b32 %1, a143\n\tv_mul_f32 %1, %3, %1\n\tv_accvgpr_write_b32 a143, %1\n\t"
                   "v_accvgpr_read_b32 %0, a144\n\tv_mul_f32 %0, %3, %0\n\tv_accvgpr_write_b32 a144, %0\n\t"
                   "v_accvgpr_read_b32 %1, a145\n\tv_mul_f32 %1, %3, %1\n\tv_accvgpr_write_b32 a145, %1\n\t"
                   "v_accvgpr_read_b32 %0, a146\n\tv_mul_f32 %0, %3, %0\n\tv_accvgpr_write_b32 a146, %0\n\t"
                   "v_accvgpr_read_b32 %1, a147\n\tv_mul_f32 %1, %3, %1\n\tv_accvgpr_write_b32 a147, %1\n\t"
                   "v_accvgpr_read_b32 %0, a148\n\tv_mul_f32 %0, %3, %0\n\tv_accvgpr_write_b32 a148, %0\n\t"
                   "v_accvgpr_read_b32 %1, a149\n\tv_mul_f32 %1, %3, %1\n\tv_accvgpr_write_b32 a149, %1\n\t"
                   "v_accvgpr_read_b32 %0, a150\n\tv_mul_f32 %0, %3, %0\n\tv_accvgpr_write_b32 a150, %0\n\t"
                   "v_accvgpr_read_b32 %1, a151\n\tv_mul_f32 %1, %3, %1\n\tv_accvgpr_write_b32 a151, %1\n\t"
                   "v_accvgpr_read_b32 %0, a152\n\tv_mul_f32 %0, %3, %0\n\tv_accvgpr_write_b32 a152, %0\n\t"
                   "v_accvgpr_read_b32 %1, a153\n\tv_mul_f32 %1, %3, %1\n\tv_accvgpr_write_b32 a153, %1\n\t"
                   "v_accvgpr_read_b32 %0, a154\n\tv_mul_f32 %0, %3, %0\n\tv_accvgpr_write_b32 a154, %0\n\t"
                   "v_accvgpr_read_b32 %1, a155\n\tv_mul_f32 %1, %3, %1\n\tv_accvgpr_write_b32 a155, %1\n\t"
                   "v_accvgpr_read_b32 %0, a156\n\tv_mul_f32 %0, %3, %0\n\tv_accvgpr_write_b32 a156, %0\n\t"
                   "v_accvgpr_read_b32 %1, a157\n\tv_mul_f32 %1, %3, %1\n\tv_accvgpr_write_b32 a157, %1\n\t"
                   "v_accvgpr_read_b32 %0, a158\n\tv_mul_f32 %0, %3, %0\n\tv_accvgpr_write_b32 a158, %0\n\t"
                   "v_accvgpr_read_b32 %1, a159\n\tv_mul_f32 %1, %3, %1\n\tv_accvgpr_write_b32 a159, %1\n\t"
                   "v_accvgpr_read_b32 %0, a160\n\tv_mul_f32 %0, %3, %0\n\tv_accvgpr_write_b32 a160, %0\n\t"
                   "v_accvgpr_read_b32 %1, a161\n\tv_mul_f32 %1, %3, %1\n\tv_accvgpr_write_b32 a161, %1\n\t"
                   "v_accvgpr_read_b32 %0, a162\n\tv_mul_f32 %0, %3, %0\n\tv_accvgpr_write_b32 a162, %0\n\t"
                   "v_accvgpr_read_b32 %1, a163\n\tv_mul_f32 %1, %3, %1\n\tv_accvgpr_write_b32 a163, %1\n\t"
                   "v_accvgpr_read_b32 %0, a164\n\tv_mul_f32 %0, %3, %0\n\tv_accvgpr_write_b32 a164, %0\n\t"
                   "v_accvgpr_read_b32 %1, a165\n\tv_mul_f32 %1, %3, %1\n\tv_accvgpr_write_b32 a165, %1\n\t"
                   "v_accvgpr_read_b32 %0, a166\n\tv_mul_f32 %0, %3, %0\n\tv_accvgpr_write_b32 a166, %0\n\t"
                   "v_accvgpr_read_b32 %1, a167\n\tv_mul_f32 %1, %3, %1\n\tv_accvgpr_write_b32 a167, %1\n\t"
                   "v_accvgpr_read_b32 %0, a168\n\tv_mul_f32 %0, %3, %0\n\tv_accvgpr_write_b32 a168, %0\n\t"
                   "v_accvgpr_read_b32 %1, a169\n\tv_mul_f32 %1, %3, %1\n\tv_accvgpr_write_b32 a169, %1\n\t"
                   "v_accvgpr_read_b32 %0, a170\n\tv_mul_f32 %0, %3, %0\n\tv_accvgpr_write_b32 a170, %0\n\t"
                   "v_accvgpr_read_b32 %1, a171\n\tv_mul_f32 %1, %3, %1\n\tv_accvgpr_write_b32 a171, %1\n\t"
                   "v_accvgpr_read_b32 %0, a172\n\tv_mul_f32 %0, %3, %0\n\tv_accvgpr_write_b32 a172, %0\n\t"
                   "v_accvgpr_read_b32 %1, a173\n\tv_mul_f32 %1, %3, %1\n\tv_accvgpr_write_b32 a173, %1\n\t"
                   "v_accvgpr_read_b32 %0, a174\n\tv_mul_f32 %0, %3, %0\n\tv_accvgpr_write_b32 a174, %0\n\t"
                   "v_accvgpr_read_b32 %1, a175\n\tv_mul_f32 %1, %3, %1\n\tv_accvgpr_write_b32 a175, %1\n\t"
                   "v_accvgpr_read_b32 %0, a176\n\tv_mul_f32 %0, %3, %0\n\tv_accvgpr_write_b32 a176, %0\n\t"
                   "v_accvgpr_read_b32 %1, a177\n\tv_mul_f32 %1, %3, %1\n\tv_accvgpr_write_b32 a177, %1\n\t"
                   "v_accvgpr_read_b32 %0, a178\n\tv_mul_f32 %0, %3, %0\n\tv_accvgpr_write_b32 a178, %0\n\t"
                   "v_accvgpr_read_b32 %1, a179\n\tv_mul_f32 %1, %3, %1\n\tv_accvgpr_write_b32 a179, %1\n\t"
                   "v_accvgpr_read_b32 %0, a180\n\tv_mul_f32 %0, %3, %0\n\tv_accvgpr_write_b32 a180, %0\n\t"
                   "v_accvgpr_read_b32 %1, a181\n\tv_mul_f32 %1, %3, %1\n\tv_accvgpr_write_b32 a181, %1\n\t"
                   "v_accvgpr_read_b32 %0, a182\n\tv_mul_f32 %0, %3, %0\n\tv_accvgpr_write_b32 a182, %0\n\t"
                   "v_accvgpr_read_b32 %1, a183\n\tv_mul_f32 %1, %3, %1\n\tv_accvgpr_write_b32 a183, %1\n\t"
                   "v_accvgpr_read_b32 %0, a184\n\tv_mul_f32 %0, %3, %0\n\tv_accvgpr_write_b32 a184, %0\n\t"
                   "v_accvgpr_read_b32 %1, a185\n\tv_mul_f32 %1, %3, %1\n\tv_accvgpr_write_b32 a185, %1\n\t"
                   "v_accvgpr_read_b32 %0, a186\n\tv_mul_f32 %0, %3, %0\n\tv_accvgpr_write_b32 a186, %0\n\t"
                   "v_accvgpr_read_b32 %1, a187\n\tv_mul_f32 %1, %3, %1\n\tv_accvgpr_write_b32 a187, %1\n\t"
                   "v_accvgpr_read_b32 %0, a188\n\tv_mul_f32 %0, %3, %0\n\tv_accvgpr_write_b32 a188, %0\n\t"
                   "v_accvgpr_read_b32 %1, a189\n\tv_mul_f32 %1, %3, %1\n\tv_accvgpr_write_b32 a189, %1\n\t"
                   "v_accvgpr_read_b32 %0, a190\n\tv_mul_f32 %0, %3, %0\n\tv_accvgpr_write_b32 a190, %0\n\t"
                   "v_accvgpr_read_b32 %1, a191\n\tv_mul_f32 %1, %3, %1\n\tv_accvgpr_write_b32 a191, %1\n\t"
                   "v_accvgpr_read_b32 %0, a192\n\tv_mul_f32 %0, %3, %0\n\tv_accvgpr_write_b32 a192, %0\n\t"
                   "v_accvgpr_read_b32 %1, a193\n\tv_mul_f32 %1, %3, %1\n\tv_accvgpr_write_b32 a193, %1\n\t"
                   "v_accvgpr_read_b32 %0, a194\n\tv_mul_f32 %0, %3, %0\n\tv_accvgpr_write_b32 a194, %0\n\t"
                   "v_accvgpr_read_b32 %1, a195\n\tv_mul_f32 %1, %3, %1\n\tv_accvgpr_write_b32 a195, %1\n\t"
                   "v_accvgpr_read_b32 %0, a196\n\tv_mul_f32 %0, %3, %0\n\tv_accvgpr_write_b32 a196, %0\n\t"
                   "v_accvgpr_read_b32 %1, a197\n\tv_mul_f32 %1, %3, %1\n\tv_accvgpr_write_b32 a197, %1\n\t"
                   "v_accvgpr_read_b32 %0, a198\n\tv_mul_f32 %0, %3, %0\n\tv_accvgpr_write_b32 a198, %0\n\t"
                   "v_accvgpr_read_b32 %1, a199\n\tv_mul_f32 %1, %3, %1\n\tv_accvgpr_write_b32 a199, %1\n\t"
                   "v_accvgpr_read_b32 %0, a200\n\tv_mul_f32 %0, %3, %0\n\tv_accvgpr_write_b32 a200, %0\n\t"
                   "v_accvgpr_read_b32 %1, a201\n\tv_mul_f32 %1, %3, %1\n\tv_accvgpr_write_b32 a201, %1\n\t"
                   "v_accvgpr_read_b32 %0, a202\n\tv_mul_f32 %0, %3, %0\n\tv_accvgpr_write_b32 a202, %0\n\t"
                   "v_accvgpr_read_b32 %1, a203\n\tv_mul_f32 %1, %3, %1\n\tv_accvgpr_write_b32 a203, %1\n\t"
                   "v_accvgpr_read_b32 %0, a204\n\tv_mul_f32 %0, %3, %0\n\tv_accvgpr_write_b32 a204, %0\n\t"
                   "v_accvgpr_read_b32 %1, a205\n\tv_mul_f32 %1, %3, %1\n\tv_accvgpr_write_b32 a205, %1\n\t"
                   "v_accvgpr_read_b32 %0, a206\n\tv_mul_f32 %0, %3, %0\n\tv_accvgpr_write_b32 a206, %0\n\t"
                   "v_accvgpr_read_b32 %1, a207\n\tv_mul_f32 %1, %3, %1\n\tv_accvgpr_write_b32 a207, %1\n\t"
                   "v_accvgpr_read_b32 %0, a208\n\tv_mul_f32 %0, %3, %0\n\tv_accvgpr_write_b32 a208, %0\n\t"
                   "v_accvgpr_read_b32 %1, a209\n\tv_mul_f32 %1, %3, %1\n\tv_accvgpr_write_b32 a209, %1\n\t"
                   "v_accvgpr_read_b32 %0, a210\n\tv_mul_f32 %0, %3, %0\n\tv_accvgpr_write_b32 a210, %0\n\t"
                   "v_accvgpr_read_b32 %1, a211\n\tv_mul_f32 %1, %3, %1\n\tv_accvgpr_write_b32 a211, %1\n\t"
                   "v_accvgpr_read_b32 %0, a212\n\tv_mul_f32 %0, %3, %0\n\tv_accvgpr_write_b32 a212, %0\n\t"
                   "v_accvgpr_read_b32 %1, a213\n\tv_mul_f32 %1, %3, %1\n\tv_accvgpr_write_b32 a213, %1\n\t"
                   "v_accvgpr_read_b32 %0, a214\n\tv_mul_f32 %0, %3, %0\n\tv_accvgpr_write_b32 a214, %0\n\t"
                   "v_accvgpr_read_b32 %1, a215\n\tv_mul_f32 %1, %3, %1\n\tv_accvgpr_write_b32 a215, %1\n\t"
                   "v_accvgpr_read_b32 %0, a216\n\tv_mul_f32 %0, %3, %0\n\tv_accvgpr_write_b32 a216, %0\n\t"
                   "v_accvgpr_read_b32 %1, a217\n\tv_mul_f32 %1, %3, %1\n\tv_accvgpr_write_b32 a217, %1\n\t"
                   "v_accvgpr_read_b32 %0, a218\n\tv_mul_f32 %0, %3, %0\n\tv_accvgpr_write_b32 a218, %0\n\t"
                   "v_accvgpr_read_b32 %1, a219\n\tv_mul_f32 %1, %3, %1\n\tv_accvgpr_write_b32 a219, %1\n\t"
                   "v_accvgpr_read_b32 %0, a220\n\tv_mul_f32 %0, %3, %0\n\tv_accvgpr_write_b32 a220, %0\n\t"
                   "v_accvgpr_read_b32 %1, a221\n\tv_mul_f32 %1, %3, %1\n\tv_accvgpr_write_b32 a221, %1\n\t"
                   "v_accvgpr_read_b32 %0, a222\n\tv_mul_f32 %0, %3, %0\n\tv_accvgpr_write_b32 a222, %0\n\t"
                   "v_accvgpr_read_b32 %1, a223\n\tv_mul_f32 %1, %3, %1\n\tv_accvgpr_write_b32 a223, %1\n\t"
                   "v_accvgpr_read_b32 %0, a224\n\tv_mul_f32 %0, %3, %0\n\tv_accvgpr_write_b32 a224, %0\n\t"
                   "v_accvgpr_read_b32 %1, a225\n\tv_mul_f32 %1, %3, %1\n\tv_accvgpr_write_b32 a225, %1\n\t"
                   "v_accvgpr_read_b32 %0, a226\n\tv_mul_f32 %0, %3, %0\n\tv_accvgpr_write_b32 a226, %0\n\t"
                   "v_accvgpr_read_b32 %1, a227\n\tv_mul_f32 %1, %3, %1\n\tv_accvgpr_write_b32 a227, %1\n\t"
                   "v_accvgpr_read_b32 %0, a228\n\tv_mul_f32 %0, %3, %0\n\tv_accvgpr_write_b32 a228, %0\n\t"
                   "v_accvgpr_read_b32 %1, a229\n\tv_mul_f32 %1, %3, %1\n\tv_accvgpr_write_b32 a229, %1\n\t"
                   "v_accvgpr_read_b32 %0, a230\n\tv_mul_f32 %0, %3, %0\n\tv_accvgpr_write_b32 a230, %0\n\t"
                   "v_accvgpr_read_b32 %1, a231\n\tv_mul_f32 %1, %3, %1\n\tv_accvgpr_write_b32 a231, %1\n\t"
                   "v_accvgpr_read_b32 %0, a232\n\tv_mul_f32 %0, %3, %0\n\tv_accvgpr_write_b32 a232, %0\n\t"
                   "v_accvgpr_read_b32 %1, a233\n\tv_mul_f32 %1, %3, %1\n\tv_accvgpr_write_b32 a233, %1\n\t"
                   "v_accvgpr_read_b32 %0, a234\n\tv_mul_f32 %0, %3, %0\n\tv_accvgpr_write_b32 a234, %0\n\t"
                   "v_accvgpr_read_b32 %1, a235\n\tv_mul_f32 %1, %3, %1\n\tv_accvgpr_write_b32 a235, %1\n\t"
                   "v_accvgpr_read_b32 %0, a236\n\tv_mul_f32 %0, %3, %0\n\tv_accvgpr_write_b32 a236, %0\n\t"
                   "v_accvgpr_read_b32 %1, a237\n\tv_mul_f32 %1, %3, %1\n\tv_accvgpr_write_b32 a237, %1\n\t"
                   "v_accvgpr_read_b32 %0, a238\n\tv_mul_f32 %0, %3, %0\n\tv_accvgpr_write_b32 a238, %0\n\t"
                   "v_accvgpr_read_b32 %1, a239\n\tv_mul_f32 %1, %3, %1\n\tv_accvgpr_write_b32 a239, %1\n\t"
                   "v_accvgpr_read_b32 %0, a240\n\tv_mul_f32 %0, %3, %0\n\tv_accvgpr_write_b32 a240, %0\n\t"
                   "v_accvgpr_read_b32 %1, a241\n\tv_mul_f32 %1, %3, %1\n\tv_accvgpr_write_b32 a241, %1\n\t"
                   "v_accvgpr_read_b32 %0, a242\n\tv_mul_f32 %0, %3, %0\n\tv_accvgpr_write_b32 a242, %0\n\t"
                   "v_accvgpr_read_b32 %1, a243\n\tv_mul_f32 %1, %3, %1\n\tv_accvgpr_write_b32 a243, %1\n\t"
                   "v_accvgpr_read_b32 %0, a244\n\tv_mul_f32 %0, %3, %0\n\tv_accvgpr_write_b32 a244, %0\n\t"
                   "v_accvgpr_read_b32 %1, a245\n\tv_mul_f32 %1, %3, %1\n\tv_accvgpr_write_b32 a245, %1\n\t"
                   "v_accvgpr_read_b32 %0, a246\n\tv_mul_f32 %0, %3, %0\n\tv_accvgpr_write_b32 a246, %0\n\t"
                   "v_accvgpr_read_b32 %1, a247\n\tv_mul_f32 %1, %3, %1\n\tv_accvgpr_write_b32 a247, %1\n\t"
                   "v_accvgpr_read_b32 %0, a248\n\tv_mul_f32 %0, %3, %0\n\tv_accvgpr_write_b32 a248, %0\n\t"
                   "v_accvgpr_read_b32 %1, a249\n\tv_mul_f32 %1, %3, %1\n\tv_accvgpr_write_b32 a249, %1\n\t"
                   "v_accvgpr_read_b32 %0, a250\n\tv_mul_f32 %0, %3, %0\n\tv_accvgpr_write_b32 a250, %0\n\t"
                   "v_accvgpr_read_b32 %1, a251\n\tv_mul_f32 %1, %3, %1\n\tv_accvgpr_write_b32 a251, %1\n\t"
                   "v_accvgpr_read_b32 %0, a252\n\tv_mul_f32 %0, %3, %0\n\tv_accvgpr_write_b32 a252, %0\n\t"
                   "v_accvgpr_read_b32 %1, a253\n\tv_mul_f32 %1, %3, %1\n\tv_accvgpr_write_b32 a253, %1\n\t"
                   "v_accvgpr_read_b32 %0, a254\n\tv_mul_f32 %0, %3, %0\n\tv_accvgpr_write_b32 a254, %0\n\t"
                   "v_accvgpr_read_b32 %1, a255\n\tv_mul_f32 %1, %3, %1\n\tv_accvgpr_write_b32 a255, %1\n\t"
                   "s_nop 1"
                   : "=&v"(t0), "=&v"(t1) : "v"(c.fct[0]), "v"(c.fct[1]) : ACLOB);
      c.pend = false;
    }
  }
  asm volatile("s_nop 1" ::: "memory");
  GAP();
}
template <int T, bool HASPV, bool HASSM> DI void phaseB5(Ctx4& c) {
  phaseB5_<T, HASPV, HASSM>(c, std::make_integer_sequence<int, 32>{});
}

__global__ __launch_bounds__(256, 1) void attn5_kernel(const u16* __restrict__ qfrag, const u16* __restrict__ kc,
                                                       const u16* __restrict__ vc, const u16* __restrict__ wo,
                                                       u16* __restrict__ o, u16* __restrict__ part,
                                                       float* __restrict__ mlg, unsigned* __restrict__ cnt DBGARG) {
  extern __shared__ __attribute__((aligned(16))) char lds[];
  STAMP(0);
  const int tid = threadIdx.x, lane = tid & 63;
  const int w = __builtin_amdgcn_readfirstlane(tid >> 6);
  const int r = lane & 31, h = lane >> 5;
  const int bid = blockIdx.x;
  const int xcd = bid & 7, j = bid >> 3;
  const int grp = (j >> 2) * 8 + xcd, sp = j & 3;
  const int b = grp >> 3, kvh = grp & 7;

  const u16* Kw = kc + ((size_t)(b * HKV + kvh) * SK + 256 * sp) * DH;
  const u16* Vw = vc + ((size_t)(b * HKV + kvh) * SK + 256 * sp) * DH;

  Ctx4 c;
  c.pfd = 0;
  c.ring = lds + w * 2048;
  c.rk = __builtin_amdgcn_make_buffer_rsrc((void*)Kw, 0, 256 * DH * 2, 0x00020000);
  c.rv = __builtin_amdgcn_make_buffer_rsrc((void*)Vw, 0, 256 * DH * 2, 0x00020000);
#pragma unroll
  for (int pi = 0; pi < 2; ++pi) {
    const int krow = 4 * (2 * w + pi) + (lane >> 4);
    c.koff[pi] = krow * 512 + (((lane & 15) ^ (krow & 15)) * 16);
    const int vrow = 2 * (2 * w + pi) + (lane >> 5);
    c.voff[pi] = vrow * 512 + (((lane & 31) ^ ((vrow & 3) << 2)) * 16);
  }
  {
    const u16* qp = qfrag + ((size_t)(grp * 8 + 2 * w) * 16 * 64 + lane) * 8;
#pragma unroll
    for (int qb = 0; qb < 2; ++qb)
#pragma unroll
      for (int s = 0; s < 16; ++s) c.qf[qb][s] = *(const bf16x8*)(qp + (size_t)(qb * 16 + s) * 512);
  }
  GAP();
  dma5_unit<0>(c); dma5_unit<1>(c); dma5_unit<2>(c); dma5_unit<3>(c); dma5_unit<4>(c); dma5_unit<5>(c); dma5_unit<6>(c);
  dma5_unit<7>(c); dma5_unit<8>(c); dma5_unit<9>(c); dma5_unit<10>(c); dma5_unit<11>(c); dma5_unit<12>(c); dma5_unit<13>(c);
#pragma unroll
  for (int qb = 0; qb < 2; ++qb)
#pragma unroll
    for (int s = 0; s < 16; ++s) asm volatile("" : "+v"(c.qf[qb][s]));
  if (w == 3) {
#pragma unroll
    for (int s = 0; s < 16; ++s) c.qf[1][s] = bf16x8{0, 0, 0, 0, 0, 0, 0, 0};
  }
  STAMP(1);
  {
    const unsigned rb = (unsigned)(size_t)lds;
#pragma unroll
    for (int ss = 0; ss < 8; ++ss) c.kaddr[ss] = rb + r * 256 + ((((2 * ss + h) ^ (r & 15))) * 16);
    const int g = lane >> 4, qq = (lane >> 2) & 3, pp = lane & 3;
#pragma unroll
    for (int d = 0; d < 4; ++d)
      c.vaddr[d] = rb + (4 * (g >> 1) + qq) * 512 + ((64 * d + 32 * (g & 1) + 8 * pp) ^ (qq << 6));
  }
  c.pend = false;
  c.fct[0] = 1.f; c.fct[1] = 1.f;

  phaseA5<0>(c); phaseB5<0, false, true>(c); STAMP(2);
  phaseA5<1>(c); phaseB5<1, true, true>(c); STAMP(3);
  phaseA5<2>(c); phaseB5<2, true, true>(c); STAMP(4);
  phaseA5<3>(c); phaseB5<3, true, true>(c); STAMP(5);
  phaseA5<4>(c); phaseB5<4, true, true>(c); STAMP(6);
  phaseA5<5>(c); phaseB5<5, true, true>(c); STAMP(7);
  phaseA5<6>(c); phaseB5<6, true, true>(c); STAMP(8);
  {
    int li = (bid * 4 + w) * 28 + (lane < 28 ? lane : 27);
    asm volatile("global_load_dword %0, %1, off" : "+v"(c.pfd) : "v"(wo + (size_t)li * 64) : "memory");
  }
  phaseA5<7>(c); phaseB5<7, true, true>(c); STAMP(9);
  phaseB5<8, true, false>(c);
  asm volatile("s_nop 7\n\ts_nop 7" :: "v"(c.pfd) : "memory");
  STAMP(13);

  float lt[2], inv[2];
#pragma unroll
  for (int qb = 0; qb < 2; ++qb) {
    auto rr = __builtin_amdgcn_permlane32_swap(__float_as_uint(c.l[qb]), __float_as_uint(c.l[qb]), false, false);
    lt[qb] = __uint_as_float(rr[0]) + __uint_as_float(rr[1]);
    inv[qb] = 1.f / lt[qb];
  }
  __amdgpu_buffer_rsrc_t rml = __builtin_amdgcn_make_buffer_rsrc((void*)(mlg + (size_t)grp * 4 * 512), 0, 4 * 512 * 4, 0x00020000);
  __amdgpu_buffer_rsrc_t rpt = __builtin_amdgcn_make_buffer_rsrc((void*)(part + (size_t)grp * 16 * 16384), 0, 16 * 32768, 0x00020000);
  if (h == 0) {
#pragma unroll
    for (int qb = 0; qb < 2; ++qb) {
      u32x2 v = {__float_as_uint(c.m[qb]), __float_as_uint(lt[qb])};
      __builtin_amdgcn_raw_buffer_store_b64(v, rml, (sp * 256 + 64 * w + 32 * qb + r) * 8, 0, 16);
    }
  }
  switch (sp) {
    case 0: {
      { f32x4 a = acc_read4<32>(), b2 = acc_read4<36>(); a *= inv[0]; b2 *= inv[0];
        u32x4 v = {cvtpk(a[0], a[1]), cvtpk(a[2], a[3]), cvtpk(b2[0], b2[1]), cvtpk(b2[2], b2[3])};
        __builtin_amdgcn_raw_buffer_store_b128(v, rpt, 32768 + w * 8192 + 0 + lane * 16, 0, 16); }
      { f32x4 a = acc_read4<40>(), b2 = acc_read4<44>(); a *= inv[0]; b2 *= inv[0];
        u32x4 v = {cvtpk(a[0], a[1]), cvtpk(a[2], a[3]), cvtpk(b2[0], b2[1]), cvtpk(b2[2], b2[3])};
        __builtin_amdgcn_raw_buffer_store_b128(v, rpt, 32768 + w * 8192 + 1024 + lane * 16, 0, 16); }
      { f32x4 a = acc_read4<48>(), b2 = acc_read4<52>(); a *= inv[0]; b2 *= inv[0];
        u32x4 v = {cvtpk(a[0], a[1]), cvtpk(a[2], a[3]), cvtpk(b2[0], b2[1]), cvtpk(b2[2], b2[3])};
        __builtin_amdgcn_raw_buffer_store_b128(v, rpt, 32768 + w * 8192 + 2048 + lane * 16, 0, 16); }
      { f32x4 a = acc_read4<56>(), b2 = acc_read4<60>(); a *= inv[0]; b2 *= inv[0];
        u32x4 v = {cvtpk(a[0], a[1]), cvtpk(a[2], a[3]), cvtpk(b2[0], b2[1]), cvtpk(b2[2], b2[3])};
        __builtin_amdgcn_raw_buffer_store_b128(v, rpt, 32768 + w * 8192 + 3072 + lane * 16, 0, 16); }
      { f32x4 a = acc_read4<160>(), b2 = acc_read4<164>(); a *= inv[1]; b2 *= inv[1];
        u32x4 v = {cvtpk(a[0], a[1]), cvtpk(a[2], a[3]), cvtpk(b2[0], b2[1]), cvtpk(b2[2], b2[3])};
        __builtin_amdgcn_raw_buffer_store_b128(v, rpt, 32768 + w * 8192 + 4096 + lane * 16, 0, 16); }
      { f32x4 a = acc_read4<168>(), b2 = acc_read4<172>(); a *= inv[1]; b2 *= inv[1];
        u32x4 v = {cvtpk(a[0], a[1]), cvtpk(a[2], a[3]), cvtpk(b2[0], b2[1]), cvtpk(b2[2], b2[3])};
        __builtin_amdgcn_raw_buffer_store_b128(v, rpt, 32768 + w * 8192 + 5120 + lane * 16, 0, 16); }
      { f32x4 a = acc_read4<176>(), b2 = acc_read4<180>(); a *= inv[1]; b2 *= inv[1];
        u32x4 v = {cvtpk(a[0], a[1]), cvtpk(a[2], a[3]), cvtpk(b2[0], b2[1]), cvtpk(b2[2], b2[3])};
        __builtin_amdgcn_raw_buffer_store_b128(v, rpt, 32768 + w * 8192 + 6144 + lane * 16, 0, 16); }
      { f32x4 a = acc_read4<184>(), b2 = acc_read4<188>(); a *= inv[1]; b2 *= inv[1];
        u32x4 v = {cvtpk(a[0], a[1]), cvtpk(a[2], a[3]), cvtpk(b2[0], b2[1]), cvtpk(b2[2], b2[3])};
        __builtin_amdgcn_raw_buffer_store_b128(v, rpt, 32768 + w * 8192 + 7168 + lane * 16, 0, 16); }
      { f32x4 a = acc_read4<64>(), b2 = acc_read4<68>(); a *= inv[0]; b2 *= inv[0];
        u32x4 v = {cvtpk(a[0], a[1]), cvtpk(a[2], a[3]), cvtpk(b2[0], b2[1]), cvtpk(b2[2], b2[3])};
        __builtin_amdgcn_raw_buffer_store_b128(v, rpt, 65536 + w * 8192 + 0 + lane * 16, 0, 16); }
      { f32x4 a = acc_read4<72>(), b2 = acc_read4<76>(); a *= inv[0]; b2 *= inv[0];
        u32x4 v = {cvtpk(a[0], a[1]), cvtpk(a[2], a[3]), cvtpk(b2[0], b2[1]), cvtpk(b2[2], b2[3])};
        __builtin_amdgcn_raw_buffer_store_b128(v, rpt, 65536 + w * 8192 + 1024 + lane * 16, 0, 16); }
      { f32x4 a = acc_read4<80>(), b2 = acc_read4<84>(); a *= inv[0]; b2 *= inv[0];
        u32x4 v = {cvtpk(a[0], a[1]), cvtpk(a[2], a[3]), cvtpk(b2[0], b2[1]), cvtpk(b2[2], b2[3])};
        __builtin_amdgcn_raw_buffer_store_b128(v, rpt, 65536 + w * 8192 + 2048 + lane * 16, 0, 16); }
      { f32x4 a = acc_read4<88>(), b2 = acc_read4<92>(); a *= inv[0]; b2 *= inv[0];
        u32x4 v = {cvtpk(a[0], a[1]), cvtpk(a[2], a[3]), cvtpk(b2[0], b2[1]), cvtpk(b2[2], b2[3])};
        __builtin_amdgcn_raw_buffer_store_b128(v, rpt, 65536 + w * 8192 + 3072 + lane * 16, 0, 16); }
      { f32x4 a = acc_read4<192>(), b2 = acc_read4<196>(); a *= inv[1]; b2 *= inv[1];
        u32x4 v = {cvtpk(a[0], a[1]), cvtpk(a[2], a[3]), cvtpk(b2[0], b2[1]), cvtpk(b2[2], b2[3])};
        __builtin_amdgcn_raw_buffer_store_b128(v, rpt, 65536 + w * 8192 + 4096 + lane * 16, 0, 16); }
      { f32x4 a = acc_read4<200>(), b2 = acc_read4<204>(); a *= inv[1]; b2 *= inv[1];
        u32x4 v = {cvtpk(a[0], a[1]), cvtpk(a[2], a[3]), cvtpk(b2[0], b2[1]), cvtpk(b2[2], b2[3])};
        __builtin_amdgcn_raw_buffer_store_b128(v, rpt, 65536 + w * 8192 + 5120 + lane * 16, 0, 16); }
      { f32x4 a = acc_read4<208>(), b2 = acc_read4<212>(); a *= inv[1]; b2 *= inv[1];
        u32x4 v = {cvtpk(a[0], a[1]), cvtpk(a[2], a[3]), cvtpk(b2[0], b2[1]), cvtpk(b2[2], b2[3])};
        __builtin_amdgcn_raw_buffer_store_b128(v, rpt, 65536 + w * 8192 + 6144 + lane * 16, 0, 16); }
      { f32x4 a = acc_read4<216>(), b2 = acc_read4<220>(); a *= inv[1]; b2 *= inv[1];
        u32x4 v = {cvtpk(a[0], a[1]), cvtpk(a[2], a[3]), cvtpk(b2[0], b2[1]), cvtpk(b2[2], b2[3])};
        __builtin_amdgcn_raw_buffer_store_b128(v, rpt, 65536 + w * 8192 + 7168 + lane * 16, 0, 16); }
      { f32x4 a = acc_read4<96>(), b2 = acc_read4<100>(); a *= inv[0]; b2 *= inv[0];
        u32x4 v = {cvtpk(a[0], a[1]), cvtpk(a[2], a[3]), cvtpk(b2[0], b2[1]), cvtpk(b2[2], b2[3])};
        __builtin_amdgcn_raw_buffer_store_b128(v, rpt, 98304 + w * 8192 + 0 + lane * 16, 0, 16); }
      { f32x4 a = acc_read4<104>(), b2 = acc_read4<108>(); a *= inv[0]; b2 *= inv[0];
        u32x4 v = {cvtpk(a[0], a[1]), cvtpk(a[2], a[3]), cvtpk(b2[0], b2[1]), cvtpk(b2[2], b2[3])};
        __builtin_amdgcn_raw_buffer_store_b128(v, rpt, 98304 + w * 8192 + 1024 + lane * 16, 0, 16); }
      { f32x4 a = acc_read4<112>(), b2 = acc_read4<116>(); a *= inv[0]; b2 *= inv[0];
        u32x4 v = {cvtpk(a[0], a[1]), cvtpk(a[2], a[3]), cvtpk(b2[0], b2[1]), cvtpk(b2[2], b2[3])};
        __builtin_amdgcn_raw_buffer_store_b128(v, rpt, 98304 + w * 8192 + 2048 + lane * 16, 0, 16); }
      { f32x4 a = acc_read4<120>(), b2 = acc_read4<124>(); a *= inv[0]; b2 *= inv[0];
        u32x4 v = {cvtpk(a[0], a[1]), cvtpk(a[2], a[3]), cvtpk(b2[0], b2[1]), cvtpk(b2[2], b2[3])};
        __builtin_amdgcn_raw_buffer_store_b128(v, rpt, 98304 + w * 8192 + 3072 + lane * 16, 0, 16); }
      { f32x4 a = acc_read4<224>(), b2 = acc_read4<228>(); a *= inv[1]; b2 *= inv[1];
        u32x4 v = {cvtpk(a[0], a[1]), cvtpk(a[2], a[3]), cvtpk(b2[0], b2[1]), cvtpk(b2[2], b2[3])};
        __builtin_amdgcn_raw_buffer_store_b128(v, rpt, 98304 + w * 8192 + 4096 + lane * 16, 0, 16); }
      { f32x4 a = acc_read4<232>(), b2 = acc_read4<236>(); a *= inv[1]; b2 *= inv[1];
        u32x4 v = {cvtpk(a[0], a[1]), cvtpk(a[2], a[3]), cvtpk(b2[0], b2[1]), cvtpk(b2[2], b2[3])};
        __builtin_amdgcn_raw_buffer_store_b128(v, rpt, 98304 + w * 8192 + 5120 + lane * 16, 0, 16); }
      { f32x4 a = acc_read4<240>(), b2 = acc_read4<244>(); a *= inv[1]; b2 *= inv[1];
        u32x4 v = {cvtpk(a[0], a[1]), cvtpk(a[2], a[3]), cvtpk(b2[0], b2[1]), cvtpk(b2[2], b2[3])};
        __builtin_amdgcn_raw_buffer_store_b128(v, rpt, 98304 + w * 8192 + 6144 + lane * 16, 0, 16); }
      { f32x4 a = acc_read4<248>(), b2 = acc_read4<252>(); a *= inv[1]; b2 *= inv[1];
        u32x4 v = {cvtpk(a[0], a[1]), cvtpk(a[2], a[3]), cvtpk(b2[0], b2[1]), cvtpk(b2[2], b2[3])};
        __builtin_amdgcn_raw_buffer_store_b128(v, rpt, 98304 + w * 8192 + 7168 + lane * 16, 0, 16); }
    } break;
    case 1: {
      { f32x4 a = acc_read4<0>(), b2 = acc_read4<4>(); a *= inv[0]; b2 *= inv[0];
        u32x4 v = {cvtpk(a[0], a[1]), cvtpk(a[2], a[3]), cvtpk(b2[0], b2[1]), cvtpk(b2[2], b2[3])};
        __builtin_amdgcn_raw_buffer_store_b128(v, rpt, 131072 + w * 8192 + 0 + lane * 16, 0, 16); }
      { f32x4 a = acc_read4<8>(), b2 = acc_read4<12>(); a *= inv[0]; b2 *= inv[0];
        u32x4 v = {cvtpk(a[0], a[1]), cvtpk(a[2], a[3]), cvtpk(b2[0], b2[1]), cvtpk(b2[2], b2[3])};
        __builtin_amdgcn_raw_buffer_store_b128(v, rpt, 131072 + w * 8192 + 1024 + lane * 16, 0, 16); }
      { f32x4 a = acc_read4<16>(), b2 = acc_read4<20>(); a *= inv[0]; b2 *= inv[0];
        u32x4 v = {cvtpk(a[0], a[1]), cvtpk(a[2], a[3]), cvtpk(b2[0], b2[1]), cvtpk(b2[2], b2[3])};
        __builtin_amdgcn_raw_buffer_store_b128(v, rpt, 131072 + w * 8192 + 2048 + lane * 16, 0, 16); }
      { f32x4 a = acc_read4<24>(), b2 = acc_read4<28>(); a *= inv[0]; b2 *= inv[0];
        u32x4 v = {cvtpk(a[0], a[1]), cvtpk(a[2], a[3]), cvtpk(b2[0], b2[1]), cvtpk(b2[2], b2[3])};
        __builtin_amdgcn_raw_buffer_store_b128(v, rpt, 131072 + w * 8192 + 3072 + lane * 16, 0, 16); }
      { f32x4 a = acc_read4<128>(), b2 = acc_read4<132>(); a *= inv[1]; b2 *= inv[1];
        u32x4 v = {cvtpk(a[0], a[1]), cvtpk(a[2], a[3]), cvtpk(b2[0], b2[1]), cvtpk(b2[2], b2[3])};
        __builtin_amdgcn_raw_buffer_store_b128(v, rpt, 131072 + w * 8192 + 4096 + lane * 16, 0, 16); }
      { f32x4 a = acc_read4<136>(), b2 = acc_read4<140>(); a *= inv[1]; b2 *= inv[1];
        u32x4 v = {cvtpk(a[0], a[1]), cvtpk(a[2], a[3]), cvtpk(b2[0], b2[1]), cvtpk(b2[2], b2[3])};
        __builtin_amdgcn_raw_buffer_store_b128(v, rpt, 131072 + w * 8192 + 5120 + lane * 16, 0, 16); }
      { f32x4 a = acc_read4<144>(), b2 = acc_read4<148>(); a *= inv[1]; b2 *= inv[1];
        u32x4 v = {cvtpk(a[0], a[1]), cvtpk(a[2], a[3]), cvtpk(b2[0], b2[1]), cvtpk(b2[2], b2[3])};
        __builtin_amdgcn_raw_buffer_store_b128(v, rpt, 131072 + w * 8192 + 6144 + lane * 16, 0, 16); }
      { f32x4 a = acc_read4<152>(), b2 = acc_read4<156>(); a *= inv[1]; b2 *= inv[1];
        u32x4 v = {cvtpk(a[0], a[1]), cvtpk(a[2], a[3]), cvtpk(b2[0], b2[1]), cvtpk(b2[2], b2[3])};
        __builtin_amdgcn_raw_buffer_store_b128(v, rpt, 131072 + w * 8192 + 7168 + lane * 16, 0, 16); }
      { f32x4 a = acc_read4<64>(), b2 = acc_read4<68>(); a *= inv[0]; b2 *= inv[0];
        u32x4 v = {cvtpk(a[0], a[1]), cvtpk(a[2], a[3]), cvtpk(b2[0], b2[1]), cvtpk(b2[2], b2[3])};
        __builtin_amdgcn_raw_buffer_store_b128(v, rpt, 196608 + w * 8192 + 0 + lane * 16, 0, 16); }
      { f32x4 a = acc_read4<72>(), b2 = acc_read4<76>(); a *= inv[0]; b2 *= inv[0];
        u32x4 v = {cvtpk(a[0], a[1]), cvtpk(a[2], a[3]), cvtpk(b2[0], b2[1]), cvtpk(b2[2], b2[3])};
        __builtin_amdgcn_raw_buffer_store_b128(v, rpt, 196608 + w * 8192 + 1024 + lane * 16, 0, 16); }
      { f32x4 a = acc_read4<80>(), b2 = acc_read4<84>(); a *= inv[0]; b2 *= inv[0];
        u32x4 v = {cvtpk(a[0], a[1]), cvtpk(a[2], a[3]), cvtpk(b2[0], b2[1]), cvtpk(b2[2], b2[3])};
        __builtin_amdgcn_raw_buffer_store_b128(v, rpt, 196608 + w * 8192 + 2048 + lane * 16, 0, 16); }
      { f32x4 a = acc_read4<88>(), b2 = acc_read4<92>(); a *= inv[0]; b2 *= inv[0];
        u32x4 v = {cvtpk(a[0], a[1]), cvtpk(a[2], a[3]), cvtpk(b2[0], b2[1]), cvtpk(b2[2], b2[3])};
        __builtin_amdgcn_raw_buffer_store_b128(v, rpt, 196608 + w * 8192 + 3072 + lane * 16, 0, 16); }
      { f32x4 a = acc_read4<192>(), b2 = acc_read4<196>(); a *= inv[1]; b2 *= inv[1];
        u32x4 v = {cvtpk(a[0], a[1]), cvtpk(a[2], a[3]), cvtpk(b2[0], b2[1]), cvtpk(b2[2], b2[3])};
        __builtin_amdgcn_raw_buffer_store_b128(v, rpt, 196608 + w * 8192 + 4096 + lane * 16, 0, 16); }
      { f32x4 a = acc_read4<200>(), b2 = acc_read4<204>(); a *= inv[1]; b2 *= inv[1];
        u32x4 v = {cvtpk(a[0], a[1]), cvtpk(a[2], a[3]), cvtpk(b2[0], b2[1]), cvtpk(b2[2], b2[3])};
        __builtin_amdgcn_raw_buffer_store_b128(v, rpt, 196608 + w * 8192 + 5120 + lane * 16, 0, 16); }
      { f32x4 a = acc_read4<208>(), b2 = acc_read4<212>(); a *= inv[1]; b2 *= inv[1];
        u32x4 v = {cvtpk(a[0], a[1]), cvtpk(a[2], a[3]), cvtpk(b2[0], b2[1]), cvtpk(b2[2], b2[3])};
        __builtin_amdgcn_raw_buffer_store_b128(v, rpt, 196608 + w * 8192 + 6144 + lane * 16, 0, 16); }
      { f32x4 a = acc_read4<216>(), b2 = acc_read4<220>(); a *= inv[1]; b2 *= inv[1];
        u32x4 v = {cvtpk(a[0], a[1]), cvtpk(a[2], a[3]), cvtpk(b2[0], b2[1]), cvtpk(b2[2], b2[3])};
        __builtin_amdgcn_raw_buffer_store_b128(v, rpt, 196608 + w * 8192 + 7168 + lane * 16, 0, 16); }
      { f32x4 a = acc_read4<96>(), b2 = acc_read4<100>(); a *= inv[0]; b2 *= inv[0];
        u32x4 v = {cvtpk(a[0], a[1]), cvtpk(a[2], a[3]), cvtpk(b2[0], b2[1]), cvtpk(b2[2], b2[3])};
        __builtin_amdgcn_raw_buffer_store_b128(v, rpt, 229376 + w * 8192 + 0 + lane * 16, 0, 16); }
      { f32x4 a = acc_read4<104>(), b2 = acc_read4<108>(); a *= inv[0]; b2 *= inv[0];
        u32x4 v = {cvtpk(a[0], a[1]), cvtpk(a[2], a[3]), cvtpk(b2[0], b2[1]), cvtpk(b2[2], b2[3])};
        __builtin_amdgcn_raw_buffer_store_b128(v, rpt, 229376 + w * 8192 + 1024 + lane * 16, 0, 16); }
      { f32x4 a = acc_read4<112>(), b2 = acc_read4<116>(); a *= inv[0]; b2 *= inv[0];
        u32x4 v = {cvtpk(a[0], a[1]), cvtpk(a[2], a[3]), cvtpk(b2[0], b2[1]), cvtpk(b2[2], b2[3])};
        __builtin_amdgcn_raw_buffer_store_b128(v, rpt, 229376 + w * 8192 + 2048 + lane * 16, 0, 16); }
      { f32x4 a = acc_read4<120>(), b2 = acc_read4<124>(); a *= inv[0]; b2 *= inv[0];
        u32x4 v = {cvtpk(a[0], a[1]), cvtpk(a[2], a[3]), cvtpk(b2[0], b2[1]), cvtpk(b2[2], b2[3])};
        __builtin_amdgcn_raw_buffer_store_b128(v, rpt, 229376 + w * 8192 + 3072 + lane * 16, 0, 16); }
      { f32x4 a = acc_read4<224>(), b2 = acc_read4<228>(); a *= inv[1]; b2 *= inv[1];
        u32x4 v = {cvtpk(a[0], a[1]), cvtpk(a[2], a[3]), cvtpk(b2[0], b2[1]), cvtpk(b2[2], b2[3])};
        __builtin_amdgcn_raw_buffer_store_b128(v, rpt, 229376 + w * 8192 + 4096 + lane * 16, 0, 16); }
      { f32x4 a = acc_read4<232>(), b2 = acc_read4<236>(); a *= inv[1]; b2 *= inv[1];
        u32x4 v = {cvtpk(a[0], a[1]), cvtpk(a[2], a[3]), cvtpk(b2[0], b2[1]), cvtpk(b2[2], b2[3])};
        __builtin_amdgcn_raw_buffer_store_b128(v, rpt, 229376 + w * 8192 + 5120 + lane * 16, 0, 16); }
      { f32x4 a = acc_read4<240>(), b2 = acc_read4<244>(); a *= inv[1]; b2 *= inv[1];
        u32x4 v = {cvtpk(a[0], a[1]), cvtpk(a[2], a[3]), cvtpk(b2[0], b2[1]), cvtpk(b2[2], b2[3])};
        __builtin_amdgcn_raw_buffer_store_b128(v, rpt, 229376 + w * 8192 + 6144 + lane * 16, 0, 16); }
      { f32x4 a = acc_read4<248>(), b2 = acc_read4<252>(); a *= inv[1]; b2 *= inv[1];
        u32x4 v = {cvtpk(a[0], a[1]), cvtpk(a[2], a[3]), cvtpk(b2[0], b2[1]), cvtpk(b2[2], b2[3])};
        __builtin_amdgcn_raw_buffer_store_b128(v, rpt, 229376 + w * 8192 + 7168 + lane * 16, 0, 16); }
    } break;
    case 2: {
      { f32x4 a = acc_read4<0>(), b2 = acc_read4<4>(); a *= inv[0]; b2 *= inv[0];
        u32x4 v = {cvtpk(a[0], a[1]), cvtpk(a[2], a[3]), cvtpk(b2[0], b2[1]), cvtpk(b2[2], b2[3])};
        __builtin_amdgcn_raw_buffer_store_b128(v, rpt, 262144 + w * 8192 + 0 + lane * 16, 0, 16); }
      { f32x4 a = acc_read4<8>(), b2 = acc_read4<12>(); a *= inv[0]; b2 *= inv[0];
        u32x4 v = {cvtpk(a[0], a[1]), cvtpk(a[2], a[3]), cvtpk(b2[0], b2[1]), cvtpk(b2[2], b2[3])};
        __builtin_amdgcn_raw_buffer_store_b128(v, rpt, 262144 + w * 8192 + 1024 + lane * 16, 0, 16); }
      { f32x4 a = acc_read4<16>(), b2 = acc_read4<20>(); a *= inv[0]; b2 *= inv[0];
        u32x4 v = {cvtpk(a[0], a[1]), cvtpk(a[2], a[3]), cvtpk(b2[0], b2[1]), cvtpk(b2[2], b2[3])};
        __builtin_amdgcn_raw_buffer_store_b128(v, rpt, 262144 + w * 8192 + 2048 + lane * 16, 0, 16); }
      { f32x4 a = acc_read4<24>(), b2 = acc_read4<28>(); a *= inv[0]; b2 *= inv[0];
        u32x4 v = {cvtpk(a[0], a[1]), cvtpk(a[2], a[3]), cvtpk(b2[0], b2[1]), cvtpk(b2[2], b2[3])};
        __builtin_amdgcn_raw_buffer_store_b128(v, rpt, 262144 + w * 8192 + 3072 + lane * 16, 0, 16); }
      { f32x4 a = acc_read4<128>(), b2 = acc_read4<132>(); a *= inv[1]; b2 *= inv[1];
        u32x4 v = {cvtpk(a[0], a[1]), cvtpk(a[2], a[3]), cvtpk(b2[0], b2[1]), cvtpk(b2[2], b2[3])};
        __builtin_amdgcn_raw_buffer_store_b128(v, rpt, 262144 + w * 8192 + 4096 + lane * 16, 0, 16); }
      { f32x4 a = acc_read4<136>(), b2 = acc_read4<140>(); a *= inv[1]; b2 *= inv[1];
        u32x4 v = {cvtpk(a[0], a[1]), cvtpk(a[2], a[3]), cvtpk(b2[0], b2[1]), cvtpk(b2[2], b2[3])};
        __builtin_amdgcn_raw_buffer_store_b128(v, rpt, 262144 + w * 8192 + 5120 + lane * 16, 0, 16); }
      { f32x4 a = acc_read4<144>(), b2 = acc_read4<148>(); a *= inv[1]; b2 *= inv[1];
        u32x4 v = {cvtpk(a[0], a[1]), cvtpk(a[2], a[3]), cvtpk(b2[0], b2[1]), cvtpk(b2[2], b2[3])};
        __builtin_amdgcn_raw_buffer_store_b128(v, rpt, 262144 + w * 8192 + 6144 + lane * 16, 0, 16); }
      { f32x4 a = acc_read4<152>(), b2 = acc_read4<156>(); a *= inv[1]; b2 *= inv[1];
        u32x4 v = {cvtpk(a[0], a[1]), cvtpk(a[2], a[3]), cvtpk(b2[0], b2[1]), cvtpk(b2[2], b2[3])};
        __builtin_amdgcn_raw_buffer_store_b128(v, rpt, 262144 + w * 8192 + 7168 + lane * 16, 0, 16); }
      { f32x4 a = acc_read4<32>(), b2 = acc_read4<36>(); a *= inv[0]; b2 *= inv[0];
        u32x4 v = {cvtpk(a[0], a[1]), cvtpk(a[2], a[3]), cvtpk(b2[0], b2[1]), cvtpk(b2[2], b2[3])};
        __builtin_amdgcn_raw_buffer_store_b128(v, rpt, 294912 + w * 8192 + 0 + lane * 16, 0, 16); }
      { f32x4 a = acc_read4<40>(), b2 = acc_read4<44>(); a *= inv[0]; b2 *= inv[0];
        u32x4 v = {cvtpk(a[0], a[1]), cvtpk(a[2], a[3]), cvtpk(b2[0], b2[1]), cvtpk(b2[2], b2[3])};
        __builtin_amdgcn_raw_buffer_store_b128(v, rpt, 294912 + w * 8192 + 1024 + lane * 16, 0, 16); }
      { f32x4 a = acc_read4<48>(), b2 = acc_read4<52>(); a *= inv[0]; b2 *= inv[0];
        u32x4 v = {cvtpk(a[0], a[1]), cvtpk(a[2], a[3]), cvtpk(b2[0], b2[1]), cvtpk(b2[2], b2[3])};
        __builtin_amdgcn_raw_buffer_store_b128(v, rpt, 294912 + w * 8192 + 2048 + lane * 16, 0, 16); }
      { f32x4 a = acc_read4<56>(), b2 = acc_read4<60>(); a *= inv[0]; b2 *= inv[0];
        u32x4 v = {cvtpk(a[0], a[1]), cvtpk(a[2], a[3]), cvtpk(b2[0], b2[1]), cvtpk(b2[2], b2[3])};
        __builtin_amdgcn_raw_buffer_store_b128(v, rpt, 294912 + w * 8192 + 3072 + lane * 16, 0, 16); }
      { f32x4 a = acc_read4<160>(), b2 = acc_read4<164>(); a *= inv[1]; b2 *= inv[1];
        u32x4 v = {cvtpk(a[0], a[1]), cvtpk(a[2], a[3]), cvtpk(b2[0], b2[1]), cvtpk(b2[2], b2[3])};
        __builtin_amdgcn_raw_buffer_store_b128(v, rpt, 294912 + w * 8192 + 4096 + lane * 16, 0, 16); }
      { f32x4 a = acc_read4<168>(), b2 = acc_read4<172>(); a *= inv[1]; b2 *= inv[1];
        u32x4 v = {cvtpk(a[0], a[1]), cvtpk(a[2], a[3]), cvtpk(b2[0], b2[1]), cvtpk(b2[2], b2[3])};
        __builtin_amdgcn_raw_buffer_store_b128(v, rpt, 294912 + w * 8192 + 5120 + lane * 16, 0, 16); }
      { f32x4 a = acc_read4<176>(), b2 = acc_read4<180>(); a *= inv[1]; b2 *= inv[1];
        u32x4 v = {cvtpk(a[0], a[1]), cvtpk(a[2], a[3]), cvtpk(b2[0], b2[1]), cvtpk(b2[2], b2[3])};
        __builtin_amdgcn_raw_buffer_store_b128(v, rpt, 294912 + w * 8192 + 6144 + lane * 16, 0, 16); }
      { f32x4 a = acc_read4<184>(), b2 = acc_read4<188>(); a *= inv[1]; b2 *= inv[1];
        u32x4 v = {cvtpk(a[0], a[1]), cvtpk(a[2], a[3]), cvtpk(b2[0], b2[1]), cvtpk(b2[2], b2[3])};
        __builtin_amdgcn_raw_buffer_store_b128(v, rpt, 294912 + w * 8192 + 7168 + lane * 16, 0, 16); }
      { f32x4 a = acc_read4<96>(), b2 = acc_read4<100>(); a *= inv[0]; b2 *= inv[0];
        u32x4 v = {cvtpk(a[0], a[1]), cvtpk(a[2], a[3]), cvtpk(b2[0], b2[1]), cvtpk(b2[2], b2[3])};
        __builtin_amdgcn_raw_buffer_store_b128(v, rpt, 360448 + w * 8192 + 0 + lane * 16, 0, 16); }
      { f32x4 a = acc_read4<104>(), b2 = acc_read4<108>(); a *= inv[0]; b2 *= inv[0];
        u32x4 v = {cvtpk(a[0], a[1]), cvtpk(a[2], a[3]), cvtpk(b2[0], b2[1]), cvtpk(b2[2], b2[3])};
        __builtin_amdgcn_raw_buffer_store_b128(v, rpt, 360448 + w * 8192 + 1024 + lane * 16, 0, 16); }
      { f32x4 a = acc_read4<112>(), b2 = acc_read4<116>(); a *= inv[0]; b2 *= inv[0];
        u32x4 v = {cvtpk(a[0], a[1]), cvtpk(a[2], a[3]), cvtpk(b2[0], b2[1]), cvtpk(b2[2], b2[3])};
        __builtin_amdgcn_raw_buffer_store_b128(v, rpt, 360448 + w * 8192 + 2048 + lane * 16, 0, 16); }
      { f32x4 a = acc_read4<120>(), b2 = acc_read4<124>(); a *= inv[0]; b2 *= inv[0];
        u32x4 v = {cvtpk(a[0], a[1]), cvtpk(a[2], a[3]), cvtpk(b2[0], b2[1]), cvtpk(b2[2], b2[3])};
        __builtin_amdgcn_raw_buffer_store_b128(v, rpt, 360448 + w * 8192 + 3072 + lane * 16, 0, 16); }
      { f32x4 a = acc_read4<224>(), b2 = acc_read4<228>(); a *= inv[1]; b2 *= inv[1];
        u32x4 v = {cvtpk(a[0], a[1]), cvtpk(a[2], a[3]), cvtpk(b2[0], b2[1]), cvtpk(b2[2], b2[3])};
        __builtin_amdgcn_raw_buffer_store_b128(v, rpt, 360448 + w * 8192 + 4096 + lane * 16, 0, 16); }
      { f32x4 a = acc_read4<232>(), b2 = acc_read4<236>(); a *= inv[1]; b2 *= inv[1];
        u32x4 v = {cvtpk(a[0], a[1]), cvtpk(a[2], a[3]), cvtpk(b2[0], b2[1]), cvtpk(b2[2], b2[3])};
        __builtin_amdgcn_raw_buffer_store_b128(v, rpt, 360448 + w * 8192 + 5120 + lane * 16, 0, 16); }
      { f32x4 a = acc_read4<240>(), b2 = acc_read4<244>(); a *= inv[1]; b2 *= inv[1];
        u32x4 v = {cvtpk(a[0], a[1]), cvtpk(a[2], a[3]), cvtpk(b2[0], b2[1]), cvtpk(b2[2], b2[3])};
        __builtin_amdgcn_raw_buffer_store_b128(v, rpt, 360448 + w * 8192 + 6144 + lane * 16, 0, 16); }
      { f32x4 a = acc_read4<248>(), b2 = acc_read4<252>(); a *= inv[1]; b2 *= inv[1];
        u32x4 v = {cvtpk(a[0], a[1]), cvtpk(a[2], a[3]), cvtpk(b2[0], b2[1]), cvtpk(b2[2], b2[3])};
        __builtin_amdgcn_raw_buffer_store_b128(v, rpt, 360448 + w * 8192 + 7168 + lane * 16, 0, 16); }
    } break;
    case 3: {
      { f32x4 a = acc_read4<0>(), b2 = acc_read4<4>(); a *= inv[0]; b2 *= inv[0];
        u32x4 v = {cvtpk(a[0], a[1]), cvtpk(a[2], a[3]), cvtpk(b2[0], b2[1]), cvtpk(b2[2], b2[3])};
        __builtin_amdgcn_raw_buffer_store_b128(v, rpt, 393216 + w * 8192 + 0 + lane * 16, 0, 16); }
      { f32x4 a = acc_read4<8>(), b2 = acc_read4<12>(); a *= inv[0]; b2 *= inv[0];
        u32x4 v = {cvtpk(a[0], a[1]), cvtpk(a[2], a[3]), cvtpk(b2[0], b2[1]), cvtpk(b2[2], b2[3])};
        __builtin_amdgcn_raw_buffer_store_b128(v, rpt, 393216 + w * 8192 + 1024 + lane * 16, 0, 16); }
      { f32x4 a = acc_read4<16>(), b2 = acc_read4<20>(); a *= inv[0]; b2 *= inv[0];
        u32x4 v = {cvtpk(a[0], a[1]), cvtpk(a[2], a[3]), cvtpk(b2[0], b2[1]), cvtpk(b2[2], b2[3])};
        __builtin_amdgcn_raw_buffer_store_b128(v, rpt, 393216 + w * 8192 + 2048 + lane * 16, 0, 16); }
      { f32x4 a = acc_read4<24>(), b2 = acc_read4<28>(); a *= inv[0]; b2 *= inv[0];
        u32x4 v = {cvtpk(a[0], a[1]), cvtpk(a[2], a[3]), cvtpk(b2[0], b2[1]), cvtpk(b2[2], b2[3])};
        __builtin_amdgcn_raw_buffer_store_b128(v, rpt, 393216 + w * 8192 + 3072 + lane * 16, 0, 16); }
      { f32x4 a = acc_read4<128>(), b2 = acc_read4<132>(); a *= inv[1]; b2 *= inv[1];
        u32x4 v = {cvtpk(a[0], a[1]), cvtpk(a[2], a[3]), cvtpk(b2[0], b2[1]), cvtpk(b2[2], b2[3])};
        __builtin_amdgcn_raw_buffer_store_b128(v, rpt, 393216 + w * 8192 + 4096 + lane * 16, 0, 16); }
      { f32x4 a = acc_read4<136>(), b2 = acc_read4<140>(); a *= inv[1]; b2 *= inv[1];
        u32x4 v = {cvtpk(a[0], a[1]), cvtpk(a[2], a[3]), cvtpk(b2[0], b2[1]), cvtpk(b2[2], b2[3])};
        __builtin_amdgcn_raw_buffer_store_b128(v, rpt, 393216 + w * 8192 + 5120 + lane * 16, 0, 16); }
      { f32x4 a = acc_read4<144>(), b2 = acc_read4<148>(); a *= inv[1]; b2 *= inv[1];
        u32x4 v = {cvtpk(a[0], a[1]), cvtpk(a[2], a[3]), cvtpk(b2[0], b2[1]), cvtpk(b2[2], b2[3])};
        __builtin_amdgcn_raw_buffer_store_b128(v, rpt, 393216 + w * 8192 + 6144 + lane * 16, 0, 16); }
      { f32x4 a = acc_read4<152>(), b2 = acc_read4<156>(); a *= inv[1]; b2 *= inv[1];
        u32x4 v = {cvtpk(a[0], a[1]), cvtpk(a[2], a[3]), cvtpk(b2[0], b2[1]), cvtpk(b2[2], b2[3])};
        __builtin_amdgcn_raw_buffer_store_b128(v, rpt, 393216 + w * 8192 + 7168 + lane * 16, 0, 16); }
      { f32x4 a = acc_read4<32>(), b2 = acc_read4<36>(); a *= inv[0]; b2 *= inv[0];
        u32x4 v = {cvtpk(a[0], a[1]), cvtpk(a[2], a[3]), cvtpk(b2[0], b2[1]), cvtpk(b2[2], b2[3])};
        __builtin_amdgcn_raw_buffer_store_b128(v, rpt, 425984 + w * 8192 + 0 + lane * 16, 0, 16); }
      { f32x4 a = acc_read4<40>(), b2 = acc_read4<44>(); a *= inv[0]; b2 *= inv[0];
        u32x4 v = {cvtpk(a[0], a[1]), cvtpk(a[2], a[3]), cvtpk(b2[0], b2[1]), cvtpk(b2[2], b2[3])};
        __builtin_amdgcn_raw_buffer_store_b128(v, rpt, 425984 + w * 8192 + 1024 + lane * 16, 0, 16); }
      { f32x4 a = acc_read4<48>(), b2 = acc_read4<52>(); a *= inv[0]; b2 *= inv[0];
        u32x4 v = {cvtpk(a[0], a[1]), cvtpk(a[2], a[3]), cvtpk(b2[0], b2[1]), cvtpk(b2[2], b2[3])};
        __builtin_amdgcn_raw_buffer_store_b128(v, rpt, 425984 + w * 8192 + 2048 + lane * 16, 0, 16); }
      { f32x4 a = acc_read4<56>(), b2 = acc_read4<60>(); a *= inv[0]; b2 *= inv[0];
        u32x4 v = {cvtpk(a[0], a[1]), cvtpk(a[2], a[3]), cvtpk(b2[0], b2[1]), cvtpk(b2[2], b2[3])};
        __builtin_amdgcn_raw_buffer_store_b128(v, rpt, 425984 + w * 8192 + 3072 + lane * 16, 0, 16); }
      { f32x4 a = acc_read4<160>(), b2 = acc_read4<164>(); a *= inv[1]; b2 *= inv[1];
        u32x4 v = {cvtpk(a[0], a[1]), cvtpk(a[2], a[3]), cvtpk(b2[0], b2[1]), cvtpk(b2[2], b2[3])};
        __builtin_amdgcn_raw_buffer_store_b128(v, rpt, 425984 + w * 8192 + 4096 + lane * 16, 0, 16); }
      { f32x4 a = acc_read4<168>(), b2 = acc_read4<172>(); a *= inv[1]; b2 *= inv[1];
        u32x4 v = {cvtpk(a[0], a[1]), cvtpk(a[2], a[3]), cvtpk(b2[0], b2[1]), cvtpk(b2[2], b2[3])};
        __builtin_amdgcn_raw_buffer_store_b128(v, rpt, 425984 + w * 8192 + 5120 + lane * 16, 0, 16); }
      { f32x4 a = acc_read4<176>(), b2 = acc_read4<180>(); a *= inv[1]; b2 *= inv[1];
        u32x4 v = {cvtpk(a[0], a[1]), cvtpk(a[2], a[3]), cvtpk(b2[0], b2[1]), cvtpk(b2[2], b2[3])};
        __builtin_amdgcn_raw_buffer_store_b128(v, rpt, 425984 + w * 8192 + 6144 + lane * 16, 0, 16); }
      { f32x4 a = acc_read4<184>(), b2 = acc_read4<188>(); a *= inv[1]; b2 *= inv[1];
        u32x4 v = {cvtpk(a[0], a[1]), cvtpk(a[2], a[3]), cvtpk(b2[0], b2[1]), cvtpk(b2[2], b2[3])};
        __builtin_amdgcn_raw_buffer_store_b128(v, rpt, 425984 + w * 8192 + 7168 + lane * 16, 0, 16); }
      { f32x4 a = acc_read4<64>(), b2 = acc_read4<68>(); a *= inv[0]; b2 *= inv[0];
        u32x4 v = {cvtpk(a[0], a[1]), cvtpk(a[2], a[3]), cvtpk(b2[0], b2[1]), cvtpk(b2[2], b2[3])};
        __builtin_amdgcn_raw_buffer_store_b128(v, rpt, 458752 + w * 8192 + 0 + lane * 16, 0, 16); }
      { f32x4 a = acc_read4<72>(), b2 = acc_read4<76>(); a *= inv[0]; b2 *= inv[0];
        u32x4 v = {cvtpk(a[0], a[1]), cvtpk(a[2], a[3]), cvtpk(b2[0], b2[1]), cvtpk(b2[2], b2[3])};
        __builtin_amdgcn_raw_buffer_store_b128(v, rpt, 458752 + w * 8192 + 1024 + lane * 16, 0, 16); }
      { f32x4 a = acc_read4<80>(), b2 = acc_read4<84>(); a *= inv[0]; b2 *= inv[0];
        u32x4 v = {cvtpk(a[0], a[1]), cvtpk(a[2], a[3]), cvtpk(b2[0], b2[1]), cvtpk(b2[2], b2[3])};
        __builtin_amdgcn_raw_buffer_store_b128(v, rpt, 458752 + w * 8192 + 2048 + lane * 16, 0, 16); }
      { f32x4 a = acc_read4<88>(), b2 = acc_read4<92>(); a *= inv[0]; b2 *= inv[0];
        u32x4 v = {cvtpk(a[0], a[1]), cvtpk(a[2], a[3]), cvtpk(b2[0], b2[1]), cvtpk(b2[2], b2[3])};
        __builtin_amdgcn_raw_buffer_store_b128(v, rpt, 458752 + w * 8192 + 3072 + lane * 16, 0, 16); }
      { f32x4 a = acc_read4<192>(), b2 = acc_read4<196>(); a *= inv[1]; b2 *= inv[1];
        u32x4 v = {cvtpk(a[0], a[1]), cvtpk(a[2], a[3]), cvtpk(b2[0], b2[1]), cvtpk(b2[2], b2[3])};
        __builtin_amdgcn_raw_buffer_store_b128(v, rpt, 458752 + w * 8192 + 4096 + lane * 16, 0, 16); }
      { f32x4 a = acc_read4<200>(), b2 = acc_read4<204>(); a *= inv[1]; b2 *= inv[1];
        u32x4 v = {cvtpk(a[0], a[1]), cvtpk(a[2], a[3]), cvtpk(b2[0], b2[1]), cvtpk(b2[2], b2[3])};
        __builtin_amdgcn_raw_buffer_store_b128(v, rpt, 458752 + w * 8192 + 5120 + lane * 16, 0, 16); }
      { f32x4 a = acc_read4<208>(), b2 = acc_read4<212>(); a *= inv[1]; b2 *= inv[1];
        u32x4 v = {cvtpk(a[0], a[1]), cvtpk(a[2], a[3]), cvtpk(b2[0], b2[1]), cvtpk(b2[2], b2[3])};
        __builtin_amdgcn_raw_buffer_store_b128(v, rpt, 458752 + w * 8192 + 6144 + lane * 16, 0, 16); }
      { f32x4 a = acc_read4<216>(), b2 = acc_read4<220>(); a *= inv[1]; b2 *= inv[1];
        u32x4 v = {cvtpk(a[0], a[1]), cvtpk(a[2], a[3]), cvtpk(b2[0], b2[1]), cvtpk(b2[2], b2[3])};
        __builtin_amdgcn_raw_buffer_store_b128(v, rpt, 458752 + w * 8192 + 7168 + lane * 16, 0, 16); }
    } break;
  }
  asm volatile("s_waitcnt vmcnt(0)" ::: "memory");
  __syncthreads();
  STAMP(14);
  if (tid == 0) {
    __hip_atomic_fetch_add(cnt + grp, 1u, __ATOMIC_RELAXED, __HIP_MEMORY_SCOPE_AGENT);
    for (int it = 0; it < (1 << 24); ++it) {
      if (__hip_atomic_load(cnt + grp, __ATOMIC_RELAXED, __HIP_MEMORY_SCOPE_AGENT) >= 4u) break;
      __builtin_amdgcn_s_sleep(1);
    }
  }
  __syncthreads();
  STAMP(15);
  float fw[2][4];
#pragma unroll
  for (int qb = 0; qb < 2; ++qb) {
    float mm[4], ll[4];
#pragma unroll
    for (int s4 = 0; s4 < 4; ++s4) {
      u32x2 v = __builtin_amdgcn_raw_buffer_load_b64(rml, (s4 * 256 + 64 * w + 32 * qb + r) * 8, 0, 16);
      mm[s4] = __uint_as_float(v[0]); ll[s4] = __uint_as_float(v[1]);
    }
    const float M = fmaxf(fmaxf(mm[0], mm[1]), fmaxf(mm[2], mm[3]));
    float e[4], L = 0.f;
#pragma unroll
    for (int s4 = 0; s4 < 4; ++s4) { e[s4] = __builtin_amdgcn_exp2f(mm[s4] - M) * ll[s4]; L += e[s4]; }
    const float iL = 1.f / L;
#pragma unroll
    for (int s4 = 0; s4 < 4; ++s4) fw[qb][s4] = e[s4] * iL;
  }
  switch (sp) {
    case 0: {
      { const int R = 64 * w + 0 + r; const int hl = R >= 112 ? 1 : 0; const int orow = b * SQ + R - 112 * hl;
        { f32x4 a = acc_read4<0>(), b2 = acc_read4<4>(); const float fo = fw[0][0] * inv[0]; a *= fo; b2 *= fo;
          { u32x4 v = __builtin_amdgcn_raw_buffer_load_b128(rpt, 131072 + w * 8192 + 0 + lane * 16, 0, 16); const float f = fw[0][1];
            a[0] += f * __uint_as_float(v[0] << 16); a[1] += f * __uint_as_float(v[0] & 0xffff0000u); a[2] += f * __uint_as_float(v[1] << 16); a[3] += f * __uint_as_float(v[1] & 0xffff0000u);
            b2[0] += f * __uint_as_float(v[2] << 16); b2[1] += f * __uint_as_float(v[2] & 0xffff0000u); b2[2] += f * __uint_as_float(v[3] << 16); b2[3] += f * __uint_as_float(v[3] & 0xffff0000u); }
          { u32x4 v = __builtin_amdgcn_raw_buffer_load_b128(rpt, 262144 + w * 8192 + 0 + lane * 16, 0, 16); const float f = fw[0][2];
            a[0] += f * __uint_as_float(v[0] << 16); a[1] += f * __uint_as_float(v[0] & 0xffff0000u); a[2] += f * __uint_as_float(v[1] << 16); a[3] += f * __uint_as_float(v[1] & 0xffff0000u);
            b2[0] += f * __uint_as_float(v[2] << 16); b2[1] += f * __uint_as_float(v[2] & 0xffff0000u); b2[2] += f * __uint_as_float(v[3] << 16); b2[3] += f * __uint_as_float(v[3] & 0xffff0000u); }
          { u32x4 v = __builtin_amdgcn_raw_buffer_load_b128(rpt, 393216 + w * 8192 + 0 + lane * 16, 0, 16); const float f = fw[0][3];
            a[0] += f * __uint_as_float(v[0] << 16); a[1] += f * __uint_as_float(v[0] & 0xffff0000u); a[2] += f * __uint_as_float(v[1] << 16); a[3] += f * __uint_as_float(v[1] & 0xffff0000u);
            b2[0] += f * __uint_as_float(v[2] << 16); b2[1] += f * __uint_as_float(v[2] & 0xffff0000u); b2[2] += f * __uint_as_float(v[3] << 16); b2[3] += f * __uint_as_float(v[3] & 0xffff0000u); }
          if (R < 224) { u16* dst = o + ((size_t)((kvh * 2 + hl) * 8 + 0) * NROW + orow) * 32 + 0 + 4 * h;
            u32x2 v0 = {cvtpk(a[0], a[1]), cvtpk(a[2], a[3])}; u32x2 v1 = {cvtpk(b2[0], b2[1]), cvtpk(b2[2], b2[3])};
            *(u32x2*)dst = v0; *(u32x2*)(dst + 8) = v1; } }
        { f32x4 a = acc_read4<8>(), b2 = acc_read4<12>(); const float fo = fw[0][0] * inv[0]; a *= fo; b2 *= fo;
          { u32x4 v = __builtin_amdgcn_raw_buffer_load_b128(rpt, 131072 + w * 8192 + 1024 + lane * 16, 0, 16); const float f = fw[0][1];
            a[0] += f * __uint_as_float(v[0] << 16); a[1] += f * __uint_as_float(v[0] & 0xffff0000u); a[2] += f * __uint_as_float(v[1] << 16); a[3] += f * __uint_as_float(v[1] & 0xffff0000u);
            b2[0] += f * __uint_as_float(v[2] << 16); b2[1] += f * __uint_as_float(v[2] & 0xffff0000u); b2[2] += f * __uint_as_float(v[3] << 16); b2[3] += f * __uint_as_float(v[3] & 0xffff0000u); }
          { u32x4 v = __builtin_amdgcn_raw_buffer_load_b128(rpt, 262144 + w * 8192 + 1024 + lane * 16, 0, 16); const float f = fw[0][2];
            a[0] += f * __uint_as_float(v[0] << 16); a[1] += f * __uint_as_float(v[0] & 0xffff0000u); a[2] += f * __uint_as_float(v[1] << 16); a[3] += f * __uint_as_float(v[1] & 0xffff0000u);
            b2[0] += f * __uint_as_float(v[2] << 16); b2[1] += f * __uint_as_float(v[2] & 0xffff0000u); b2[2] += f * __uint_as_float(v[3] << 16); b2[3] += f * __uint_as_float(v[3] & 0xffff0000u); }
          { u32x4 v = __builtin_amdgcn_raw_buffer_load_b128(rpt, 393216 + w * 8192 + 1024 + lane * 16, 0, 16); const float f = fw[0][3];
            a[0] += f * __uint_as_float(v[0] << 16); a[1] += f * __uint_as_float(v[0] & 0xffff0000u); a[2] += f * __uint_as_float(v[1] << 16); a[3] += f * __uint_as_float(v[1] & 0xffff0000u);
            b2[0] += f * __uint_as_float(v[2] << 16); b2[1] += f * __uint_as_float(v[2] & 0xffff0000u); b2[2] += f * __uint_as_float(v[3] << 16); b2[3] += f * __uint_as_float(v[3] & 0xffff0000u); }
          if (R < 224) { u16* dst = o + ((size_t)((kvh * 2 + hl) * 8 + 0) * NROW + orow) * 32 + 16 + 4 * h;
            u32x2 v0 = {cvtpk(a[0], a[1]), cvtpk(a[2], a[3])}; u32x2 v1 = {cvtpk(b2[0], b2[1]), cvtpk(b2[2], b2[3])};
            *(u32x2*)dst = v0; *(u32x2*)(dst + 8) = v1; } }
        { f32x4 a = acc_read4<16>(), b2 = acc_read4<20>(); const float fo = fw[0][0] * inv[0]; a *= fo; b2 *= fo;
          { u32x4 v = __builtin_amdgcn_raw_buffer_load_b128(rpt, 131072 + w * 8192 + 2048 + lane * 16, 0, 16); const float f = fw[0][1];
            a[0] += f * __uint_as_float(v[0] << 16); a[1] += f * __uint_as_float(v[0] & 0xffff0000u); a[2] += f * __uint_as_float(v[1] << 16); a[3] += f * __uint_as_float(v[1] & 0xffff0000u);
            b2[0] += f * __uint_as_float(v[2] << 16); b2[1] += f * __uint_as_float(v[2] & 0xffff0000u); b2[2] += f * __uint_as_float(v[3] << 16); b2[3] += f * __uint_as_float(v[3] & 0xffff0000u); }
          { u32x4 v = __builtin_amdgcn_raw_buffer_load_b128(rpt, 262144 + w * 8192 + 2048 + lane * 16, 0, 16); const float f = fw[0][2];
            a[0] += f * __uint_as_float(v[0] << 16); a[1] += f * __uint_as_float(v[0] & 0xffff0000u); a[2] += f * __uint_as_float(v[1] << 16); a[3] += f * __uint_as_float(v[1] & 0xffff0000u);
            b2[0] += f * __uint_as_float(v[2] << 16); b2[1] += f * __uint_as_float(v[2] & 0xffff0000u); b2[2] += f * __uint_as_float(v[3] << 16); b2[3] += f * __uint_as_float(v[3] & 0xffff0000u); }
          { u32x4 v = __builtin_amdgcn_raw_buffer_load_b128(rpt, 393216 + w * 8192 + 2048 + lane * 16, 0, 16); const float f = fw[0][3];
            a[0] += f * __uint_as_float(v[0] << 16); a[1] += f * __uint_as_float(v[0] & 0xffff0000u); a[2] += f * __uint_as_float(v[1] << 16); a[3] += f * __uint_as_float(v[1] & 0xffff0000u);
            b2[0] += f * __uint_as_float(v[2] << 16); b2[1] += f * __uint_as_float(v[2] & 0xffff0000u); b2[2] += f * __uint_as_float(v[3] << 16); b2[3] += f * __uint_as_float(v[3] & 0xffff0000u); }
          if (R < 224) { u16* dst = o + ((size_t)((kvh * 2 + hl) * 8 + 1) * NROW + orow) * 32 + 0 + 4 * h;
            u32x2 v0 = {cvtpk(a[0], a[1]), cvtpk(a[2], a[3])}; u32x2 v1 = {cvtpk(b2[0], b2[1]), cvtpk(b2[2], b2[3])};
            *(u32x2*)dst = v0; *(u32x2*)(dst + 8) = v1; } }
        { f32x4 a = acc_read4<24>(), b2 = acc_read4<28>(); const float fo = fw[0][0] * inv[0]; a *= fo; b2 *= fo;
          { u32x4 v = __builtin_amdgcn_raw_buffer_load_b128(rpt, 131072 + w * 8192 + 3072 + lane * 16, 0, 16); const float f = fw[0][1];
            a[0] += f * __uint_as_float(v[0] << 16); a[1] += f * __uint_as_float(v[0] & 0xffff0000u); a[2] += f * __uint_as_float(v[1] << 16); a[3] += f * __uint_as_float(v[1] & 0xffff0000u);
            b2[0] += f * __uint_as_float(v[2] << 16); b2[1] += f * __uint_as_float(v[2] & 0xffff0000u); b2[2] += f * __uint_as_float(v[3] << 16); b2[3] += f * __uint_as_float(v[3] & 0xffff0000u); }
          { u32x4 v = __builtin_amdgcn_raw_buffer_load_b128(rpt, 262144 + w * 8192 + 3072 + lane * 16, 0, 16); const float f = fw[0][2];
            a[0] += f * __uint_as_float(v[0] << 16); a[1] += f * __uint_as_float(v[0] & 0xffff0000u); a[2] += f * __uint_as_float(v[1] << 16); a[3] += f * __uint_as_float(v[1] & 0xffff0000u);
            b2[0] += f * __uint_as_float(v[2] << 16); b2[1] += f * __uint_as_float(v[2] & 0xffff0000u); b2[2] += f * __uint_as_float(v[3] << 16); b2[3] += f * __uint_as_float(v[3] & 0xffff0000u); }
          { u32x4 v = __builtin_amdgcn_raw_buffer_load_b128(rpt, 393216 + w * 8192 + 3072 + lane * 16, 0, 16); const float f = fw[0][3];
            a[0] += f * __uint_as_float(v[0] << 16); a[1] += f * __uint_as_float(v[0] & 0xffff0000u); a[2] += f * __uint_as_float(v[1] << 16); a[3] += f * __uint_as_float(v[1] & 0xffff0000u);
            b2[0] += f * __uint_as_float(v[2] << 16); b2[1] += f * __uint_as_float(v[2] & 0xffff0000u); b2[2] += f * __uint_as_float(v[3] << 16); b2[3] += f * __uint_as_float(v[3] & 0xffff0000u); }
          if (R < 224) { u16* dst = o + ((size_t)((kvh * 2 + hl) * 8 + 1) * NROW + orow) * 32 + 16 + 4 * h;
            u32x2 v0 = {cvtpk(a[0], a[1]), cvtpk(a[2], a[3])}; u32x2 v1 = {cvtpk(b2[0], b2[1]), cvtpk(b2[2], b2[3])};
            *(u32x2*)dst = v0; *(u32x2*)(dst + 8) = v1; } }
      }
      { const int R = 64 * w + 32 + r; const int hl = R >= 112 ? 1 : 0; const int orow = b * SQ + R - 112 * hl;
        { f32x4 a = acc_read4<128>(), b2 = acc_read4<132>(); const float fo = fw[1][0] * inv[1]; a *= fo; b2 *= fo;
          { u32x4 v = __builtin_amdgcn_raw_buffer_load_b128(rpt, 131072 + w * 8192 + 4096 + lane * 16, 0, 16); const float f = fw[1][1];
            a[0] += f * __uint_as_float(v[0] << 16); a[1] += f * __uint_as_float(v[0] & 0xffff0000u); a[2] += f * __uint_as_float(v[1] << 16); a[3] += f * __uint_as_float(v[1] & 0xffff0000u);
            b2[0] += f * __uint_as_float(v[2] << 16); b2[1] += f * __uint_as_float(v[2] & 0xffff0000u); b2[2] += f * __uint_as_float(v[3] << 16); b2[3] += f * __uint_as_float(v[3] & 0xffff0000u); }
          { u32x4 v = __builtin_amdgcn_raw_buffer_load_b128(rpt, 262144 + w * 8192 + 4096 + lane * 16, 0, 16); const float f = fw[1][2];
            a[0] += f * __uint_as_float(v[0] << 16); a[1] += f * __uint_as_float(v[0] & 0xffff0000u); a[2] += f * __uint_as_float(v[1] << 16); a[3] += f * __uint_as_float(v[1] & 0xffff0000u);
            b2[0] += f * __uint_as_float(v[2] << 16); b2[1] += f * __uint_as_float(v[2] & 0xffff0000u); b2[2] += f * __uint_as_float(v[3] << 16); b2[3] += f * __uint_as_float(v[3] & 0xffff0000u); }
          { u32x4 v = __builtin_amdgcn_raw_buffer_load_b128(rpt, 393216 + w * 8192 + 4096 + lane * 16, 0, 16); const float f = fw[1][3];
            a[0] += f * __uint_as_float(v[0] << 16); a[1] += f * __uint_as_float(v[0] & 0xffff0000u); a[2] += f * __uint_as_float(v[1] << 16); a[3] += f * __uint_as_float(v[1] & 0xffff0000u);
            b2[0] += f * __uint_as_float(v[2] << 16); b2[1] += f * __uint_as_float(v[2] & 0xffff0000u); b2[2] += f * __uint_as_float(v[3] << 16); b2[3] += f * __uint_as_float(v[3] & 0xffff0000u); }
          if (R < 224) { u16* dst = o + ((size_t)((kvh * 2 + hl) * 8 + 0) * NROW + orow) * 32 + 0 + 4 * h;
            u32x2 v0 = {cvtpk(a[0], a[1]), cvtpk(a[2], a[3])}; u32x2 v1 = {cvtpk(b2[0], b2[1]), cvtpk(b2[2], b2[3])};
            *(u32x2*)dst = v0; *(u32x2*)(dst + 8) = v1; } }
        { f32x4 a = acc_read4<136>(), b2 = acc_read4<140>(); const float fo = fw[1][0] * inv[1]; a *= fo; b2 *= fo;
          { u32x4 v = __builtin_amdgcn_raw_buffer_load_b128(rpt, 131072 + w * 8192 + 5120 + lane * 16, 0, 16); const float f = fw[1][1];
            a[0] += f * __uint_as_float(v[0] << 16); a[1] += f * __uint_as_float(v[0] & 0xffff0000u); a[2] += f * __uint_as_float(v[1] << 16); a[3] += f * __uint_as_float(v[1] & 0xffff0000u);
            b2[0] += f * __uint_as_float(v[2] << 16); b2[1] += f * __uint_as_float(v[2] & 0xffff0000u); b2[2] += f * __uint_as_float(v[3] << 16); b2[3] += f * __uint_as_float(v[3] & 0xffff0000u); }
          { u32x4 v = __builtin_amdgcn_raw_buffer_load_b128(rpt, 262144 + w * 8192 + 5120 + lane * 16, 0, 16); const float f = fw[1][2];
            a[0] += f * __uint_as_float(v[0] << 16); a[1] += f * __uint_as_float(v[0] & 0xffff0000u); a[2] += f * __uint_as_float(v[1] << 16); a[3] += f * __uint_as_float(v[1] & 0xffff0000u);
            b2[0] += f * __uint_as_float(v[2] << 16); b2[1] += f * __uint_as_float(v[2] & 0xffff0000u); b2[2] += f * __uint_as_float(v[3] << 16); b2[3] += f * __uint_as_float(v[3] & 0xffff0000u); }
          { u32x4 v = __builtin_amdgcn_raw_buffer_load_b128(rpt, 393216 + w * 8192 + 5120 + lane * 16, 0, 16); const float f = fw[1][3];
            a[0] += f * __uint_as_float(v[0] << 16); a[1] += f * __uint_as_float(v[0] & 0xffff0000u); a[2] += f * __uint_as_float(v[1] << 16); a[3] += f * __uint_as_float(v[1] & 0xffff0000u);
            b2[0] += f * __uint_as_float(v[2] << 16); b2[1] += f * __uint_as_float(v[2] & 0xffff0000u); b2[2] += f * __uint_as_float(v[3] << 16); b2[3] += f * __uint_as_float(v[3] & 0xffff0000u); }
          if (R < 224) { u16* dst = o + ((size_t)((kvh * 2 + hl) * 8 + 0) * NROW + orow) * 32 + 16 + 4 * h;
            u32x2 v0 = {cvtpk(a[0], a[1]), cvtpk(a[2], a[3])}; u32x2 v1 = {cvtpk(b2[0], b2[1]), cvtpk(b2[2], b2[3])};
            *(u32x2*)dst = v0; *(u32x2*)(dst + 8) = v1; } }
        { f32x4 a = acc_read4<144>(), b2 = acc_read4<148>(); const float fo = fw[1][0] * inv[1]; a *= fo; b2 *= fo;
          { u32x4 v = __builtin_amdgcn_raw_buffer_load_b128(rpt, 131072 + w * 8192 + 6144 + lane * 16, 0, 16); const float f = fw[1][1];
            a[0] += f * __uint_as_float(v[0] << 16); a[1] += f * __uint_as_float(v[0] & 0xffff0000u); a[2] += f * __uint_as_float(v[1] << 16); a[3] += f * __uint_as_float(v[1] & 0xffff0000u);
            b2[0] += f * __uint_as_float(v[2] << 16); b2[1] += f * __uint_as_float(v[2] & 0xffff0000u); b2[2] += f * __uint_as_float(v[3] << 16); b2[3] += f * __uint_as_float(v[3] & 0xffff0000u); }
          { u32x4 v = __builtin_amdgcn_raw_buffer_load_b128(rpt, 262144 + w * 8192 + 6144 + lane * 16, 0, 16); const float f = fw[1][2];
            a[0] += f * __uint_as_float(v[0] << 16); a[1] += f * __uint_as_float(v[0] & 0xffff0000u); a[2] += f * __uint_as_float(v[1] << 16); a[3] += f * __uint_as_float(v[1] & 0xffff0000u);
            b2[0] += f * __uint_as_float(v[2] << 16); b2[1] += f * __uint_as_float(v[2] & 0xffff0000u); b2[2] += f * __uint_as_float(v[3] << 16); b2[3] += f * __uint_as_float(v[3] & 0xffff0000u); }
          { u32x4 v = __builtin_amdgcn_raw_buffer_load_b128(rpt, 393216 + w * 8192 + 6144 + lane * 16, 0, 16); const float f = fw[1][3];
            a[0] += f * __uint_as_float(v[0] << 16); a[1] += f * __uint_as_float(v[0] & 0xffff0000u); a[2] += f * __uint_as_float(v[1] << 16); a[3] += f * __uint_as_float(v[1] & 0xffff0000u);
            b2[0] += f * __uint_as_float(v[2] << 16); b2[1] += f * __uint_as_float(v[2] & 0xffff0000u); b2[2] += f * __uint_as_float(v[3] << 16); b2[3] += f * __uint_as_float(v[3] & 0xffff0000u); }
          if (R < 224) { u16* dst = o + ((size_t)((kvh * 2 + hl) * 8 + 1) * NROW + orow) * 32 + 0 + 4 * h;
            u32x2 v0 = {cvtpk(a[0], a[1]), cvtpk(a[2], a[3])}; u32x2 v1 = {cvtpk(b2[0], b2[1]), cvtpk(b2[2], b2[3])};
            *(u32x2*)dst = v0; *(u32x2*)(dst + 8) = v1; } }
        { f32x4 a = acc_read4<152>(), b2 = acc_read4<156>(); const float fo = fw[1][0] * inv[1]; a *= fo; b2 *= fo;
          { u32x4 v = __builtin_amdgcn_raw_buffer_load_b128(rpt, 131072 + w * 8192 + 7168 + lane * 16, 0, 16); const float f = fw[1][1];
            a[0] += f * __uint_as_float(v[0] << 16); a[1] += f * __uint_as_float(v[0] & 0xffff0000u); a[2] += f * __uint_as_float(v[1] << 16); a[3] += f * __uint_as_float(v[1] & 0xffff0000u);
            b2[0] += f * __uint_as_float(v[2] << 16); b2[1] += f * __uint_as_float(v[2] & 0xffff0000u); b2[2] += f * __uint_as_float(v[3] << 16); b2[3] += f * __uint_as_float(v[3] & 0xffff0000u); }
          { u32x4 v = __builtin_amdgcn_raw_buffer_load_b128(rpt, 262144 + w * 8192 + 7168 + lane * 16, 0, 16); const float f = fw[1][2];
            a[0] += f * __uint_as_float(v[0] << 16); a[1] += f * __uint_as_float(v[0] & 0xffff0000u); a[2] += f * __uint_as_float(v[1] << 16); a[3] += f * __uint_as_float(v[1] & 0xffff0000u);
            b2[0] += f * __uint_as_float(v[2] << 16); b2[1] += f * __uint_as_float(v[2] & 0xffff0000u); b2[2] += f * __uint_as_float(v[3] << 16); b2[3] += f * __uint_as_float(v[3] & 0xffff0000u); }
          { u32x4 v = __builtin_amdgcn_raw_buffer_load_b128(rpt, 393216 + w * 8192 + 7168 + lane * 16, 0, 16); const float f = fw[1][3];
            a[0] += f * __uint_as_float(v[0] << 16); a[1] += f * __uint_as_float(v[0] & 0xffff0000u); a[2] += f * __uint_as_float(v[1] << 16); a[3] += f * __uint_as_float(v[1] & 0xffff0000u);
            b2[0] += f * __uint_as_float(v[2] << 16); b2[1] += f * __uint_as_float(v[2] & 0xffff0000u); b2[2] += f * __uint_as_float(v[3] << 16); b2[3] += f * __uint_as_float(v[3] & 0xffff0000u); }
          if (R < 224) { u16* dst = o + ((size_t)((kvh * 2 + hl) * 8 + 1) * NROW + orow) * 32 + 16 + 4 * h;
            u32x2 v0 = {cvtpk(a[0], a[1]), cvtpk(a[2], a[3])}; u32x2 v1 = {cvtpk(b2[0], b2[1]), cvtpk(b2[2], b2[3])};
            *(u32x2*)dst = v0; *(u32x2*)(dst + 8) = v1; } }
      }
    } break;
    case 1: {
      { const int R = 64 * w + 0 + r; const int hl = R >= 112 ? 1 : 0; const int orow = b * SQ + R - 112 * hl;
        { f32x4 a = acc_read4<32>(), b2 = acc_read4<36>(); const float fo = fw[0][1] * inv[0]; a *= fo; b2 *= fo;
          { u32x4 v = __builtin_amdgcn_raw_buffer_load_b128(rpt, 32768 + w * 8192 + 0 + lane * 16, 0, 16); const float f = fw[0][0];
            a[0] += f * __uint_as_float(v[0] << 16); a[1] += f * __uint_as_float(v[0] & 0xffff0000u); a[2] += f * __uint_as_float(v[1] << 16); a[3] += f * __uint_as_float(v[1] & 0xffff0000u);
            b2[0] += f * __uint_as_float(v[2] << 16); b2[1] += f * __uint_as_float(v[2] & 0xffff0000u); b2[2] += f * __uint_as_float(v[3] << 16); b2[3] += f * __uint_as_float(v[3] & 0xffff0000u); }
          { u32x4 v = __builtin_amdgcn_raw_buffer_load_b128(rpt, 294912 + w * 8192 + 0 + lane * 16, 0, 16); const float f = fw[0][2];
            a[0] += f * __uint_as_float(v[0] << 16); a[1] += f * __uint_as_float(v[0] & 0xffff0000u); a[2] += f * __uint_as_float(v[1] << 16); a[3] += f * __uint_as_float(v[1] & 0xffff0000u);
            b2[0] += f * __uint_as_float(v[2] << 16); b2[1] += f * __uint_as_float(v[2] & 0xffff0000u); b2[2] += f * __uint_as_float(v[3] << 16); b2[3] += f * __uint_as_float(v[3] & 0xffff0000u); }
          { u32x4 v = __builtin_amdgcn_raw_buffer_load_b128(rpt, 425984 + w * 8192 + 0 + lane * 16, 0, 16); const float f = fw[0][3];
            a[0] += f * __uint_as_float(v[0] << 16); a[1] += f * __uint_as_float(v[0] & 0xffff0000u); a[2] += f * __uint_as_float(v[1] << 16); a[3] += f * __uint_as_float(v[1] & 0xffff0000u);
            b2[0] += f * __uint_as_float(v[2] << 16); b2[1] += f * __uint_as_float(v[2] & 0xffff0000u); b2[2] += f * __uint_as_float(v[3] << 16); b2[3] += f * __uint_as_float(v[3] & 0xffff0000u); }
          if (R < 224) { u16* dst = o + ((size_t)((kvh * 2 + hl) * 8 + 2) * NROW + orow) * 32 + 0 + 4 * h;
            u32x2 v0 = {cvtpk(a[0], a[1]), cvtpk(a[2], a[3])}; u32x2 v1 = {cvtpk(b2[0], b2[1]), cvtpk(b2[2], b2[3])};
            *(u32x2*)dst = v0; *(u32x2*)(dst + 8) = v1; } }
        { f32x4 a = acc_read4<40>(), b2 = acc_read4<44>(); const float fo = fw[0][1] * inv[0]; a *= fo; b2 *= fo;
          { u32x4 v = __builtin_amdgcn_raw_buffer_load_b128(rpt, 32768 + w * 8192 + 1024 + lane * 16, 0, 16); const float f = fw[0][0];
            a[0] += f * __uint_as_float(v[0] << 16); a[1] += f * __uint_as_float(v[0] & 0xffff0000u); a[2] += f * __uint_as_float(v[1] << 16); a[3] += f * __uint_as_float(v[1] & 0xffff0000u);
            b2[0] += f * __uint_as_float(v[2] << 16); b2[1] += f * __uint_as_float(v[2] & 0xffff0000u); b2[2] += f * __uint_as_float(v[3] << 16); b2[3] += f * __uint_as_float(v[3] & 0xffff0000u); }
          { u32x4 v = __builtin_amdgcn_raw_buffer_load_b128(rpt, 294912 + w * 8192 + 1024 + lane * 16, 0, 16); const float f = fw[0][2];
            a[0] += f * __uint_as_float(v[0] << 16); a[1] += f * __uint_as_float(v[0] & 0xffff0000u); a[2] += f * __uint_as_float(v[1] << 16); a[3] += f * __uint_as_float(v[1] & 0xffff0000u);
            b2[0] += f * __uint_as_float(v[2] << 16); b2[1] += f * __uint_as_float(v[2] & 0xffff0000u); b2[2] += f * __uint_as_float(v[3] << 16); b2[3] += f * __uint_as_float(v[3] & 0xffff0000u); }
          { u32x4 v = __builtin_amdgcn_raw_buffer_load_b128(rpt, 425984 + w * 8192 + 1024 + lane * 16, 0, 16); const float f = fw[0][3];
            a[0] += f * __uint_as_float(v[0] << 16); a[1] += f * __uint_as_float(v[0] & 0xffff0000u); a[2] += f * __uint_as_float(v[1] << 16); a[3] += f * __uint_as_float(v[1] & 0xffff0000u);
            b2[0] += f * __uint_as_float(v[2] << 16); b2[1] += f * __uint_as_float(v[2] & 0xffff0000u); b2[2] += f * __uint_as_float(v[3] << 16); b2[3] += f * __uint_as_float(v[3] & 0xffff0000u); }
          if (R < 224) { u16* dst = o + ((size_t)((kvh * 2 + hl) * 8 + 2) * NROW + orow) * 32 + 16 + 4 * h;
            u32x2 v0 = {cvtpk(a[0], a[1]), cvtpk(a[2], a[3])}; u32x2 v1 = {cvtpk(b2[0], b2[1]), cvtpk(b2[2], b2[3])};
            *(u32x2*)dst = v0; *(u32x2*)(dst + 8) = v1; } }
        { f32x4 a = acc_read4<48>(), b2 = acc_read4<52>(); const float fo = fw[0][1] * inv[0]; a *= fo; b2 *= fo;
          { u32x4 v = __builtin_amdgcn_raw_buffer_load_b128(rpt, 32768 + w * 8192 + 2048 + lane * 16, 0, 16); const float f = fw[0][0];
            a[0] += f * __uint_as_float(v[0] << 16); a[1] += f * __uint_as_float(v[0] & 0xffff0000u); a[2] += f * __uint_as_float(v[1] << 16); a[3] += f * __uint_as_float(v[1] & 0xffff0000u);
            b2[0] += f * __uint_as_float(v[2] << 16); b2[1] += f * __uint_as_float(v[2] & 0xffff0000u); b2[2] += f * __uint_as_float(v[3] << 16); b2[3] += f * __uint_as_float(v[3] & 0xffff0000u); }
          { u32x4 v = __builtin_amdgcn_raw_buffer_load_b128(rpt, 294912 + w * 8192 + 2048 + lane * 16, 0, 16); const float f = fw[0][2];
            a[0] += f * __uint_as_float(v[0] << 16); a[1] += f * __uint_as_float(v[0] & 0xffff0000u); a[2] += f * __uint_as_float(v[1] << 16); a[3] += f * __uint_as_float(v[1] & 0xffff0000u);
            b2[0] += f * __uint_as_float(v[2] << 16); b2[1] += f * __uint_as_float(v[2] & 0xffff0000u); b2[2] += f * __uint_as_float(v[3] << 16); b2[3] += f * __uint_as_float(v[3] & 0xffff0000u); }
          { u32x4 v = __builtin_amdgcn_raw_buffer_load_b128(rpt, 425984 + w * 8192 + 2048 + lane * 16, 0, 16); const float f = fw[0][3];
            a[0] += f * __uint_as_float(v[0] << 16); a[1] += f * __uint_as_float(v[0] & 0xffff0000u); a[2] += f * __uint_as_float(v[1] << 16); a[3] += f * __uint_as_float(v[1] & 0xffff0000u);
            b2[0] += f * __uint_as_float(v[2] << 16); b2[1] += f * __uint_as_float(v[2] & 0xffff0000u); b2[2] += f * __uint_as_float(v[3] << 16); b2[3] += f * __uint_as_float(v[3] & 0xffff0000u); }
          if (R < 224) { u16* dst = o + ((size_t)((kvh * 2 + hl) * 8 + 3) * NROW + orow) * 32 + 0 + 4 * h;
            u32x2 v0 = {cvtpk(a[0], a[1]), cvtpk(a[2], a[3])}; u32x2 v1 = {cvtpk(b2[0], b2[1]), cvtpk(b2[2], b2[3])};
            *(u32x2*)dst = v0; *(u32x2*)(dst + 8) = v1; } }
        { f32x4 a = acc_read4<56>(), b2 = acc_read4<60>(); const float fo = fw[0][1] * inv[0]; a *= fo; b2 *= fo;
          { u32x4 v = __builtin_amdgcn_raw_buffer_load_b128(rpt, 32768 + w * 8192 + 3072 + lane * 16, 0, 16); const float f = fw[0][0];
            a[0] += f * __uint_as_float(v[0] << 16); a[1] += f * __uint_as_float(v[0] & 0xffff0000u); a[2] += f * __uint_as_float(v[1] << 16); a[3] += f * __uint_as_float(v[1] & 0xffff0000u);
            b2[0] += f * __uint_as_float(v[2] << 16); b2[1] += f * __uint_as_float(v[2] & 0xffff0000u); b2[2] += f * __uint_as_float(v[3] << 16); b2[3] += f * __uint_as_float(v[3] & 0xffff0000u); }
          { u32x4 v = __builtin_amdgcn_raw_buffer_load_b128(rpt, 294912 + w * 8192 + 3072 + lane * 16, 0, 16); const float f = fw[0][2];
            a[0] += f * __uint_as_float(v[0] << 16); a[1] += f * __uint_as_float(v[0] & 0xffff0000u); a[2] += f * __uint_as_float(v[1] << 16); a[3] += f * __uint_as_float(v[1] & 0xffff0000u);
            b2[0] += f * __uint_as_float(v[2] << 16); b2[1] += f * __uint_as_float(v[2] & 0xffff0000u); b2[2] += f * __uint_as_float(v[3] << 16); b2[3] += f * __uint_as_float(v[3] & 0xffff0000u); }
          { u32x4 v = __builtin_amdgcn_raw_buffer_load_b128(rpt, 425984 + w * 8192 + 3072 + lane * 16, 0, 16); const float f = fw[0][3];
            a[0] += f * __uint_as_float(v[0] << 16); a[1] += f * __uint_as_float(v[0] & 0xffff0000u); a[2] += f * __uint_as_float(v[1] << 16); a[3] += f * __uint_as_float(v[1] & 0xffff0000u);
            b2[0] += f * __uint_as_float(v[2] << 16); b2[1] += f * __uint_as_float(v[2] & 0xffff0000u); b2[2] += f * __uint_as_float(v[3] << 16); b2[3] += f * __uint_as_float(v[3] & 0xffff0000u); }
          if (R < 224) { u16* dst = o + ((size_t)((kvh * 2 + hl) * 8 + 3) * NROW + orow) * 32 + 16 + 4 * h;
            u32x2 v0 = {cvtpk(a[0], a[1]), cvtpk(a[2], a[3])}; u32x2 v1 = {cvtpk(b2[0], b2[1]), cvtpk(b2[2], b2[3])};
            *(u32x2*)dst = v0; *(u32x2*)(dst + 8) = v1; } }
      }
      { const int R = 64 * w + 32 + r; const int hl = R >= 112 ? 1 : 0; const int orow = b * SQ + R - 112 * hl;
        { f32x4 a = acc_read4<160>(), b2 = acc_read4<164>(); const float fo = fw[1][1] * inv[1]; a *= fo; b2 *= fo;
          { u32x4 v = __builtin_amdgcn_raw_buffer_load_b128(rpt, 32768 + w * 8192 + 4096 + lane * 16, 0, 16); const float f = fw[1][0];
            a[0] += f * __uint_as_float(v[0] << 16); a[1] += f * __uint_as_float(v[0] & 0xffff0000u); a[2] += f * __uint_as_float(v[1] << 16); a[3] += f * __uint_as_float(v[1] & 0xffff0000u);
            b2[0] += f * __uint_as_float(v[2] << 16); b2[1] += f * __uint_as_float(v[2] & 0xffff0000u); b2[2] += f * __uint_as_float(v[3] << 16); b2[3] += f * __uint_as_float(v[3] & 0xffff0000u); }
          { u32x4 v = __builtin_amdgcn_raw_buffer_load_b128(rpt, 294912 + w * 8192 + 4096 + lane * 16, 0, 16); const float f = fw[1][2];
            a[0] += f * __uint_as_float(v[0] << 16); a[1] += f * __uint_as_float(v[0] & 0xffff0000u); a[2] += f * __uint_as_float(v[1] << 16); a[3] += f * __uint_as_float(v[1] & 0xffff0000u);
            b2[0] += f * __uint_as_float(v[2] << 16); b2[1] += f * __uint_as_float(v[2] & 0xffff0000u); b2[2] += f * __uint_as_float(v[3] << 16); b2[3] += f * __uint_as_float(v[3] & 0xffff0000u); }
          { u32x4 v = __builtin_amdgcn_raw_buffer_load_b128(rpt, 425984 + w * 8192 + 4096 + lane * 16, 0, 16); const float f = fw[1][3];
            a[0] += f * __uint_as_float(v[0] << 16); a[1] += f * __uint_as_float(v[0] & 0xffff0000u); a[2] += f * __uint_as_float(v[1] << 16); a[3] += f * __uint_as_float(v[1] & 0xffff0000u);
            b2[0] += f * __uint_as_float(v[2] << 16); b2[1] += f * __uint_as_float(v[2] & 0xffff0000u); b2[2] += f * __uint_as_float(v[3] << 16); b2[3] += f * __uint_as_float(v[3] & 0xffff0000u); }
          if (R < 224) { u16* dst = o + ((size_t)((kvh * 2 + hl) * 8 + 2) * NROW + orow) * 32 + 0 + 4 * h;
            u32x2 v0 = {cvtpk(a[0], a[1]), cvtpk(a[2], a[3])}; u32x2 v1 = {cvtpk(b2[0], b2[1]), cvtpk(b2[2], b2[3])};
            *(u32x2*)dst = v0; *(u32x2*)(dst + 8) = v1; } }
        { f32x4 a = acc_read4<168>(), b2 = acc_read4<172>(); const float fo = fw[1][1] * inv[1]; a *= fo; b2 *= fo;
          { u32x4 v = __builtin_amdgcn_raw_buffer_load_b128(rpt, 32768 + w * 8192 + 5120 + lane * 16, 0, 16); const float f = fw[1][0];
            a[0] += f * __uint_as_float(v[0] << 16); a[1] += f * __uint_as_float(v[0] & 0xffff0000u); a[2] += f * __uint_as_float(v[1] << 16); a[3] += f * __uint_as_float(v[1] & 0xffff0000u);
            b2[0] += f * __uint_as_float(v[2] << 16); b2[1] += f * __uint_as_float(v[2] & 0xffff0000u); b2[2] += f * __uint_as_float(v[3] << 16); b2[3] += f * __uint_as_float(v[3] & 0xffff0000u); }
          { u32x4 v = __builtin_amdgcn_raw_buffer_load_b128(rpt, 294912 + w * 8192 + 5120 + lane * 16, 0, 16); const float f = fw[1][2];
            a[0] += f * __uint_as_float(v[0] << 16); a[1] += f * __uint_as_float(v[0] & 0xffff0000u); a[2] += f * __uint_as_float(v[1] << 16); a[3] += f * __uint_as_float(v[1] & 0xffff0000u);
            b2[0] += f * __uint_as_float(v[2] << 16); b2[1] += f * __uint_as_float(v[2] & 0xffff0000u); b2[2] += f * __uint_as_float(v[3] << 16); b2[3] += f * __uint_as_float(v[3] & 0xffff0000u); }
          { u32x4 v = __builtin_amdgcn_raw_buffer_load_b128(rpt, 425984 + w * 8192 + 5120 + lane * 16, 0, 16); const float f = fw[1][3];
            a[0] += f * __uint_as_float(v[0] << 16); a[1] += f * __uint_as_float(v[0] & 0xffff0000u); a[2] += f * __uint_as_float(v[1] << 16); a[3] += f * __uint_as_float(v[1] & 0xffff0000u);
            b2[0] += f * __uint_as_float(v[2] << 16); b2[1] += f * __uint_as_float(v[2] & 0xffff0000u); b2[2] += f * __uint_as_float(v[3] << 16); b2[3] += f * __uint_as_float(v[3] & 0xffff0000u); }
          if (R < 224) { u16* dst = o + ((size_t)((kvh * 2 + hl) * 8 + 2) * NROW + orow) * 32 + 16 + 4 * h;
            u32x2 v0 = {cvtpk(a[0], a[1]), cvtpk(a[2], a[3])}; u32x2 v1 = {cvtpk(b2[0], b2[1]), cvtpk(b2[2], b2[3])};
            *(u32x2*)dst = v0; *(u32x2*)(dst + 8) = v1; } }
        { f32x4 a = acc_read4<176>(), b2 = acc_read4<180>(); const float fo = fw[1][1] * inv[1]; a *= fo; b2 *= fo;
          { u32x4 v = __builtin_amdgcn_raw_buffer_load_b128(rpt, 32768 + w * 8192 + 6144 + lane * 16, 0, 16); const float f = fw[1][0];
            a[0] += f * __uint_as_float(v[0] << 16); a[1] += f * __uint_as_float(v[0] & 0xffff0000u); a[2] += f * __uint_as_float(v[1] << 16); a[3] += f * __uint_as_float(v[1] & 0xffff0000u);
            b2[0] += f * __uint_as_float(v[2] << 16); b2[1] += f * __uint_as_float(v[2] & 0xffff0000u); b2[2] += f * __uint_as_float(v[3] << 16); b2[3] += f * __uint_as_float(v[3] & 0xffff0000u); }
          { u32x4 v = __builtin_amdgcn_raw_buffer_load_b128(rpt, 294912 + w * 8192 + 6144 + lane * 16, 0, 16); const float f = fw[1][2];
            a[0] += f * __uint_as_float(v[0] << 16); a[1] += f * __uint_as_float(v[0] & 0xffff0000u); a[2] += f * __uint_as_float(v[1] << 16); a[3] += f * __uint_as_float(v[1] & 0xffff0000u);
            b2[0] += f * __uint_as_float(v[2] << 16); b2[1] += f * __uint_as_float(v[2] & 0xffff0000u); b2[2] += f * __uint_as_float(v[3] << 16); b2[3] += f * __uint_as_float(v[3] & 0xffff0000u); }
          { u32x4 v = __builtin_amdgcn_raw_buffer_load_b128(rpt, 425984 + w * 8192 + 6144 + lane * 16, 0, 16); const float f = fw[1][3];
            a[0] += f * __uint_as_float(v[0] << 16); a[1] += f * __uint_as_float(v[0] & 0xffff0000u); a[2] += f * __uint_as_float(v[1] << 16); a[3] += f * __uint_as_float(v[1] & 0xffff0000u);
            b2[0] += f * __uint_as_float(v[2] << 16); b2[1] += f * __uint_as_float(v[2] & 0xffff0000u); b2[2] += f * __uint_as_float(v[3] << 16); b2[3] += f * __uint_as_float(v[3] & 0xffff0000u); }
          if (R < 224) { u16* dst = o + ((size_t)((kvh * 2 + hl) * 8 + 3) * NROW + orow) * 32 + 0 + 4 * h;
            u32x2 v0 = {cvtpk(a[0], a[1]), cvtpk(a[2], a[3])}; u32x2 v1 = {cvtpk(b2[0], b2[1]), cvtpk(b2[2], b2[3])};
            *(u32x2*)dst = v0; *(u32x2*)(dst + 8) = v1; } }
        { f32x4 a = acc_read4<184>(), b2 = acc_read4<188>(); const float fo = fw[1][1] * inv[1]; a *= fo; b2 *= fo;
          { u32x4 v = __builtin_amdgcn_raw_buffer_load_b128(rpt, 32768 + w * 8192 + 7168 + lane * 16, 0, 16); const float f = fw[1][0];
            a[0] += f * __uint_as_float(v[0] << 16); a[1] += f * __uint_as_float(v[0] & 0xffff0000u); a[2] += f * __uint_as_float(v[1] << 16); a[3] += f * __uint_as_float(v[1] & 0xffff0000u);
            b2[0] += f * __uint_as_float(v[2] << 16); b2[1] += f * __uint_as_float(v[2] & 0xffff0000u); b2[2] += f * __uint_as_float(v[3] << 16); b2[3] += f * __uint_as_float(v[3] & 0xffff0000u); }
          { u32x4 v = __builtin_amdgcn_raw_buffer_load_b128(rpt, 294912 + w * 8192 + 7168 + lane * 16, 0, 16); const float f = fw[1][2];
            a[0] += f * __uint_as_float(v[0] << 16); a[1] += f * __uint_as_float(v[0] & 0xffff0000u); a[2] += f * __uint_as_float(v[1] << 16); a[3] += f * __uint_as_float(v[1] & 0xffff0000u);
            b2[0] += f * __uint_as_float(v[2] << 16); b2[1] += f * __uint_as_float(v[2] & 0xffff0000u); b2[2] += f * __uint_as_float(v[3] << 16); b2[3] += f * __uint_as_float(v[3] & 0xffff0000u); }
          { u32x4 v = __builtin_amdgcn_raw_buffer_load_b128(rpt, 425984 + w * 8192 + 7168 + lane * 16, 0, 16); const float f = fw[1][3];
            a[0] += f * __uint_as_float(v[0] << 16); a[1] += f * __uint_as_float(v[0] & 0xffff0000u); a[2] += f * __uint_as_float(v[1] << 16); a[3] += f * __uint_as_float(v[1] & 0xffff0000u);
            b2[0] += f * __uint_as_float(v[2] << 16); b2[1] += f * __uint_as_float(v[2] & 0xffff0000u); b2[2] += f * __uint_as_float(v[3] << 16); b2[3] += f * __uint_as_float(v[3] & 0xffff0000u); }
          if (R < 224) { u16* dst = o + ((size_t)((kvh * 2 + hl) * 8 + 3) * NROW + orow) * 32 + 16 + 4 * h;
            u32x2 v0 = {cvtpk(a[0], a[1]), cvtpk(a[2], a[3])}; u32x2 v1 = {cvtpk(b2[0], b2[1]), cvtpk(b2[2], b2[3])};
            *(u32x2*)dst = v0; *(u32x2*)(dst + 8) = v1; } }
      }
    } break;
    case 2: {
      { const int R = 64 * w + 0 + r; const int hl = R >= 112 ? 1 : 0; const int orow = b * SQ + R - 112 * hl;
        { f32x4 a = acc_read4<64>(), b2 = acc_read4<68>(); const float fo = fw[0][2] * inv[0]; a *= fo; b2 *= fo;
          { u32x4 v = __builtin_amdgcn_raw_buffer_load_b128(rpt, 65536 + w * 8192 + 0 + lane * 16, 0, 16); const float f = fw[0][0];
            a[0] += f * __uint_as_float(v[0] << 16); a[1] += f * __uint_as_float(v[0] & 0xffff0000u); a[2] += f * __uint_as_float(v[1] << 16); a[3] += f * __uint_as_float(v[1] & 0xffff0000u);
            b2[0] += f * __uint_as_float(v[2] << 16); b2[1] += f * __uint_as_float(v[2] & 0xffff0000u); b2[2] += f * __uint_as_float(v[3] << 16); b2[3] += f * __uint_as_float(v[3] & 0xffff0000u); }
          { u32x4 v = __builtin_amdgcn_raw_buffer_load_b128(rpt, 196608 + w * 8192 + 0 + lane * 16, 0, 16); const float f = fw[0][1];
            a[0] += f * __uint_as_float(v[0] << 16); a[1] += f * __uint_as_float(v[0] & 0xffff0000u); a[2] += f * __uint_as_float(v[1] << 16); a[3] += f * __uint_as_float(v[1] & 0xffff0000u);
            b2[0] += f * __uint_as_float(v[2] << 16); b2[1] += f * __uint_as_float(v[2] & 0xffff0000u); b2[2] += f * __uint_as_float(v[3] << 16); b2[3] += f * __uint_as_float(v[3] & 0xffff0000u); }
          { u32x4 v = __builtin_amdgcn_raw_buffer_load_b128(rpt, 458752 + w * 8192 + 0 + lane * 16, 0, 16); const float f = fw[0][3];
            a[0] += f * __uint_as_float(v[0] << 16); a[1] += f * __uint_as_float(v[0] & 0xffff0000u); a[2] += f * __uint_as_float(v[1] << 16); a[3] += f * __uint_as_float(v[1] & 0xffff0000u);
            b2[0] += f * __uint_as_float(v[2] << 16); b2[1] += f * __uint_as_float(v[2] & 0xffff0000u); b2[2] += f * __uint_as_float(v[3] << 16); b2[3] += f * __uint_as_float(v[3] & 0xffff0000u); }
          if (R < 224) { u16* dst = o + ((size_t)((kvh * 2 + hl) * 8 + 4) * NROW + orow) * 32 + 0 + 4 * h;
            u32x2 v0 = {cvtpk(a[0], a[1]), cvtpk(a[2], a[3])}; u32x2 v1 = {cvtpk(b2[0], b2[1]), cvtpk(b2[2], b2[3])};
            *(u32x2*)dst = v0; *(u32x2*)(dst + 8) = v1; } }
        { f32x4 a = acc_read4<72>(), b2 = acc_read4<76>(); const float fo = fw[0][2] * inv[0]; a *= fo; b2 *= fo;
          { u32x4 v = __builtin_amdgcn_raw_buffer_load_b128(rpt, 65536 + w * 8192 + 1024 + lane * 16, 0, 16); const float f = fw[0][0];
            a[0] += f * __uint_as_float(v[0] << 16); a[1] += f * __uint_as_float(v[0] & 0xffff0000u); a[2] += f * __uint_as_float(v[1] << 16); a[3] += f * __uint_as_float(v[1] & 0xffff0000u);
            b2[0] += f * __uint_as_float(v[2] << 16); b2[1] += f * __uint_as_float(v[2] & 0xffff0000u); b2[2] += f * __uint_as_float(v[3] << 16); b2[3] += f * __uint_as_float(v[3] & 0xffff0000u); }
          { u32x4 v = __builtin_amdgcn_raw_buffer_load_b128(rpt, 196608 + w * 8192 + 1024 + lane * 16, 0, 16); const float f = fw[0][1];
            a[0] += f * __uint_as_float(v[0] << 16); a[1] += f * __uint_as_float(v[0] & 0xffff0000u); a[2] += f * __uint_as_float(v[1] << 16); a[3] += f * __uint_as_float(v[1] & 0xffff0000u);
            b2[0] += f * __uint_as_float(v[2] << 16); b2[1] += f * __uint_as_float(v[2] & 0xffff0000u); b2[2] += f * __uint_as_float(v[3] << 16); b2[3] += f * __uint_as_float(v[3] & 0xffff0000u); }
          { u32x4 v = __builtin_amdgcn_raw_buffer_load_b128(rpt, 458752 + w * 8192 + 1024 + lane * 16, 0, 16); const float f = fw[0][3];
            a[0] += f * __uint_as_float(v[0] << 16); a[1] += f * __uint_as_float(v[0] & 0xffff0000u); a[2] += f * __uint_as_float(v[1] << 16); a[3] += f * __uint_as_float(v[1] & 0xffff0000u);
            b2[0] += f * __uint_as_float(v[2] << 16); b2[1] += f * __uint_as_float(v[2] & 0xffff0000u); b2[2] += f * __uint_as_float(v[3] << 16); b2[3] += f * __uint_as_float(v[3] & 0xffff0000u); }
          if (R < 224) { u16* dst = o + ((size_t)((kvh * 2 + hl) * 8 + 4) * NROW + orow) * 32 + 16 + 4 * h;
            u32x2 v0 = {cvtpk(a[0], a[1]), cvtpk(a[2], a[3])}; u32x2 v1 = {cvtpk(b2[0], b2[1]), cvtpk(b2[2], b2[3])};
            *(u32x2*)dst = v0; *(u32x2*)(dst + 8) = v1; } }
        { f32x4 a = acc_read4<80>(), b2 = acc_read4<84>(); const float fo = fw[0][2] * inv[0]; a *= fo; b2 *= fo;
          { u32x4 v = __builtin_amdgcn_raw_buffer_load_b128(rpt, 65536 + w * 8192 + 2048 + lane * 16, 0, 16); const float f = fw[0][0];
            a[0] += f * __uint_as_float(v[0] << 16); a[1] += f * __uint_as_float(v[0] & 0xffff0000u); a[2] += f * __uint_as_float(v[1] << 16); a[3] += f * __uint_as_float(v[1] & 0xffff0000u);
            b2[0] += f * __uint_as_float(v[2] << 16); b2[1] += f * __uint_as_float(v[2] & 0xffff0000u); b2[2] += f * __uint_as_float(v[3] << 16); b2[3] += f * __uint_as_float(v[3] & 0xffff0000u); }
          { u32x4 v = __builtin_amdgcn_raw_buffer_load_b128(rpt, 196608 + w * 8192 + 2048 + lane * 16, 0, 16); const float f = fw[0][1];
            a[0] += f * __uint_as_float(v[0] << 16); a[1] += f * __uint_as_float(v[0] & 0xffff0000u); a[2] += f * __uint_as_float(v[1] << 16); a[3] += f * __uint_as_float(v[1] & 0xffff0000u);
            b2[0] += f * __uint_as_float(v[2] << 16); b2[1] += f * __uint_as_float(v[2] & 0xffff0000u); b2[2] += f * __uint_as_float(v[3] << 16); b2[3] += f * __uint_as_float(v[3] & 0xffff0000u); }
          { u32x4 v = __builtin_amdgcn_raw_buffer_load_b128(rpt, 458752 + w * 8192 + 2048 + lane * 16, 0, 16); const float f = fw[0][3];
            a[0] += f * __uint_as_float(v[0] << 16); a[1] += f * __uint_as_float(v[0] & 0xffff0000u); a[2] += f * __uint_as_float(v[1] << 16); a[3] += f * __uint_as_float(v[1] & 0xffff0000u);
            b2[0] += f * __uint_as_float(v[2] << 16); b2[1] += f * __uint_as_float(v[2] & 0xffff0000u); b2[2] += f * __uint_as_float(v[3] << 16); b2[3] += f * __uint_as_float(v[3] & 0xffff0000u); }
          if (R < 224) { u16* dst = o + ((size_t)((kvh * 2 + hl) * 8 + 5) * NROW + orow) * 32 + 0 + 4 * h;
            u32x2 v0 = {cvtpk(a[0], a[1]), cvtpk(a[2], a[3])}; u32x2 v1 = {cvtpk(b2[0], b2[1]), cvtpk(b2[2], b2[3])};
            *(u32x2*)dst = v0; *(u32x2*)(dst + 8) = v1; } }
        { f32x4 a = acc_read4<88>(), b2 = acc_read4<92>(); const float fo = fw[0][2] * inv[0]; a *= fo; b2 *= fo;
          { u32x4 v = __builtin_amdgcn_raw_buffer_load_b128(rpt, 65536 + w * 8192 + 3072 + lane * 16, 0, 16); const float f = fw[0][0];
            a[0] += f * __uint_as_float(v[0] << 16); a[1] += f * __uint_as_float(v[0] & 0xffff0000u); a[2] += f * __uint_as_float(v[1] << 16); a[3] += f * __uint_as_float(v[1] & 0xffff0000u);
            b2[0] += f * __uint_as_float(v[2] << 16); b2[1] += f * __uint_as_float(v[2] & 0xffff0000u); b2[2] += f * __uint_as_float(v[3] << 16); b2[3] += f * __uint_as_float(v[3] & 0xffff0000u); }
          { u32x4 v = __builtin_amdgcn_raw_buffer_load_b128(rpt, 196608 + w * 8192 + 3072 + lane * 16, 0, 16); const float f = fw[0][1];
            a[0] += f * __uint_as_float(v[0] << 16); a[1] += f * __uint_as_float(v[0] & 0xffff0000u); a[2] += f * __uint_as_float(v[1] << 16); a[3] += f * __uint_as_float(v[1] & 0xffff0000u);
            b2[0] += f * __uint_as_float(v[2] << 16); b2[1] += f * __uint_as_float(v[2] & 0xffff0000u); b2[2] += f * __uint_as_float(v[3] << 16); b2[3] += f * __uint_as_float(v[3] & 0xffff0000u); }
          { u32x4 v = __builtin_amdgcn_raw_buffer_load_b128(rpt, 458752 + w * 8192 + 3072 + lane * 16, 0, 16); const float f = fw[0][3];
            a[0] += f * __uint_as_float(v[0] << 16); a[1] += f * __uint_as_float(v[0] & 0xffff0000u); a[2] += f * __uint_as_float(v[1] << 16); a[3] += f * __uint_as_float(v[1] & 0xffff0000u);
            b2[0] += f * __uint_as_float(v[2] << 16); b2[1] += f * __uint_as_float(v[2] & 0xffff0000u); b2[2] += f * __uint_as_float(v[3] << 16); b2[3] += f * __uint_as_float(v[3] & 0xffff0000u); }
          if (R < 224) { u16* dst = o + ((size_t)((kvh * 2 + hl) * 8 + 5) * NROW + orow) * 32 + 16 + 4 * h;
            u32x2 v0 = {cvtpk(a[0], a[1]), cvtpk(a[2], a[3])}; u32x2 v1 = {cvtpk(b2[0], b2[1]), cvtpk(b2[2], b2[3])};
            *(u32x2*)dst = v0; *(u32x2*)(dst + 8) = v1; } }
      }
      { const int R = 64 * w + 32 + r; const int hl = R >= 112 ? 1 : 0; const int orow = b * SQ + R - 112 * hl;
        { f32x4 a = acc_read4<192>(), b2 = acc_read4<196>(); const float fo = fw[1][2] * inv[1]; a *= fo; b2 *= fo;
          { u32x4 v = __builtin_amdgcn_raw_buffer_load_b128(rpt, 65536 + w * 8192 + 4096 + lane * 16, 0, 16); const float f = fw[1][0];
            a[0] += f * __uint_as_float(v[0] << 16); a[1] += f * __uint_as_float(v[0] & 0xffff0000u); a[2] += f * __uint_as_float(v[1] << 16); a[3] += f * __uint_as_float(v[1] & 0xffff0000u);
            b2[0] += f * __uint_as_float(v[2] << 16); b2[1] += f * __uint_as_float(v[2] & 0xffff0000u); b2[2] += f * __uint_as_float(v[3] << 16); b2[3] += f * __uint_as_float(v[3] & 0xffff0000u); }
          { u32x4 v = __builtin_amdgcn_raw_buffer_load_b128(rpt, 196608 + w * 8192 + 4096 + lane * 16, 0, 16); const float f = fw[1][1];
            a[0] += f * __uint_as_float(v[0] << 16); a[1] += f * __uint_as_float(v[0] & 0xffff0000u); a[2] += f * __uint_as_float(v[1] << 16); a[3] += f * __uint_as_float(v[1] & 0xffff0000u);
            b2[0] += f * __uint_as_float(v[2] << 16); b2[1] += f * __uint_as_float(v[2] & 0xffff0000u); b2[2] += f * __uint_as_float(v[3] << 16); b2[3] += f * __uint_as_float(v[3] & 0xffff0000u); }
          { u32x4 v = __builtin_amdgcn_raw_buffer_load_b128(rpt, 458752 + w * 8192 + 4096 + lane * 16, 0, 16); const float f = fw[1][3];
            a[0] += f * __uint_as_float(v[0] << 16); a[1] += f * __uint_as_float(v[0] & 0xffff0000u); a[2] += f * __uint_as_float(v[1] << 16); a[3] += f * __uint_as_float(v[1] & 0xffff0000u);
            b2[0] += f * __uint_as_float(v[2] << 16); b2[1] += f * __uint_as_float(v[2] & 0xffff0000u); b2[2] += f * __uint_as_float(v[3] << 16); b2[3] += f * __uint_as_float(v[3] & 0xffff0000u); }
          if (R < 224) { u16* dst = o + ((size_t)((kvh * 2 + hl) * 8 + 4) * NROW + orow) * 32 + 0 + 4 * h;
            u32x2 v0 = {cvtpk(a[0], a[1]), cvtpk(a[2], a[3])}; u32x2 v1 = {cvtpk(b2[0], b2[1]), cvtpk(b2[2], b2[3])};
            *(u32x2*)dst = v0; *(u32x2*)(dst + 8) = v1; } }
        { f32x4 a = acc_read4<200>(), b2 = acc_read4<204>(); const float fo = fw[1][2] * inv[1]; a *= fo; b2 *= fo;
          { u32x4 v = __builtin_amdgcn_raw_buffer_load_b128(rpt, 65536 + w * 8192 + 5120 + lane * 16, 0, 16); const float f = fw[1][0];
            a[0] += f * __uint_as_float(v[0] << 16); a[1] += f * __uint_as_float(v[0] & 0xffff0000u); a[2] += f * __uint_as_float(v[1] << 16); a[3] += f * __uint_as_float(v[1] & 0xffff0000u);
            b2[0] += f * __uint_as_float(v[2] << 16); b2[1] += f * __uint_as_float(v[2] & 0xffff0000u); b2[2] += f * __uint_as_float(v[3] << 16); b2[3] += f * __uint_as_float(v[3] & 0xffff0000u); }
          { u32x4 v = __builtin_amdgcn_raw_buffer_load_b128(rpt, 196608 + w * 8192 + 5120 + lane * 16, 0, 16); const float f = fw[1][1];
            a[0] += f * __uint_as_float(v[0] << 16); a[1] += f * __uint_as_float(v[0] & 0xffff0000u); a[2] += f * __uint_as_float(v[1] << 16); a[3] += f * __uint_as_float(v[1] & 0xffff0000u);
            b2[0] += f * __uint_as_float(v[2] << 16); b2[1] += f * __uint_as_float(v[2] & 0xffff0000u); b2[2] += f * __uint_as_float(v[3] << 16); b2[3] += f * __uint_as_float(v[3] & 0xffff0000u); }
          { u32x4 v = __builtin_amdgcn_raw_buffer_load_b128(rpt, 458752 + w * 8192 + 5120 + lane * 16, 0, 16); const float f = fw[1][3];
            a[0] += f * __uint_as_float(v[0] << 16); a[1] += f * __uint_as_float(v[0] & 0xffff0000u); a[2] += f * __uint_as_float(v[1] << 16); a[3] += f * __uint_as_float(v[1] & 0xffff0000u);
            b2[0] += f * __uint_as_float(v[2] << 16); b2[1] += f * __uint_as_float(v[2] & 0xffff0000u); b2[2] += f * __uint_as_float(v[3] << 16); b2[3] += f * __uint_as_float(v[3] & 0xffff0000u); }
          if (R < 224) { u16* dst = o + ((size_t)((kvh * 2 + hl) * 8 + 4) * NROW + orow) * 32 + 16 + 4 * h;
            u32x2 v0 = {cvtpk(a[0], a[1]), cvtpk(a[2], a[3])}; u32x2 v1 = {cvtpk(b2[0], b2[1]), cvtpk(b2[2], b2[3])};
            *(u32x2*)dst = v0; *(u32x2*)(dst + 8) = v1; } }
        { f32x4 a = acc_read4<208>(), b2 = acc_read4<212>(); const float fo = fw[1][2] * inv[1]; a *= fo; b2 *= fo;
          { u32x4 v = __builtin_amdgcn_raw_buffer_load_b128(rpt, 65536 + w * 8192 + 6144 + lane * 16, 0, 16); const float f = fw[1][0];
            a[0] += f * __uint_as_float(v[0] << 16); a[1] += f * __uint_as_float(v[0] & 0xffff0000u); a[2] += f * __uint_as_float(v[1] << 16); a[3] += f * __uint_as_float(v[1] & 0xffff0000u);
            b2[0] += f * __uint_as_float(v[2] << 16); b2[1] += f * __uint_as_float(v[2] & 0xffff0000u); b2[2] += f * __uint_as_float(v[3] << 16); b2[3] += f * __uint_as_float(v[3] & 0xffff0000u); }
          { u32x4 v = __builtin_amdgcn_raw_buffer_load_b128(rpt, 196608 + w * 8192 + 6144 + lane * 16, 0, 16); const float f = fw[1][1];
            a[0] += f * __uint_as_float(v[0] << 16); a[1] += f * __uint_as_float(v[0] & 0xffff0000u); a[2] += f * __uint_as_float(v[1] << 16); a[3] += f * __uint_as_float(v[1] & 0xffff0000u);
            b2[0] += f * __uint_as_float(v[2] << 16); b2[1] += f * __uint_as_float(v[2] & 0xffff0000u); b2[2] += f * __uint_as_float(v[3] << 16); b2[3] += f * __uint_as_float(v[3] & 0xffff0000u); }
          { u32x4 v = __builtin_amdgcn_raw_buffer_load_b128(rpt, 458752 + w * 8192 + 6144 + lane * 16, 0, 16); const float f = fw[1][3];
            a[0] += f * __uint_as_float(v[0] << 16); a[1] += f * __uint_as_float(v[0] & 0xffff0000u); a[2] += f * __uint_as_float(v[1] << 16); a[3] += f * __uint_as_float(v[1] & 0xffff0000u);
            b2[0] += f * __uint_as_float(v[2] << 16); b2[1] += f * __uint_as_float(v[2] & 0xffff0000u); b2[2] += f * __uint_as_float(v[3] << 16); b2[3] += f * __uint_as_float(v[3] & 0xffff0000u); }
          if (R < 224) { u16* dst = o + ((size_t)((kvh * 2 + hl) * 8 + 5) * NROW + orow) * 32 + 0 + 4 * h;
            u32x2 v0 = {cvtpk(a[0], a[1]), cvtpk(a[2], a[3])}; u32x2 v1 = {cvtpk(b2[0], b2[1]), cvtpk(b2[2], b2[3])};
            *(u32x2*)dst = v0; *(u32x2*)(dst + 8) = v1; } }
        { f32x4 a = acc_read4<216>(), b2 = acc_read4<220>(); const float fo = fw[1][2] * inv[1]; a *= fo; b2 *= fo;
          { u32x4 v = __builtin_amdgcn_raw_buffer_load_b128(rpt, 65536 + w * 8192 + 7168 + lane * 16, 0, 16); const float f = fw[1][0];
            a[0] += f * __uint_as_float(v[0] << 16); a[1] += f * __uint_as_float(v[0] & 0xffff0000u); a[2] += f * __uint_as_float(v[1] << 16); a[3] += f * __uint_as_float(v[1] & 0xffff0000u);
            b2[0] += f * __uint_as_float(v[2] << 16); b2[1] += f * __uint_as_float(v[2] & 0xffff0000u); b2[2] += f * __uint_as_float(v[3] << 16); b2[3] += f * __uint_as_float(v[3] & 0xffff0000u); }
          { u32x4 v = __builtin_amdgcn_raw_buffer_load_b128(rpt, 196608 + w * 8192 + 7168 + lane * 16, 0, 16); const float f = fw[1][1];
            a[0] += f * __uint_as_float(v[0] << 16); a[1] += f * __uint_as_float(v[0] & 0xffff0000u); a[2] += f * __uint_as_float(v[1] << 16); a[3] += f * __uint_as_float(v[1] & 0xffff0000u);
            b2[0] += f * __uint_as_float(v[2] << 16); b2[1] += f * __uint_as_float(v[2] & 0xffff0000u); b2[2] += f * __uint_as_float(v[3] << 16); b2[3] += f * __uint_as_float(v[3] & 0xffff0000u); }
          { u32x4 v = __builtin_amdgcn_raw_buffer_load_b128(rpt, 458752 + w * 8192 + 7168 + lane * 16, 0, 16); const float f = fw[1][3];
            a[0] += f * __uint_as_float(v[0] << 16); a[1] += f * __uint_as_float(v[0] & 0xffff0000u); a[2] += f * __uint_as_float(v[1] << 16); a[3] += f * __uint_as_float(v[1] & 0xffff0000u);
            b2[0] += f * __uint_as_float(v[2] << 16); b2[1] += f * __uint_as_float(v[2] & 0xffff0000u); b2[2] += f * __uint_as_float(v[3] << 16); b2[3] += f * __uint_as_float(v[3] & 0xffff0000u); }
          if (R < 224) { u16* dst = o + ((size_t)((kvh * 2 + hl) * 8 + 5) * NROW + orow) * 32 + 16 + 4 * h;
            u32x2 v0 = {cvtpk(a[0], a[1]), cvtpk(a[2], a[3])}; u32x2 v1 = {cvtpk(b2[0], b2[1]), cvtpk(b2[2], b2[3])};
            *(u32x2*)dst = v0; *(u32x2*)(dst + 8) = v1; } }
      }
    } break;
    case 3: {
      { const int R = 64 * w + 0 + r; const int hl = R >= 112 ? 1 : 0; const int orow = b * SQ + R - 112 * hl;
        { f32x4 a = acc_read4<96>(), b2 = acc_read4<100>(); const float fo = fw[0][3] * inv[0]; a *= fo; b2 *= fo;
          { u32x4 v = __builtin_amdgcn_raw_buffer_load_b128(rpt, 98304 + w * 8192 + 0 + lane * 16, 0, 16); const float f = fw[0][0];
            a[0] += f * __uint_as_float(v[0] << 16); a[1] += f * __uint_as_float(v[0] & 0xffff0000u); a[2] += f * __uint_as_float(v[1] << 16); a[3] += f * __uint_as_float(v[1] & 0xffff0000u);
            b2[0] += f * __uint_as_float(v[2] << 16); b2[1] += f * __uint_as_float(v[2] & 0xffff0000u); b2[2] += f * __uint_as_float(v[3] << 16); b2[3] += f * __uint_as_float(v[3] & 0xffff0000u); }
          { u32x4 v = __builtin_amdgcn_raw_buffer_load_b128(rpt, 229376 + w * 8192 + 0 + lane * 16, 0, 16); const float f = fw[0][1];
            a[0] += f * __uint_as_float(v[0] << 16); a[1] += f * __uint_as_float(v[0] & 0xffff0000u); a[2] += f * __uint_as_float(v[1] << 16); a[3] += f * __uint_as_float(v[1] & 0xffff0000u);
            b2[0] += f * __uint_as_float(v[2] << 16); b2[1] += f * __uint_as_float(v[2] & 0xffff0000u); b2[2] += f * __uint_as_float(v[3] << 16); b2[3] += f * __uint_as_float(v[3] & 0xffff0000u); }
          { u32x4 v = __builtin_amdgcn_raw_buffer_load_b128(rpt, 360448 + w * 8192 + 0 + lane * 16, 0, 16); const float f = fw[0][2];
            a[0] += f * __uint_as_float(v[0] << 16); a[1] += f * __uint_as_float(v[0] & 0xffff0000u); a[2] += f * __uint_as_float(v[1] << 16); a[3] += f * __uint_as_float(v[1] & 0xffff0000u);
            b2[0] += f * __uint_as_float(v[2] << 16); b2[1] += f * __uint_as_float(v[2] & 0xffff0000u); b2[2] += f * __uint_as_float(v[3] << 16); b2[3] += f * __uint_as_float(v[3] & 0xffff0000u); }
          if (R < 224) { u16* dst = o + ((size_t)((kvh * 2 + hl) * 8 + 6) * NROW + orow) * 32 + 0 + 4 * h;
            u32x2 v0 = {cvtpk(a[0], a[1]), cvtpk(a[2], a[3])}; u32x2 v1 = {cvtpk(b2[0], b2[1]), cvtpk(b2[2], b2[3])};
            *(u32x2*)dst = v0; *(u32x2*)(dst + 8) = v1; } }
        { f32x4 a = acc_read4<104>(), b2 = acc_read4<108>(); const float fo = fw[0][3] * inv[0]; a *= fo; b2 *= fo;
          { u32x4 v = __builtin_amdgcn_raw_buffer_load_b128(rpt, 98304 + w * 8192 + 1024 + lane * 16, 0, 16); const float f = fw[0][0];
            a[0] += f * __uint_as_float(v[0] << 16); a[1] += f * __uint_as_float(v[0] & 0xffff0000u); a[2] += f * __uint_as_float(v[1] << 16); a[3] += f * __uint_as_float(v[1] & 0xffff0000u);
            b2[0] += f * __uint_as_float(v[2] << 16); b2[1] += f * __uint_as_float(v[2] & 0xffff0000u); b2[2] += f * __uint_as_float(v[3] << 16); b2[3] += f * __uint_as_float(v[3] & 0xffff0000u); }
          { u32x4 v = __builtin_amdgcn_raw_buffer_load_b128(rpt, 229376 + w * 8192 + 1024 + lane * 16, 0, 16); const float f = fw[0][1];
            a[0] += f * __uint_as_float(v[0] << 16); a[1] += f * __uint_as_float(v[0] & 0xffff0000u); a[2] += f * __uint_as_float(v[1] << 16); a[3] += f * __uint_as_float(v[1] & 0xffff0000u);
            b2[0] += f * __uint_as_float(v[2] << 16); b2[1] += f * __uint_as_float(v[2] & 0xffff0000u); b2[2] += f * __uint_as_float(v[3] << 16); b2[3] += f * __uint_as_float(v[3] & 0xffff0000u); }
          { u32x4 v = __builtin_amdgcn_raw_buffer_load_b128(rpt, 360448 + w * 8192 + 1024 + lane * 16, 0, 16); const float f = fw[0][2];
            a[0] += f * __uint_as_float(v[0] << 16); a[1] += f * __uint_as_float(v[0] & 0xffff0000u); a[2] += f * __uint_as_float(v[1] << 16); a[3] += f * __uint_as_float(v[1] & 0xffff0000u);
            b2[0] += f * __uint_as_float(v[2] << 16); b2[1] += f * __uint_as_float(v[2] & 0xffff0000u); b2[2] += f * __uint_as_float(v[3] << 16); b2[3] += f * __uint_as_float(v[3] & 0xffff0000u); }
          if (R < 224) { u16* dst = o + ((size_t)((kvh * 2 + hl) * 8 + 6) * NROW + orow) * 32 + 16 + 4 * h;
            u32x2 v0 = {cvtpk(a[0], a[1]), cvtpk(a[2], a[3])}; u32x2 v1 = {cvtpk(b2[0], b2[1]), cvtpk(b2[2], b2[3])};
            *(u32x2*)dst = v0; *(u32x2*)(dst + 8) = v1; } }
        { f32x4 a = acc_read4<112>(), b2 = acc_read4<116>(); const float fo = fw[0][3] * inv[0]; a *= fo; b2 *= fo;
          { u32x4 v = __builtin_amdgcn_raw_buffer_load_b128(rpt, 98304 + w * 8192 + 2048 + lane * 16, 0, 16); const float f = fw[0][0];
            a[0] += f * __uint_as_float(v[0] << 16); a[1] += f * __uint_as_float(v[0] & 0xffff0000u); a[2] += f * __uint_as_float(v[1] << 16); a[3] += f * __uint_as_float(v[1] & 0xffff0000u);
            b2[0] += f * __uint_as_float(v[2] << 16); b2[1] += f * __uint_as_float(v[2] & 0xffff0000u); b2[2] += f * __uint_as_float(v[3] << 16); b2[3] += f * __uint_as_float(v[3] & 0xffff0000u); }
          { u32x4 v = __builtin_amdgcn_raw_buffer_load_b128(rpt, 229376 + w * 8192 + 2048 + lane * 16, 0, 16); const float f = fw[0][1];
            a[0] += f * __uint_as_float(v[0] << 16); a[1] += f * __uint_as_float(v[0] & 0xffff0000u); a[2] += f * __uint_as_float(v[1] << 16); a[3] += f * __uint_as_float(v[1] & 0xffff0000u);
            b2[0] += f * __uint_as_float(v[2] << 16); b2[1] += f * __uint_as_float(v[2] & 0xffff0000u); b2[2] += f * __uint_as_float(v[3] << 16); b2[3] += f * __uint_as_float(v[3] & 0xffff0000u); }
          { u32x4 v = __builtin_amdgcn_raw_buffer_load_b128(rpt, 360448 + w * 8192 + 2048 + lane * 16, 0, 16); const float f = fw[0][2];
            a[0] += f * __uint_as_float(v[0] << 16); a[1] += f * __uint_as_float(v[0] & 0xffff0000u); a[2] += f * __uint_as_float(v[1] << 16); a[3] += f * __uint_as_float(v[1] & 0xffff0000u);
            b2[0] += f * __uint_as_float(v[2] << 16); b2[1] += f * __uint_as_float(v[2] & 0xffff0000u); b2[2] += f * __uint_as_float(v[3] << 16); b2[3] += f * __uint_as_float(v[3] & 0xffff0000u); }
          if (R < 224) { u16* dst = o + ((size_t)((kvh * 2 + hl) * 8 + 7) * NROW + orow) * 32 + 0 + 4 * h;
            u32x2 v0 = {cvtpk(a[0], a[1]), cvtpk(a[2], a[3])}; u32x2 v1 = {cvtpk(b2[0], b2[1]), cvtpk(b2[2], b2[3])};
            *(u32x2*)dst = v0; *(u32x2*)(dst + 8) = v1; } }
        { f32x4 a = acc_read4<120>(), b2 = acc_read4<124>(); const float fo = fw[0][3] * inv[0]; a *= fo; b2 *= fo;
          { u32x4 v = __builtin_amdgcn_raw_buffer_load_b128(rpt, 98304 + w * 8192 + 3072 + lane * 16, 0, 16); const float f = fw[0][0];
            a[0] += f * __uint_as_float(v[0] << 16); a[1] += f * __uint_as_float(v[0] & 0xffff0000u); a[2] += f * __uint_as_float(v[1] << 16); a[3] += f * __uint_as_float(v[1] & 0xffff0000u);
            b2[0] += f * __uint_as_float(v[2] << 16); b2[1] += f * __uint_as_float(v[2] & 0xffff0000u); b2[2] += f * __uint_as_float(v[3] << 16); b2[3] += f * __uint_as_float(v[3] & 0xffff0000u); }
          { u32x4 v = __builtin_amdgcn_raw_buffer_load_b128(rpt, 229376 + w * 8192 + 3072 + lane * 16, 0, 16); const float f = fw[0][1];
            a[0] += f * __uint_as_float(v[0] << 16); a[1] += f * __uint_as_float(v[0] & 0xffff0000u); a[2] += f * __uint_as_float(v[1] << 16); a[3] += f * __uint_as_float(v[1] & 0xffff0000u);
            b2[0] += f * __uint_as_float(v[2] << 16); b2[1] += f * __uint_as_float(v[2] & 0xffff0000u); b2[2] += f * __uint_as_float(v[3] << 16); b2[3] += f * __uint_as_float(v[3] & 0xffff0000u); }
          { u32x4 v = __builtin_amdgcn_raw_buffer_load_b128(rpt, 360448 + w * 8192 + 3072 + lane * 16, 0, 16); const float f = fw[0][2];
            a[0] += f * __uint_as_float(v[0] << 16); a[1] += f * __uint_as_float(v[0] & 0xffff0000u); a[2] += f * __uint_as_float(v[1] << 16); a[3] += f * __uint_as_float(v[1] & 0xffff0000u);
            b2[0] += f * __uint_as_float(v[2] << 16); b2[1] += f * __uint_as_float(v[2] & 0xffff0000u); b2[2] += f * __uint_as_float(v[3] << 16); b2[3] += f * __uint_as_float(v[3] & 0xffff0000u); }
          if (R < 224) { u16* dst = o + ((size_t)((kvh * 2 + hl) * 8 + 7) * NROW + orow) * 32 + 16 + 4 * h;
            u32x2 v0 = {cvtpk(a[0], a[1]), cvtpk(a[2], a[3])}; u32x2 v1 = {cvtpk(b2[0], b2[1]), cvtpk(b2[2], b2[3])};
            *(u32x2*)dst = v0; *(u32x2*)(dst + 8) = v1; } }
      }
      { const int R = 64 * w + 32 + r; const int hl = R >= 112 ? 1 : 0; const int orow = b * SQ + R - 112 * hl;
        { f32x4 a = acc_read4<224>(), b2 = acc_read4<228>(); const float fo = fw[1][3] * inv[1]; a *= fo; b2 *= fo;
          { u32x4 v = __builtin_amdgcn_raw_buffer_load_b128(rpt, 98304 + w * 8192 + 4096 + lane * 16, 0, 16); const float f = fw[1][0];
            a[0] += f * __uint_as_float(v[0] << 16); a[1] += f * __uint_as_float(v[0] & 0xffff0000u); a[2] += f * __uint_as_float(v[1] << 16); a[3] += f * __uint_as_float(v[1] & 0xffff0000u);
            b2[0] += f * __uint_as_float(v[2] << 16); b2[1] += f * __uint_as_float(v[2] & 0xffff0000u); b2[2] += f * __uint_as_float(v[3] << 16); b2[3] += f * __uint_as_float(v[3] & 0xffff0000u); }
          { u32x4 v = __builtin_amdgcn_raw_buffer_load_b128(rpt, 229376 + w * 8192 + 4096 + lane * 16, 0, 16); const float f = fw[1][1];
            a[0] += f * __uint_as_float(v[0] << 16); a[1] += f * __uint_as_float(v[0] & 0xffff0000u); a[2] += f * __uint_as_float(v[1] << 16); a[3] += f * __uint_as_float(v[1] & 0xffff0000u);
            b2[0] += f * __uint_as_float(v[2] << 16); b2[1] += f * __uint_as_float(v[2] & 0xffff0000u); b2[2] += f * __uint_as_float(v[3] << 16); b2[3] += f * __uint_as_float(v[3] & 0xffff0000u); }
          { u32x4 v = __builtin_amdgcn_raw_buffer_load_b128(rpt, 360448 + w * 8192 + 4096 + lane * 16, 0, 16); const float f = fw[1][2];
            a[0] += f * __uint_as_float(v[0] << 16); a[1] += f * __uint_as_float(v[0] & 0xffff0000u); a[2] += f * __uint_as_float(v[1] << 16); a[3] += f * __uint_as_float(v[1] & 0xffff0000u);
            b2[0] += f * __uint_as_float(v[2] << 16); b2[1] += f * __uint_as_float(v[2] & 0xffff0000u); b2[2] += f * __uint_as_float(v[3] << 16); b2[3] += f * __uint_as_float(v[3] & 0xffff0000u); }
          if (R < 224) { u16* dst = o + ((size_t)((kvh * 2 + hl) * 8 + 6) * NROW + orow) * 32 + 0 + 4 * h;
            u32x2 v0 = {cvtpk(a[0], a[1]), cvtpk(a[2], a[3])}; u32x2 v1 = {cvtpk(b2[0], b2[1]), cvtpk(b2[2], b2[3])};
            *(u32x2*)dst = v0; *(u32x2*)(dst + 8) = v1; } }
        { f32x4 a = acc_read4<232>(), b2 = acc_read4<236>(); const float fo = fw[1][3] * inv[1]; a *= fo; b2 *= fo;
          { u32x4 v = __builtin_amdgcn_raw_buffer_load_b128(rpt, 98304 + w * 8192 + 5120 + lane * 16, 0, 16); const float f = fw[1][0];
            a[0] += f * __uint_as_float(v[0] << 16); a[1] += f * __uint_as_float(v[0] & 0xffff0000u); a[2] += f * __uint_as_float(v[1] << 16); a[3] += f * __uint_as_float(v[1] & 0xffff0000u);
            b2[0] += f * __uint_as_float(v[2] << 16); b2[1] += f * __uint_as_float(v[2] & 0xffff0000u); b2[2] += f * __uint_as_float(v[3] << 16); b2[3] += f * __uint_as_float(v[3] & 0xffff0000u); }
          { u32x4 v = __builtin_amdgcn_raw_buffer_load_b128(rpt, 229376 + w * 8192 + 5120 + lane * 16, 0, 16); const float f = fw[1][1];
            a[0] += f * __uint_as_float(v[0] << 16); a[1] += f * __uint_as_float(v[0] & 0xffff0000u); a[2] += f * __uint_as_float(v[1] << 16); a[3] += f * __uint_as_float(v[1] & 0xffff0000u);
            b2[0] += f * __uint_as_float(v[2] << 16); b2[1] += f * __uint_as_float(v[2] & 0xffff0000u); b2[2] += f * __uint_as_float(v[3] << 16); b2[3] += f * __uint_as_float(v[3] & 0xffff0000u); }
          { u32x4 v = __builtin_amdgcn_raw_buffer_load_b128(rpt, 360448 + w * 8192 + 5120 + lane * 16, 0, 16); const float f = fw[1][2];
            a[0] += f * __uint_as_float(v[0] << 16); a[1] += f * __uint_as_float(v[0] & 0xffff0000u); a[2] += f * __uint_as_float(v[1] << 16); a[3] += f * __uint_as_float(v[1] & 0xffff0000u);
            b2[0] += f * __uint_as_float(v[2] << 16); b2[1] += f * __uint_as_float(v[2] & 0xffff0000u); b2[2] += f * __uint_as_float(v[3] << 16); b2[3] += f * __uint_as_float(v[3] & 0xffff0000u); }
          if (R < 224) { u16* dst = o + ((size_t)((kvh * 2 + hl) * 8 + 6) * NROW + orow) * 32 + 16 + 4 * h;
            u32x2 v0 = {cvtpk(a[0], a[1]), cvtpk(a[2], a[3])}; u32x2 v1 = {cvtpk(b2[0], b2[1]), cvtpk(b2[2], b2[3])};
            *(u32x2*)dst = v0; *(u32x2*)(dst + 8) = v1; } }
        { f32x4 a = acc_read4<240>(), b2 = acc_read4<244>(); const float fo = fw[1][3] * inv[1]; a *= fo; b2 *= fo;
          { u32x4 v = __builtin_amdgcn_raw_buffer_load_b128(rpt, 98304 + w * 8192 + 6144 + lane * 16, 0, 16); const float f = fw[1][0];
            a[0] += f * __uint_as_float(v[0] << 16); a[1] += f * __uint_as_float(v[0] & 0xffff0000u); a[2] += f * __uint_as_float(v[1] << 16); a[3] += f * __uint_as_float(v[1] & 0xffff0000u);
            b2[0] += f * __uint_as_float(v[2] << 16); b2[1] += f * __uint_as_float(v[2] & 0xffff0000u); b2[2] += f * __uint_as_float(v[3] << 16); b2[3] += f * __uint_as_float(v[3] & 0xffff0000u); }
          { u32x4 v = __builtin_amdgcn_raw_buffer_load_b128(rpt, 229376 + w * 8192 + 6144 + lane * 16, 0, 16); const float f = fw[1][1];
            a[0] += f * __uint_as_float(v[0] << 16); a[1] += f * __uint_as_float(v[0] & 0xffff0000u); a[2] += f * __uint_as_float(v[1] << 16); a[3] += f * __uint_as_float(v[1] & 0xffff0000u);
            b2[0] += f * __uint_as_float(v[2] << 16); b2[1] += f * __uint_as_float(v[2] & 0xffff0000u); b2[2] += f * __uint_as_float(v[3] << 16); b2[3] += f * __uint_as_float(v[3] & 0xffff0000u); }
          { u32x4 v = __builtin_amdgcn_raw_buffer_load_b128(rpt, 360448 + w * 8192 + 6144 + lane * 16, 0, 16); const float f = fw[1][2];
            a[0] += f * __uint_as_float(v[0] << 16); a[1] += f * __uint_as_float(v[0] & 0xffff0000u); a[2] += f * __uint_as_float(v[1] << 16); a[3] += f * __uint_as_float(v[1] & 0xffff0000u);
            b2[0] += f * __uint_as_float(v[2] << 16); b2[1] += f * __uint_as_float(v[2] & 0xffff0000u); b2[2] += f * __uint_as_float(v[3] << 16); b2[3] += f * __uint_as_float(v[3] & 0xffff0000u); }
          if (R < 224) { u16* dst = o + ((size_t)((kvh * 2 + hl) * 8 + 7) * NROW + orow) * 32 + 0 + 4 * h;
            u32x2 v0 = {cvtpk(a[0], a[1]), cvtpk(a[2], a[3])}; u32x2 v1 = {cvtpk(b2[0], b2[1]), cvtpk(b2[2], b2[3])};
            *(u32x2*)dst = v0; *(u32x2*)(dst + 8) = v1; } }
        { f32x4 a = acc_read4<248>(), b2 = acc_read4<252>(); const float fo = fw[1][3] * inv[1]; a *= fo; b2 *= fo;
          { u32x4 v = __builtin_amdgcn_raw_buffer_load_b128(rpt, 98304 + w * 8192 + 7168 + lane * 16, 0, 16); const float f = fw[1][0];
            a[0] += f * __uint_as_float(v[0] << 16); a[1] += f * __uint_as_float(v[0] & 0xffff0000u); a[2] += f * __uint_as_float(v[1] << 16); a[3] += f * __uint_as_float(v[1] & 0xffff0000u);
            b2[0] += f * __uint_as_float(v[2] << 16); b2[1] += f * __uint_as_float(v[2] & 0xffff0000u); b2[2] += f * __uint_as_float(v[3] << 16); b2[3] += f * __uint_as_float(v[3] & 0xffff0000u); }
          { u32x4 v = __builtin_amdgcn_raw_buffer_load_b128(rpt, 229376 + w * 8192 + 7168 + lane * 16, 0, 16); const float f = fw[1][1];
            a[0] += f * __uint_as_float(v[0] << 16); a[1] += f * __uint_as_float(v[0] & 0xffff0000u); a[2] += f * __uint_as_float(v[1] << 16); a[3] += f * __uint_as_float(v[1] & 0xffff0000u);
            b2[0] += f * __uint_as_float(v[2] << 16); b2[1] += f * __uint_as_float(v[2] & 0xffff0000u); b2[2] += f * __uint_as_float(v[3] << 16); b2[3] += f * __uint_as_float(v[3] & 0xffff0000u); }
          { u32x4 v = __builtin_amdgcn_raw_buffer_load_b128(rpt, 360448 + w * 8192 + 7168 + lane * 16, 0, 16); const float f = fw[1][2];
            a[0] += f * __uint_as_float(v[0] << 16); a[1] += f * __uint_as_float(v[0] & 0xffff0000u); a[2] += f * __uint_as_float(v[1] << 16); a[3] += f * __uint_as_float(v[1] & 0xffff0000u);
            b2[0] += f * __uint_as_float(v[2] << 16); b2[1] += f * __uint_as_float(v[2] & 0xffff0000u); b2[2] += f * __uint_as_float(v[3] << 16); b2[3] += f * __uint_as_float(v[3] & 0xffff0000u); }
          if (R < 224) { u16* dst = o + ((size_t)((kvh * 2 + hl) * 8 + 7) * NROW + orow) * 32 + 16 + 4 * h;
            u32x2 v0 = {cvtpk(a[0], a[1]), cvtpk(a[2], a[3])}; u32x2 v1 = {cvtpk(b2[0], b2[1]), cvtpk(b2[2], b2[3])};
            *(u32x2*)dst = v0; *(u32x2*)(dst + 8) = v1; } }
      }
    } break;
  }
  STAMP(10);
}
__global__ __launch_bounds__(512, 2) void oproj2_kernel(const u16* __restrict__ o, const u16* __restrict__ wo,
                                                        u16* __restrict__ out) {
  extern __shared__ __attribute__((aligned(16))) char lds[];
  const int tid = threadIdx.x, lane = tid & 63;
  const int w = __builtin_amdgcn_readfirstlane(tid >> 6);
  const int r = lane & 31, h = lane >> 5;
  const int xcd = blockIdx.x & 7, idx = blockIdx.x >> 3;
  const int rt = xcd + 8 * (idx / 7), ct = idx % 7;
  if (rt >= 28) return;
  const int col0 = ct * 64;
  char* ring = lds + w * 18432;
  __amdgpu_buffer_rsrc_t rw = __builtin_amdgcn_make_buffer_rsrc((void*)(wo + (size_t)(512 * w) * DM + col0), 0,
                                                                 (512 * DM - col0) * 2, 0x00020000);
  __amdgpu_buffer_rsrc_t ra = __builtin_amdgcn_make_buffer_rsrc((void*)(o + ((size_t)(16 * w) * NROW + rt * 32) * 32), 0,
                                                                 (16 * NROW - rt * 32) * 64, 0x00020000);
  const int drow = lane >> 3;
  const int wvoff = drow * (DM * 2) + (((lane & 7) ^ (((drow >> 1) & 1) << 2)) * 16);
  const int arow = lane >> 2;
  const int avoff = arow * 64 + (((lane & 3) ^ ((arow >> 2) & 3)) * 16);
  auto issue = [&](int u) {
    char* dst = ring + (u % 3) * 6144;
#pragma unroll
    for (int p = 0; p < 4; ++p)
      __builtin_amdgcn_raw_ptr_buffer_load_lds(rw, (lds_vp)(dst + p * 1024), 16, wvoff, (32 * u + 8 * p) * (DM * 2), 0, 0);
#pragma unroll
    for (int p = 0; p < 2; ++p)
      __builtin_amdgcn_raw_ptr_buffer_load_lds(ra, (lds_vp)(dst + 4096 + p * 1024), 16, avoff, 1024 * p + u * (NROW * 64), 0, 0);
  };
  issue(0); issue(1); issue(2);
  const int g = lane >> 4, qq = (lane >> 2) & 3, pp = lane & 3;
  const unsigned rb = (unsigned)(size_t)ring;
  const unsigned trb = rb + (8 * (g >> 1) + qq) * 128 + ((32 * (g & 1) + 8 * pp) ^ ((qq >> 1) << 6));
  const unsigned ab0 = rb + 4096 + r * 64 + (((0 + h) ^ ((r >> 2) & 3)) * 16);
  const unsigned ab1 = rb + 4096 + r * 64 + (((2 + h) ^ ((r >> 2) & 3)) * 16);
  f32x16 acc[2];
#pragma unroll
  for (int b2 = 0; b2 < 2; ++b2)
#pragma unroll
    for (int i = 0; i < 16; ++i) acc[b2][i] = 0.f;
#pragma unroll
  for (int u = 0; u < 16; ++u) {
    if (u <= 13) WAITV(12); else if (u == 14) WAITV(6); else WAITV(0);
    const unsigned so = (u % 3) * 6144;
    s16x4 a[2][4];
    bf16x8 xb[2];
#pragma unroll
    for (int s = 0; s < 2; ++s) {
      a[s][0] = trread(trb + so + s * 2048);
      a[s][1] = trread(trb + so + s * 2048 + 512);
      a[s][2] = trread((trb + so + s * 2048) ^ 64);
      a[s][3] = trread((trb + so + s * 2048 + 512) ^ 64);
    }
    asm volatile("ds_read_b128 %0, %1" : "=v"(xb[0]) : "v"(ab0 + so) : "memory");
    asm volatile("ds_read_b128 %0, %1" : "=v"(xb[1]) : "v"(ab1 + so) : "memory");
    asm volatile("s_waitcnt lgkmcnt(0)"
                 : "+v"(a[0][0]), "+v"(a[0][1]), "+v"(a[0][2]), "+v"(a[0][3]), "+v"(a[1][0]), "+v"(a[1][1]),
                   "+v"(a[1][2]), "+v"(a[1][3]), "+v"(xb[0]), "+v"(xb[1])::"memory");
    if (u + 3 < 16) issue(u + 3);
#pragma unroll
    for (int s = 0; s < 2; ++s) {
      acc[0] = __builtin_amdgcn_mfma_f32_32x32x16_bf16(cat8(a[s][0], a[s][1]), xb[s], acc[0], 0, 0, 0);
      acc[1] = __builtin_amdgcn_mfma_f32_32x32x16_bf16(cat8(a[s][2], a[s][3]), xb[s], acc[1], 0, 0, 0);
    }
  }
  __syncthreads();
  float* red = (float*)lds;
#pragma unroll
  for (int cb = 0; cb < 2; ++cb)
#pragma unroll
    for (int gg = 0; gg < 4; ++gg) {
      f32x4 v = {acc[cb][4 * gg], acc[cb][4 * gg + 1], acc[cb][4 * gg + 2], acc[cb][4 * gg + 3]};
      *(f32x4*)(red + ((w * 8 + cb * 4 + gg) * 64 + lane) * 4) = v;
    }
  __syncthreads();
  {
    const int cg = w, cb = cg >> 2, gg = cg & 3;
    f32x4 s = {0.f, 0.f, 0.f, 0.f};
#pragma unroll
    for (int ww = 0; ww < 8; ++ww) s += *(const f32x4*)(red + ((ww * 8 + cg) * 64 + lane) * 4);
    u32x2 v = {cvtpk(s[0], s[1]), cvtpk(s[2], s[3])};
    *(u32x2*)(out + (size_t)(rt * 32 + r) * DM + col0 + 32 * cb + 8 * gg + 4 * h) = v;
  }
}
__global__ __launch_bounds__(512, 2) void oproj3_kernel(const u16* __restrict__ o, const u16* __restrict__ wo,
                                                        u16* __restrict__ out, float* __restrict__ slab,
                                                        unsigned* __restrict__ cnt DBGARG) {
  extern __shared__ __attribute__((aligned(16))) char lds[];
  STAMP(0);
  const int tid = threadIdx.x, lane = tid & 63;
  const int w = __builtin_amdgcn_readfirstlane(tid >> 6);
  const int r = lane & 31, h = lane >> 5;
  const int xcd = blockIdx.x & 7, idx = blockIdx.x >> 3;
  const int tile = (idx >> 1) * 8 + xcd, kh = idx & 1;
  if (tile >= 98) return;
  const int rt = tile / 7, ct = tile % 7;
  const int col0 = ct * 64, k0 = kh * 2048 + 256 * w;
  char* ring = lds + w * 16384;
  __amdgpu_buffer_rsrc_t rw = __builtin_amdgcn_make_buffer_rsrc((void*)(wo + (size_t)k0 * DM + col0), 0,
                                                                 (256 * DM - col0) * 2, 0x00020000);
  __amdgpu_buffer_rsrc_t ra = __builtin_amdgcn_make_buffer_rsrc((void*)(o + (size_t)(rt * 64) * NQ + k0), 0,
                                                                 (64 * NQ - k0) * 2, 0x00020000);
  const int drow = lane >> 3;
  const int wvoff = drow * (DM * 2) + (((lane & 7) ^ (((drow >> 1) & 1) << 2)) * 16);
  const int arow = lane >> 2;
  const int avoff = arow * (NQ * 2) + (((lane & 3) ^ ((arow >> 2) & 3)) * 16);
  auto issue = [&](int u) {
    char* dst = ring + (u & 1) * 8192;
#pragma unroll
    for (int p = 0; p < 4; ++p)
      __builtin_amdgcn_raw_ptr_buffer_load_lds(rw, (lds_vp)(dst + p * 1024), 16, wvoff, (32 * u + 8 * p) * (DM * 2), 0, 0);
#pragma unroll
    for (int p = 0; p < 4; ++p)
      __builtin_amdgcn_raw_ptr_buffer_load_lds(ra, (lds_vp)(dst + 4096 + p * 1024), 16, avoff, 16 * p * (NQ * 2) + 64 * u, 0, 0);
  };
  issue(0); issue(1);
  const int g = lane >> 4, qq = (lane >> 2) & 3, pp = lane & 3;
  const unsigned rb = (unsigned)(size_t)ring;
  const unsigned trb = rb + (8 * (g >> 1) + qq) * 128 + ((32 * (g & 1) + 8 * pp) ^ ((qq >> 1) << 6));
  const unsigned ab0 = rb + 4096 + r * 64 + (((0 + h) ^ ((r >> 2) & 3)) * 16);
  const unsigned ab1 = rb + 4096 + r * 64 + (((2 + h) ^ ((r >> 2) & 3)) * 16);
  f32x16 acc[2][2];
#pragma unroll
  for (int a2 = 0; a2 < 2; ++a2)
#pragma unroll
    for (int b2 = 0; b2 < 2; ++b2)
#pragma unroll
      for (int i = 0; i < 16; ++i) acc[a2][b2][i] = 0.f;
#pragma unroll
  for (int u = 0; u < 8; ++u) {
    if (u <= 6) WAITV(8); else WAITV(0);
    const unsigned so = (u & 1) * 8192;
    s16x4 a[2][4];
    bf16x8 xb[2][2];
#pragma unroll
    for (int s = 0; s < 2; ++s) {
      a[s][0] = trread(trb + so + s * 2048);
      a[s][1] = trread(trb + so + s * 2048 + 512);
      a[s][2] = trread((trb + so + s * 2048) ^ 64);
      a[s][3] = trread((trb + so + s * 2048 + 512) ^ 64);
    }
    asm volatile("ds_read_b128 %0, %1" : "=v"(xb[0][0]) : "v"(ab0 + so) : "memory");
    asm volatile("ds_read_b128 %0, %1" : "=v"(xb[1][0]) : "v"(ab1 + so) : "memory");
    asm volatile("ds_read_b128 %0, %1 offset:2048" : "=v"(xb[0][1]) : "v"(ab0 + so) : "memory");
    asm volatile("ds_read_b128 %0, %1 offset:2048" : "=v"(xb[1][1]) : "v"(ab1 + so) : "memory");
    asm volatile("s_waitcnt lgkmcnt(0)"
                 : "+v"(a[0][0]), "+v"(a[0][1]), "+v"(a[0][2]), "+v"(a[0][3]), "+v"(a[1][0]), "+v"(a[1][1]),
                   "+v"(a[1][2]), "+v"(a[1][3]), "+v"(xb[0][0]), "+v"(xb[1][0]), "+v"(xb[0][1]), "+v"(xb[1][1])::"memory");
    if (u + 2 < 8) issue(u + 2);
#pragma unroll
    for (int s = 0; s < 2; ++s)
#pragma unroll
      for (int rb2 = 0; rb2 < 2; ++rb2) {
        acc[rb2][0] = __builtin_amdgcn_mfma_f32_32x32x16_bf16(cat8(a[s][0], a[s][1]), xb[s][rb2], acc[rb2][0], 0, 0, 0);
        acc[rb2][1] = __builtin_amdgcn_mfma_f32_32x32x16_bf16(cat8(a[s][2], a[s][3]), xb[s][rb2], acc[rb2][1], 0, 0, 0);
      }
  }
  STAMP(1);
  __syncthreads();
  float* red = (float*)lds;
#pragma unroll
  for (int rb2 = 0; rb2 < 2; ++rb2)
#pragma unroll
    for (int cb = 0; cb < 2; ++cb)
#pragma unroll
      for (int gg = 0; gg < 4; ++gg) {
        f32x4 v = {acc[rb2][cb][4 * gg], acc[rb2][cb][4 * gg + 1], acc[rb2][cb][4 * gg + 2], acc[rb2][cb][4 * gg + 3]};
        *(f32x4*)(red + ((w * 16 + rb2 * 8 + cb * 4 + gg) * 64 + lane) * 4) = v;
      }
  __syncthreads();
  f32x4 sum[2];
#pragma unroll
  for (int jj = 0; jj < 2; ++jj) {
    const int grp = 2 * w + jj;
    f32x4 s = {0.f, 0.f, 0.f, 0.f};
#pragma unroll
    for (int ww = 0; ww < 8; ++ww) s += *(const f32x4*)(red + ((ww * 16 + grp) * 64 + lane) * 4);
    sum[jj] = s;
  }
  STAMP(2);
  __amdgpu_buffer_rsrc_t rs = __builtin_amdgcn_make_buffer_rsrc((void*)(slab + (size_t)tile * 2 * 4096), 0, 2 * 4096 * 4, 0x00020000);
#pragma unroll
  for (int jj = 0; jj < 2; ++jj)
    __builtin_amdgcn_raw_buffer_store_b128(__builtin_bit_cast(u32x4, sum[jj]), rs, (kh * 16 + 2 * w + jj) * 1024 + lane * 16, 0, 16);
  asm volatile("s_waitcnt vmcnt(0)" ::: "memory");
  __syncthreads();
  unsigned* flag = (unsigned*)(lds + 131072);
  if (tid == 0) *flag = __hip_atomic_fetch_add(cnt + tile, 1u, __ATOMIC_RELAXED, __HIP_MEMORY_SCOPE_AGENT);
  __syncthreads();
  STAMP(3);
  if (*flag == 1u) {
#pragma unroll
    for (int jj = 0; jj < 2; ++jj) {
      u32x4 pv = __builtin_amdgcn_raw_buffer_load_b128(rs, ((kh ^ 1) * 16 + 2 * w + jj) * 1024 + lane * 16, 0, 16);
      f32x4 t = sum[jj] + __builtin_bit_cast(f32x4, pv);
      const int grp = 2 * w + jj, rb2 = grp >> 3, cb = (grp >> 2) & 1, gg = grp & 3;
      u32x2 v = {cvtpk(t[0], t[1]), cvtpk(t[2], t[3])};
      *(u32x2*)(out + (size_t)(rt * 64 + 32 * rb2 + r) * DM + col0 + 32 * cb + 8 * gg + 4 * h) = v;
    }
  }
  STAMP(4);
}
extern "C" void kernel_launch(void* const* d_in, const int* in_sizes, int n_in, void* d_out, int out_size,
                              void* d_ws, size_t ws_size, hipStream_t stream) {
  const u16* x = (const u16*)d_in[0];
  const u16* kc = (const u16*)d_in[1];
  const u16* vc = (const u16*)d_in[2];
  const u16* wq = (const u16*)d_in[3];
  const u16* wo = (const u16*)d_in[4];
  u16* out = (u16*)d_out;
  u16* qws = (u16*)d_ws;
  u16* ows = (u16*)((char*)d_ws + (8  __H0__ ));
  float* slab = (float*)((char*)d_ws + (16 << 20));
  unsigned* cnt = (unsigned*)((char*)d_ws + (24 << 20));
  u16* part = (u16*)((char*)d_ws + (32 << 20));
  float* mlg = (float*)((char*)d_ws + (25 << 20));
  (void)hipFuncSetAttribute((const void*)qproj5_kernel, hipFuncAttributeMaxDynamicSharedMemorySize, 163840);
  (void)hipFuncSetAttribute((const void*)attn5_kernel, hipFuncAttributeMaxDynamicSharedMemorySize, 131072);
  hipLaunchKernelGGL(qproj5_kernel, dim3(256), dim3(256), 163840, stream, x, wq, qws, cnt);
  hipLaunchKernelGGL(attn5_kernel, dim3(256), dim3(256), 131072, stream, (const u16*)qws, kc, vc, wo, ows, part, mlg, cnt
  );
  (void)hipFuncSetAttribute((const void*)oproj2_kernel, hipFuncAttributeMaxDynamicSharedMemorySize, 147456);
  hipLaunchKernelGGL(oproj2_kernel, dim3(8 * 28), dim3(512), 147456, stream, (const u16*)ows, wo, out);
  return;
  (void)hipFuncSetAttribute((const void*)oproj3_kernel, hipFuncAttributeMaxDynamicSharedMemorySize, 131328);
  hipLaunchKernelGGL(oproj3_kernel, dim3(8 * 26), dim3(512), 131328, stream, (const u16*)ows, wo, out, slab, cnt
  );
}
```

```cpp
#include <hip/hip_runtime.h>
#include <hip/hip_bf16.h>
#include <utility>

typedef unsigned short u16;
typedef __attribute__((ext_vector_type(8))) short bf16x8;
typedef __attribute__((ext_vector_type(4))) short s16x4;
typedef __attribute__((ext_vector_type(16))) float f32x16;
typedef __attribute__((ext_vector_type(4))) float f32x4;
typedef __attribute__((ext_vector_type(2))) float f32x2;
typedef __attribute__((ext_vector_type(2))) __bf16 bf16x2v;
typedef __attribute__((ext_vector_type(2))) unsigned u32x2;
typedef __attribute__((ext_vector_type(4))) unsigned u32x4;
#define DI __device__ __forceinline__
#define LDSP(p) ((__attribute__((address_space(3))) unsigned*)(p))

constexpr int NB = 8, SQ = 112, DM = 448, HQ = 16, HKV = 8, SK = 1024, DH = 256;
constexpr int NROW = NB * SQ;
constexpr int NQ = HQ * DH;

DI unsigned cvtpk(float lo, float hi) {
  f32x2 v = {lo, hi};
  bf16x2v b = __builtin_convertvector(v, bf16x2v);
  return __builtin_bit_cast(unsigned, b);
}
DI void glds16(const void* g, void* l) {
  __builtin_amdgcn_global_load_lds((const unsigned*)g, LDSP(l), 16, 0, 0);
}
#define WAITV(n) asm volatile("s_waitcnt vmcnt(" #n ")" ::: "memory")
#define WAITL0() asm volatile("s_waitcnt lgkmcnt(0)" ::: "memory")

DI s16x4 trread(unsigned addr) {
  s16x4 r;
  asm volatile("ds_read_b64_tr_b16 %0, %1" : "=v"(r) : "v"(addr) : "memory");
  return r;
}
template <int OFF> DI s16x4 trread_o(unsigned addr) {
  s16x4 r;
  asm volatile("ds_read_b64_tr_b16 %0, %1 offset:%2" : "=v"(r) : "v"(addr), "i"(OFF) : "memory");
  return r;
}
DI bf16x8 cat8(s16x4 a, s16x4 b) { return __builtin_shufflevector(a, b, 0, 1, 2, 3, 4, 5, 6, 7); }

template <int RB, int NCH>
DI void wave_gemm(const u16* __restrict__ act, int lda, const int (&arow)[RB], const u16* __restrict__ W, int ldw,
                  char* wl, f32x16 (&acc)[RB][2], int lane) {
  const int r = lane & 31, h = lane >> 5;
  const int g = lane >> 4, q = (lane >> 2) & 3, pp = lane & 3;
  const unsigned ldsb = (unsigned)(size_t)wl;
  const unsigned trb = ldsb + (8 * (g >> 1) + q) * 128 + ((32 * (g & 1) + 8 * pp) ^ ((q >> 1) << 6));
  const int drow = lane >> 3;
  const int dchk = (lane & 7) ^ (((drow >> 1) & 1) << 2);
  const u16* wsrc = W + (size_t)drow * ldw + dchk * 8;
  auto issue = [&](int c, int buf) {
#pragma unroll
    for (int p = 0; p < 8; ++p) glds16(wsrc + (size_t)(64 * c + 8 * p) * ldw, wl + buf * 8192 + p * 1024);
  };
  issue(0, 0);
#pragma unroll
  for (int c = 0; c < NCH; ++c) {
    const int buf = c & 1;
    bf16x8 bfr[4][RB];
#pragma unroll
    for (int s = 0; s < 4; ++s)
#pragma unroll
      for (int rb = 0; rb < RB; ++rb)
        bfr[s][rb] = *(const bf16x8*)(act + (size_t)arow[rb] * lda + 64 * c + 16 * s + 8 * h);
    WAITV(0);
    if (c + 1 < NCH) issue(c + 1, buf ^ 1);
#pragma unroll
    for (int s = 0; s < 4; ++s) {
      s16x4 a00 = trread(trb + buf * 8192 + s * 2048 + 0);
      s16x4 a01 = trread(trb + buf * 8192 + s * 2048 + 512);
      s16x4 a10 = trread((trb + buf * 8192 + s * 2048 + 0) ^ 64);
      s16x4 a11 = trread((trb + buf * 8192 + s * 2048 + 512) ^ 64);
      asm volatile("s_waitcnt lgkmcnt(0)" : "+v"(a00), "+v"(a01), "+v"(a10), "+v"(a11)::"memory");
      bf16x8 A0 = cat8(a00, a01), A1 = cat8(a10, a11);
#pragma unroll
      for (int rb = 0; rb < RB; ++rb) {
        acc[rb][0] = __builtin_amdgcn_mfma_f32_32x32x16_bf16(A0, bfr[s][rb], acc[rb][0], 0, 0, 0);
        acc[rb][1] = __builtin_amdgcn_mfma_f32_32x32x16_bf16(A1, bfr[s][rb], acc[rb][1], 0, 0, 0);
      }
    }
  }
  (void)r;
}

__global__ __launch_bounds__(256) void qproj_kernel(const u16* __restrict__ x, const u16* __restrict__ wq,
                                                    u16* __restrict__ qout) {
  extern __shared__ __attribute__((aligned(16))) char lds[];
  const int tid = threadIdx.x, lane = tid & 63;
  const int w = __builtin_amdgcn_readfirstlane(tid >> 6);
  const int r = lane & 31, h = lane >> 5;
  const int rt = blockIdx.x % 14, head = blockIdx.x / 14;
  const int col0 = head * 256 + 64 * w;
  f32x16 acc[2][2];
#pragma unroll
  for (int a = 0; a < 2; ++a)
#pragma unroll
    for (int b = 0; b < 2; ++b)
#pragma unroll
      for (int i = 0; i < 16; ++i) acc[a][b][i] = 0.f;
  const int arow[2] = {rt * 64 + r, rt * 64 + 32 + r};
  wave_gemm<2, 7>(x, DM, arow, wq + col0, NQ, lds + w * 16384, acc, lane);
#pragma unroll
  for (int rb = 0; rb < 2; ++rb)
#pragma unroll
    for (int cb = 0; cb < 2; ++cb)
#pragma unroll
      for (int g = 0; g < 4; ++g) {
        u32x2 v = {cvtpk(acc[rb][cb][4 * g + 0], acc[rb][cb][4 * g + 1]),
                   cvtpk(acc[rb][cb][4 * g + 2], acc[rb][cb][4 * g + 3])};
        *(u32x2*)(qout + (size_t)arow[rb] * NQ + col0 + 32 * cb + 8 * g + 4 * h) = v;
      }
}

__global__ __launch_bounds__(256) void oproj_kernel(const u16* __restrict__ o, const u16* __restrict__ wo,
                                                    u16* __restrict__ out) {
  extern __shared__ __attribute__((aligned(16))) char lds[];
  const int tid = threadIdx.x, lane = tid & 63;
  const int w = __builtin_amdgcn_readfirstlane(tid >> 6);
  const int r = lane & 31, h = lane >> 5;
  const int rt = blockIdx.x % 28, ct = blockIdx.x / 28;
  const int col0 = ct * 64;
  f32x16 acc[1][2];
#pragma unroll
  for (int b = 0; b < 2; ++b)
#pragma unroll
    for (int i = 0; i < 16; ++i) acc[0][b][i] = 0.f;
  const int arow[1] = {rt * 32 + r};
  wave_gemm<1, 16>(o + 1024 * w, NQ, arow, wo + (size_t)(1024 * w) * DM + col0, DM, lds + w * 16384, acc, lane);
  __syncthreads();
  float* red = (float*)lds;
#pragma unroll
  for (int cb = 0; cb < 2; ++cb)
#pragma unroll
    for (int g = 0; g < 4; ++g) {
      f32x4 v = {acc[0][cb][4 * g], acc[0][cb][4 * g + 1], acc[0][cb][4 * g + 2], acc[0][cb][4 * g + 3]};
      *(f32x4*)(red + ((w * 8 + cb * 4 + g) * 64 + lane) * 4) = v;
    }
  __syncthreads();
#pragma unroll
  for (int j = 0; j < 2; ++j) {
    const int cg = 2 * w + j, cb = cg >> 2, g = cg & 3;
    f32x4 s = {0.f, 0.f, 0.f, 0.f};
#pragma unroll
    for (int ww = 0; ww < 4; ++ww) {
      f32x4 v = *(const f32x4*)(red + ((ww * 8 + cg) * 64 + lane) * 4);
      s += v;
    }
    u32x2 v = {cvtpk(s[0], s[1]), cvtpk(s[2], s[3])};
    *(u32x2*)(out + (size_t)arow[0] * DM + col0 + 32 * cb + 8 * g + 4 * h) = v;
  }
}

constexpr float SCL = 0.0625f * 1.4426950408889634f;
constexpr float THR = 8.0f;

typedef __attribute__((address_space(3))) void* lds_vp;
struct DmaCtx {
  __amdgpu_buffer_rsrc_t rk, rv;
  int koff[4], voff[4];
  char* dst;
};
template <int U>
DI void issue_unit(const DmaCtx& c) {
  constexpr int t = U >> 2, kind = U & 3, slot = U % 5;
  if constexpr (kind < 2) {
#pragma unroll
    for (int pi = 0; pi < 4; ++pi)
      __builtin_amdgcn_raw_ptr_buffer_load_lds(c.rk, (lds_vp)(c.dst + slot * 8192 + pi * 1024), 16, c.koff[pi],
                                               32 * t * 512 + kind * 256, 0, 0);
  } else {
    constexpr int ks = kind - 2;
#pragma unroll
    for (int pi = 0; pi < 4; ++pi)
      __builtin_amdgcn_raw_ptr_buffer_load_lds(c.rv, (lds_vp)(c.dst + slot * 8192 + pi * 1024), 16, c.voff[pi],
                                               (32 * t + 16 * ks) * 512, 0, 0);
  }
}
template <int U> DI void wait_unit_bar() {
  constexpr int after = (U + 3 > 31 ? 31 : U + 3) - U;
  if constexpr (after == 3) WAITV(12);
  else if constexpr (after == 2) WAITV(8);
  else if constexpr (after == 1) WAITV(4);
  else WAITV(0);
  WAITL0();
  __builtin_amdgcn_s_barrier();
}

struct AttnState {
  bf16x8 qf[16];
  f32x16 oacc[8];
  float m, l;
};

template <int T, int DHF>
DI void qk_half(AttnState& st, f32x16& s, const DmaCtx& c, char* ring, unsigned kaddr) {
  constexpr int U = 4 * T + DHF, slot = U % 5;
  wait_unit_bar<U>();
  if constexpr (U + 4 < 32) issue_unit<U + 4>(c);
#pragma unroll
  for (int ss = 0; ss < 8; ++ss) {
    bf16x8 kf = *(const bf16x8*)(ring + slot * 8192 + (kaddr ^ (ss * 32)));
    s = __builtin_amdgcn_mfma_f32_32x32x16_bf16(kf, st.qf[DHF * 8 + ss], s, 0, 0, 0);
  }
}
template <int T, int KS, int HF>
DI void pv_quarter(AttnState& st, const bf16x8& pf, const unsigned (&vaddr)[4]) {
  constexpr int U = 4 * T + 2 + KS, slot = U % 5;
  constexpr int OFF = slot * 8192 + HF * 256;
  s16x4 v0[4], v1[4];
  v0[0] = trread_o<OFF>(vaddr[0]); v1[0] = trread_o<OFF + 4096>(vaddr[0]);
  v0[1] = trread_o<OFF>(vaddr[1]); v1[1] = trread_o<OFF + 4096>(vaddr[1]);
  v0[2] = trread_o<OFF>(vaddr[2]); v1[2] = trread_o<OFF + 4096>(vaddr[2]);
  v0[3] = trread_o<OFF>(vaddr[3]); v1[3] = trread_o<OFF + 4096>(vaddr[3]);
  asm volatile("s_waitcnt lgkmcnt(0)"
               : "+v"(v0[0]), "+v"(v0[1]), "+v"(v0[2]), "+v"(v0[3]), "+v"(v1[0]), "+v"(v1[1]), "+v"(v1[2]),
                 "+v"(v1[3])::"memory");
#pragma unroll
  for (int d = 0; d < 4; ++d) {
    bf16x8 vf = cat8(v0[d], v1[d]);
    st.oacc[HF * 4 + d] = __builtin_amdgcn_mfma_f32_32x32x16_bf16(vf, pf, st.oacc[HF * 4 + d], 0, 0, 0);
  }
}
template <int T, int KS>
DI void pv_half(AttnState& st, const bf16x8& pf, const DmaCtx& c, const unsigned (&vaddr)[4]) {
  constexpr int U = 4 * T + 2 + KS;
  wait_unit_bar<U>();
  if constexpr (U + 4 < 32) issue_unit<U + 4>(c);
  pv_quarter<T, KS, 0>(st, pf, vaddr);
  pv_quarter<T, KS, 1>(st, pf, vaddr);
}

template <int T>
DI void attn_tile(AttnState& st, const DmaCtx& c, char* ring, unsigned kaddr, const unsigned (&vaddr)[4]) {
  f32x16 s;
#pragma unroll
  for (int i = 0; i < 16; ++i) s[i] = 0.f;
  qk_half<T, 0>(st, s, c, ring, kaddr);
  qk_half<T, 1>(st, s, c, ring, kaddr);
  bf16x8 pf[2];
  {
    float a = s[0];
#pragma unroll
    for (int i = 1; i < 16; ++i) a = fmaxf(a, s[i]);
    auto rr = __builtin_amdgcn_permlane32_swap(__float_as_uint(a), __float_as_uint(a), false, false);
    const float mx = fmaxf(__uint_as_float(rr[0]), __uint_as_float(rr[1])) * SCL;
    const bool grew = (mx - st.m > THR);
    if (T == 0 || __any((int)grew)) {
      const float mn = fmaxf(st.m, mx);
      const float alpha = __builtin_amdgcn_exp2f(st.m - mn);
      st.m = mn;
      st.l *= alpha;
      if (T != 0) {
#pragma unroll
        for (int db = 0; db < 8; ++db)
#pragma unroll
          for (int i = 0; i < 16; ++i) st.oacc[db][i] *= alpha;
      }
    }
    const float nm = -st.m;
    float ps = 0.f;
#pragma unroll
    for (int i = 0; i < 16; ++i) {
      s[i] = __builtin_amdgcn_exp2f(fmaf(s[i], SCL, nm));
      ps += s[i];
    }
    st.l += ps;
#pragma unroll
    for (int ks = 0; ks < 2; ++ks) {
      u32x4 wv = {cvtpk(s[8 * ks + 0], s[8 * ks + 1]), cvtpk(s[8 * ks + 2], s[8 * ks + 3]),
                  cvtpk(s[8 * ks + 4], s[8 * ks + 5]), cvtpk(s[8 * ks + 6], s[8 * ks + 7])};
      pf[ks] = __builtin_bit_cast(bf16x8, wv);
    }
  }
  pv_half<T, 0>(st, pf[0], c, vaddr);
  pv_half<T, 1>(st, pf[1], c, vaddr);
}

#define STAMP(i)
#define DBGARG
__global__ __launch_bounds__(512, 2) void attn_kernel(const u16* __restrict__ q, const u16* __restrict__ kc,
                                                      const u16* __restrict__ vc, u16* __restrict__ o DBGARG) {
  extern __shared__ __attribute__((aligned(16))) char lds[];
  STAMP(0);
  const int tid = threadIdx.x, lane = tid & 63;
  const int w = __builtin_amdgcn_readfirstlane(tid >> 6);
  const int rb = w >> 2, kq = w & 3;
  const int r = lane & 31, h = lane >> 5;
  const int bid = blockIdx.x;
  const int xcd = bid & 7, j = bid >> 3;
  const int grp = (j >> 2) * 8 + xcd, sub = j & 3;
  const int b = grp >> 3, kvh = grp & 7;
  const int head = kvh * 2 + (sub >> 1), rhalf = sub & 1;
  const int row0 = b * SQ + rhalf * 56;

  const u16* Kw = kc + ((size_t)(b * HKV + kvh) * SK + 256 * kq) * DH;
  const u16* Vw = vc + ((size_t)(b * HKV + kvh) * SK + 256 * kq) * DH;
  char* ring = lds + kq * 40960;

  AttnState st;
  {
    int rr = 32 * rb + r;
    rr = rr > 55 ? 55 : rr;
    const u16* qrow = q + (size_t)(row0 + rr) * NQ + head * DH + 8 * h;
#pragma unroll
    for (int s = 0; s < 16; ++s) st.qf[s] = *(const bf16x8*)(qrow + 16 * s);
    st.m = -__builtin_inff();
    st.l = 0.f;
#pragma unroll
    for (int db = 0; db < 8; ++db)
#pragma unroll
      for (int i = 0; i < 16; ++i) st.oacc[db][i] = 0.f;
  }
#pragma unroll
  for (int s = 0; s < 16; ++s) asm volatile("" : "+v"(st.qf[s]));
  WAITV(0);

  STAMP(1);
  DmaCtx c;
  c.rk = __builtin_amdgcn_make_buffer_rsrc((void*)Kw, 0, 256 * DH * 2, 0x00020000);
  c.rv = __builtin_amdgcn_make_buffer_rsrc((void*)Vw, 0, 256 * DH * 2, 0x00020000);
  c.dst = ring + rb * 4096;
#pragma unroll
  for (int pi = 0; pi < 4; ++pi) {
    const int krow = 16 * rb + 4 * pi + (lane >> 4);
    c.koff[pi] = krow * 512 + (((lane & 15) ^ (krow & 15)) * 16);
    const int vrow = 8 * rb + 2 * pi + (lane >> 5);
    c.voff[pi] = vrow * 512 + (((lane & 31) ^ ((vrow & 3) << 2)) * 16);
  }
  issue_unit<0>(c);
  issue_unit<1>(c);
  issue_unit<2>(c);
  issue_unit<3>(c);

  const unsigned kaddr = r * 256 + ((h ^ (r & 15)) * 16);
  const int g = lane >> 4, qq = (lane >> 2) & 3, pp = lane & 3;
  unsigned vaddr[4];
#pragma unroll
  for (int d = 0; d < 4; ++d)
    vaddr[d] = (unsigned)(size_t)ring + (4 * (g >> 1) + qq) * 512 + ((64 * d + 32 * (g & 1) + 8 * pp) ^ (qq << 6));

  attn_tile<0>(st, c, ring, kaddr, vaddr);
  STAMP(2);
  attn_tile<1>(st, c, ring, kaddr, vaddr);
  STAMP(3);
  attn_tile<2>(st, c, ring, kaddr, vaddr);
  STAMP(4);
  attn_tile<3>(st, c, ring, kaddr, vaddr);
  STAMP(5);
  attn_tile<4>(st, c, ring, kaddr, vaddr);
  STAMP(6);
  attn_tile<5>(st, c, ring, kaddr, vaddr);
  STAMP(7);
  attn_tile<6>(st, c, ring, kaddr, vaddr);
  STAMP(8);
  attn_tile<7>(st, c, ring, kaddr, vaddr);
  STAMP(9);

  __syncthreads();
  float* ml = (float*)lds;
  {
    auto rr = __builtin_amdgcn_permlane32_swap(__float_as_uint(st.l), __float_as_uint(st.l), false, false);
    const float lt = __uint_as_float(rr[0]) + __uint_as_float(rr[1]);
    if (h == 0) {
      ml[(w * 32 + r) * 2 + 0] = st.m;
      ml[(w * 32 + r) * 2 + 1] = lt;
    }
  }
  __syncthreads();
  float f;
  {
    float mm[4], ll[4];
#pragma unroll
    for (int k = 0; k < 4; ++k) {
      mm[k] = ml[((rb * 4 + k) * 32 + r) * 2 + 0];
      ll[k] = ml[((rb * 4 + k) * 32 + r) * 2 + 1];
    }
    const float M = fmaxf(fmaxf(mm[0], mm[1]), fmaxf(mm[2], mm[3]));
    float L = 0.f;
#pragma unroll
    for (int k = 0; k < 4; ++k) L += __builtin_amdgcn_exp2f(mm[k] - M) * ll[k];
    f = __builtin_amdgcn_exp2f(st.m - M) / L;
  }
  float* xch = (float*)(lds + 4096);
#pragma unroll
  for (int rnd = 0; rnd < 2; ++rnd) {
#pragma unroll
    for (int db = 0; db < 8; ++db) {
      if ((db & 1) != rnd) continue;
      const int owner = db >> 1;
      if (owner == kq) continue;
      const int dslot = owner < kq ? owner : owner - 1;
#pragma unroll
      for (int gg = 0; gg < 4; ++gg) {
        f32x4 v = {st.oacc[db][4 * gg] * f, st.oacc[db][4 * gg + 1] * f, st.oacc[db][4 * gg + 2] * f,
                   st.oacc[db][4 * gg + 3] * f};
        *(f32x4*)(xch + (((w * 3 + dslot) * 4 + gg) * 64 + lane) * 4) = v;
      }
    }
    __syncthreads();
#pragma unroll
    for (int db = 0; db < 8; ++db) {
      if ((db & 1) != rnd) continue;
      if ((db >> 1) != kq) continue;
#pragma unroll
      for (int gg = 0; gg < 4; ++gg) {
        f32x4 sum = {st.oacc[db][4 * gg] * f, st.oacc[db][4 * gg + 1] * f, st.oacc[db][4 * gg + 2] * f,
                     st.oacc[db][4 * gg + 3] * f};
#pragma unroll
        for (int sk = 0; sk < 4; ++sk) {
          if (sk == kq) continue;
          const int dslot = kq < sk ? kq : kq - 1;
          f32x4 v = *(const f32x4*)(xch + ((((rb * 4 + sk) * 3 + dslot) * 4 + gg) * 64 + lane) * 4);
          sum += v;
        }
        const int rr = 32 * rb + r;
        if (rr < 56) {
          u32x2 v = {cvtpk(sum[0], sum[1]), cvtpk(sum[2], sum[3])};
          *(u32x2*)(o + (size_t)(row0 + rr) * NQ + head * DH + 32 * db + 8 * gg + 4 * h) = v;
        }
      }
    }
    __syncthreads();
  }
  STAMP(10);
}

#define GAP() __builtin_amdgcn_sched_barrier(0)
#define ACLOB "a0","a1","a2","a3","a4","a5","a6","a7","a8","a9","a10","a11","a12","a13","a14","a15","a16","a17","a18","a19","a20","a21","a22","a23","a24","a25","a26","a27","a28","a29","a30","a31","a32","a33","a34","a35","a36","a37","a38","a39","a40","a41","a42","a43","a44","a45","a46","a47","a48","a49","a50","a51","a52","a53","a54","a55","a56","a57","a58","a59","a60","a61","a62","a63","a64","a65","a66","a67","a68","a69","a70","a71","a72","a73","a74","a75","a76","a77","a78","a79","a80","a81","a82","a83","a84","a85","a86","a87","a88","a89","a90","a91","a92","a93","a94","a95","a96","a97","a98","a99","a100","a101","a102","a103","a104","a105","a106","a107","a108","a109","a110","a111","a112","a113","a114","a115","a116","a117","a118","a119","a120","a121","a122","a123","a124","a125","a126","a127","a128","a129","a130","a131","a132","a133","a134","a135","a136","a137","a138","a139","a140","a141","a142","a143","a144","a145","a146","a147","a148","a149","a150","a151","a152","a153","a154","a155","a156","a157","a158","a159","a160","a161","a162","a163","a164","a165","a166","a167","a168","a169","a170","a171","a172","a173","a174","a175","a176","a177","a178","a179","a180","a181","a182","a183","a184","a185","a186","a187","a188","a189","a190","a191","a192","a193","a194","a195","a196","a197","a198","a199","a200","a201","a202","a203","a204","a205","a206","a207","a208","a209","a210","a211","a212","a213","a214","a215","a216","a217","a218","a219","a220","a221","a222","a223","a224","a225","a226","a227","a228","a229","a230","a231","a232","a233","a234","a235","a236","a237","a238","a239","a240","a241","a242","a243","a244","a245","a246","a247","a248","a249","a250","a251","a252","a253","a254","a255"

template <int AO, bool ZERO> DI void pv_mfma(const bf16x8& vf, const bf16x8& pf) {
  if constexpr (ZERO)
    asm volatile("v_mfma_f32_32x32x16_bf16 a[%c2:%c3], %0, %1, 0" ::"v"(vf), "v"(pf), "i"(AO), "i"(AO + 15) : ACLOB);
  else
    asm volatile("v_mfma_f32_32x32x16_bf16 a[%c2:%c3], %0, %1, a[%c2:%c3]" ::"v"(vf), "v"(pf), "i"(AO), "i"(AO + 15)
                 : ACLOB);
}
template <bool FIRST> DI void qk_mfma(f32x16& s, const bf16x8& kf, const bf16x8& qf) {
  if constexpr (FIRST)
    asm volatile("v_mfma_f32_32x32x16_bf16 %0, %1, %2, 0" : "=&v"(s) : "v"(kf), "v"(qf) : ACLOB);
  else
    asm volatile("v_mfma_f32_32x32x16_bf16 %0, %1, %2, %0" : "+v"(s) : "v"(kf), "v"(qf) : ACLOB);
}
DI float max3f(float a, float b, float c) { float r; asm("v_max3_f32 %0, %1, %2, %3" : "=v"(r) : "v"(a), "v"(b), "v"(c)); return r; }
DI float max2f(float a, float b) { float r; asm("v_max_f32_e32 %0, %1, %2" : "=v"(r) : "v"(a), "v"(b)); return r; }
DI float fadd_s(float a, float b) { float r; asm("v_add_f32_e32 %0, %1, %2" : "=v"(r) : "v"(a), "v"(b)); return r; }
DI float ffma_s(float a, float b, float c) { float r; asm("v_fma_f32 %0, %1, %2, %3" : "=v"(r) : "v"(a), "v"(b), "v"(c)); return r; }

constexpr int unit_is_v(int n) { return n < 2 ? 0 : (n >= 30 ? 1 : (((n - 2) & 3) >= 2)); }
constexpr int unit_tile(int n) { return n < 2 ? 0 : (n >= 30 ? 7 : (((n - 2) & 3) >= 2 ? (n - 2) / 4 : (n - 2) / 4 + 1)); }
constexpr int unit_half(int n) { return n & 1; }
constexpr int unit_k(int t, int hf) { return t == 0 ? hf : 4 * (t - 1) + 2 + hf; }
constexpr int unit_v(int t, int hf) { return t == 7 ? 30 + hf : 4 * t + 4 + hf; }

struct Ctx4 {
  __amdgpu_buffer_rsrc_t rk, rv;
  int koff[4], voff[2];
  char* ring;
  unsigned kaddr[8];
  unsigned vaddr[4];
  bf16x8 qf[2][16];
  f32x16 s[2];
  bf16x8 pf[2][2];
  unsigned pn[2][8];
  float m[2], l[2], nm[2], mx[2], fct[2];
  float rs[2];
  bool pend;
  const u16* Kp; const u16* Vp;
  unsigned pfoff;
  int sub;
  unsigned pfd;
};
DI void prefetch_unit(Ctx4& c, int n) {
}

template <int NS, int N, int P> DI void dma_piece(const Ctx4& c) {
  if constexpr (N < 32) {
    constexpr int slot = N % NS, t = unit_tile(N), hf = unit_half(N);
    if constexpr (!unit_is_v(N))
      __builtin_amdgcn_raw_ptr_buffer_load_lds(c.rk, (lds_vp)(c.ring + slot * 8192 + P * 1024), 16, c.koff[P & 3],
                                               (32 * t + 16 * (P >> 2)) * 512 + hf * 256, 0, 0);
    else
      __builtin_amdgcn_raw_ptr_buffer_load_lds(c.rv, (lds_vp)(c.ring + slot * 8192 + P * 1024), 16, c.voff[P & 1],
                                               (32 * t + 16 * hf + 4 * (P >> 1)) * 512, 0, 0);
  }
}
template <int NS, int N> DI void dma_unit(const Ctx4& c) {
  dma_piece<NS, N, 0>(c); dma_piece<NS, N, 1>(c); dma_piece<NS, N, 2>(c); dma_piece<NS, N, 3>(c);
  dma_piece<NS, N, 4>(c); dma_piece<NS, N, 5>(c); dma_piece<NS, N, 6>(c); dma_piece<NS, N, 7>(c);
}
template <int N, int AHEAD> DI void wait_unit4() {
  constexpr int last = N + AHEAD > 31 ? 31 : N + AHEAD;
  constexpr int after = last - N;
  asm volatile("s_waitcnt vmcnt(%0)" ::"i"(8 * after) : "memory");
}

template <int NS, int T, int J> DI bf16x8 kread(const Ctx4& c) {
  constexpr int n = unit_k(T, J >> 3), slot = n % NS;
  bf16x8 r;
  asm volatile("ds_read_b128 %0, %1 offset:%2" : "=v"(r) : "v"(c.kaddr[J & 7]), "i"(slot * 8192) : "memory");
  return r;
}
template <int T, int GA> DI void sm_sliceA(Ctx4& c);
template <int NS, int T, int I> DI void phaseA_step(Ctx4& c, bf16x8 (&kf)[6]) {
  constexpr int n0 = unit_k(T, 0), n1 = unit_k(T, 1);
  constexpr int qb = I >> 4, J = I & 15;
  constexpr int ahead = (31 - I) < 5 ? (31 - I) : 5;
  asm volatile("s_waitcnt lgkmcnt(%1)" : "+v"(kf[I % 6]) : "i"(ahead) : "memory");
  qk_mfma<J == 0>(c.s[qb], kf[I % 6], c.qf[qb][J]);
  if constexpr (I + 6 == 8) wait_unit4<n1, NS - 2>();
  if constexpr (I + 6 < 32) kf[I % 6] = kread<NS, T, (I + 6) & 15>(c);
  if constexpr (I >= 24) { dma_piece<NS, n0 + NS, I - 24>(c); }
  if constexpr (T >= 1 && I < 16) sm_sliceA<T - 1, I>(c);
  GAP();
}
template <int NS, int T, int... Is> DI void phaseA_(Ctx4& c, std::integer_sequence<int, Is...>) {
  bf16x8 kf[6];
  wait_unit4<unit_k(T, 0), NS - 1>();
  kf[0] = kread<NS, T, 0>(c); kf[1] = kread<NS, T, 1>(c); kf[2] = kread<NS, T, 2>(c);
  kf[3] = kread<NS, T, 3>(c); kf[4] = kread<NS, T, 4>(c); kf[5] = kread<NS, T, 5>(c);
  GAP();
  (phaseA_step<NS, T, Is>(c, kf), ...);
}
template <int NS, int T> DI void phaseA(Ctx4& c) { phaseA_<NS, T>(c, std::make_integer_sequence<int, 32>{}); }

constexpr int egap(int e) { return 6 + (e * 25) / 32; }
template <int T, int G> DI void sm_slice(Ctx4& c) {
  if constexpr (G < 4) {
    constexpr int qb = G >> 1;
    if constexpr ((G & 1) == 0) {
      float a = max3f(c.s[qb][0], c.s[qb][1], c.s[qb][2]);
      a = max3f(a, c.s[qb][3], c.s[qb][4]);
      a = max3f(a, c.s[qb][5], c.s[qb][6]);
      c.mx[qb] = max3f(a, c.s[qb][7], c.s[qb][8]);
    } else {
      float a = max3f(c.mx[qb], c.s[qb][9], c.s[qb][10]);
      a = max3f(a, c.s[qb][11], c.s[qb][12]);
      a = max3f(a, c.s[qb][13], c.s[qb][14]);
      c.mx[qb] = max2f(a, c.s[qb][15]);
    }
  } else if constexpr (G == 4) {
#pragma unroll
    for (int qb = 0; qb < 2; ++qb) {
      auto rr = __builtin_amdgcn_permlane32_swap(__float_as_uint(c.mx[qb]), __float_as_uint(c.mx[qb]), false, false);
      c.mx[qb] = max2f(__uint_as_float(rr[0]), __uint_as_float(rr[1])) * SCL;
    }
  } else if constexpr (G == 5) {
    if constexpr (T == 0) {
      c.m[0] = c.mx[0]; c.m[1] = c.mx[1];
      c.l[0] = 0.f; c.l[1] = 0.f;
    } else {
      const bool grew = (c.mx[0] - c.m[0] > THR) | (c.mx[1] - c.m[1] > THR);
      if (__builtin_expect(__any((int)grew), 0)) {
#pragma unroll
        for (int qb = 0; qb < 2; ++qb) {
          const float mn = fmaxf(c.m[qb], c.mx[qb]);
          const float al = __builtin_amdgcn_exp2f(c.m[qb] - mn);
          c.m[qb] = mn;
          c.l[qb] *= al;
          c.fct[qb] = al;
        }
        c.pend = true;
      }
    }
    c.nm[0] = -c.m[0]; c.nm[1] = -c.m[1];
    c.rs[0] = 0.f; c.rs[1] = 0.f;
  }
  if constexpr (G >= 6) {
#pragma unroll
    for (int e = 0; e < 32; ++e) {
      if (egap(e) == G) {
        const int qb = e >> 4, i = e & 15;
        c.s[qb][i] = __builtin_amdgcn_exp2f(ffma_s(c.s[qb][i], SCL, c.nm[qb]));
      }
    }
#pragma unroll
    for (int e = 0; e < 32; ++e) {
      const int ge = egap(e);
      if (ge == G - 1 || (G == 31 && ge == 31)) {
        const int qb = e >> 4, i = e & 15;
        c.rs[qb] += c.s[qb][i];
      }
    }
#pragma unroll
    for (int d = 0; d < 16; ++d) {
      const int ge = egap(2 * d + 1);
      if (ge == G - 1 || (G == 31 && ge == 31)) {
        const int qb = d >> 3, i = (2 * d) & 15;
        c.pn[qb][d & 7] = cvtpk(c.s[qb][i], c.s[qb][i + 1]);
      }
    }
  }
}

constexpr int egap0(int e) { return 6 + (e * 25) / 16; }
template <int T, int G> DI void sm_sliceB(Ctx4& c) {
  if constexpr (G < 6) sm_slice<T, G>(c);
  else {
#pragma unroll
    for (int e = 0; e < 16; ++e)
      if (egap0(e) == G) c.s[0][e] = __builtin_amdgcn_exp2f(ffma_s(c.s[0][e], SCL, c.nm[0]));
#pragma unroll
    for (int e = 0; e < 16; ++e) {
      const int ge = egap0(e);
      if (ge == G - 1 || (G == 31 && ge == 31)) c.rs[0] += c.s[0][e];
    }
#pragma unroll
    for (int d = 0; d < 8; ++d) {
      const int ge = egap0(2 * d + 1);
      if (ge == G - 1 || (G == 31 && ge == 31)) c.pn[0][d] = cvtpk(c.s[0][2 * d], c.s[0][2 * d + 1]);
    }
  }
}
template <int T, int GA> DI void sm_sliceA(Ctx4& c) {
  c.s[1][GA] = __builtin_amdgcn_exp2f(ffma_s(c.s[1][GA], SCL, c.nm[1]));
  if constexpr (GA >= 1) c.rs[1] += c.s[1][GA - 1];
  if constexpr (GA >= 2 && (GA & 1) == 0) c.pn[1][(GA - 2) >> 1] = cvtpk(c.s[1][GA - 2], c.s[1][GA - 1]);
  if constexpr (GA == 15) {
    c.rs[1] += c.s[1][15];
    c.pn[1][7] = cvtpk(c.s[1][14], c.s[1][15]);
    c.l[1] += c.rs[1];
#pragma unroll
    for (int ks = 0; ks < 2; ++ks) {
      u32x4 wv = {c.pn[1][4 * ks], c.pn[1][4 * ks + 1], c.pn[1][4 * ks + 2], c.pn[1][4 * ks + 3]};
      c.pf[1][ks] = __builtin_bit_cast(bf16x8, wv);
    }
  }
}

template <int NS, int TV, int S> DI void vread_set(const Ctx4& c, s16x4 (&v)[4]) {
  constexpr int ks = S >> 2, dbp = S & 3;
  constexpr int n = unit_v(TV, ks), slot = n % NS;
  constexpr int db0 = 2 * dbp, db1 = 2 * dbp + 1;
  constexpr int o0 = slot * 8192 + (db0 >> 2) * 256, o1 = slot * 8192 + (db1 >> 2) * 256;
  v[0] = trread_o<o0>(c.vaddr[db0 & 3]);
  v[1] = trread_o<o0 + 4096>(c.vaddr[db0 & 3]);
  v[2] = trread_o<o1>(c.vaddr[db1 & 3]);
  v[3] = trread_o<o1 + 4096>(c.vaddr[db1 & 3]);
}
template <int NS, int T, bool HASPV, bool HASSM, int M> DI void phaseB_gap(Ctx4& c, s16x4 (&va)[4], s16x4 (&vb)[4]) {
  constexpr int TV = T - 1;
  constexpr int S = M >> 2, ks = S >> 2, dbp = S & 3, dd = (M >> 1) & 1, qb = M & 1;
  if constexpr (HASPV) {
    if constexpr ((M & 3) == 0) {
      if constexpr (S + 1 < 8) {
        if constexpr (S & 1) asm volatile("s_waitcnt lgkmcnt(4)" : "+v"(vb[0]), "+v"(vb[1]), "+v"(vb[2]), "+v"(vb[3])::"memory");
        else asm volatile("s_waitcnt lgkmcnt(4)" : "+v"(va[0]), "+v"(va[1]), "+v"(va[2]), "+v"(va[3])::"memory");
      } else {
        asm volatile("s_waitcnt lgkmcnt(0)" : "+v"(vb[0]), "+v"(vb[1]), "+v"(vb[2]), "+v"(vb[3])::"memory");
      }
    }
    {
      constexpr int db = 2 * dbp + dd;
      const bf16x8 vf = (S & 1) ? cat8(vb[2 * dd], vb[2 * dd + 1]) : cat8(va[2 * dd], va[2 * dd + 1]);
      pv_mfma<(qb * 8 + db) * 16, (TV == 0 && ks == 0)>(vf, c.pf[qb][ks]);
    }
    if constexpr ((M & 3) == 3 && S + 2 < 8) {
      if constexpr (S + 2 == 4) wait_unit4<unit_v(TV, 1), NS - 2>();
      if constexpr (S & 1) vread_set<NS, TV, S + 2>(c, vb); else vread_set<NS, TV, S + 2>(c, va);
    }
    if constexpr (M >= 16 && M < 24) dma_piece<NS, unit_v(TV, 0) + NS, M - 16>(c);
  }
  if constexpr (HASSM) { if constexpr (T == 7) sm_slice<T, M>(c); else sm_sliceB<T, M>(c); }
  GAP();
}
template <int NS, int T, bool HASPV, bool HASSM, int... Ms> DI void phaseB_(Ctx4& c, std::integer_sequence<int, Ms...>) {
  constexpr int TV = T - 1;
  s16x4 va[4], vb[4];
  if constexpr (HASPV) {
    wait_unit4<unit_v(TV, 0), NS - 2>();
    vread_set<NS, TV, 0>(c, va);
    vread_set<NS, TV, 1>(c, vb);
  }
  if constexpr (HASSM) dma_unit<NS, unit_k(T, 1) + NS>(c);
  if constexpr (HASSM) {
    asm volatile("s_nop 7\n\ts_nop 7" : "+v"(c.s[0]), "+v"(c.s[1]));
  }
  GAP();
  (phaseB_gap<NS, T, HASPV, HASSM, Ms>(c, va, vb), ...);
  if constexpr (HASPV) dma_unit<NS, unit_v(TV, 1) + NS>(c);
  if constexpr (HASSM) {
    c.l[0] += c.rs[0];
    if constexpr (T == 7) c.l[1] += c.rs[1];
#pragma unroll
    for (int qb = 0; qb < (T == 7 ? 2 : 1); ++qb)
#pragma unroll
      for (int ks = 0; ks < 2; ++ks) {
        u32x4 wv = {c.pn[qb][4 * ks], c.pn[qb][4 * ks + 1], c.pn[qb][4 * ks + 2], c.pn[qb][4 * ks + 3]};
        c.pf[qb][ks] = __builtin_bit_cast(bf16x8, wv);
      }
  }
  if constexpr (HASPV && HASSM) {
    if (__builtin_expect(c.pend, 0)) {
      float t0, t1;
      asm volatile("s_nop 7\n\ts_nop 7\n\t"
                   "v_accvgpr_read_b32 %0, a0\n\tv_mul_f32 %0, %2, %0\n\tv_accvgpr_write_b32 a0, %0\n\t"
                   "v_accvgpr_read_b32 %1, a1\n\tv_mul_f32 %1, %2, %1\n\tv_accvgpr_write_b32 a1, %1\n\t"
                   "v_accvgpr_read_b32 %0, a2\n\tv_mul_f32 %0, %2, %0\n\tv_accvgpr_write_b32 a2, %0\n\t"
                   "v_accvgpr_read_b32 %1, a3\n\tv_mul_f32 %1, %2, %1\n\tv_accvgpr_write_b32 a3, %1\n\t"
                   "v_accvgpr_read_b32 %0, a4\n\tv_mul_f32 %0, %2, %0\n\tv_accvgpr_write_b32 a4, %0\n\t"
                   "v_accvgpr_read_b32 %1, a5\n\tv_mul_f32 %1, %2, %1\n\tv_accvgpr_write_b32 a5, %1\n\t"
                   "v_accvgpr_read_b32 %0, a6\n\tv_mul_f32 %0, %2, %0\n\tv_accvgpr_write_b32 a6, %0\n\t"
                   "v_accvgpr_read_b32 %1, a7\n\tv_mul_f32 %1, %2, %1\n\tv_accvgpr_write_b32 a7, %1\n\t"
                   "v_accvgpr_read_b32 %0, a8\n\tv_mul_f32 %0, %2, %0\n\tv_accvgpr_write_b32 a8, %0\n\t"
                   "v_accvgpr_read_b32 %1, a9\n\tv_mul_f32 %1, %2, %1\n\tv_accvgpr_write_b32 a9, %1\n\t"
                   "v_accvgpr_read_b32 %0, a10\n\tv_mul_f32 %0, %2, %0\n\tv_accvgpr_write_b32 a10, %0\n\t"
                   "v_accvgpr_read_b32 %1, a11\n\tv_mul_f32 %1, %2, %1\n\tv_accvgpr_write_b32 a11, %1\n\t"
                   "v_accvgpr_read_b32 %0, a12\n\tv_mul_f32 %0, %2, %0\n\tv_accvgpr_write_b32 a12, %0\n\t"
                   "v_accvgpr_read_b32 %1, a13\n\tv_mul_f32 %1, %2, %1\n\tv_accvgpr_write_b32 a13, %1\n\t"
                   "v_accvgpr_read_b32 %0, a14\n\tv_mul_f32 %0, %2, %0\n\tv_accvgpr_write_b32 a14, %0\n\t"
                   "v_accvgpr_read_b32 %1, a15\n\tv_mul_f32 %1, %2, %1\n\tv_accvgpr_write_b32 a15, %1\n\t"
                   "v_accvgpr_read_b32 %0, a16\n\tv_mul_f32 %0, %2, %0\n\tv_accvgpr_write_b32 a16, %0\n\t"
                   "v_accvgpr_read_b32 %1, a17\n\tv_mul_f32 %1, %2, %1\n\tv_accvgpr_write_b32 a17, %1\n\t"
                   "v_accvgpr_read_b32 %0, a18\n\tv_mul_f32 %0, %2, %0\n\tv_accvgpr_write_b32 a18, %0\n\t"
                   "v_accvgpr_read_b32 %1, a19\n\tv_mul_f32 %1, %2, %1\n\tv_accvgpr_write_b32 a19, %1\n\t"
                   "v_accvgpr_read_b32 %0, a20\n\tv_mul_f32 %0, %2, %0\n\tv_accvgpr_write_b32 a20, %0\n\t"
                   "v_accvgpr_read_b32 %1, a21\n\tv_mul_f32 %1, %2, %1\n\tv_accvgpr_write_b32 a21, %1\n\t"
                   "v_accvgpr_read_b32 %0, a22\n\tv_mul_f32 %0, %2, %0\n\tv_accvgpr_write_b32 a22, %0\n\t"
                   "v_accvgpr_read_b32 %1, a23\n\tv_mul_f32 %1, %2, %1\n\tv_accvgpr_write_b32 a23, %1\n\t"
                   "v_accvgpr_read_b32 %0, a24\n\tv_mul_f32 %0, %2, %0\n\tv_accvgpr_write_b32 a24, %0\n\t"
                   "v_accvgpr_read_b32 %1, a25\n\tv_mul_f32 %1, %2, %1\n\tv_accvgpr_write_b32 a25, %1\n\t"
                   "v_accvgpr_read_b32 %0, a26\n\tv_mul_f32 %0, %2, %0\n\tv_accvgpr_write_b32 a26, %0\n\t"
                   "v_accvgpr_read_b32 %1, a27\n\tv_mul_f32 %1, %2, %1\n\tv_accvgpr_write_b32 a27, %1\n\t"
                   "v_accvgpr_read_b32 %0, a28\n\tv_mul_f32 %0, %2, %0\n\tv_accvgpr_write_b32 a28, %0\n\t"
                   "v_accvgpr_read_b32 %1, a29\n\tv_mul_f32 %1, %2, %1\n\tv_accvgpr_write_b32 a29, %1\n\t"
                   "v_accvgpr_read_b32 %0, a30\n\tv_mul_f32 %0, %2, %0\n\tv_accvgpr_write_b32 a30, %0\n\t"
                   "v_accvgpr_read_b32 %1, a31\n\tv_mul_f32 %1, %2, %1\n\tv_accvgpr_write_b32 a31, %1\n\t"
                   "v_accvgpr_read_b32 %0, a32\n\tv_mul_f32 %0, %2, %0\n\tv_accvgpr_write_b32 a32, %0\n\t"
                   "v_accvgpr_read_b32 %1, a33\n\tv_mul_f32 %1, %2, %1\n\tv_accvgpr_write_b32 a33, %1\n\t"
                   "v_accvgpr_read_b32 %0, a34\n\tv_mul_f32 %0, %2, %0\n\tv_accvgpr_write_b32 a34, %0\n\t"
                   "v_accvgpr_read_b32 %1, a35\n\tv_mul_f32 %1, %2, %1\n\tv_accvgpr_write_b32 a35, %1\n\t"
                   "v_accvgpr_read_b32 %0, a36\n\tv_mul_f32 %0, %2, %0\n\tv_accvgpr_write_b32 a36, %0\n\t"
                   "v_accvgpr_read_b32 %1, a37\n\tv_mul_f32 %1, %2, %1\n\tv_accvgpr_write_b32 a37, %1\n\t"
                   "v_accvgpr_read_b32 %0, a38\n\tv_mul_f32 %0, %2, %0\n\tv_accvgpr_write_b32 a38, %0\n\t"
                   "v_accvgpr_read_b32 %1, a39\n\tv_mul_f32 %1, %2, %1\n\tv_accvgpr_write_b32 a39, %1\n\t"
                   "v_accvgpr_read_b32 %0, a40\n\tv_mul_f32 %0, %2, %0\n\tv_accvgpr_write_b32 a40, %0\n\t"
                   "v_accvgpr_read_b32 %1, a41\n\tv_mul_f32 %1, %2, %1\n\tv_accvgpr_write_b32 a41, %1\n\t"
                   "v_accvgpr_read_b32 %0, a42\n\tv_mul_f32 %0, %2, %0\n\tv_accvgpr_write_b32 a42, %0\n\t"
                   "v_accvgpr_read_b32 %1, a43\n\tv_mul_f32 %1, %2, %1\n\tv_accvgpr_write_b32 a43, %1\n\t"
                   "v_accvgpr_read_b32 %0, a44\n\tv_mul_f32 %0, %2, %0\n\tv_accvgpr_write_b32 a44, %0\n\t"
                   "v_accvgpr_read_b32 %1, a45\n\tv_mul_f32 %1, %2, %1\n\tv_accvgpr_write_b32 a45, %1\n\t"
                   "v_accvgpr_read_b32 %0, a46\n\tv_mul_f32 %0, %2, %0\n\tv_accvgpr_write_b32 a46, %0\n\t"
                   "v_accvgpr_read_b32 %1, a47\n\tv_mul_f32 %1, %2, %1\n\tv_accvgpr_write_b32 a47, %1\n\t"
                   "v_accvgpr_read_b32 %0, a48\n\tv_mul_f32 %0, %2, %0\n\tv_accvgpr_write_b32 a48, %0\n\t"
                   "v_accvgpr_read_b32 %1, a49\n\tv_mul_f32 %1, %2, %1\n\tv_accvgpr_write_b32 a49, %1\n\t"
                   "v_accvgpr_read_b32 %0, a50\n\tv_mul_f32 %0, %2, %0\n\tv_accvgpr_write_b32 a50, %0\n\t"
                   "v_accvgpr_read_b32 %1, a51\n\tv_mul_f32 %1, %2, %1\n\tv_accvgpr_write_b32 a51, %1\n\t"
                   "v_accvgpr_read_b32 %0, a52\n\tv_mul_f32 %0, %2, %0\n\tv_accvgpr_write_b32 a52, %0\n\t"
                   "v_accvgpr_read_b32 %1, a53\n\tv_mul_f32 %1, %2, %1\n\tv_accvgpr_write_b32 a53, %1\n\t"
                   "v_accvgpr_read_b32 %0, a54\n\tv_mul_f32 %0, %2, %0\n\tv_accvgpr_write_b32 a54, %0\n\t"
                   "v_accvgpr_read_b32 %1, a55\n\tv_mul_f32 %1, %2, %1\n\tv_accvgpr_write_b32 a55, %1\n\t"
                   "v_accvgpr_read_b32 %0, a56\n\tv_mul_f32 %0, %2, %0\n\tv_accvgpr_write_b32 a56, %0\n\t"
                   "v_accvgpr_read_b32 %1, a57\n\tv_mul_f32 %1, %2, %1\n\tv_accvgpr_write_b32 a57, %1\n\t"
                   "v_accvgpr_read_b32 %0, a58\n\tv_mul_f32 %0, %2, %0\n\tv_accvgpr_write_b32 a58, %0\n\t"
                   "v_accvgpr_read_b32 %1, a59\n\tv_mul_f32 %1, %2, %1\n\tv_accvgpr_write_b32 a59, %1\n\t"
                   "v_accvgpr_read_b32 %0, a60\n\tv_mul_f32 %0, %2, %0\n\tv_accvgpr_write_b32 a60, %0\n\t"
                   "v_accvgpr_read_b32 %1, a61\n\tv_mul_f32 %1, %2, %1\n\tv_accvgpr_write_b32 a61, %1\n\t"
                   "v_accvgpr_read_b32 %0, a62\n\tv_mul_f32 %0, %2, %0\n\tv_accvgpr_write_b32 a62, %0\n\t"
                   "v_accvgpr_read_b32 %1, a63\n\tv_mul_f32 %1, %2, %1\n\tv_accvgpr_write_b32 a63, %1\n\t"
                   "v_accvgpr_read_b32 %0, a64\n\tv_mul_f32 %0, %2, %0\n\tv_accvgpr_write_b32 a64, %0\n\t"
                   "v_accvgpr_read_b32 %1, a65\n\tv_mul_f32 %1, %2, %1\n\tv_accvgpr_write_b32 a65, %1\n\t"
                   "v_accvgpr_read_b32 %0, a66\n\tv_mul_f32 %0, %2, %0\n\tv_accvgpr_write_b32 a66, %0\n\t"
                   "v_accvgpr_read_b32 %1, a67\n\tv_mul_f32 %1, %2, %1\n\tv_accvgpr_write_b32 a67, %1\n\t"
                   "v_accvgpr_read_b32 %0, a68\n\tv_mul_f32 %0, %2, %0\n\tv_accvgpr_write_b32 a68, %0\n\t"
                   "v_accvgpr_read_b32 %1, a69\n\tv_mul_f32 %1, %2, %1\n\tv_accvgpr_write_b32 a69, %1\n\t"
                   "v_accvgpr_read_b32 %0, a70\n\tv_mul_f32 %0, %2, %0\n\tv_accvgpr_write_b32 a70, %0\n\t"
                   "v_accvgpr_read_b32 %1, a71\n\tv_mul_f32 %1, %2, %1\n\tv_accvgpr_write_b32 a71, %1\n\t"
                   "v_accvgpr_read_b32 %0, a72\n\tv_mul_f32 %0, %2, %0\n\tv_accvgpr_write_b32 a72, %0\n\t"
                   "v_accvgpr_read_b32 %1, a73\n\tv_mul_f32 %1, %2, %1\n\tv_accvgpr_write_b32 a73, %1\n\t"
                   "v_accvgpr_read_b32 %0, a74\n\tv_mul_f32 %0, %2, %0\n\tv_accvgpr_write_b32 a74, %0\n\t"
                   "v_accvgpr_read_b32 %1, a75\n\tv_mul_f32 %1, %2, %1\n\tv_accvgpr_write_b32 a75, %1\n\t"
                   "v_accvgpr_read_b32 %0, a76\n\tv_mul_f32 %0, %2, %0\n\tv_accvgpr_write_b32 a76, %0\n\t"
                   "v_accvgpr_read_b32 %1, a77\n\tv_mul_f32 %1, %2, %1\n\tv_accvgpr_write_b32 a77, %1\n\t"
                   "v_accvgpr_read_b32 %0, a78\n\tv_mul_f32 %0, %2, %0\n\tv_accvgpr_write_b32 a78, %0\n\t"
                   "v_accvgpr_read_b32 %1, a79\n\tv_mul_f32 %1, %2, %1\n\tv_accvgpr_write_b32 a79, %1\n\t"
                   "v_accvgpr_read_b32 %0, a80\n\tv_mul_f32 %0, %2, %0\n\tv_accvgpr_write_b32 a80, %0\n\t"
                   "v_accvgpr_read_b32 %1, a81\n\tv_mul_f32 %1, %2, %1\n\tv_accvgpr_write_b32 a81, %1\n\t"
                   "v_accvgpr_read_b32 %0, a82\n\tv_mul_f32 %0, %2, %0\n\tv_accvgpr_write_b32 a82, %0\n\t"
                   "v_accvgpr_read_b32 %1, a83\n\tv_mul_f32 %1, %2, %1\n\tv_accvgpr_write_b32 a83, %1\n\t"
                   "v_accvgpr_read_b32 %0, a84\n\tv_mul_f32 %0, %2, %0\n\tv_accvgpr_write_b32 a84, %0\n\t"
                   "v_accvgpr_read_b32 %1, a85\n\tv_mul_f32 %1, %2, %1\n\tv_accvgpr_write_b32 a85, %1\n\t"
                   "v_accvgpr_read_b32 %0, a86\n\tv_mul_f32 %0, %2, %0\n\tv_accvgpr_write_b32 a86, %0\n\t"
                   "v_accvgpr_read_b32 %1, a87\n\tv_mul_f32 %1, %2, %1\n\tv_accvgpr_write_b32 a87, %1\n\t"
                   "v_accvgpr_read_b32 %0, a88\n\tv_mul_f32 %0, %2, %0\n\tv_accvgpr_write_b32 a88, %0\n\t"
                   "v_accvgpr_read_b32 %1, a89\n\tv_mul_f32 %1, %2, %1\n\tv_accvgpr_write_b32 a89, %1\n\t"
                   "v_accvgpr_read_b32 %0, a90\n\tv_mul_f32 %0, %2, %0\n\tv_accvgpr_write_b32 a90, %0\n\t"
                   "v_accvgpr_read_b32 %1, a91\n\tv_mul_f32 %1, %2, %1\n\tv_accvgpr_write_b32 a91, %1\n\t"
                   "v_accvgpr_read_b32 %0, a92\n\tv_mul_f32 %0, %2, %0\n\tv_accvgpr_write_b32 a92, %0\n\t"
                   "v_accvgpr_read_b32 %1, a93\n\tv_mul_f32 %1, %2, %1\n\tv_accvgpr_write_b32 a93, %1\n\t"
                   "v_accvgpr_read_b32 %0, a94\n\tv_mul_f32 %0, %2, %0\n\tv_accvgpr_write_b32 a94, %0\n\t"
                   "v_accvgpr_read_b32 %1, a95\n\tv_mul_f32 %1, %2, %1\n\tv_accvgpr_write_b32 a95, %1\n\t"
                   "v_accvgpr_read_b32 %0, a96\n\tv_mul_f32 %0, %2, %0\n\tv_accvgpr_write_b32 a96, %0\n\t"
                   "v_accvgpr_read_b32 %1, a97\n\tv_mul_f32 %1, %2, %1\n\tv_accvgpr_write_b32 a97, %1\n\t"
                   "v_accvgpr_read_b32 %0, a98\n\tv_mul_f32 %0, %2, %0\n\tv_accvgpr_write_b32 a98, %0\n\t"
                   "v_accvgpr_read_b32 %1, a99\n\tv_mul_f32 %1, %2, %1\n\tv_accvgpr_write_b32 a99, %1\n\t"
                   "v_accvgpr_read_b32 %0, a100\n\tv_mul_f32 %0, %2, %0\n\tv_accvgpr_write_b32 a100, %0\n\t"
                   "v_accvgpr_read_b32 %1, a101\n\tv_mul_f32 %1, %2, %1\n\tv_accvgpr_write_b32 a101, %1\n\t"
                   "v_accvgpr_read_b32 %0, a102\n\tv_mul_f32 %0, %2, %0\n\tv_accvgpr_write_b32 a102, %0\n\t"
                   "v_accvgpr_read_b32 %1, a103\n\tv_mul_f32 %1, %2, %1\n\tv_accvgpr_write_b32 a103, %1\n\t"
                   "v_accvgpr_read_b32 %0, a104\n\tv_mul_f32 %0, %2, %0\n\tv_accvgpr_write_b32 a104, %0\n\t"
                   "v_accvgpr_read_b32 %1, a105\n\tv_mul_f32 %1, %2, %1\n\tv_accvgpr_write_b32 a105, %1\n\t"
                   "v_accvgpr_read_b32 %0, a106\n\tv_mul_f32 %0, %2, %0\n\tv_accvgpr_write_b32 a106, %0\n\t"
                   "v_accvgpr_read_b32 %1, a107\n\tv_mul_f32 %1, %2, %1\n\tv_accvgpr_write_b32 a107, %1\n\t"
                   "v_accvgpr_read_b32 %0, a108\n\tv_mul_f32 %0, %2, %0\n\tv_accvgpr_write_b32 a108, %0\n\t"
                   "v_accvgpr_read_b32 %1, a109\n\tv_mul_f32 %1, %2, %1\n\tv_accvgpr_write_b32 a109, %1\n\t"
                   "v_accvgpr_read_b32 %0, a110\n\tv_mul_f32 %0, %2, %0\n\tv_accvgpr_write_b32 a110, %0\n\t"
                   "v_accvgpr_read_b32 %1, a111\n\tv_mul_f32 %1, %2, %1\n\tv_accvgpr_write_b32 a111, %1\n\t"
                   "v_accvgpr_read_b32 %0, a112\n\tv_mul_f32 %0, %2, %0\n\tv_accvgpr_write_b32 a112, %0\n\t"
                   "v_accvgpr_read_b32 %1, a113\n\tv_mul_f32 %1, %2, %1\n\tv_accvgpr_write_b32 a113, %1\n\t"
                   "v_accvgpr_read_b32 %0, a114\n\tv_mul_f32 %0, %2, %0\n\tv_accvgpr_write_b32 a114, %0\n\t"
                   "v_accvgpr_read_b32 %1, a115\n\tv_mul_f32 %1, %2, %1\n\tv_accvgpr_write_b32 a115, %1\n\t"
                   "v_accvgpr_read_b32 %0, a116\n\tv_mul_f32 %0, %2, %0\n\tv_accvgpr_write_b32 a116, %0\n\t"
                   "v_accvgpr_read_b32 %1, a117\n\tv_mul_f32 %1, %2, %1\n\tv_accvgpr_write_b32 a117, %1\n\t"
                   "v_accvgpr_read_b32 %0, a118\n\tv_mul_f32 %0, %2, %0\n\tv_accvgpr_write_b32 a118, %0\n\t"
                   "v_accvgpr_read_b32 %1, a119\n\tv_mul_f32 %1, %2, %1\n\tv_accvgpr_write_b32 a119, %1\n\t"
                   "v_accvgpr_read_b32 %0, a120\n\tv_mul_f32 %0, %2, %0\n\tv_accvgpr_write_b32 a120, %0\n\t"
                   "v_accvgpr_read_b32 %1, a121\n\tv_mul_f32 %1, %2, %1\n\tv_accvgpr_write_b32 a121, %1\n\t"
                   "v_accvgpr_read_b32 %0, a122\n\tv_mul_f32 %0, %2, %0\n\tv_accvgpr_write_b32 a122, %0\n\t"
                   "v_accvgpr_read_b32 %1, a123\n\tv_mul_f32 %1, %2, %1\n\tv_accvgpr_write_b32 a123, %1\n\t"
                   "v_accvgpr_read_b32 %0, a124\n\tv_mul_f32 %0, %2, %0\n\tv_accvgpr_write_b32 a124, %0\n\t"
                   "v_accvgpr_read_b32 %1, a125\n\tv_mul_f32 %1, %2, %1\n\tv_accvgpr_write_b32 a125, %1\n\t"
                   "v_accvgpr_read_b32 %0, a126\n\tv_mul_f32 %0, %2, %0\n\tv_accvgpr_write_b32 a126, %0\n\t"
                   "v_accvgpr_read_b32 %1, a127\n\tv_mul_f32 %1, %2, %1\n\tv_accvgpr_write_b32 a127, %1\n\t"
                   "v_accvgpr_read_b32 %0, a128\n\tv_mul_f32 %0, %3, %0\n\tv_accvgpr_write_b32 a128, %0\n\t"
                   "v_accvgpr_read_b32 %1, a129\n\tv_mul_f32 %1, %3, %1\n\tv_accvgpr_write_b32 a129, %1\n\t"
                   "v_accvgpr_read_b32 %0, a130\n\tv_mul_f32 %0, %3, %0\n\tv_accvgpr_write_b32 a130, %0\n\t"
                   "v_accvgpr_read_b32 %1, a131\n\tv_mul_f32 %1, %3, %1\n\tv_accvgpr_write_b32 a131, %1\n\t"
                   "v_accvgpr_read_b32 %0, a132\n\tv_mul_f32 %0, %3, %0\n\tv_accvgpr_write_b32 a132, %0\n\t"
                   "v_accvgpr_read_b32 %1, a133\n\tv_mul_f32 %1, %3, %1\n\tv_accvgpr_write_b32 a133, %1\n\t"
                   "v_accvgpr_read_b32 %0, a134\n\tv_mul_f32 %0, %3, %0\n\tv_accvgpr_write_b32 a134, %0\n\t"
                   "v_accvgpr_read_b32 %1, a135\n\tv_mul_f32 %1, %3, %1\n\tv_accvgpr_write_b32 a135, %1\n\t"
                   "v_accvgpr_read_b32 %0, a136\n\tv_mul_f32 %0, %3, %0\n\tv_accvgpr_write_b32 a136, %0\n\t"
                   "v_accvgpr_read_b32 %1, a137\n\tv_mul_f32 %1, %3, %1\n\tv_accvgpr_write_b32 a137, %1\n\t"
                   "v_accvgpr_read_b32 %0, a138\n\tv_mul_f32 %0, %3, %0\n\tv_accvgpr_write_b32 a138, %0\n\t"
                   "v_accvgpr_read_b32 %1, a139\n\tv_mul_f32 %1, %3, %1\n\tv_accvgpr_write_b32 a139, %1\n\t"
                   "v_accvgpr_read_b32 %0, a140\n\tv_mul_f32 %0, %3, %0\n\tv_accvgpr_write_b32 a140, %0\n\t"
                   "v_accvgpr_read_b32 %1, a141\n\tv_mul_f32 %1, %3, %1\n\tv_accvgpr_write_b32 a141, %1\n\t"
                   "v_accvgpr_read_b32 %0, a142\n\tv_mul_f32 %0, %3, %0\n\tv_accvgpr_write_b32 a142, %0\n\t"
                   "v_accvgpr_read_b32 %1, a143\n\tv_mul_f32 %1, %3, %1\n\tv_accvgpr_write_b32 a143, %1\n\t"
                   "v_accvgpr_read_b32 %0, a144\n\tv_mul_f32 %0, %3, %0\n\tv_accvgpr_write_b32 a144, %0\n\t"
                   "v_accvgpr_read_b32 %1, a145\n\tv_mul_f32 %1, %3, %1\n\tv_accvgpr_write_b32 a145, %1\n\t"
                   "v_accvgpr_read_b32 %0, a146\n\tv_mul_f32 %0, %3, %0\n\tv_accvgpr_write_b32 a146, %0\n\t"
                   "v_accvgpr_read_b32 %1, a147\n\tv_mul_f32 %1, %3, %1\n\tv_accvgpr_write_b32 a147, %1\n\t"
                   "v_accvgpr_read_b32 %0, a148\n\tv_mul_f32 %0, %3, %0\n\tv_accvgpr_write_b32 a148, %0\n\t"
                   "v_accvgpr_read_b32 %1, a149\n\tv_mul_f32 %1, %3, %1\n\tv_accvgpr_write_b32 a149, %1\n\t"
                   "v_accvgpr_read_b32 %0, a150\n\tv_mul_f32 %0, %3, %0\n\tv_accvgpr_write_b32 a150, %0\n\t"
                   "v_accvgpr_read_b32 %1, a151\n\tv_mul_f32 %1, %3, %1\n\tv_accvgpr_write_b32 a151, %1\n\t"
                   "v_accvgpr_read_b32 %0, a152\n\tv_mul_f32 %0, %3, %0\n\tv_accvgpr_write_b32 a152, %0\n\t"
                   "v_accvgpr_read_b32 %1, a153\n\tv_mul_f32 %1, %3, %1\n\tv_accvgpr_write_b32 a153, %1\n\t"
                   "v_accvgpr_read_b32 %0, a154\n\tv_mul_f32 %0, %3, %0\n\tv_accvgpr_write_b32 a154, %0\n\t"
                   "v_accvgpr_read_b32 %1, a155\n\tv_mul_f32 %1, %3, %1\n\tv_accvgpr_write_b32 a155, %1\n\t"
                   "v_accvgpr_read_b32 %0, a156\n\tv_mul_f32 %0, %3, %0\n\tv_accvgpr_write_b32 a156, %0\n\t"
                   "v_accvgpr_read_b32 %1, a157\n\tv_mul_f32 %1, %3, %1\n\tv_accvgpr_write_b32 a157, %1\n\t"
                   "v_accvgpr_read_b32 %0, a158\n\tv_mul_f32 %0, %3, %0\n\tv_accvgpr_write_b32 a158, %0\n\t"
                   "v_accvgpr_read_b32 %1, a159\n\tv_mul_f32 %1, %3, %1\n\tv_accvgpr_write_b32 a159, %1\n\t"
                   "v_accvgpr_read_b32 %0, a160\n\tv_mul_f32 %0, %3, %0\n\tv_accvgpr_write_b32 a160, %0\n\t"
                   "v_accvgpr_read_b32 %1, a161\n\tv_mul_f32 %1, %3, %1\n\tv_accvgpr_write_b32 a161, %1\n\t"
                   "v_accvgpr_read_b32 %0, a162\n\tv_mul_f32 %0, %3, %0\n\tv_accvgpr_write_b32 a162, %0\n\t"
                   "v_accvgpr_read_b32 %1, a163\n\tv_mul_f32 %1, %3, %1\n\tv_accvgpr_write_b32 a163, %1\n\t"
                   "v_accvgpr_read_b32 %0, a164\n\tv_mul_f32 %0, %3, %0\n\tv_accvgpr_write_b32 a164, %0\n\t"
                   "v_accvgpr_read_b32 %1, a165\n\tv_mul_f32 %1, %3, %1\n\tv_accvgpr_write_b32 a165, %1\n\t"
                   "v_accvgpr_read_b32 %0, a166\n\tv_mul_f32 %0, %3, %0\n\tv_accvgpr_write_b32 a166, %0\n\t"
                   "v_accvgpr_read_b32 %1, a167\n\tv_mul_f32 %1, %3, %1\n\tv_accvgpr_write_b32 a167, %1\n\t"
                   "v_accvgpr_read_b32 %0, a168\n\tv_mul_f32 %0, %3, %0\n\tv_accvgpr_write_b32 a168, %0\n\t"
                   "v_accvgpr_read_b32 %1, a169\n\tv_mul_f32 %1, %3, %1\n\tv_accvgpr_write_b32 a169, %1\n\t"
                   "v_accvgpr_read_b32 %0, a170\n\tv_mul_f32 %0, %3, %0\n\tv_accvgpr_write_b32 a170, %0\n\t"
                   "v_accvgpr_read_b32 %1, a171\n\tv_mul_f32 %1, %3, %1\n\tv_accvgpr_write_b32 a171, %1\n\t"
                   "v_accvgpr_read_b32 %0, a172\n\tv_mul_f32 %0, %3, %0\n\tv_accvgpr_write_b32 a172, %0\n\t"
                   "v_accvgpr_read_b32 %1, a173\n\tv_mul_f32 %1, %3, %1\n\tv_accvgpr_write_b32 a173, %1\n\t"
                   "v_accvgpr_read_b32 %0, a174\n\tv_mul_f32 %0, %3, %0\n\tv_accvgpr_write_b32 a174, %0\n\t"
                   "v_accvgpr_read_b32 %1, a175\n\tv_mul_f32 %1, %3, %1\n\tv_accvgpr_write_b32 a175, %1\n\t"
                   "v_accvgpr_read_b32 %0, a176\n\tv_mul_f32 %0, %3, %0\n\tv_accvgpr_write_b32 a176, %0\n\t"
                   "v_accvgpr_read_b32 %1, a177\n\tv_mul_f32 %1, %3, %1\n\tv_accvgpr_write_b32 a177, %1\n\t"
                   "v_accvgpr_read_b32 %0, a178\n\tv_mul_f32 %0, %3, %0\n\tv_accvgpr_write_b32 a178, %0\n\t"
                   "v_accvgpr_read_b32 %1, a179\n\tv_mul_f32 %1, %3, %1\n\tv_accvgpr_write_b32 a179, %1\n\t"
                   "v_accvgpr_read_b32 %0, a180\n\tv_mul_f32 %0, %3, %0\n\tv_accvgpr_write_b32 a180, %0\n\t"
                   "v_accvgpr_read_b32 %1, a181\n\tv_mul_f32 %1, %3, %1\n\tv_accvgpr_write_b32 a181, %1\n\t"
                   "v_accvgpr_read_b32 %0, a182\n\tv_mul_f32 %0, %3, %0\n\tv_accvgpr_write_b32 a182, %0\n\t"
                   "v_accvgpr_read_b32 %1, a183\n\tv_mul_f32 %1, %3, %1\n\tv_accvgpr_write_b32 a183, %1\n\t"
                   "v_accvgpr_read_b32 %0, a184\n\tv_mul_f32 %0, %3, %0\n\tv_accvgpr_write_b32 a184, %0\n\t"
                   "v_accvgpr_read_b32 %1, a185\n\tv_mul_f32 %1, %3, %1\n\tv_accvgpr_write_b32 a185, %1\n\t"
                   "v_accvgpr_read_b32 %0, a186\n\tv_mul_f32 %0, %3, %0\n\tv_accvgpr_write_b32 a186, %0\n\t"
                   "v_accvgpr_read_b32 %1, a187\n\tv_mul_f32 %1, %3, %1\n\tv_accvgpr_write_b32 a187, %1\n\t"
                   "v_accvgpr_read_b32 %0, a188\n\tv_mul_f32 %0, %3, %0\n\tv_accvgpr_write_b32 a188, %0\n\t"
                   "v_accvgpr_read_b32 %1, a189\n\tv_mul_f32 %1, %3, %1\n\tv_accvgpr_write_b32 a189, %1\n\t"
                   "v_accvgpr_read_b32 %0, a190\n\tv_mul_f32 %0, %3, %0\n\tv_accvgpr_write_b32 a190, %0\n\t"
                   "v_accvgpr_read_b32 %1, a191\n\tv_mul_f32 %1, %3, %1\n\tv_accvgpr_write_b32 a191, %1\n\t"
                   "v_accvgpr_read_b32 %0, a192\n\tv_mul_f32 %0, %3, %0\n\tv_accvgpr_write_b32 a192, %0\n\t"
                   "v_accvgpr_read_b32 %1, a193\n\tv_mul_f32 %1, %3, %1\n\tv_accvgpr_write_b32 a193, %1\n\t"
                   "v_accvgpr_read_b32 %0, a194\n\tv_mul_f32 %0, %3, %0\n\tv_accvgpr_write_b32 a194, %0\n\t"
                   "v_accvgpr_read_b32 %1, a195\n\tv_mul_f32 %1, %3, %1\n\tv_accvgpr_write_b32 a195, %1\n\t"
                   "v_accvgpr_read_b32 %0, a196\n\tv_mul_f32 %0, %3, %0\n\tv_accvgpr_write_b32 a196, %0\n\t"
                   "v_accvgpr_read_b32 %1, a197\n\tv_mul_f32 %1, %3, %1\n\tv_accvgpr_write_b32 a197, %1\n\t"
                   "v_accvgpr_read_b32 %0, a198\n\tv_mul_f32 %0, %3, %0\n\tv_accvgpr_write_b32 a198, %0\n\t"
                   "v_accvgpr_read_b32 %1, a199\n\tv_mul_f32 %1, %3, %1\n\tv_accvgpr_write_b32 a199, %1\n\t"
                   "v_accvgpr_read_b32 %0, a200\n\tv_mul_f32 %0, %3, %0\n\tv_accvgpr_write_b32 a200, %0\n\t"
                   "v_accvgpr_read_b32 %1, a201\n\tv_mul_f32 %1, %3, %1\n\tv_accvgpr_write_b32 a201, %1\n\t"
                   "v_accvgpr_read_b32 %0, a202\n\tv_mul_f32 %0, %3, %0\n\tv_accvgpr_write_b32 a202, %0\n\t"
                   "v_accvgpr_read_b32 %1, a203\n\tv_mul_f32 %1, %3, %1\n\tv_accvgpr_write_b32 a203, %1\n\t"
                   "v_accvgpr_read_b32 %0, a204\n\tv_mul_f32 %0, %3, %0\n\tv_accvgpr_write_b32 a204, %0\n\t"
                   "v_accvgpr_read_b32 %1, a205\n\tv_mul_f32 %1, %3, %1\n\tv_accvgpr_write_b32 a205, %1\n\t"
                   "v_accvgpr_read_b32 %0, a206\n\tv_mul_f32 %0, %3, %0\n\tv_accvgpr_write_b32 a206, %0\n\t"
                   "v_accvgpr_read_b32 %1, a207\n\tv_mul_f32 %1, %3, %1\n\tv_accvgpr_write_b32 a207, %1\n\t"
                   "v_accvgpr_read_b32 %0, a208\n\tv_mul_f32 %0, %3, %0\n\tv_accvgpr_write_b32 a208, %0\n\t"
                   "v_accvgpr_read_b32 %1, a209\n\tv_mul_f32 %1, %3, %1\n\tv_accvgpr_write_b32 a209, %1\n\t"
                   "v_accvgpr_read_b32 %0, a210\n\tv_mul_f32 %0, %3, %0\n\tv_accvgpr_write_b32 a210, %0\n\t"
                   "v_accvgpr_read_b32 %1, a211\n\tv_mul_f32 %1, %3, %1\n\tv_accvgpr_write_b32 a211, %1\n\t"
                   "v_accvgpr_read_b32 %0, a212\n\tv_mul_f32 %0, %3, %0\n\tv_accvgpr_write_b32 a212, %0\n\t"
                   "v_accvgpr_read_b32 %1, a213\n\tv_mul_f32 %1, %3, %1\n\tv_accvgpr_write_b32 a213, %1\n\t"
                   "v_accvgpr_read_b32 %0, a214\n\tv_mul_f32 %0, %3, %0\n\tv_accvgpr_write_b32 a214, %0\n\t"
                   "v_accvgpr_read_b32 %1, a215\n\tv_mul_f32 %1, %3, %1\n\tv_accvgpr_write_b32 a215, %1\n\t"
                   "v_accvgpr_read_b32 %0, a216\n\tv_mul_f32 %0, %3, %0\n\tv_accvgpr_write_b32 a216, %0\n\t"
                   "v_accvgpr_read_b32 %1, a217\n\tv_mul_f32 %1, %3, %1\n\tv_accvgpr_write_b32 a217, %1\n\t"
                   "v_accvgpr_read_b32 %0, a218\n\tv_mul_f32 %0, %3, %0\n\tv_accvgpr_write_b32 a218, %0\n\t"
                   "v_accvgpr_read_b32 %1, a219\n\tv_mul_f32 %1, %3, %1\n\tv_accvgpr_write_b32 a219, %1\n\t"
                   "v_accvgpr_read_b32 %0, a220\n\tv_mul_f32 %0, %3, %0\n\tv_accvgpr_write_b32 a220, %0\n\t"
                   "v_accvgpr_read_b32 %1, a221\n\tv_mul_f32 %1, %3, %1\n\tv_accvgpr_write_b32 a221, %1\n\t"
                   "v_accvgpr_read_b32 %0, a222\n\tv_mul_f32 %0, %3, %0\n\tv_accvgpr_write_b32 a222, %0\n\t"
                   "v_accvgpr_read_b32 %1, a223\n\tv_mul_f32 %1, %3, %1\n\tv_accvgpr_write_b32 a223, %1\n\t"
                   "v_accvgpr_read_b32 %0, a224\n\tv_mul_f32 %0, %3, %0\n\tv_accvgpr_write_b32 a224, %0\n\t"
                   "v_accvgpr_read_b32 %1, a225\n\tv_mul_f32 %1, %3, %1\n\tv_accvgpr_write_b32 a225, %1\n\t"
                   "v_accvgpr_read_b32 %0, a226\n\tv_mul_f32 %0, %3, %0\n\tv_accvgpr_write_b32 a226, %0\n\t"
                   "v_accvgpr_read_b32 %1, a227\n\tv_mul_f32 %1, %3, %1\n\tv_accvgpr_write_b32 a227, %1\n\t"
                   "v_accvgpr_read_b32 %0, a228\n\tv_mul_f32 %0, %3, %0\n\tv_accvgpr_write_b32 a228, %0\n\t"
                   "v_accvgpr_read_b32 %1, a229\n\tv_mul_f32 %1, %3, %1\n\tv_accvgpr_write_b32 a229, %1\n\t"
                   "v_accvgpr_read_b32 %0, a230\n\tv_mul_f32 %0, %3, %0\n\tv_accvgpr_write_b32 a230, %0\n\t"
                   "v_accvgpr_read_b32 %1, a231\n\tv_mul_f32 %1, %3, %1\n\tv_accvgpr_write_b32 a231, %1\n\t"
                   "v_accvgpr_read_b32 %0, a232\n\tv_mul_f32 %0, %3, %0\n\tv_accvgpr_write_b32 a232, %0\n\t"
                   "v_accvgpr_read_b32 %1, a233\n\tv_mul_f32 %1, %3, %1\n\tv_accvgpr_write_b32 a233, %1\n\t"
                   "v_accvgpr_read_b32 %0, a234\n\tv_mul_f32 %0, %3, %0\n\tv_accvgpr_write_b32 a234, %0\n\t"
                   "v_accvgpr_read_b32 %1, a235\n\tv_mul_f32 %1, %3, %1\n\tv_accvgpr_write_b32 a235, %1\n\t"
                   "v_accvgpr_read_b32 %0, a236\n\tv_mul_f32 %0, %3, %0\n\tv_accvgpr_write_b32 a236, %0\n\t"
                   "v_accvgpr_read_b32 %1, a237\n\tv_mul_f32 %1, %3, %1\n\tv_accvgpr_write_b32 a237, %1\n\t"
                   "v_accvgpr_read_b32 %0, a238\n\tv_mul_f32 %0, %3, %0\n\tv_accvgpr_write_b32 a238, %0\n\t"
                   "v_accvgpr_read_b32 %1, a239\n\tv_mul_f32 %1, %3, %1\n\tv_accvgpr_write_b32 a239, %1\n\t"
                   "v_accvgpr_read_b32 %0, a240\n\tv_mul_f32 %0, %3, %0\n\tv_accvgpr_write_b32 a240, %0\n\t"
                   "v_accvgpr_read_b32 %1, a241\n\tv_mul_f32 %1, %3, %1\n\tv_accvgpr_write_b32 a241, %1\n\t"
                   "v_accvgpr_read_b32 %0, a242\n\tv_mul_f32 %0, %3, %0\n\tv_accvgpr_write_b32 a242, %0\n\t"
                   "v_accvgpr_read_b32 %1, a243\n\tv_mul_f32 %1, %3, %1\n\tv_accvgpr_write_b32 a243, %1\n\t"
                   "v_accvgpr_read_b32 %0, a244\n\tv_mul_f32 %0, %3, %0\n\tv_accvgpr_write_b32 a244, %0\n\t"
                   "v_accvgpr_read_b32 %1, a245\n\tv_mul_f32 %1, %3, %1\n\tv_accvgpr_write_b32 a245, %1\n\t"
                   "v_accvgpr_read_b32 %0, a246\n\tv_mul_f32 %0, %3, %0\n\tv_accvgpr_write_b32 a246, %0\n\t"
                   "v_accvgpr_read_b32 %1, a247\n\tv_mul_f32 %1, %3, %1\n\tv_accvgpr_write_b32 a247, %1\n\t"
                   "v_accvgpr_read_b32 %0, a248\n\tv_mul_f32 %0, %3, %0\n\tv_accvgpr_write_b32 a248, %0\n\t"
                   "v_accvgpr_read_b32 %1, a249\n\tv_mul_f32 %1, %3, %1\n\tv_accvgpr_write_b32 a249, %1\n\t"
                   "v_accvgpr_read_b32 %0, a250\n\tv_mul_f32 %0, %3, %0\n\tv_accvgpr_write_b32 a250, %0\n\t"
                   "v_accvgpr_read_b32 %1, a251\n\tv_mul_f32 %1, %3, %1\n\tv_accvgpr_write_b32 a251, %1\n\t"
                   "v_accvgpr_read_b32 %0, a252\n\tv_mul_f32 %0, %3, %0\n\tv_accvgpr_write_b32 a252, %0\n\t"
                   "v_accvgpr_read_b32 %1, a253\n\tv_mul_f32 %1, %3, %1\n\tv_accvgpr_write_b32 a253, %1\n\t"
                   "v_accvgpr_read_b32 %0, a254\n\tv_mul_f32 %0, %3, %0\n\tv_accvgpr_write_b32 a254, %0\n\t"
                   "v_accvgpr_read_b32 %1, a255\n\tv_mul_f32 %1, %3, %1\n\tv_accvgpr_write_b32 a255, %1\n\t"
                   "s_nop 1"
                   : "=&v"(t0), "=&v"(t1) : "v"(c.fct[0]), "v"(c.fct[1]) : ACLOB);
      c.pend = false;
    }
  }
  asm volatile("s_nop 1" ::: "memory");
  GAP();
}
template <int NS, int T, bool HASPV, bool HASSM> DI void phaseB(Ctx4& c) {
  phaseB_<NS, T, HASPV, HASSM>(c, std::make_integer_sequence<int, 32>{});
}
template <int NS, int... Us> DI void first_units(const Ctx4& c, std::integer_sequence<int, Us...>) { (dma_unit<NS, Us>(c), ...); }
template <int NS> DI void main_loop(Ctx4& c, const u16* __restrict__ wo, int bid, int w, int lane) {
  first_units<NS>(c, std::make_integer_sequence<int, NS>{});
  phaseA<NS, 0>(c); phaseB<NS, 0, false, true>(c);
  phaseA<NS, 1>(c); phaseB<NS, 1, true, true>(c);
  phaseA<NS, 2>(c); phaseB<NS, 2, true, true>(c);
  phaseA<NS, 3>(c); phaseB<NS, 3, true, true>(c);
  phaseA<NS, 4>(c); phaseB<NS, 4, true, true>(c);
  phaseA<NS, 5>(c); phaseB<NS, 5, true, true>(c);
  phaseA<NS, 6>(c); phaseB<NS, 6, true, true>(c);
  {
    int li = (bid * 4 + w) * 28 + (lane < 28 ? lane : 27);
    asm volatile("global_load_dword %0, %1, off" : "+v"(c.pfd) : "v"(wo + (size_t)li * 64) : "memory");
  }
  phaseA<NS, 7>(c); phaseB<NS, 7, true, true>(c);
  phaseB<NS, 8, true, false>(c);
}

template <int A, int OFF> DI void acc_dswrite4(unsigned addr) {
  asm volatile("ds_write_b128 %0, a[%c1:%c2] offset:%c3" ::"v"(addr), "i"(A), "i"(A + 3), "i"(OFF) : "memory", ACLOB);
}
template <int A> DI f32x4 acc_read4() {
  f32x4 r;
  asm volatile("v_accvgpr_read_b32 %0, a[%c4]\n\tv_accvgpr_read_b32 %1, a[%c5]\n\tv_accvgpr_read_b32 %2, a[%c6]\n\t"
               "v_accvgpr_read_b32 %3, a[%c7]"
               : "=v"(r[0]), "=v"(r[1]), "=v"(r[2]), "=v"(r[3]) : "i"(A), "i"(A + 1), "i"(A + 2), "i"(A + 3) : ACLOB);
  return r;
}

__global__ __launch_bounds__(256, 1) void attn4_kernel(const u16* __restrict__ x, const u16* __restrict__ wq,
                                                       const u16* __restrict__ kc, const u16* __restrict__ vc,
                                                       const u16* __restrict__ wo, u16* __restrict__ o,
                                                       unsigned* __restrict__ cnt DBGARG) {
  extern __shared__ __attribute__((aligned(16))) char lds[];
  STAMP(0);
  if (blockIdx.x == 0 && threadIdx.x < 128) cnt[threadIdx.x] = 0u;
  const int tid = threadIdx.x, lane = tid & 63;
  const int w = __builtin_amdgcn_readfirstlane(tid >> 6);
  const int r = lane & 31, h = lane >> 5;
  const int bid = blockIdx.x;
  const int xcd = bid & 7, j = bid >> 3;
  const int grp = (j >> 2) * 8 + xcd, sub = j & 3;
  const int b = grp >> 3, kvh = grp & 7;
  const int head = kvh * 2 + (sub >> 1), rhalf = sub & 1;
  const int row0 = b * SQ + rhalf * 56;

  const u16* Kw = kc + ((size_t)(b * HKV + kvh) * SK + 256 * w) * DH;
  const u16* Vw = vc + ((size_t)(b * HKV + kvh) * SK + 256 * w) * DH;

  Ctx4 c;
  c.pfd = 0; c.sub = sub;
  c.Kp = Kw; c.Vp = Vw; c.pfoff = lane * 128;
  c.ring = lds + w * 40960;
  c.rk = __builtin_amdgcn_make_buffer_rsrc((void*)Kw, 0, 256 * DH * 2, 0x00020000);
  c.rv = __builtin_amdgcn_make_buffer_rsrc((void*)Vw, 0, 256 * DH * 2, 0x00020000);
#pragma unroll
  for (int pi = 0; pi < 4; ++pi) {
    const int krow = 4 * pi + (lane >> 4);
    c.koff[pi] = krow * 512 + (((lane & 15) ^ (krow & 15)) * 16);
  }
#pragma unroll
  for (int pi = 0; pi < 2; ++pi) {
    const int vrow = 2 * pi + (lane >> 5);
    c.voff[pi] = vrow * 512 + (((lane & 31) ^ ((vrow & 3) << 2)) * 16);
  }
  {
    __amdgpu_buffer_rsrc_t rx =
        __builtin_amdgcn_make_buffer_rsrc((void*)(x + (size_t)row0 * DM), 0, 56 * DM * 2, 0x00020000);
#pragma unroll
    for (int p = 0; p < 14; ++p) {
      const int ci = 64 * p + lane;
      const int lr = ci / 56, cp = ci - lr * 56;
      int gr = 16 * w + lr; gr = gr > 55 ? 55 : gr;
      const int cs = (cp & ~7) | ((cp ^ (lr >> 1)) & 7);
      __builtin_amdgcn_raw_ptr_buffer_load_lds(rx, (lds_vp)(c.ring + 3 * 8192 + p * 1024), 16, gr * (DM * 2) + cs * 16,
                                               0, 0, 0);
    }
  }
  {
    const int colw = head * DH + 64 * w;
    __amdgpu_buffer_rsrc_t rw =
        __builtin_amdgcn_make_buffer_rsrc((void*)(wq + colw), 0, (DM * NQ - colw) * 2, 0x00020000);
    const int drow = lane >> 3;
    const int wvoff = drow * (NQ * 2) + (((lane & 7) ^ (((drow >> 1) & 1) << 2)) * 16);
    auto wissue = [&](int ch) {
#pragma unroll
      for (int p = 0; p < 8; ++p)
        __builtin_amdgcn_raw_ptr_buffer_load_lds(rw, (lds_vp)(c.ring + ((ch + 2) % 3) * 8192 + p * 1024), 16, wvoff,
                                                 (64 * ch + 8 * p) * (NQ * 2), 0, 0);
    };
    wissue(0); wissue(1); wissue(2);
    const int g = lane >> 4, qq = (lane >> 2) & 3, pp = lane & 3;
    const unsigned trb = (unsigned)(size_t)c.ring + (8 * (g >> 1) + qq) * 128 + ((32 * (g & 1) + 8 * pp) ^ ((qq >> 1) << 6));
    const char* xa[2][4];
#pragma unroll
    for (int qb = 0; qb < 2; ++qb) {
      const int R = 32 * qb + r, lr = R & 15;
#pragma unroll
      for (int v = 0; v < 4; ++v)
        xa[qb][v] = lds + (R >> 4) * 40960 + 3 * 8192 + lr * 896 + ((((2 * v + h) ^ (lr >> 1)) & 7) * 16);
    }
    f32x16 qa[2][2];
#pragma unroll
    for (int a2 = 0; a2 < 2; ++a2)
#pragma unroll
      for (int b2 = 0; b2 < 2; ++b2)
#pragma unroll
        for (int i = 0; i < 16; ++i) qa[a2][b2][i] = 0.f;
    s16x4 fa[2][4];
    bf16x8 fx[2][2];
    auto rdstep = [&](int i, int bsel) {
      const int ch = i >> 2, s4 = i & 3;
      const unsigned tb = trb + ((ch + 2) % 3) * 8192 + s4 * 2048;
      fa[bsel][0] = trread(tb);
      fa[bsel][1] = trread(tb + 512);
      fa[bsel][2] = trread(tb ^ 64);
      fa[bsel][3] = trread((tb + 512) ^ 64);
      asm volatile("ds_read_b128 %0, %1" : "=v"(fx[bsel][0]) : "v"((unsigned)(size_t)(xa[0][s4] + ch * 128)) : "memory");
      asm volatile("ds_read_b128 %0, %1" : "=v"(fx[bsel][1]) : "v"((unsigned)(size_t)(xa[1][s4] + ch * 128)) : "memory");
    };
    auto refill_piece = [&](int k, int p) {
      if (k <= 3) {
        __builtin_amdgcn_raw_ptr_buffer_load_lds(rw, (lds_vp)(c.ring + ((k + 5) % 3) * 8192 + p * 1024), 16, wvoff,
                                                 (64 * (k + 3) + 8 * p) * (NQ * 2), 0, 0);
      }
    };
    WAITV(16);
    __builtin_amdgcn_s_barrier();
    rdstep(0, 0);
#pragma unroll
    for (int i = 0; i < 28; ++i) {
      const int bs = i & 1, ch = i >> 2, s4 = i & 3;
      if (i + 1 < 28) {
        if (s4 == 3) { if (ch == 0) WAITV(8); else if (ch <= 4) WAITV(6); else WAITV(0); }
        rdstep(i + 1, bs ^ 1);
        asm volatile("s_waitcnt lgkmcnt(6)" : "+v"(fa[bs][0]), "+v"(fa[bs][1]), "+v"(fa[bs][2]), "+v"(fa[bs][3]), "+v"(fx[bs][0]), "+v"(fx[bs][1])::"memory");
      } else {
        asm volatile("s_waitcnt lgkmcnt(0)" : "+v"(fa[bs][0]), "+v"(fa[bs][1]), "+v"(fa[bs][2]), "+v"(fa[bs][3]), "+v"(fx[bs][0]), "+v"(fx[bs][1])::"memory");
      }
      bf16x8 A0 = cat8(fa[bs][0], fa[bs][1]), A1 = cat8(fa[bs][2], fa[bs][3]);
      qa[0][0] = __builtin_amdgcn_mfma_f32_32x32x16_bf16(A0, fx[bs][0], qa[0][0], 0, 0, 0);
      GAP();
      if (ch >= 1) {
        if (ch - 1 <= 3) refill_piece(ch - 1, 2 * s4);
      }
      GAP();
      qa[0][1] = __builtin_amdgcn_mfma_f32_32x32x16_bf16(A1, fx[bs][0], qa[0][1], 0, 0, 0);
      qa[1][0] = __builtin_amdgcn_mfma_f32_32x32x16_bf16(A0, fx[bs][1], qa[1][0], 0, 0, 0);
      GAP();
      if (ch >= 1) {
        if (ch - 1 <= 3) refill_piece(ch - 1, 2 * s4 + 1);
      }
      GAP();
      qa[1][1] = __builtin_amdgcn_mfma_f32_32x32x16_bf16(A1, fx[bs][1], qa[1][1], 0, 0, 0);
    }
    STAMP(12);
    __builtin_amdgcn_s_barrier();
#pragma unroll
    for (int qb = 0; qb < 2; ++qb) {
      const int R = 32 * qb + r;
      char* base = lds + (R >> 4) * 40960 + 4 * 8192 + (R & 15) * 512 + 8 * h;
#pragma unroll
      for (int cb = 0; cb < 2; ++cb)
#pragma unroll
        for (int gg = 0; gg < 4; ++gg) {
          u32x2 v = {cvtpk(qa[qb][cb][4 * gg], qa[qb][cb][4 * gg + 1]), cvtpk(qa[qb][cb][4 * gg + 2], qa[qb][cb][4 * gg + 3])};
          *(u32x2*)(base + (((8 * w + 4 * cb + gg) ^ (R & 15)) * 16)) = v;
        }
    }
    WAITL0();
    __builtin_amdgcn_s_barrier();
  }
  {
#pragma unroll
    for (int qb = 0; qb < 2; ++qb) {
      const int R = 32 * qb + r;
      const char* base = lds + (R >> 4) * 40960 + 4 * 8192 + (R & 15) * 512;
#pragma unroll
      for (int s = 0; s < 16; ++s) c.qf[qb][s] = *(const bf16x8*)(base + (((2 * s + h) ^ (R & 15)) * 16));
    }
#pragma unroll
    for (int qb = 0; qb < 2; ++qb)
#pragma unroll
      for (int s = 0; s < 16; ++s) asm volatile("" : "+v"(c.qf[qb][s]));
  }
  WAITL0();
  __builtin_amdgcn_s_barrier();
  {
    int off = 0;
#pragma unroll
    for (int v = 0; v < 3; ++v) if (v < w) off += (v == sub) ? 65536 : 32768;
    c.ring = lds + off;
  }
  STAMP(1);

  {
    const unsigned rb = (unsigned)(size_t)c.ring;
#pragma unroll
    for (int ss = 0; ss < 8; ++ss) c.kaddr[ss] = rb + r * 256 + ((((2 * ss + h) ^ (r & 15))) * 16);
    const int g = lane >> 4, qq = (lane >> 2) & 3, pp = lane & 3;
#pragma unroll
    for (int d = 0; d < 4; ++d)
      c.vaddr[d] = rb + (4 * (g >> 1) + qq) * 512 + ((64 * d + 32 * (g & 1) + 8 * pp) ^ (qq << 6));
  }
  c.pend = false;
  c.fct[0] = 1.f; c.fct[1] = 1.f;

  if (w == sub) main_loop<8>(c, wo, bid, w, lane); else main_loop<4>(c, wo, bid, w, lane);
  STAMP(9);
  asm volatile("s_nop 7\n\ts_nop 7" ::: "memory");
  STAMP(13);

  __syncthreads();
  float* ml = (float*)lds;
#pragma unroll
  for (int qb = 0; qb < 2; ++qb) {
    auto rr = __builtin_amdgcn_permlane32_swap(__float_as_uint(c.l[qb]), __float_as_uint(c.l[qb]), false, false);
    const float lt = __uint_as_float(rr[0]) + __uint_as_float(rr[1]);
    if (h == 0) {
      ml[((w * 2 + qb) * 32 + r) * 2 + 0] = c.m[qb];
      ml[((w * 2 + qb) * 32 + r) * 2 + 1] = lt;
    }
  }
  __syncthreads();
  float fw[2][4];
#pragma unroll
  for (int qb = 0; qb < 2; ++qb) {
    float mm[4], ll[4];
#pragma unroll
    for (int ww = 0; ww < 4; ++ww) {
      mm[ww] = ml[((ww * 2 + qb) * 32 + r) * 2 + 0];
      ll[ww] = ml[((ww * 2 + qb) * 32 + r) * 2 + 1];
    }
    const float M = fmaxf(fmaxf(mm[0], mm[1]), fmaxf(mm[2], mm[3]));
    float e[4], L = 0.f;
#pragma unroll
    for (int ww = 0; ww < 4; ++ww) { e[ww] = __builtin_amdgcn_exp2f(mm[ww] - M); L += e[ww] * ll[ww]; }
    const float inv = 1.f / L;
#pragma unroll
    for (int ww = 0; ww < 4; ++ww) fw[qb][ww] = e[ww] * inv;
  }
  STAMP(14);
  const unsigned xw = (unsigned)(size_t)lds + 4096 + lane * 16;
  const f32x4* xr = (const f32x4*)(lds + 4096) + lane;
  char* otile = lds + 102400;
  {
    switch (w) {
      case 0: {
        acc_dswrite4<32, 0>(xw + 0);
        acc_dswrite4<36, 1024>(xw + 0);
        acc_dswrite4<40, 2048>(xw + 0);
        acc_dswrite4<44, 3072>(xw + 0);
        acc_dswrite4<160, 4096>(xw + 0);
        acc_dswrite4<164, 5120>(xw + 0);
        acc_dswrite4<168, 6144>(xw + 0);
        acc_dswrite4<172, 7168>(xw + 0);
        acc_dswrite4<64, 8192>(xw + 0);
        acc_dswrite4<68, 9216>(xw + 0);
        acc_dswrite4<72, 10240>(xw + 0);
        acc_dswrite4<76, 11264>(xw + 0);
        acc_dswrite4<192, 12288>(xw + 0);
        acc_dswrite4<196, 13312>(xw + 0);
        acc_dswrite4<200, 14336>(xw + 0);
        acc_dswrite4<204, 15360>(xw + 0);
        acc_dswrite4<96, 16384>(xw + 0);
        acc_dswrite4<100, 17408>(xw + 0);
        acc_dswrite4<104, 18432>(xw + 0);
        acc_dswrite4<108, 19456>(xw + 0);
        acc_dswrite4<224, 20480>(xw + 0);
        acc_dswrite4<228, 21504>(xw + 0);
        acc_dswrite4<232, 22528>(xw + 0);
        acc_dswrite4<236, 23552>(xw + 0);
      } break;
      case 1: {
        acc_dswrite4<0, 24576>(xw + 0);
        acc_dswrite4<4, 25600>(xw + 0);
        acc_dswrite4<8, 26624>(xw + 0);
        acc_dswrite4<12, 27648>(xw + 0);
        acc_dswrite4<128, 28672>(xw + 0);
        acc_dswrite4<132, 29696>(xw + 0);
        acc_dswrite4<136, 30720>(xw + 0);
        acc_dswrite4<140, 31744>(xw + 0);
        acc_dswrite4<64, 32768>(xw + 0);
        acc_dswrite4<68, 33792>(xw + 0);
        acc_dswrite4<72, 34816>(xw + 0);
        acc_dswrite4<76, 35840>(xw + 0);
        acc_dswrite4<192, 36864>(xw + 0);
        acc_dswrite4<196, 37888>(xw + 0);
        acc_dswrite4<200, 38912>(xw + 0);
        acc_dswrite4<204, 39936>(xw + 0);
        acc_dswrite4<96, 40960>(xw + 0);
        acc_dswrite4<100, 41984>(xw + 0);
        acc_dswrite4<104, 43008>(xw + 0);
        acc_dswrite4<108, 44032>(xw + 0);
        acc_dswrite4<224, 45056>(xw + 0);
        acc_dswrite4<228, 46080>(xw + 0);
        acc_dswrite4<232, 47104>(xw + 0);
        acc_dswrite4<236, 48128>(xw + 0);
      } break;
      case 2: {
        acc_dswrite4<0, 49152>(xw + 0);
        acc_dswrite4<4, 50176>(xw + 0);
        acc_dswrite4<8, 51200>(xw + 0);
        acc_dswrite4<12, 52224>(xw + 0);
        acc_dswrite4<128, 53248>(xw + 0);
        acc_dswrite4<132, 54272>(xw + 0);
        acc_dswrite4<136, 55296>(xw + 0);
        acc_dswrite4<140, 56320>(xw + 0);
        acc_dswrite4<32, 57344>(xw + 0);
        acc_dswrite4<36, 58368>(xw + 0);
        acc_dswrite4<40, 59392>(xw + 0);
        acc_dswrite4<44, 60416>(xw + 0);
        acc_dswrite4<160, 61440>(xw + 0);
        acc_dswrite4<164, 62464>(xw + 0);
        acc_dswrite4<168, 63488>(xw + 0);
        acc_dswrite4<172, 64512>(xw + 0);
        acc_dswrite4<96, 0>(xw + 65536);
        acc_dswrite4<100, 1024>(xw + 65536);
        acc_dswrite4<104, 2048>(xw + 65536);
        acc_dswrite4<108, 3072>(xw + 65536);
        acc_dswrite4<224, 4096>(xw + 65536);
        acc_dswrite4<228, 5120>(xw + 65536);
        acc_dswrite4<232, 6144>(xw + 65536);
        acc_dswrite4<236, 7168>(xw + 65536);
      } break;
      case 3: {
        acc_dswrite4<0, 8192>(xw + 65536);
        acc_dswrite4<4, 9216>(xw + 65536);
        acc_dswrite4<8, 10240>(xw + 65536);
        acc_dswrite4<12, 11264>(xw + 65536);
        acc_dswrite4<128, 12288>(xw + 65536);
        acc_dswrite4<132, 13312>(xw + 65536);
        acc_dswrite4<136, 14336>(xw + 65536);
        acc_dswrite4<140, 15360>(xw + 65536);
        acc_dswrite4<32, 16384>(xw + 65536);
        acc_dswrite4<36, 17408>(xw + 65536);
        acc_dswrite4<40, 18432>(xw + 65536);
        acc_dswrite4<44, 19456>(xw + 65536);
        acc_dswrite4<160, 20480>(xw + 65536);
        acc_dswrite4<164, 21504>(xw + 65536);
        acc_dswrite4<168, 22528>(xw + 65536);
        acc_dswrite4<172, 23552>(xw + 65536);
        acc_dswrite4<64, 24576>(xw + 65536);
        acc_dswrite4<68, 25600>(xw + 65536);
        acc_dswrite4<72, 26624>(xw + 65536);
        acc_dswrite4<76, 27648>(xw + 65536);
        acc_dswrite4<192, 28672>(xw + 65536);
        acc_dswrite4<196, 29696>(xw + 65536);
        acc_dswrite4<200, 30720>(xw + 65536);
        acc_dswrite4<204, 31744>(xw + 65536);
      } break;
    }
    WAITL0();
    __builtin_amdgcn_s_barrier();
    switch (w) {
      case 0: {
        { f32x4 sum = acc_read4<0>(); sum *= fw[0][0];
          sum += xr[1536] * fw[0][1];
          sum += xr[3072] * fw[0][2];
          sum += xr[4608] * fw[0][3];
          u32x2 v = {cvtpk(sum[0], sum[1]), cvtpk(sum[2], sum[3])};
          *(u32x2*)(otile + (0 + r) * 512 + (((0) ^ ((0 + r) & 31)) * 16) + 8 * h) = v; }
        { f32x4 sum = acc_read4<4>(); sum *= fw[0][0];
          sum += xr[1600] * fw[0][1];
          sum += xr[3136] * fw[0][2];
          sum += xr[4672] * fw[0][3];
          u32x2 v = {cvtpk(sum[0], sum[1]), cvtpk(sum[2], sum[3])};
          *(u32x2*)(otile + (0 + r) * 512 + (((1) ^ ((0 + r) & 31)) * 16) + 8 * h) = v; }
        { f32x4 sum = acc_read4<8>(); sum *= fw[0][0];
          sum += xr[1664] * fw[0][1];
          sum += xr[3200] * fw[0][2];
          sum += xr[4736] * fw[0][3];
          u32x2 v = {cvtpk(sum[0], sum[1]), cvtpk(sum[2], sum[3])};
          *(u32x2*)(otile + (0 + r) * 512 + (((2) ^ ((0 + r) & 31)) * 16) + 8 * h) = v; }
        { f32x4 sum = acc_read4<12>(); sum *= fw[0][0];
          sum += xr[1728] * fw[0][1];
          sum += xr[3264] * fw[0][2];
          sum += xr[4800] * fw[0][3];
          u32x2 v = {cvtpk(sum[0], sum[1]), cvtpk(sum[2], sum[3])};
          *(u32x2*)(otile + (0 + r) * 512 + (((3) ^ ((0 + r) & 31)) * 16) + 8 * h) = v; }
        { f32x4 sum = acc_read4<128>(); sum *= fw[1][0];
          sum += xr[1792] * fw[1][1];
          sum += xr[3328] * fw[1][2];
          sum += xr[4864] * fw[1][3];
          u32x2 v = {cvtpk(sum[0], sum[1]), cvtpk(sum[2], sum[3])};
          *(u32x2*)(otile + (32 + r) * 512 + (((0) ^ ((32 + r) & 31)) * 16) + 8 * h) = v; }
        { f32x4 sum = acc_read4<132>(); sum *= fw[1][0];
          sum += xr[1856] * fw[1][1];
          sum += xr[3392] * fw[1][2];
          sum += xr[4928] * fw[1][3];
          u32x2 v = {cvtpk(sum[0], sum[1]), cvtpk(sum[2], sum[3])};
          *(u32x2*)(otile + (32 + r) * 512 + (((1) ^ ((32 + r) & 31)) * 16) + 8 * h) = v; }
        { f32x4 sum = acc_read4<136>(); sum *= fw[1][0];
          sum += xr[1920] * fw[1][1];
          sum += xr[3456] * fw[1][2];
          sum += xr[4992] * fw[1][3];
          u32x2 v = {cvtpk(sum[0], sum[1]), cvtpk(sum[2], sum[3])};
          *(u32x2*)(otile + (32 + r) * 512 + (((2) ^ ((32 + r) & 31)) * 16) + 8 * h) = v; }
        { f32x4 sum = acc_read4<140>(); sum *= fw[1][0];
          sum += xr[1984] * fw[1][1];
          sum += xr[3520] * fw[1][2];
          sum += xr[5056] * fw[1][3];
          u32x2 v = {cvtpk(sum[0], sum[1]), cvtpk(sum[2], sum[3])};
          *(u32x2*)(otile + (32 + r) * 512 + (((3) ^ ((32 + r) & 31)) * 16) + 8 * h) = v; }
      } break;
      case 1: {
        { f32x4 sum = acc_read4<32>(); sum *= fw[0][1];
          sum += xr[0] * fw[0][0];
          sum += xr[3584] * fw[0][2];
          sum += xr[5120] * fw[0][3];
          u32x2 v = {cvtpk(sum[0], sum[1]), cvtpk(sum[2], sum[3])};
          *(u32x2*)(otile + (0 + r) * 512 + (((8) ^ ((0 + r) & 31)) * 16) + 8 * h) = v; }
        { f32x4 sum = acc_read4<36>(); sum *= fw[0][1];
          sum += xr[64] * fw[0][0];
          sum += xr[3648] * fw[0][2];
          sum += xr[5184] * fw[0][3];
          u32x2 v = {cvtpk(sum[0], sum[1]), cvtpk(sum[2], sum[3])};
          *(u32x2*)(otile + (0 + r) * 512 + (((9) ^ ((0 + r) & 31)) * 16) + 8 * h) = v; }
        { f32x4 sum = acc_read4<40>(); sum *= fw[0][1];
          sum += xr[128] * fw[0][0];
          sum += xr[3712] * fw[0][2];
          sum += xr[5248] * fw[0][3];
          u32x2 v = {cvtpk(sum[0], sum[1]), cvtpk(sum[2], sum[3])};
          *(u32x2*)(otile + (0 + r) * 512 + (((10) ^ ((0 + r) & 31)) * 16) + 8 * h) = v; }
        { f32x4 sum = acc_read4<44>(); sum *= fw[0][1];
          sum += xr[192] * fw[0][0];
          sum += xr[3776] * fw[0][2];
          sum += xr[5312] * fw[0][3];
          u32x2 v = {cvtpk(sum[0], sum[1]), cvtpk(sum[2], sum[3])};
          *(u32x2*)(otile + (0 + r) * 512 + (((11) ^ ((0 + r) & 31)) * 16) + 8 * h) = v; }
        { f32x4 sum = acc_read4<160>(); sum *= fw[1][1];
          sum += xr[256] * fw[1][0];
          sum += xr[3840] * fw[1][2];
          sum += xr[5376] * fw[1][3];
          u32x2 v = {cvtpk(sum[0], sum[1]), cvtpk(sum[2], sum[3])};
          *(u32x2*)(otile + (32 + r) * 512 + (((8) ^ ((32 + r) & 31)) * 16) + 8 * h) = v; }
        { f32x4 sum = acc_read4<164>(); sum *= fw[1][1];
          sum += xr[320] * fw[1][0];
          sum += xr[3904] * fw[1][2];
          sum += xr[5440] * fw[1][3];
          u32x2 v = {cvtpk(sum[0], sum[1]), cvtpk(sum[2], sum[3])};
          *(u32x2*)(otile + (32 + r) * 512 + (((9) ^ ((32 + r) & 31)) * 16) + 8 * h) = v; }
        { f32x4 sum = acc_read4<168>(); sum *= fw[1][1];
          sum += xr[384] * fw[1][0];
          sum += xr[3968] * fw[1][2];
          sum += xr[5504] * fw[1][3];
          u32x2 v = {cvtpk(sum[0], sum[1]), cvtpk(sum[2], sum[3])};
          *(u32x2*)(otile + (32 + r) * 512 + (((10) ^ ((32 + r) & 31)) * 16) + 8 * h) = v; }
        { f32x4 sum = acc_read4<172>(); sum *= fw[1][1];
          sum += xr[448] * fw[1][0];
          sum += xr[4032] * fw[1][2];
          sum += xr[5568] * fw[1][3];
          u32x2 v = {cvtpk(sum[0], sum[1]), cvtpk(sum[2], sum[3])};
          *(u32x2*)(otile + (32 + r) * 512 + (((11) ^ ((32 + r) & 31)) * 16) + 8 * h) = v; }
      } break;
      case 2: {
        { f32x4 sum = acc_read4<64>(); sum *= fw[0][2];
          sum += xr[512] * fw[0][0];
          sum += xr[2048] * fw[0][1];
          sum += xr[5632] * fw[0][3];
          u32x2 v = {cvtpk(sum[0], sum[1]), cvtpk(sum[2], sum[3])};
          *(u32x2*)(otile + (0 + r) * 512 + (((16) ^ ((0 + r) & 31)) * 16) + 8 * h) = v; }
        { f32x4 sum = acc_read4<68>(); sum *= fw[0][2];
          sum += xr[576] * fw[0][0];
          sum += xr[2112] * fw[0][1];
          sum += xr[5696] * fw[0][3];
          u32x2 v = {cvtpk(sum[0], sum[1]), cvtpk(sum[2], sum[3])};
          *(u32x2*)(otile + (0 + r) * 512 + (((17) ^ ((0 + r) & 31)) * 16) + 8 * h) = v; }
        { f32x4 sum = acc_read4<72>(); sum *= fw[0][2];
          sum += xr[640] * fw[0][0];
          sum += xr[2176] * fw[0][1];
          sum += xr[5760] * fw[0][3];
          u32x2 v = {cvtpk(sum[0], sum[1]), cvtpk(sum[2], sum[3])};
          *(u32x2*)(otile + (0 + r) * 512 + (((18) ^ ((0 + r) & 31)) * 16) + 8 * h) = v; }
        { f32x4 sum = acc_read4<76>(); sum *= fw[0][2];
          sum += xr[704] * fw[0][0];
          sum += xr[2240] * fw[0][1];
          sum += xr[5824] * fw[0][3];
          u32x2 v = {cvtpk(sum[0], sum[1]), cvtpk(sum[2], sum[3])};
          *(u32x2*)(otile + (0 + r) * 512 + (((19) ^ ((0 + r) & 31)) * 16) + 8 * h) = v; }
        { f32x4 sum = acc_read4<192>(); sum *= fw[1][2];
          sum += xr[768] * fw[1][0];
          sum += xr[2304] * fw[1][1];
          sum += xr[5888] * fw[1][3];
          u32x2 v = {cvtpk(sum[0], sum[1]), cvtpk(sum[2], sum[3])};
          *(u32x2*)(otile + (32 + r) * 512 + (((16) ^ ((32 + r) & 31)) * 16) + 8 * h) = v; }
        { f32x4 sum = acc_read4<196>(); sum *= fw[1][2];
          sum += xr[832] * fw[1][0];
          sum += xr[2368] * fw[1][1];
          sum += xr[5952] * fw[1][3];
          u32x2 v = {cvtpk(sum[0], sum[1]), cvtpk(sum[2], sum[3])};
          *(u32x2*)(otile + (32 + r) * 512 + (((17) ^ ((32 + r) & 31)) * 16) + 8 * h) = v; }
        { f32x4 sum = acc_read4<200>(); sum *= fw[1][2];
          sum += xr[896] * fw[1][0];
          sum += xr[2432] * fw[1][1];
          sum += xr[6016] * fw[1][3];
          u32x2 v = {cvtpk(sum[0], sum[1]), cvtpk(sum[2], sum[3])};
          *(u32x2*)(otile + (32 + r) * 512 + (((18) ^ ((32 + r) & 31)) * 16) + 8 * h) = v; }
        { f32x4 sum = acc_read4<204>(); sum *= fw[1][2];
          sum += xr[960] * fw[1][0];
          sum += xr[2496] * fw[1][1];
          sum += xr[6080] * fw[1][3];
          u32x2 v = {cvtpk(sum[0], sum[1]), cvtpk(sum[2], sum[3])};
          *(u32x2*)(otile + (32 + r) * 512 + (((19) ^ ((32 + r) & 31)) * 16) + 8 * h) = v; }
      } break;
      case 3: {
        { f32x4 sum = acc_read4<96>(); sum *= fw[0][3];
          sum += xr[1024] * fw[0][0];
          sum += xr[2560] * fw[0][1];
          sum += xr[4096] * fw[0][2];
          u32x2 v = {cvtpk(sum[0], sum[1]), cvtpk(sum[2], sum[3])};
          *(u32x2*)(otile + (0 + r) * 512 + (((24) ^ ((0 + r) & 31)) * 16) + 8 * h) = v; }
        { f32x4 sum = acc_read4<100>(); sum *= fw[0][3];
          sum += xr[1088] * fw[0][0];
          sum += xr[2624] * fw[0][1];
          sum += xr[4160] * fw[0][2];
          u32x2 v = {cvtpk(sum[0], sum[1]), cvtpk(sum[2], sum[3])};
          *(u32x2*)(otile + (0 + r) * 512 + (((25) ^ ((0 + r) & 31)) * 16) + 8 * h) = v; }
        { f32x4 sum = acc_read4<104>(); sum *= fw[0][3];
          sum += xr[1152] * fw[0][0];
          sum += xr[2688] * fw[0][1];
          sum += xr[4224] * fw[0][2];
          u32x2 v = {cvtpk(sum[0], sum[1]), cvtpk(sum[2], sum[3])};
          *(u32x2*)(otile + (0 + r) * 512 + (((26) ^ ((0 + r) & 31)) * 16) + 8 * h) = v; }
        { f32x4 sum = acc_read4<108>(); sum *= fw[0][3];
          sum += xr[1216] * fw[0][0];
          sum += xr[2752] * fw[0][1];
          sum += xr[4288] * fw[0][2];
          u32x2 v = {cvtpk(sum[0], sum[1]), cvtpk(sum[2], sum[3])};
          *(u32x2*)(otile + (0 + r) * 512 + (((27) ^ ((0 + r) & 31)) * 16) + 8 * h) = v; }
        { f32x4 sum = acc_read4<224>(); sum *= fw[1][3];
          sum += xr[1280] * fw[1][0];
          sum += xr[2816] * fw[1][1];
          sum += xr[4352] * fw[1][2];
          u32x2 v = {cvtpk(sum[0], sum[1]), cvtpk(sum[2], sum[3])};
          *(u32x2*)(otile + (32 + r) * 512 + (((24) ^ ((32 + r) & 31)) * 16) + 8 * h) = v; }
        { f32x4 sum = acc_read4<228>(); sum *= fw[1][3];
          sum += xr[1344] * fw[1][0];
          sum += xr[2880] * fw[1][1];
          sum += xr[4416] * fw[1][2];
          u32x2 v = {cvtpk(sum[0], sum[1]), cvtpk(sum[2], sum[3])};
          *(u32x2*)(otile + (32 + r) * 512 + (((25) ^ ((32 + r) & 31)) * 16) + 8 * h) = v; }
        { f32x4 sum = acc_read4<232>(); sum *= fw[1][3];
          sum += xr[1408] * fw[1][0];
          sum += xr[2944] * fw[1][1];
          sum += xr[4480] * fw[1][2];
          u32x2 v = {cvtpk(sum[0], sum[1]), cvtpk(sum[2], sum[3])};
          *(u32x2*)(otile + (32 + r) * 512 + (((26) ^ ((32 + r) & 31)) * 16) + 8 * h) = v; }
        { f32x4 sum = acc_read4<236>(); sum *= fw[1][3];
          sum += xr[1472] * fw[1][0];
          sum += xr[3008] * fw[1][1];
          sum += xr[4544] * fw[1][2];
          u32x2 v = {cvtpk(sum[0], sum[1]), cvtpk(sum[2], sum[3])};
          *(u32x2*)(otile + (32 + r) * 512 + (((27) ^ ((32 + r) & 31)) * 16) + 8 * h) = v; }
      } break;
    }
    WAITL0();
    __builtin_amdgcn_s_barrier();
  }
  {
    switch (w) {
      case 0: {
        acc_dswrite4<48, 0>(xw + 0);
        acc_dswrite4<52, 1024>(xw + 0);
        acc_dswrite4<56, 2048>(xw + 0);
        acc_dswrite4<60, 3072>(xw + 0);
        acc_dswrite4<176, 4096>(xw + 0);
        acc_dswrite4<180, 5120>(xw + 0);
        acc_dswrite4<184, 6144>(xw + 0);
        acc_dswrite4<188, 7168>(xw + 0);
        acc_dswrite4<80, 8192>(xw + 0);
        acc_dswrite4<84, 9216>(xw + 0);
        acc_dswrite4<88, 10240>(xw + 0);
        acc_dswrite4<92, 11264>(xw + 0);
        acc_dswrite4<208, 12288>(xw + 0);
        acc_dswrite4<212, 13312>(xw + 0);
        acc_dswrite4<216, 14336>(xw + 0);
        acc_dswrite4<220, 15360>(xw + 0);
        acc_dswrite4<112, 16384>(xw + 0);
        acc_dswrite4<116, 17408>(xw + 0);
        acc_dswrite4<120, 18432>(xw + 0);
        acc_dswrite4<124, 19456>(xw + 0);
        acc_dswrite4<240, 20480>(xw + 0);
        acc_dswrite4<244, 21504>(xw + 0);
        acc_dswrite4<248, 22528>(xw + 0);
        acc_dswrite4<252, 23552>(xw + 0);
      } break;
      case 1: {
        acc_dswrite4<16, 24576>(xw + 0);
        acc_dswrite4<20, 25600>(xw + 0);
        acc_dswrite4<24, 26624>(xw + 0);
        acc_dswrite4<28, 27648>(xw + 0);
        acc_dswrite4<144, 28672>(xw + 0);
        acc_dswrite4<148, 29696>(xw + 0);
        acc_dswrite4<152, 30720>(xw + 0);
        acc_dswrite4<156, 31744>(xw + 0);
        acc_dswrite4<80, 32768>(xw + 0);
        acc_dswrite4<84, 33792>(xw + 0);
        acc_dswrite4<88, 34816>(xw + 0);
        acc_dswrite4<92, 35840>(xw + 0);
        acc_dswrite4<208, 36864>(xw + 0);
        acc_dswrite4<212, 37888>(xw + 0);
        acc_dswrite4<216, 38912>(xw + 0);
        acc_dswrite4<220, 39936>(xw + 0);
        acc_dswrite4<112, 40960>(xw + 0);
        acc_dswrite4<116, 41984>(xw + 0);
        acc_dswrite4<120, 43008>(xw + 0);
        acc_dswrite4<124, 44032>(xw + 0);
        acc_dswrite4<240, 45056>(xw + 0);
        acc_dswrite4<244, 46080>(xw + 0);
        acc_dswrite4<248, 47104>(xw + 0);
        acc_dswrite4<252, 48128>(xw + 0);
      } break;
      case 2: {
        acc_dswrite4<16, 49152>(xw + 0);
        acc_dswrite4<20, 50176>(xw + 0);
        acc_dswrite4<24, 51200>(xw + 0);
        acc_dswrite4<28, 52224>(xw + 0);
        acc_dswrite4<144, 53248>(xw + 0);
        acc_dswrite4<148, 54272>(xw + 0);
        acc_dswrite4<152, 55296>(xw + 0);
        acc_dswrite4<156, 56320>(xw + 0);
        acc_dswrite4<48, 57344>(xw + 0);
        acc_dswrite4<52, 58368>(xw + 0);
        acc_dswrite4<56, 59392>(xw + 0);
        acc_dswrite4<60, 60416>(xw + 0);
        acc_dswrite4<176, 61440>(xw + 0);
        acc_dswrite4<180, 62464>(xw + 0);
        acc_dswrite4<184, 63488>(xw + 0);
        acc_dswrite4<188, 64512>(xw + 0);
        acc_dswrite4<112, 0>(xw + 65536);
        acc_dswrite4<116, 1024>(xw + 65536);
        acc_dswrite4<120, 2048>(xw + 65536);
        acc_dswrite4<124, 3072>(xw + 65536);
        acc_dswrite4<240, 4096>(xw + 65536);
        acc_dswrite4<244, 5120>(xw + 65536);
        acc_dswrite4<248, 6144>(xw + 65536);
        acc_dswrite4<252, 7168>(xw + 65536);
      } break;
      case 3: {
        acc_dswrite4<16, 8192>(xw + 65536);
        acc_dswrite4<20, 9216>(xw + 65536);
        acc_dswrite4<24, 10240>(xw + 65536);
        acc_dswrite4<28, 11264>(xw + 65536);
        acc_dswrite4<144, 12288>(xw + 65536);
        acc_dswrite4<148, 13312>(xw + 65536);
        acc_dswrite4<152, 14336>(xw + 65536);
        acc_dswrite4<156, 15360>(xw + 65536);
        acc_dswrite4<48, 16384>(xw + 65536);
        acc_dswrite4<52, 17408>(xw + 65536);
        acc_dswrite4<56, 18432>(xw + 65536);
        acc_dswrite4<60, 19456>(xw + 65536);
        acc_dswrite4<176, 20480>(xw + 65536);
        acc_dswrite4<180, 21504>(xw + 65536);
        acc_dswrite4<184, 22528>(xw + 65536);
        acc_dswrite4<188, 23552>(xw + 65536);
        acc_dswrite4<80, 24576>(xw + 65536);
        acc_dswrite4<84, 25600>(xw + 65536);
        acc_dswrite4<88, 26624>(xw + 65536);
        acc_dswrite4<92, 27648>(xw + 65536);
        acc_dswrite4<208, 28672>(xw + 65536);
        acc_dswrite4<212, 29696>(xw + 65536);
        acc_dswrite4<216, 30720>(xw + 65536);
        acc_dswrite4<220, 31744>(xw + 65536);
      } break;
    }
    WAITL0();
    __builtin_amdgcn_s_barrier();
    switch (w) {
      case 0: {
        { f32x4 sum = acc_read4<16>(); sum *= fw[0][0];
          sum += xr[1536] * fw[0][1];
          sum += xr[3072] * fw[0][2];
          sum += xr[4608] * fw[0][3];
          u32x2 v = {cvtpk(sum[0], sum[1]), cvtpk(sum[2], sum[3])};
          *(u32x2*)(otile + (0 + r) * 512 + (((4) ^ ((0 + r) & 31)) * 16) + 8 * h) = v; }
        { f32x4 sum = acc_read4<20>(); sum *= fw[0][0];
          sum += xr[1600] * fw[0][1];
          sum += xr[3136] * fw[0][2];
          sum += xr[4672] * fw[0][3];
          u32x2 v = {cvtpk(sum[0], sum[1]), cvtpk(sum[2], sum[3])};
          *(u32x2*)(otile + (0 + r) * 512 + (((5) ^ ((0 + r) & 31)) * 16) + 8 * h) = v; }
        { f32x4 sum = acc_read4<24>(); sum *= fw[0][0];
          sum += xr[1664] * fw[0][1];
          sum += xr[3200] * fw[0][2];
          sum += xr[4736] * fw[0][3];
          u32x2 v = {cvtpk(sum[0], sum[1]), cvtpk(sum[2], sum[3])};
          *(u32x2*)(otile + (0 + r) * 512 + (((6) ^ ((0 + r) & 31)) * 16) + 8 * h) = v; }
        { f32x4 sum = acc_read4<28>(); sum *= fw[0][0];
          sum += xr[1728] * fw[0][1];
          sum += xr[3264] * fw[0][2];
          sum += xr[4800] * fw[0][3];
          u32x2 v = {cvtpk(sum[0], sum[1]), cvtpk(sum[2], sum[3])};
          *(u32x2*)(otile + (0 + r) * 512 + (((7) ^ ((0 + r) & 31)) * 16) + 8 * h) = v; }
        { f32x4 sum = acc_read4<144>(); sum *= fw[1][0];
          sum += xr[1792] * fw[1][1];
          sum += xr[3328] * fw[1][2];
          sum += xr[4864] * fw[1][3];
          u32x2 v = {cvtpk(sum[0], sum[1]), cvtpk(sum[2], sum[3])};
          *(u32x2*)(otile + (32 + r) * 512 + (((4) ^ ((32 + r) & 31)) * 16) + 8 * h) = v; }
        { f32x4 sum = acc_read4<148>(); sum *= fw[1][0];
          sum += xr[1856] * fw[1][1];
          sum += xr[3392] * fw[1][2];
          sum += xr[4928] * fw[1][3];
          u32x2 v = {cvtpk(sum[0], sum[1]), cvtpk(sum[2], sum[3])};
          *(u32x2*)(otile + (32 + r) * 512 + (((5) ^ ((32 + r) & 31)) * 16) + 8 * h) = v; }
        { f32x4 sum = acc_read4<152>(); sum *= fw[1][0];
          sum += xr[1920] * fw[1][1];
          sum += xr[3456] * fw[1][2];
          sum += xr[4992] * fw[1][3];
          u32x2 v = {cvtpk(sum[0], sum[1]), cvtpk(sum[2], sum[3])};
          *(u32x2*)(otile + (32 + r) * 512 + (((6) ^ ((32 + r) & 31)) * 16) + 8 * h) = v; }
        { f32x4 sum = acc_read4<156>(); sum *= fw[1][0];
          sum += xr[1984] * fw[1][1];
          sum += xr[3520] * fw[1][2];
          sum += xr[5056] * fw[1][3];
          u32x2 v = {cvtpk(sum[0], sum[1]), cvtpk(sum[2], sum[3])};
          *(u32x2*)(otile + (32 + r) * 512 + (((7) ^ ((32 + r) & 31)) * 16) + 8 * h) = v; }
      } break;
      case 1: {
        { f32x4 sum = acc_read4<48>(); sum *= fw[0][1];
          sum += xr[0] * fw[0][0];
          sum += xr[3584] * fw[0][2];
          sum += xr[5120] * fw[0][3];
          u32x2 v = {cvtpk(sum[0], sum[1]), cvtpk(sum[2], sum[3])};
          *(u32x2*)(otile + (0 + r) * 512 + (((12) ^ ((0 + r) & 31)) * 16) + 8 * h) = v; }
        { f32x4 sum = acc_read4<52>(); sum *= fw[0][1];
          sum += xr[64] * fw[0][0];
          sum += xr[3648] * fw[0][2];
          sum += xr[5184] * fw[0][3];
          u32x2 v = {cvtpk(sum[0], sum[1]), cvtpk(sum[2], sum[3])};
          *(u32x2*)(otile + (0 + r) * 512 + (((13) ^ ((0 + r) & 31)) * 16) + 8 * h) = v; }
        { f32x4 sum = acc_read4<56>(); sum *= fw[0][1];
          sum += xr[128] * fw[0][0];
          sum += xr[3712] * fw[0][2];
          sum += xr[5248] * fw[0][3];
          u32x2 v = {cvtpk(sum[0], sum[1]), cvtpk(sum[2], sum[3])};
          *(u32x2*)(otile + (0 + r) * 512 + (((14) ^ ((0 + r) & 31)) * 16) + 8 * h) = v; }
        { f32x4 sum = acc_read4<60>(); sum *= fw[0][1];
          sum += xr[192] * fw[0][0];
          sum += xr[3776] * fw[0][2];
          sum += xr[5312] * fw[0][3];
          u32x2 v = {cvtpk(sum[0], sum[1]), cvtpk(sum[2], sum[3])};
          *(u32x2*)(otile + (0 + r) * 512 + (((15) ^ ((0 + r) & 31)) * 16) + 8 * h) = v; }
        { f32x4 sum = acc_read4<176>(); sum *= fw[1][1];
          sum += xr[256] * fw[1][0];
          sum += xr[3840] * fw[1][2];
          sum += xr[5376] * fw[1][3];
          u32x2 v = {cvtpk(sum[0], sum[1]), cvtpk(sum[2], sum[3])};
          *(u32x2*)(otile + (32 + r) * 512 + (((12) ^ ((32 + r) & 31)) * 16) + 8 * h) = v; }
        { f32x4 sum = acc_read4<180>(); sum *= fw[1][1];
          sum += xr[320] * fw[1][0];
          sum += xr[3904] * fw[1][2];
          sum += xr[5440] * fw[1][3];
          u32x2 v = {cvtpk(sum[0], sum[1]), cvtpk(sum[2], sum[3])};
          *(u32x2*)(otile + (32 + r) * 512 + (((13) ^ ((32 + r) & 31)) * 16) + 8 * h) = v; }
        { f32x4 sum = acc_read4<184>(); sum *= fw[1][1];
          sum += xr[384] * fw[1][0];
          sum += xr[3968] * fw[1][2];
          sum += xr[5504] * fw[1][3];
          u32x2 v = {cvtpk(sum[0], sum[1]), cvtpk(sum[2], sum[3])};
          *(u32x2*)(otile + (32 + r) * 512 + (((14) ^ ((32 + r) & 31)) * 16) + 8 * h) = v; }
        { f32x4 sum = acc_read4<188>(); sum *= fw[1][1];
          sum += xr[448] * fw[1][0];
          sum += xr[4032] * fw[1][2];
          sum += xr[5568] * fw[1][3];
          u32x2 v = {cvtpk(sum[0], sum[1]), cvtpk(sum[2], sum[3])};
          *(u32x2*)(otile + (32 + r) * 512 + (((15) ^ ((32 + r) & 31)) * 16) + 8 * h) = v; }
      } break;
      case 2: {
        { f32x4 sum = acc_read4<80>(); sum *= fw[0][2];
          sum += xr[512] * fw[0][0];
          sum += xr[2048] * fw[0][1];
          sum += xr[5632] * fw[0][3];
          u32x2 v = {cvtpk(sum[0], sum[1]), cvtpk(sum[2], sum[3])};
          *(u32x2*)(otile + (0 + r) * 512 + (((20) ^ ((0 + r) & 31)) * 16) + 8 * h) = v; }
        { f32x4 sum = acc_read4<84>(); sum *= fw[0][2];
          sum += xr[576] * fw[0][0];
          sum += xr[2112] * fw[0][1];
          sum += xr[5696] * fw[0][3];
          u32x2 v = {cvtpk(sum[0], sum[1]), cvtpk(sum[2], sum[3])};
          *(u32x2*)(otile + (0 + r) * 512 + (((21) ^ ((0 + r) & 31)) * 16) + 8 * h) = v; }
        { f32x4 sum = acc_read4<88>(); sum *= fw[0][2];
          sum += xr[640] * fw[0][0];
          sum += xr[2176] * fw[0][1];
          sum += xr[5760] * fw[0][3];
          u32x2 v = {cvtpk(sum[0], sum[1]), cvtpk(sum[2], sum[3])};
          *(u32x2*)(otile + (0 + r) * 512 + (((22) ^ ((0 + r) & 31)) * 16) + 8 * h) = v; }
        { f32x4 sum = acc_read4<92>(); sum *= fw[0][2];
          sum += xr[704] * fw[0][0];
          sum += xr[2240] * fw[0][1];
          sum += xr[5824] * fw[0][3];
          u32x2 v = {cvtpk(sum[0], sum[1]), cvtpk(sum[2], sum[3])};
          *(u32x2*)(otile + (0 + r) * 512 + (((23) ^ ((0 + r) & 31)) * 16) + 8 * h) = v; }
        { f32x4 sum = acc_read4<208>(); sum *= fw[1][2];
          sum += xr[768] * fw[1][0];
          sum += xr[2304] * fw[1][1];
          sum += xr[5888] * fw[1][3];
          u32x2 v = {cvtpk(sum[0], sum[1]), cvtpk(sum[2], sum[3])};
          *(u32x2*)(otile + (32 + r) * 512 + (((20) ^ ((32 + r) & 31)) * 16) + 8 * h) = v; }
        { f32x4 sum = acc_read4<212>(); sum *= fw[1][2];
          sum += xr[832] * fw[1][0];
          sum += xr[2368] * fw[1][1];
          sum += xr[5952] * fw[1][3];
          u32x2 v = {cvtpk(sum[0], sum[1]), cvtpk(sum[2], sum[3])};
          *(u32x2*)(otile + (32 + r) * 512 + (((21) ^ ((32 + r) & 31)) * 16) + 8 * h) = v; }
        { f32x4 sum = acc_read4<216>(); sum *= fw[1][2];
          sum += xr[896] * fw[1][0];
          sum += xr[2432] * fw[1][1];
          sum += xr[6016] * fw[1][3];
          u32x2 v = {cvtpk(sum[0], sum[1]), cvtpk(sum[2], sum[3])};
          *(u32x2*)(otile + (32 + r) * 512 + (((22) ^ ((32 + r) & 31)) * 16) + 8 * h) = v; }
        { f32x4 sum = acc_read4<220>(); sum *= fw[1][2];
          sum += xr[960] * fw[1][0];
          sum += xr[2496] * fw[1][1];
          sum += xr[6080] * fw[1][3];
          u32x2 v = {cvtpk(sum[0], sum[1]), cvtpk(sum[2], sum[3])};
          *(u32x2*)(otile + (32 + r) * 512 + (((23) ^ ((32 + r) & 31)) * 16) + 8 * h) = v; }
      } break;
      case 3: {
        { f32x4 sum = acc_read4<112>(); sum *= fw[0][3];
          sum += xr[1024] * fw[0][0];
          sum += xr[2560] * fw[0][1];
          sum += xr[4096] * fw[0][2];
          u32x2 v = {cvtpk(sum[0], sum[1]), cvtpk(sum[2], sum[3])};
          *(u32x2*)(otile + (0 + r) * 512 + (((28) ^ ((0 + r) & 31)) * 16) + 8 * h) = v; }
        { f32x4 sum = acc_read4<116>(); sum *= fw[0][3];
          sum += xr[1088] * fw[0][0];
          sum += xr[2624] * fw[0][1];
          sum += xr[4160] * fw[0][2];
          u32x2 v = {cvtpk(sum[0], sum[1]), cvtpk(sum[2], sum[3])};
          *(u32x2*)(otile + (0 + r) * 512 + (((29) ^ ((0 + r) & 31)) * 16) + 8 * h) = v; }
        { f32x4 sum = acc_read4<120>(); sum *= fw[0][3];
          sum += xr[1152] * fw[0][0];
          sum += xr[2688] * fw[0][1];
          sum += xr[4224] * fw[0][2];
          u32x2 v = {cvtpk(sum[0], sum[1]), cvtpk(sum[2], sum[3])};
          *(u32x2*)(otile + (0 + r) * 512 + (((30) ^ ((0 + r) & 31)) * 16) + 8 * h) = v; }
        { f32x4 sum = acc_read4<124>(); sum *= fw[0][3];
          sum += xr[1216] * fw[0][0];
          sum += xr[2752] * fw[0][1];
          sum += xr[4288] * fw[0][2];
          u32x2 v = {cvtpk(sum[0], sum[1]), cvtpk(sum[2], sum[3])};
          *(u32x2*)(otile + (0 + r) * 512 + (((31) ^ ((0 + r) & 31)) * 16) + 8 * h) = v; }
        { f32x4 sum = acc_read4<240>(); sum *= fw[1][3];
          sum += xr[1280] * fw[1][0];
          sum += xr[2816] * fw[1][1];
          sum += xr[4352] * fw[1][2];
          u32x2 v = {cvtpk(sum[0], sum[1]), cvtpk(sum[2], sum[3])};
          *(u32x2*)(otile + (32 + r) * 512 + (((28) ^ ((32 + r) & 31)) * 16) + 8 * h) = v; }
        { f32x4 sum = acc_read4<244>(); sum *= fw[1][3];
          sum += xr[1344] * fw[1][0];
          sum += xr[2880] * fw[1][1];
          sum += xr[4416] * fw[1][2];
          u32x2 v = {cvtpk(sum[0], sum[1]), cvtpk(sum[2], sum[3])};
          *(u32x2*)(otile + (32 + r) * 512 + (((29) ^ ((32 + r) & 31)) * 16) + 8 * h) = v; }
        { f32x4 sum = acc_read4<248>(); sum *= fw[1][3];
          sum += xr[1408] * fw[1][0];
          sum += xr[2944] * fw[1][1];
          sum += xr[4480] * fw[1][2];
          u32x2 v = {cvtpk(sum[0], sum[1]), cvtpk(sum[2], sum[3])};
          *(u32x2*)(otile + (32 + r) * 512 + (((30) ^ ((32 + r) & 31)) * 16) + 8 * h) = v; }
        { f32x4 sum = acc_read4<252>(); sum *= fw[1][3];
          sum += xr[1472] * fw[1][0];
          sum += xr[3008] * fw[1][1];
          sum += xr[4544] * fw[1][2];
          u32x2 v = {cvtpk(sum[0], sum[1]), cvtpk(sum[2], sum[3])};
          *(u32x2*)(otile + (32 + r) * 512 + (((31) ^ ((32 + r) & 31)) * 16) + 8 * h) = v; }
      } break;
    }
  }
  __syncthreads();
  asm volatile("s_waitcnt vmcnt(0)" :: "v"(c.pfd) : "memory");
  STAMP(15);
#pragma unroll
  for (int k = 0; k < 7; ++k) {
    const int R = 14 * w + 2 * k + h;
    u32x4 v = *(const u32x4*)(otile + R * 512 + ((r ^ (R & 31)) * 16));
    *(u32x4*)(o + ((size_t)(head * 8 + (r >> 2)) * NROW + row0 + R) * 32 + (r & 3) * 8) = v;
  }
  STAMP(10);
}
__global__ __launch_bounds__(256, 1) void qproj5_kernel(const u16* __restrict__ x, const u16* __restrict__ wq,
                                                         u16* __restrict__ qfrag, unsigned* __restrict__ cnt) {
  extern __shared__ __attribute__((aligned(16))) char lds[];
  const int tid = threadIdx.x, lane = tid & 63;
  const int w = __builtin_amdgcn_readfirstlane(tid >> 6);
  const int r = lane & 31, h = lane >> 5;
  const int bid = blockIdx.x;
  if (bid == 0 && tid < 64) cnt[tid] = 0u;
  const int xcd = bid & 7, j = bid >> 3;
  const int grp = (j >> 2) * 8 + xcd, sub = j & 3;
  const int b = grp >> 3, kvh = grp & 7;
  const int hl = sub >> 1, head = kvh * 2 + hl, rhalf = sub & 1;
  const int row0 = b * SQ + rhalf * 56;
  char* ring = lds + w * 40960;
  {
    __amdgpu_buffer_rsrc_t rx =
        __builtin_amdgcn_make_buffer_rsrc((void*)(x + (size_t)row0 * DM), 0, 56 * DM * 2, 0x00020000);
#pragma unroll
    for (int p = 0; p < 14; ++p) {
      const int ci = 64 * p + lane;
      const int lr = ci / 56, cp = ci - lr * 56;
      int gr = 16 * w + lr; gr = gr > 55 ? 55 : gr;
      const int cs = (cp & ~7) | ((cp ^ (lr >> 1)) & 7);
      __builtin_amdgcn_raw_ptr_buffer_load_lds(rx, (lds_vp)(ring + 3 * 8192 + p * 1024), 16, gr * (DM * 2) + cs * 16, 0, 0, 0);
    }
  }
  const int colw = head * DH + 64 * w;
  __amdgpu_buffer_rsrc_t rw = __builtin_amdgcn_make_buffer_rsrc((void*)(wq + colw), 0, (DM * NQ - colw) * 2, 0x00020000);
  const int drow = lane >> 3;
  const int wvoff = drow * (NQ * 2) + (((lane & 7) ^ (((drow >> 1) & 1) << 2)) * 16);
  auto wpiece = [&](int ch, int p) {
    __builtin_amdgcn_raw_ptr_buffer_load_lds(rw, (lds_vp)(ring + (ch % 3) * 8192 + p * 1024), 16, wvoff,
                                             (64 * ch + 8 * p) * (NQ * 2), 0, 0);
  };
#pragma unroll
  for (int ch = 0; ch < 3; ++ch)
#pragma unroll
    for (int p = 0; p < 8; ++p) wpiece(ch, p);
  const int g = lane >> 4, qq = (lane >> 2) & 3, pp = lane & 3;
  const unsigned trb = (unsigned)(size_t)ring + (8 * (g >> 1) + qq) * 128 + ((32 * (g & 1) + 8 * pp) ^ ((qq >> 1) << 6));
  const char* xa[2][4];
#pragma unroll
  for (int qb = 0; qb < 2; ++qb) {
    const int R = 32 * qb + r, lr = R & 15;
#pragma unroll
    for (int v = 0; v < 4; ++v)
      xa[qb][v] = lds + (R >> 4) * 40960 + 3 * 8192 + lr * 896 + ((((2 * v + h) ^ (lr >> 1)) & 7) * 16);
  }
  f32x16 qa[2][2];
#pragma unroll
  for (int a2 = 0; a2 < 2; ++a2)
#pragma unroll
    for (int b2 = 0; b2 < 2; ++b2)
#pragma unroll
      for (int i = 0; i < 16; ++i) qa[a2][b2][i] = 0.f;
  s16x4 fa[2][4];
  bf16x8 fx[2][2];
  auto rdstep = [&](int i, int bsel) {
    const int ch = i >> 2, s4 = i & 3;
    const unsigned tb = trb + (ch % 3) * 8192 + s4 * 2048;
    fa[bsel][0] = trread(tb);
    fa[bsel][1] = trread(tb + 512);
    fa[bsel][2] = trread(tb ^ 64);
    fa[bsel][3] = trread((tb + 512) ^ 64);
    asm volatile("ds_read_b128 %0, %1" : "=v"(fx[bsel][0]) : "v"((unsigned)(size_t)(xa[0][s4] + ch * 128)) : "memory");
    asm volatile("ds_read_b128 %0, %1" : "=v"(fx[bsel][1]) : "v"((unsigned)(size_t)(xa[1][s4] + ch * 128)) : "memory");
  };
  WAITV(16);
  __builtin_amdgcn_s_barrier();
  rdstep(0, 0);
#pragma unroll
  for (int i = 0; i < 28; ++i) {
    const int bs = i & 1, ch = i >> 2, s4 = i & 3;
    if (i + 1 < 28) {
      if (s4 == 3) { if (ch == 0) WAITV(8); else if (ch <= 4) WAITV(6); else WAITV(0); }
      rdstep(i + 1, bs ^ 1);
      asm volatile("s_waitcnt lgkmcnt(6)" : "+v"(fa[bs][0]), "+v"(fa[bs][1]), "+v"(fa[bs][2]), "+v"(fa[bs][3]), "+v"(fx[bs][0]), "+v"(fx[bs][1])::"memory");
    } else {
      asm volatile("s_waitcnt lgkmcnt(0)" : "+v"(fa[bs][0]), "+v"(fa[bs][1]), "+v"(fa[bs][2]), "+v"(fa[bs][3]), "+v"(fx[bs][0]), "+v"(fx[bs][1])::"memory");
    }
    bf16x8 A0 = cat8(fa[bs][0], fa[bs][1]), A1 = cat8(fa[bs][2], fa[bs][3]);
    qa[0][0] = __builtin_amdgcn_mfma_f32_32x32x16_bf16(A0, fx[bs][0], qa[0][0], 0, 0, 0);
    GAP();
    if (ch >= 1 && ch <= 4) wpiece(ch + 2, 2 * s4);
    GAP();
    qa[0][1] = __builtin_amdgcn_mfma_f32_32x32x16_bf16(A1, fx[bs][0], qa[0][1], 0, 0, 0);
    qa[1][0] = __builtin_amdgcn_mfma_f32_32x32x16_bf16(A0, fx[bs][1], qa[1][0], 0, 0, 0);
    GAP();
    if (ch >= 1 && ch <= 4) wpiece(ch + 2, 2 * s4 + 1);
    GAP();
    qa[1][1] = __builtin_amdgcn_mfma_f32_32x32x16_bf16(A1, fx[bs][1], qa[1][1], 0, 0, 0);
  }
#pragma unroll
  for (int qb = 0; qb < 2; ++qb) {
    const int lrow = 32 * qb + r;
    if (lrow < 56) {
      const int R = hl * 112 + rhalf * 56 + lrow;
      u16* base = qfrag + ((size_t)(grp * 8 + (R >> 5)) * 16 * 64 + (R & 31)) * 8 + 4 * h;
#pragma unroll
      for (int cb = 0; cb < 2; ++cb)
#pragma unroll
        for (int gg = 0; gg < 4; ++gg) {
          const int s = 4 * w + 2 * cb + (gg >> 1), hp = gg & 1;
          u32x2 v = {cvtpk(qa[qb][cb][4 * gg], qa[qb][cb][4 * gg + 1]), cvtpk(qa[qb][cb][4 * gg + 2], qa[qb][cb][4 * gg + 3])};
          *(u32x2*)(base + (s * 64 + 32 * hp) * 8) = v;
        }
    }
  }
}

template <int N> DI void dma5_unit(const Ctx4& c) {
  if constexpr (N < 32) {
    constexpr int slot = N % 16, t = unit_tile(N), hf = unit_half(N);
    if constexpr (!unit_is_v(N)) {
      __builtin_amdgcn_raw_ptr_buffer_load_lds(c.rk, (lds_vp)(c.ring + slot * 8192), 16, c.koff[0], 32 * t * 512 + hf * 256, 0, 0);
      __builtin_amdgcn_raw_ptr_buffer_load_lds(c.rk, (lds_vp)(c.ring + slot * 8192 + 1024), 16, c.koff[1], 32 * t * 512 + hf * 256, 0, 0);
    } else {
      __builtin_amdgcn_raw_ptr_buffer_load_lds(c.rv, (lds_vp)(c.ring + slot * 8192), 16, c.voff[0], (32 * t + 16 * hf) * 512, 0, 0);
      __builtin_amdgcn_raw_ptr_buffer_load_lds(c.rv, (lds_vp)(c.ring + slot * 8192 + 1024), 16, c.voff[1], (32 * t + 16 * hf) * 512, 0, 0);
    }
  }
}
template <int T, int J> DI bf16x8 kread5(const Ctx4& c) {
  constexpr int n = unit_k(T, J >> 3), slot = n % 16;
  bf16x8 r;
  asm volatile("ds_read_b128 %0, %1 offset:%2" : "=v"(r) : "v"(c.kaddr[J & 7] + (slot >> 3) * 65536), "i"((slot & 7) * 8192) : "memory");
  return r;
}
template <int T, int J> DI void phaseA5_step(Ctx4& c, bf16x8 (&kf)[4]) {
  if constexpr (J + 3 < 16) asm volatile("s_waitcnt lgkmcnt(3)" : "+v"(kf[J & 3])::"memory");
  else if constexpr (J + 3 == 16) asm volatile("s_waitcnt lgkmcnt(2)" : "+v"(kf[J & 3])::"memory");
  else if constexpr (J + 3 == 17) asm volatile("s_waitcnt lgkmcnt(1)" : "+v"(kf[J & 3])::"memory");
  else asm volatile("s_waitcnt lgkmcnt(0)" : "+v"(kf[J & 3])::"memory");
  qk_mfma<J == 0>(c.s[0], kf[J & 3], c.qf[0][J]);
  if constexpr (T >= 1 && (J & 3) == 1 && J < 16) dma5_unit<4 * T + 10 + (J >> 2)>(c);
  GAP();
  qk_mfma<J == 0>(c.s[1], kf[J & 3], c.qf[1][J]);
  if constexpr (J + 4 < 16) kf[J & 3] = kread5<T, J + 4>(c);
  GAP();
}
template <int T, int... Js> DI void phaseA5_(Ctx4& c, std::integer_sequence<int, Js...>) {
  bf16x8 kf[4];
  {
    constexpr int need = 4 * T + 1 > 31 ? 31 : 4 * T + 1;
    constexpr int lastiss = (T == 0 ? 13 : 4 * T + 9) > 31 ? 31 : (T == 0 ? 13 : 4 * T + 9);
    constexpr int after = 2 * (lastiss - need);
    asm volatile("s_waitcnt vmcnt(%0)" ::"i"(after) : "memory");
    __builtin_amdgcn_s_barrier();
  }
  kf[0] = kread5<T, 0>(c); kf[1] = kread5<T, 1>(c); kf[2] = kread5<T, 2>(c); kf[3] = kread5<T, 3>(c);
  GAP();
  (phaseA5_step<T, Js>(c, kf), ...);
}
template <int T> DI void phaseA5(Ctx4& c) { phaseA5_<T>(c, std::make_integer_sequence<int, 16>{}); }

template <int TV, int S> DI void vread5_set(const Ctx4& c, s16x4 (&v)[4]) {
  constexpr int ks = S >> 2, dbp = S & 3;
  constexpr int n = unit_v(TV, ks), slot = n % 16;
  constexpr int db0 = 2 * dbp, db1 = 2 * dbp + 1;
  constexpr int o0 = (slot & 7) * 8192 + (db0 >> 2) * 256, o1 = (slot & 7) * 8192 + (db1 >> 2) * 256;
  constexpr unsigned hi = (slot >> 3) * 65536;
  v[0] = trread_o<o0>(c.vaddr[db0 & 3] + hi);
  v[1] = trread_o<o0 + 4096>(c.vaddr[db0 & 3] + hi);
  v[2] = trread_o<o1>(c.vaddr[db1 & 3] + hi);
  v[3] = trread_o<o1 + 4096>(c.vaddr[db1 & 3] + hi);
}
template <int T, bool HASPV, bool HASSM, int M> DI void phaseB5_gap(Ctx4& c, s16x4 (&va)[4], s16x4 (&vb)[4]) {
  constexpr int TV = T - 1;
  constexpr int S = M >> 2, ks = S >> 2, dbp = S & 3, dd = (M >> 1) & 1, qb = M & 1;
  if constexpr (HASPV) {
    if constexpr ((M & 3) == 0) {
      if constexpr (S + 1 < 8) {
        if constexpr (S & 1) asm volatile("s_waitcnt lgkmcnt(4)" : "+v"(vb[0]), "+v"(vb[1]), "+v"(vb[2]), "+v"(vb[3])::"memory");
        else asm volatile("s_waitcnt lgkmcnt(4)" : "+v"(va[0]), "+v"(va[1]), "+v"(va[2]), "+v"(va[3])::"memory");
      } else {
        asm volatile("s_waitcnt lgkmcnt(0)" : "+v"(vb[0]), "+v"(vb[1]), "+v"(vb[2]), "+v"(vb[3])::"memory");
      }
    }
    {
      constexpr int db = 2 * dbp + dd;
      const bf16x8 vf = (S & 1) ? cat8(vb[2 * dd], vb[2 * dd + 1]) : cat8(va[2 * dd], va[2 * dd + 1]);
      pv_mfma<(qb * 8 + db) * 16, (TV == 0 && ks == 0)>(vf, c.pf[qb][ks]);
    }
    if constexpr ((M & 3) == 3 && S + 2 < 8) {
      if constexpr (S & 1) vread5_set<TV, S + 2>(c, vb); else vread5_set<TV, S + 2>(c, va);
    }
  }
  if constexpr (HASSM) sm_slice<T, M>(c);
  GAP();
}
template <int T, bool HASPV, bool HASSM, int... Ms> DI void phaseB5_(Ctx4& c, std::integer_sequence<int, Ms...>) {
  constexpr int TV = T - 1;
  s16x4 va[4], vb[4];
  if constexpr (!HASSM) {
    asm volatile("s_waitcnt vmcnt(0)" ::: "memory");
    __builtin_amdgcn_s_barrier();
  }
  if constexpr (HASPV) {
    vread5_set<TV, 0>(c, va);
    vread5_set<TV, 1>(c, vb);
  }
  if constexpr (HASSM) asm volatile("s_nop 7\n\ts_nop 7" : "+v"(c.s[0]), "+v"(c.s[1]));
  GAP();
  (phaseB5_gap<T, HASPV, HASSM, Ms>(c, va, vb), ...);
  if constexpr (HASSM) {
    c.l[0] += c.rs[0]; c.l[1] += c.rs[1];
#pragma unroll
    for (int qb = 0; qb < 2; ++qb)
#pragma unroll
      for (int ks = 0; ks < 2; ++ks) {
        u32x4 wv = {c.pn[qb][4 * ks], c.pn[qb][4 * ks + 1], c.pn[qb][4 * ks + 2], c.pn[qb][4 * ks + 3]};
        c.pf[qb][ks] = __builtin_bit_cast(bf16x8, wv);
      }
  }
  if constexpr (HASPV && HASSM) {
    if (__builtin_expect(c.pend, 0)) {
      float t0, t1;
      asm volatile("s_nop 7\n\ts_nop 7\n\t"
                   "v_accvgpr_read_b32 %0, a0\n\tv_mul_f32 %0, %2, %0\n\tv_accvgpr_write_b32 a0, %0\n\t"
                   "v_accvgpr_read_b32 %1, a1\n\tv_mul_f32 %1, %2, %1\n\tv_accvgpr_write_b32 a1, %1\n\t"
                   "v_accvgpr_read_b32 %0, a2\n\tv_mul_f32 %0, %2, %0\n\tv_accvgpr_write_b32 a2, %0\n\t"
                   "v_accvgpr_read_b32 %1, a3\n\tv_mul_f32 %1, %2, %1\n\tv_accvgpr_write_b32 a3, %1\n\t"
                   "v_accvgpr_read_b32 %0, a4\n\tv_mul_f32 %0, %2, %0\n\tv_accvgpr_write_b32 a4, %0\n\t"
                   "v_accvgpr_read_b32 %1, a5\n\tv_mul_f32 %1, %2, %1\n\tv_accvgpr_write_b32 a5, %1\n\t"
                   "v_accvgpr_read_b32 %0, a6\n\tv_mul_f32 %0, %2, %0\n\tv_accvgpr_write_b32 a6, %0\n\t"
                   "v_accvgpr_read_b32 %1, a7\n\tv_mul_f32 %1, %2, %1\n\tv_accvgpr_write_b32 a7, %1\n\t"
                   "v_accvgpr_read_b32 %0, a8\n\tv_mul_f32 %0, %2, %0\n\tv_accvgpr_write_b32 a8, %0\n\t"
                   "v_accvgpr_read_b32 %1, a9\n\tv_mul_f32 %1, %2, %1\n\tv_accvgpr_write_b32 a9, %1\n\t"
                   "v_accvgpr_read_b32 %0, a10\n\tv_mul_f32 %0, %2, %0\n\tv_accvgpr_write_b32 a10, %0\n\t"
                   "v_accvgpr_read_b32 %1, a11\n\tv_mul_f32 %1, %2, %1\n\tv_accvgpr_write_b32 a11, %1\n\t"
                   "v_accvgpr_read_b32 %0, a12\n\tv_mul_f32 %0, %2, %0\n\tv_accvgpr_write_b32 a12, %0\n\t"
                   "v_accvgpr_read_b32 %1, a13\n\tv_mul_f32 %1, %2, %1\n\tv_accvgpr_write_b32 a13, %1\n\t"
                   "v_accvgpr_read_b32 %0, a14\n\tv_mul_f32 %0, %2, %0\n\tv_accvgpr_write_b32 a14, %0\n\t"
                   "v_accvgpr_read_b32 %1, a15\n\tv_mul_f32 %1, %2, %1\n\tv_accvgpr_write_b32 a15, %1\n\t"
                   "v_accvgpr_read_b32 %0, a16\n\tv_mul_f32 %0, %2, %0\n\tv_accvgpr_write_b32 a16, %0\n\t"
                   "v_accvgpr_read_b32 %1, a17\n\tv_mul_f32 %1, %2, %1\n\tv_accvgpr_write_b32 a17, %1\n\t"
                   "v_accvgpr_read_b32 %0, a18\n\tv_mul_f32 %0, %2, %0\n\tv_accvgpr_write_b32 a18, %0\n\t"
                   "v_accvgpr_read_b32 %1, a19\n\tv_mul_f32 %1, %2, %1\n\tv_accvgpr_write_b32 a19, %1\n\t"
                   "v_accvgpr_read_b32 %0, a20\n\tv_mul_f32 %0, %2, %0\n\tv_accvgpr_write_b32 a20, %0\n\t"
                   "v_accvgpr_read_b32 %1, a21\n\tv_mul_f32 %1, %2, %1\n\tv_accvgpr_write_b32 a21, %1\n\t"
                   "v_accvgpr_read_b32 %0, a22\n\tv_mul_f32 %0, %2, %0\n\tv_accvgpr_write_b32 a22, %0\n\t"
                   "v_accvgpr_read_b32 %1, a23\n\tv_mul_f32 %1, %2, %1\n\tv_accvgpr_write_b32 a23, %1\n\t"
                   "v_accvgpr_read_b32 %0, a24\n\tv_mul_f32 %0, %2, %0\n\tv_accvgpr_write_b32 a24, %0\n\t"
                   "v_accvgpr_read_b32 %1, a25\n\tv_mul_f32 %1, %2, %1\n\tv_accvgpr_write_b32 a25, %1\n\t"
                   "v_accvgpr_read_b32 %0, a26\n\tv_mul_f32 %0, %2, %0\n\tv_accvgpr_write_b32 a26, %0\n\t"
                   "v_accvgpr_read_b32 %1, a27\n\tv_mul_f32 %1, %2, %1\n\tv_accvgpr_write_b32 a27, %1\n\t"
                   "v_accvgpr_read_b32 %0, a28\n\tv_mul_f32 %0, %2, %0\n\tv_accvgpr_write_b32 a28, %0\n\t"
                   "v_accvgpr_read_b32 %1, a29\n\tv_mul_f32 %1, %2, %1\n\tv_accvgpr_write_b32 a29, %1\n\t"
                   "v_accvgpr_read_b32 %0, a30\n\tv_mul_f32 %0, %2, %0\n\tv_accvgpr_write_b32 a30, %0\n\t"
                   "v_accvgpr_read_b32 %1, a31\n\tv_mul_f32 %1, %2, %1\n\tv_accvgpr_write_b32 a31, %1\n\t"
                   "v_accvgpr_read_b32 %0, a32\n\tv_mul_f32 %0, %2, %0\n\tv_accvgpr_write_b32 a32, %0\n\t"
                   "v_accvgpr_read_b32 %1, a33\n\tv_mul_f32 %1, %2, %1\n\tv_accvgpr_write_b32 a33, %1\n\t"
                   "v_accvgpr_read_b32 %0, a34\n\tv_mul_f32 %0, %2, %0\n\tv_accvgpr_write_b32 a34, %0\n\t"
                   "v_accvgpr_read_b32 %1, a35\n\tv_mul_f32 %1, %2, %1\n\tv_accvgpr_write_b32 a35, %1\n\t"
                   "v_accvgpr_read_b32 %0, a36\n\tv_mul_f32 %0, %2, %0\n\tv_accvgpr_write_b32 a36, %0\n\t"
                   "v_accvgpr_read_b32 %1, a37\n\tv_mul_f32 %1, %2, %1\n\tv_accvgpr_write_b32 a37, %1\n\t"
                   "v_accvgpr_read_b32 %0, a38\n\tv_mul_f32 %0, %2, %0\n\tv_accvgpr_write_b32 a38, %0\n\t"
                   "v_accvgpr_read_b32 %1, a39\n\tv_mul_f32 %1, %2, %1\n\tv_accvgpr_write_b32 a39, %1\n\t"
                   "v_accvgpr_read_b32 %0, a40\n\tv_mul_f32 %0, %2, %0\n\tv_accvgpr_write_b32 a40, %0\n\t"
                   "v_accvgpr_read_b32 %1, a41\n\tv_mul_f32 %1, %2, %1\n\tv_accvgpr_write_b32 a41, %1\n\t"
                   "v_accvgpr_read_b32 %0, a42\n\tv_mul_f32 %0, %2, %0\n\tv_accvgpr_write_b32 a42, %0\n\t"
                   "v_accvgpr_read_b32 %1, a43\n\tv_mul_f32 %1, %2, %1\n\tv_accvgpr_write_b32 a43, %1\n\t"
                   "v_accvgpr_read_b32 %0, a44\n\tv_mul_f32 %0, %2, %0\n\tv_accvgpr_write_b32 a44, %0\n\t"
                   "v_accvgpr_read_b32 %1, a45\n\tv_mul_f32 %1, %2, %1\n\tv_accvgpr_write_b32 a45, %1\n\t"
                   "v_accvgpr_read_b32 %0, a46\n\tv_mul_f32 %0, %2, %0\n\tv_accvgpr_write_b32 a46, %0\n\t"
                   "v_accvgpr_read_b32 %1, a47\n\tv_mul_f32 %1, %2, %1\n\tv_accvgpr_write_b32 a47, %1\n\t"
                   "v_accvgpr_read_b32 %0, a48\n\tv_mul_f32 %0, %2, %0\n\tv_accvgpr_write_b32 a48, %0\n\t"
                   "v_accvgpr_read_b32 %1, a49\n\tv_mul_f32 %1, %2, %1\n\tv_accvgpr_write_b32 a49, %1\n\t"
                   "v_accvgpr_read_b32 %0, a50\n\tv_mul_f32 %0, %2, %0\n\tv_accvgpr_write_b32 a50, %0\n\t"
                   "v_accvgpr_read_b32 %1, a51\n\tv_mul_f32 %1, %2, %1\n\tv_accvgpr_write_b32 a51, %1\n\t"
                   "v_accvgpr_read_b32 %0, a52\n\tv_mul_f32 %0, %2, %0\n\tv_accvgpr_write_b32 a52, %0\n\t"
                   "v_accvgpr_read_b32 %1, a53\n\tv_mul_f32 %1, %2, %1\n\tv_accvgpr_write_b32 a53, %1\n\t"
                   "v_accvgpr_read_b32 %0, a54\n\tv_mul_f32 %0, %2, %0\n\tv_accvgpr_write_b32 a54, %0\n\t"
                   "v_accvgpr_read_b32 %1, a55\n\tv_mul_f32 %1, %2, %1\n\tv_accvgpr_write_b32 a55, %1\n\t"
                   "v_accvgpr_read_b32 %0, a56\n\tv_mul_f32 %0, %2, %0\n\tv_accvgpr_write_b32 a56, %0\n\t"
                   "v_accvgpr_read_b32 %1, a57\n\tv_mul_f32 %1, %2, %1\n\tv_accvgpr_write_b32 a57, %1\n\t"
                   "v_accvgpr_read_b32 %0, a58\n\tv_mul_f32 %0, %2, %0\n\tv_accvgpr_write_b32 a58, %0\n\t"
                   "v_accvgpr_read_b32 %1, a59\n\tv_mul_f32 %1, %2, %1\n\tv_accvgpr_write_b32 a59, %1\n\t"
                   "v_accvgpr_read_b32 %0, a60\n\tv_mul_f32 %0, %2, %0\n\tv_accvgpr_write_b32 a60, %0\n\t"
                   "v_accvgpr_read_b32 %1, a61\n\tv_mul_f32 %1, %2, %1\n\tv_accvgpr_write_b32 a61, %1\n\t"
                   "v_accvgpr_read_b32 %0, a62\n\tv_mul_f32 %0, %2, %0\n\tv_accvgpr_write_b32 a62, %0\n\t"
                   "v_accvgpr_read_b32 %1, a63\n\tv_mul_f32 %1, %2, %1\n\tv_accvgpr_write_b32 a63, %1\n\t"
                   "v_accvgpr_read_b32 %0, a64\n\tv_mul_f32 %0, %2, %0\n\tv_accvgpr_write_b32 a64, %0\n\t"
                   "v_accvgpr_read_b32 %1, a65\n\tv_mul_f32 %1, %2, %1\n\tv_accvgpr_write_b32 a65, %1\n\t"
                   "v_accvgpr_read_b32 %0, a66\n\tv_mul_f32 %0, %2, %0\n\tv_accvgpr_write_b32 a66, %0\n\t"
                   "v_accvgpr_read_b32 %1, a67\n\tv_mul_f32 %1, %2, %1\n\tv_accvgpr_write_b32 a67, %1\n\t"
                   "v_accvgpr_read_b32 %0, a68\n\tv_mul_f32 %0, %2, %0\n\tv_accvgpr_write_b32 a68, %0\n\t"
                   "v_accvgpr_read_b32 %1, a69\n\tv_mul_f32 %1, %2, %1\n\tv_accvgpr_write_b32 a69, %1\n\t"
                   "v_accvgpr_read_b32 %0, a70\n\tv_mul_f32 %0, %2, %0\n\tv_accvgpr_write_b32 a70, %0\n\t"
                   "v_accvgpr_read_b32 %1, a71\n\tv_mul_f32 %1, %2, %1\n\tv_accvgpr_write_b32 a71, %1\n\t"
                   "v_accvgpr_read_b32 %0, a72\n\tv_mul_f32 %0, %2, %0\n\tv_accvgpr_write_b32 a72, %0\n\t"
                   "v_accvgpr_read_b32 %1, a73\n\tv_mul_f32 %1, %2, %1\n\tv_accvgpr_write_b32 a73, %1\n\t"
                   "v_accvgpr_read_b32 %0, a74\n\tv_mul_f32 %0, %2, %0\n\tv_accvgpr_write_b32 a74, %0\n\t"
                   "v_accvgpr_read_b32 %1, a75\n\tv_mul_f32 %1, %2, %1\n\tv_accvgpr_write_b32 a75, %1\n\t"
                   "v_accvgpr_read_b32 %0, a76\n\tv_mul_f32 %0, %2, %0\n\tv_accvgpr_write_b32 a76, %0\n\t"
                   "v_accvgpr_read_b32 %1, a77\n\tv_mul_f32 %1, %2, %1\n\tv_accvgpr_write_b32 a77, %1\n\t"
                   "v_accvgpr_read_b32 %0, a78\n\tv_mul_f32 %0, %2, %0\n\tv_accvgpr_write_b32 a78, %0\n\t"
                   "v_accvgpr_read_b32 %1, a79\n\tv_mul_f32 %1, %2, %1\n\tv_accvgpr_write_b32 a79, %1\n\t"
                   "v_accvgpr_read_b32 %0, a80\n\tv_mul_f32 %0, %2, %0\n\tv_accvgpr_write_b32 a80, %0\n\t"
                   "v_accvgpr_read_b32 %1, a81\n\tv_mul_f32 %1, %2, %1\n\tv_accvgpr_write_b32 a81, %1\n\t"
                   "v_accvgpr_read_b32 %0, a82\n\tv_mul_f32 %0, %2, %0\n\tv_accvgpr_write_b32 a82, %0\n\t"
                   "v_accvgpr_read_b32 %1, a83\n\tv_mul_f32 %1, %2, %1\n\tv_accvgpr_write_b32 a83, %1\n\t"
                   "v_accvgpr_read_b32 %0, a84\n\tv_mul_f32 %0, %2, %0\n\tv_accvgpr_write_b32 a84, %0\n\t"
                   "v_accvgpr_read_b32 %1, a85\n\tv_mul_f32 %1, %2, %1\n\tv_accvgpr_write_b32 a85, %1\n\t"
                   "v_accvgpr_read_b32 %0, a86\n\tv_mul_f32 %0, %2, %0\n\tv_accvgpr_write_b32 a86, %0\n\t"
                   "v_accvgpr_read_b32 %1, a87\n\tv_mul_f32 %1, %2, %1\n\tv_accvgpr_write_b32 a87, %1\n\t"
                   "v_accvgpr_read_b32 %0, a88\n\tv_mul_f32 %0, %2, %0\n\tv_accvgpr_write_b32 a88, %0\n\t"
                   "v_accvgpr_read_b32 %1, a89\n\tv_mul_f32 %1, %2, %1\n\tv_accvgpr_write_b32 a89, %1\n\t"
                   "v_accvgpr_read_b32 %0, a90\n\tv_mul_f32 %0, %2, %0\n\tv_accvgpr_write_b32 a90, %0\n\t"
                   "v_accvgpr_read_b32 %1, a91\n\tv_mul_f32 %1, %2, %1\n\tv_accvgpr_write_b32 a91, %1\n\t"
                   "v_accvgpr_read_b32 %0, a92\n\tv_mul_f32 %0, %2, %0\n\tv_accvgpr_write_b32 a92, %0\n\t"
                   "v_accvgpr_read_b32 %1, a93\n\tv_mul_f32 %1, %2, %1\n\tv_accvgpr_write_b32 a93, %1\n\t"
                   "v_accvgpr_read_b32 %0, a94\n\tv_mul_f32 %0, %2, %0\n\tv_accvgpr_write_b32 a94, %0\n\t"
                   "v_accvgpr_read_b32 %1, a95\n\tv_mul_f32 %1, %2, %1\n\tv_accvgpr_write_b32 a95, %1\n\t"
                   "v_accvgpr_read_b32 %0, a96\n\tv_mul_f32 %0, %2, %0\n\tv_accvgpr_write_b32 a96, %0\n\t"
                   "v_accvgpr_read_b32 %1, a97\n\tv_mul_f32 %1, %2, %1\n\tv_accvgpr_write_b32 a97, %1\n\t"
                   "v_accvgpr_read_b32 %0, a98\n\tv_mul_f32 %0, %2, %0\n\tv_accvgpr_write_b32 a98, %0\n\t"
                   "v_accvgpr_read_b32 %1, a99\n\tv_mul_f32 %1, %2, %1\n\tv_accvgpr_write_b32 a99, %1\n\t"
                   "v_accvgpr_read_b32 %0, a100\n\tv_mul_f32 %0, %2, %0\n\tv_accvgpr_write_b32 a100, %0\n\t"
                   "v_accvgpr_read_b32 %1, a101\n\tv_mul_f32 %1, %2, %1\n\tv_accvgpr_write_b32 a101, %1\n\t"
                   "v_accvgpr_read_b32 %0, a102\n\tv_mul_f32 %0, %2, %0\n\tv_accvgpr_write_b32 a102, %0\n\t"
                   "v_accvgpr_read_b32 %1, a103\n\tv_mul_f32 %1, %2, %1\n\tv_accvgpr_write_b32 a103, %1\n\t"
                   "v_accvgpr_read_b32 %0, a104\n\tv_mul_f32 %0, %2, %0\n\tv_accvgpr_write_b32 a104, %0\n\t"
                   "v_accvgpr_read_b32 %1, a105\n\tv_mul_f32 %1, %2, %1\n\tv_accvgpr_write_b32 a105, %1\n\t"
                   "v_accvgpr_read_b32 %0, a106\n\tv_mul_f32 %0, %2, %0\n\tv_accvgpr_write_b32 a106, %0\n\t"
                   "v_accvgpr_read_b32 %1, a107\n\tv_mul_f32 %1, %2, %1\n\tv_accvgpr_write_b32 a107, %1\n\t"
                   "v_accvgpr_read_b32 %0, a108\n\tv_mul_f32 %0, %2, %0\n\tv_accvgpr_write_b32 a108, %0\n\t"
                   "v_accvgpr_read_b32 %1, a109\n\tv_mul_f32 %1, %2, %1\n\tv_accvgpr_write_b32 a109, %1\n\t"
                   "v_accvgpr_read_b32 %0, a110\n\tv_mul_f32 %0, %2, %0\n\tv_accvgpr_write_b32 a110, %0\n\t"
                   "v_accvgpr_read_b32 %1, a111\n\tv_mul_f32 %1, %2, %1\n\tv_accvgpr_write_b32 a111, %1\n\t"
                   "v_accvgpr_read_b32 %0, a112\n\tv_mul_f32 %0, %2, %0\n\tv_accvgpr_write_b32 a112, %0\n\t"
                   "v_accvgpr_read_b32 %1, a113\n\tv_mul_f32 %1, %2, %1\n\tv_accvgpr_write_b32 a113, %1\n\t"
                   "v_accvgpr_read_b32 %0, a114\n\tv_mul_f32 %0, %2, %0\n\tv_accvgpr_write_b32 a114, %0\n\t"
                   "v_accvgpr_read_b32 %1, a115\n\tv_mul_f32 %1, %2, %1\n\tv_accvgpr_write_b32 a115, %1\n\t"
                   "v_accvgpr_read_b32 %0, a116\n\tv_mul_f32 %0, %2, %0\n\tv_accvgpr_write_b32 a116, %0\n\t"
                   "v_accvgpr_read_b32 %1, a117\n\tv_mul_f32 %1, %2, %1\n\tv_accvgpr_write_b32 a117, %1\n\t"
                   "v_accvgpr_read_b32 %0, a118\n\tv_mul_f32 %0, %2, %0\n\tv_accvgpr_write_b32 a118, %0\n\t"
                   "v_accvgpr_read_b32 %1, a119\n\tv_mul_f32 %1, %2, %1\n\tv_accvgpr_write_b32 a119, %1\n\t"
                   "v_accvgpr_read_b32 %0, a120\n\tv_mul_f32 %0, %2, %0\n\tv_accvgpr_write_b32 a120, %0\n\t"
                   "v_accvgpr_read_b32 %1, a121\n\tv_mul_f32 %1, %2, %1\n\tv_accvgpr_write_b32 a121, %1\n\t"
                   "v_accvgpr_read_b32 %0, a122\n\tv_mul_f32 %0, %2, %0\n\tv_accvgpr_write_b32 a122, %0\n\t"
                   "v_accvgpr_read_b32 %1, a123\n\tv_mul_f32 %1, %2, %1\n\tv_accvgpr_write_b32 a123, %1\n\t"
                   "v_accvgpr_read_b32 %0, a124\n\tv_mul_f32 %0, %2, %0\n\tv_accvgpr_write_b32 a124, %0\n\t"
                   "v_accvgpr_read_b32 %1, a125\n\tv_mul_f32 %1, %2, %1\n\tv_accvgpr_write_b32 a125, %1\n\t"
                   "v_accvgpr_read_b32 %0, a126\n\tv_mul_f32 %0, %2, %0\n\tv_accvgpr_write_b32 a126, %0\n\t"
                   "v_accvgpr_read_b32 %1, a127\n\tv_mul_f32 %1, %2, %1\n\tv_accvgpr_write_b32 a127, %1\n\t"
                   "v_accvgpr_read_b32 %0, a128\n\tv_mul_f32 %0, %3, %0\n\tv_accvgpr_write_b32 a128, %0\n\t"
                   "v_accvgpr_read_b32 %1, a129\n\tv_mul_f32 %1, %3, %1\n\tv_accvgpr_write_b32 a129, %1\n\t"
                   "v_accvgpr_read_b32 %0, a130\n\tv_mul_f32 %0, %3, %0\n\tv_accvgpr_write_b32 a130, %0\n\t"
                   "v_accvgpr_read_b32 %1, a131\n\tv_mul_f32 %1, %3, %1\n\tv_accvgpr_write_b32 a131, %1\n\t"
                   "v_accvgpr_read_b32 %0, a132\n\tv_mul_f32 %0, %3, %0\n\tv_accvgpr_write_b32 a132, %0\n\t"
                   "v_accvgpr_read_b32 %1, a133\n\tv_mul_f32 %1, %3, %1\n\tv_accvgpr_write_b32 a133, %1\n\t"
                   "v_accvgpr_read_b32 %0, a134\n\tv_mul_f32 %0, %3, %0\n\tv_accvgpr_write_b32 a134, %0\n\t"
                   "v_accvgpr_read_b32 %1, a135\n\tv_mul_f32 %1, %3, %1\n\tv_accvgpr_write_b32 a135, %1\n\t"
                   "v_accvgpr_read_b32 %0, a136\n\tv_mul_f32 %0, %3, %0\n\tv_accvgpr_write_b32 a136, %0\n\t"
                   "v_accvgpr_read_b32 %1, a137\n\tv_mul_f32 %1, %3, %1\n\tv_accvgpr_write_b32 a137, %1\n\t"
                   "v_accvgpr_read_b32 %0, a138\n\tv_mul_f32 %0, %3, %0\n\tv_accvgpr_write_b32 a138, %0\n\t"
                   "v_accvgpr_read_b32 %1, a139\n\tv_mul_f32 %1, %3, %1\n\tv_accvgpr_write_b32 a139, %1\n\t"
                   "v_accvgpr_read_b32 %0, a140\n\tv_mul_f32 %0, %3, %0\n\tv_accvgpr_write_b32 a140, %0\n\t"
                   "v_accvgpr_read_b32 %1, a141\n\tv_mul_f32 %1, %3, %1\n\tv_accvgpr_write_b32 a141, %1\n\t"
                   "v_accvgpr_read_b32 %0, a142\n\tv_mul_f32 %0, %3, %0\n\tv_accvgpr_write_b32 a142, %0\n\t"
                   "v_accvgpr_read_b32 %1, a143\n\tv_mul_f32 %1, %3, %1\n\tv_accvgpr_write_b32 a143, %1\n\t"
                   "v_accvgpr_read_b32 %0, a144\n\tv_mul_f32 %0, %3, %0\n\tv_accvgpr_write_b32 a144, %0\n\t"
                   "v_accvgpr_read_b32 %1, a145\n\tv_mul_f32 %1, %3, %1\n\tv_accvgpr_write_b32 a145, %1\n\t"
                   "v_accvgpr_read_b32 %0, a146\n\tv_mul_f32 %0, %3, %0\n\tv_accvgpr_write_b32 a146, %0\n\t"
                   "v_accvgpr_read_b32 %1, a147\n\tv_mul_f32 %1, %3, %1\n\tv_accvgpr_write_b32 a147, %1\n\t"
                   "v_accvgpr_read_b32 %0, a148\n\tv_mul_f32 %0, %3, %0\n\tv_accvgpr_write_b32 a148, %0\n\t"
                   "v_accvgpr_read_b32 %1, a149\n\tv_mul_f32 %1, %3, %1\n\tv_accvgpr_write_b32 a149, %1\n\t"
                   "v_accvgpr_read_b32 %0, a150\n\tv_mul_f32 %0, %3, %0\n\tv_accvgpr_write_b32 a150, %0\n\t"
                   "v_accvgpr_read_b32 %1, a151\n\tv_mul_f32 %1, %3, %1\n\tv_accvgpr_write_b32 a151, %1\n\t"
                   "v_accvgpr_read_b32 %0, a152\n\tv_mul_f32 %0, %3, %0\n\tv_accvgpr_write_b32 a152, %0\n\t"
                   "v_accvgpr_read_b32 %1, a153\n\tv_mul_f32 %1, %3, %1\n\tv_accvgpr_write_b32 a153, %1\n\t"
                   "v_accvgpr_read_b32 %0, a154\n\tv_mul_f32 %0, %3, %0\n\tv_accvgpr_write_b32 a154, %0\n\t"
                   "v_accvgpr_read_b32 %1, a155\n\tv_mul_f32 %1, %3, %1\n\tv_accvgpr_write_b32 a155, %1\n\t"
                   "v_accvgpr_read_b32 %0, a156\n\tv_mul_f32 %0, %3, %0\n\tv_accvgpr_write_b32 a156, %0\n\t"
                   "v_accvgpr_read_b32 %1, a157\n\tv_mul_f32 %1, %3, %1\n\tv_accvgpr_write_b32 a157, %1\n\t"
                   "v_accvgpr_read_b32 %0, a158\n\tv_mul_f32 %0, %3, %0\n\tv_accvgpr_write_b32 a158, %0\n\t"
                   "v_accvgpr_read_b32 %1, a159\n\tv_mul_f32 %1, %3, %1\n\tv_accvgpr_write_b32 a159, %1\n\t"
                   "v_accvgpr_read_b32 %0, a160\n\tv_mul_f32 %0, %3, %0\n\tv_accvgpr_write_b32 a160, %0\n\t"
                   "v_accvgpr_read_b32 %1, a161\n\tv_mul_f32 %1, %3, %1\n\tv_accvgpr_write_b32 a161, %1\n\t"
                   "v_accvgpr_read_b32 %0, a162\n\tv_mul_f32 %0, %3, %0\n\tv_accvgpr_write_b32 a162, %0\n\t"
                   "v_accvgpr_read_b32 %1, a163\n\tv_mul_f32 %1, %3, %1\n\tv_accvgpr_write_b32 a163, %1\n\t"
                   "v_accvgpr_read_b32 %0, a164\n\tv_mul_f32 %0, %3, %0\n\tv_accvgpr_write_b32 a164, %0\n\t"
                   "v_accvgpr_read_b32 %1, a165\n\tv_mul_f32 %1, %3, %1\n\tv_accvgpr_write_b32 a165, %1\n\t"
                   "v_accvgpr_read_b32 %0, a166\n\tv_mul_f32 %0, %3, %0\n\tv_accvgpr_write_b32 a166, %0\n\t"
                   "v_accvgpr_read_b32 %1, a167\n\tv_mul_f32 %1, %3, %1\n\tv_accvgpr_write_b32 a167, %1\n\t"
                   "v_accvgpr_read_b32 %0, a168\n\tv_mul_f32 %0, %3, %0\n\tv_accvgpr_write_b32 a168, %0\n\t"
                   "v_accvgpr_read_b32 %1, a169\n\tv_mul_f32 %1, %3, %1\n\tv_accvgpr_write_b32 a169, %1\n\t"
                   "v_accvgpr_read_b32 %0, a170\n\tv_mul_f32 %0, %3, %0\n\tv_accvgpr_write_b32 a170, %0\n\t"
                   "v_accvgpr_read_b32 %1, a171\n\tv_mul_f32 %1, %3, %1\n\tv_accvgpr_write_b32 a171, %1\n\t"
                   "v_accvgpr_read_b32 %0, a172\n\tv_mul_f32 %0, %3, %0\n\tv_accvgpr_write_b32 a172, %0\n\t"
                   "v_accvgpr_read_b32 %1, a173\n\tv_mul_f32 %1, %3, %1\n\tv_accvgpr_write_b32 a173, %1\n\t"
                   "v_accvgpr_read_b32 %0, a174\n\tv_mul_f32 %0, %3, %0\n\tv_accvgpr_write_b32 a174, %0\n\t"
                   "v_accvgpr_read_b32 %1, a175\n\tv_mul_f32 %1, %3, %1\n\tv_accvgpr_write_b32 a175, %1\n\t"
                   "v_accvgpr_read_b32 %0, a176\n\tv_mul_f32 %0, %3, %0\n\tv_accvgpr_write_b32 a176, %0\n\t"
                   "v_accvgpr_read_b32 %1, a177\n\tv_mul_f32 %1, %3, %1\n\tv_accvgpr_write_b32 a177, %1\n\t"
                   "v_accvgpr_read_b32 %0, a178\n\tv_mul_f32 %0, %3, %0\n\tv_accvgpr_write_b32 a178, %0\n\t"
                   "v_accvgpr_read_b32 %1, a179\n\tv_mul_f32 %1, %3, %1\n\tv_accvgpr_write_b32 a179, %1\n\t"
                   "v_accvgpr_read_b32 %0, a180\n\tv_mul_f32 %0, %3, %0\n\tv_accvgpr_write_b32 a180, %0\n\t"
                   "v_accvgpr_read_b32 %1, a181\n\tv_mul_f32 %1, %3, %1\n\tv_accvgpr_write_b32 a181, %1\n\t"
                   "v_accvgpr_read_b32 %0, a182\n\tv_mul_f32 %0, %3, %0\n\tv_accvgpr_write_b32 a182, %0\n\t"
                   "v_accvgpr_read_b32 %1, a183\n\tv_mul_f32 %1, %3, %1\n\tv_accvgpr_write_b32 a183, %1\n\t"
                   "v_accvgpr_read_b32 %0, a184\n\tv_mul_f32 %0, %3, %0\n\tv_accvgpr_write_b32 a184, %0\n\t"
                   "v_accvgpr_read_b32 %1, a185\n\tv_mul_f32 %1, %3, %1\n\tv_accvgpr_write_b32 a185, %1\n\t"
                   "v_accvgpr_read_b32 %0, a186\n\tv_mul_f32 %0, %3, %0\n\tv_accvgpr_write_b32 a186, %0\n\t"
                   "v_accvgpr_read_b32 %1, a187\n\tv_mul_f32 %1, %3, %1\n\tv_accvgpr_write_b32 a187, %1\n\t"
                   "v_accvgpr_read_b32 %0, a188\n\tv_mul_f32 %0, %3, %0\n\tv_accvgpr_write_b32 a188, %0\n\t"
                   "v_accvgpr_read_b32 %1, a189\n\tv_mul_f32 %1, %3, %1\n\tv_accvgpr_write_b32 a189, %1\n\t"
                   "v_accvgpr_read_b32 %0, a190\n\tv_mul_f32 %0, %3, %0\n\tv_accvgpr_write_b32 a190, %0\n\t"
                   "v_accvgpr_read_b32 %1, a191\n\tv_mul_f32 %1, %3, %1\n\tv_accvgpr_write_b32 a191, %1\n\t"
                   "v_accvgpr_read_b32 %0, a192\n\tv_mul_f32 %0, %3, %0\n\tv_accvgpr_write_b32 a192, %0\n\t"
                   "v_accvgpr_read_b32 %1, a193\n\tv_mul_f32 %1, %3, %1\n\tv_accvgpr_write_b32 a193, %1\n\t"
                   "v_accvgpr_read_b32 %0, a194\n\tv_mul_f32 %0, %3, %0\n\tv_accvgpr_write_b32 a194, %0\n\t"
                   "v_accvgpr_read_b32 %1, a195\n\tv_mul_f32 %1, %3, %1\n\tv_accvgpr_write_b32 a195, %1\n\t"
                   "v_accvgpr_read_b32 %0, a196\n\tv_mul_f32 %0, %3, %0\n\tv_accvgpr_write_b32 a196, %0\n\t"
                   "v_accvgpr_read_b32 %1, a197\n\tv_mul_f32 %1, %3, %1\n\tv_accvgpr_write_b32 a197, %1\n\t"
                   "v_accvgpr_read_b32 %0, a198\n\tv_mul_f32 %0, %3, %0\n\tv_accvgpr_write_b32 a198, %0\n\t"
                   "v_accvgpr_read_b32 %1, a199\n\tv_mul_f32 %1, %3, %1\n\tv_accvgpr_write_b32 a199, %1\n\t"
                   "v_accvgpr_read_b32 %0, a200\n\tv_mul_f32 %0, %3, %0\n\tv_accvgpr_write_b32 a200, %0\n\t"
                   "v_accvgpr_read_b32 %1, a201\n\tv_mul_f32 %1, %3, %1\n\tv_accvgpr_write_b32 a201, %1\n\t"
                   "v_accvgpr_read_b32 %0, a202\n\tv_mul_f32 %0, %3, %0\n\tv_accvgpr_write_b32 a202, %0\n\t"
                   "v_accvgpr_read_b32 %1, a203\n\tv_mul_f32 %1, %3, %1\n\tv_accvgpr_write_b32 a203, %1\n\t"
                   "v_accvgpr_read_b32 %0, a204\n\tv_mul_f32 %0, %3, %0\n\tv_accvgpr_write_b32 a204, %0\n\t"
                   "v_accvgpr_read_b32 %1, a205\n\tv_mul_f32 %1, %3, %1\n\tv_accvgpr_write_b32 a205, %1\n\t"
                   "v_accvgpr_read_b32 %0, a206\n\tv_mul_f32 %0, %3, %0\n\tv_accvgpr_write_b32 a206, %0\n\t"
                   "v_accvgpr_read_b32 %1, a207\n\tv_mul_f32 %1, %3, %1\n\tv_accvgpr_write_b32 a207, %1\n\t"
                   "v_accvgpr_read_b32 %0, a208\n\tv_mul_f32 %0, %3, %0\n\tv_accvgpr_write_b32 a208, %0\n\t"
                   "v_accvgpr_read_b32 %1, a209\n\tv_mul_f32 %1, %3, %1\n\tv_accvgpr_write_b32 a209, %1\n\t"
                   "v_accvgpr_read_b32 %0, a210\n\tv_mul_f32 %0, %3, %0\n\tv_accvgpr_write_b32 a210, %0\n\t"
                   "v_accvgpr_read_b32 %1, a211\n\tv_mul_f32 %1, %3, %1\n\tv_accvgpr_write_b32 a211, %1\n\t"
                   "v_accvgpr_read_b32 %0, a212\n\tv_mul_f32 %0, %3, %0\n\tv_accvgpr_write_b32 a212, %0\n\t"
                   "v_accvgpr_read_b32 %1, a213\n\tv_mul_f32 %1, %3, %1\n\tv_accvgpr_write_b32 a213, %1\n\t"
                   "v_accvgpr_read_b32 %0, a214\n\tv_mul_f32 %0, %3, %0\n\tv_accvgpr_write_b32 a214, %0\n\t"
                   "v_accvgpr_read_b32 %1, a215\n\tv_mul_f32 %1, %3, %1\n\tv_accvgpr_write_b32 a215, %1\n\t"
                   "v_accvgpr_read_b32 %0, a216\n\tv_mul_f32 %0, %3, %0\n\tv_accvgpr_write_b32 a216, %0\n\t"
                   "v_accvgpr_read_b32 %1, a217\n\tv_mul_f32 %1, %3, %1\n\tv_accvgpr_write_b32 a217, %1\n\t"
                   "v_accvgpr_read_b32 %0, a218\n\tv_mul_f32 %0, %3, %0\n\tv_accvgpr_write_b32 a218, %0\n\t"
                   "v_accvgpr_read_b32 %1, a219\n\tv_mul_f32 %1, %3, %1\n\tv_accvgpr_write_b32 a219, %1\n\t"
                   "v_accvgpr_read_b32 %0, a220\n\tv_mul_f32 %0, %3, %0\n\tv_accvgpr_write_b32 a220, %0\n\t"
                   "v_accvgpr_read_b32 %1, a221\n\tv_mul_f32 %1, %3, %1\n\tv_accvgpr_write_b32 a221, %1\n\t"
                   "v_accvgpr_read_b32 %0, a222\n\tv_mul_f32 %0, %3, %0\n\tv_accvgpr_write_b32 a222, %0\n\t"
                   "v_accvgpr_read_b32 %1, a223\n\tv_mul_f32 %1, %3, %1\n\tv_accvgpr_write_b32 a223, %1\n\t"
                   "v_accvgpr_read_b32 %0, a224\n\tv_mul_f32 %0, %3, %0\n\tv_accvgpr_write_b32 a224, %0\n\t"
                   "v_accvgpr_read_b32 %1, a225\n\tv_mul_f32 %1, %3, %1\n\tv_accvgpr_write_b32 a225, %1\n\t"
                   "v_accvgpr_read_b32 %0, a226\n\tv_mul_f32 %0, %3, %0\n\tv_accvgpr_write_b32 a226, %0\n\t"
                   "v_accvgpr_read_b32 %1, a227\n\tv_mul_f32 %1, %3, %1\n\tv_accvgpr_write_b32 a227, %1\n\t"
                   "v_accvgpr_read_b32 %0, a228\n\tv_mul_f32 %0, %3, %0\n\tv_accvgpr_write_b32 a228, %0\n\t"
                   "v_accvgpr_read_b32 %1, a229\n\tv_mul_f32 %1, %3, %1\n\tv_accvgpr_write_b32 a229, %1\n\t"
                   "v_accvgpr_read_b32 %0, a230\n\tv_mul_f32 %0, %3, %0\n\tv_accvgpr_write_b32 a230, %0\n\t"
                   "v_accvgpr_read_b32 %1, a231\n\tv_mul_f32 %1, %3, %1\n\tv_accvgpr_write_b32 a231, %1\n\t"
                   "v_accvgpr_read_b32 %0, a232\n\tv_mul_f32 %0, %3, %0\n\tv_accvgpr_write_b32 a232, %0\n\t"
                   "v_accvgpr_read_b32 %1, a233\n\tv_mul_f32 %1, %3, %1\n\tv_accvgpr_write_b32 a233, %1\n\t"
                   "v_accvgpr_read_b32 %0, a234\n\tv_mul_f32 %0, %3, %0\n\tv_accvgpr_write_b32 a234, %0\n\t"
                   "v_accvgpr_read_b32 %1, a235\n\tv_mul_f32 %1, %3, %1\n\tv_accvgpr_write_b32 a235, %1\n\t"
                   "v_accvgpr_read_b32 %0, a236\n\tv_mul_f32 %0, %3, %0\n\tv_accvgpr_write_b32 a236, %0\n\t"
                   "v_accvgpr_read_b32 %1, a237\n\tv_mul_f32 %1, %3, %1\n\tv_accvgpr_write_b32 a237, %1\n\t"
                   "v_accvgpr_read_b32 %0, a238\n\tv_mul_f32 %0, %3, %0\n\tv_accvgpr_write_b32 a238, %0\n\t"
                   "v_accvgpr_read_b32 %1, a239\n\tv_mul_f32 %1, %3, %1\n\tv_accvgpr_write_b32 a239, %1\n\t"
                   "v_accvgpr_read_b32 %0, a240\n\tv_mul_f32 %0, %3, %0\n\tv_accvgpr_write_b32 a240, %0\n\t"
                   "v_accvgpr_read_b32 %1, a241\n\tv_mul_f32 %1, %3, %1\n\tv_accvgpr_write_b32 a241, %1\n\t"
                   "v_accvgpr_read_b32 %0, a242\n\tv_mul_f32 %0, %3, %0\n\tv_accvgpr_write_b32 a242, %0\n\t"
                   "v_accvgpr_read_b32 %1, a243\n\tv_mul_f32 %1, %3, %1\n\tv_accvgpr_write_b32 a243, %1\n\t"
                   "v_accvgpr_read_b32 %0, a244\n\tv_mul_f32 %0, %3, %0\n\tv_accvgpr_write_b32 a244, %0\n\t"
                   "v_accvgpr_read_b32 %1, a245\n\tv_mul_f32 %1, %3, %1\n\tv_accvgpr_write_b32 a245, %1\n\t"
                   "v_accvgpr_read_b32 %0, a246\n\tv_mul_f32 %0, %3, %0\n\tv_accvgpr_write_b32 a246, %0\n\t"
                   "v_accvgpr_read_b32 %1, a247\n\tv_mul_f32 %1, %3, %1\n\tv_accvgpr_write_b32 a247, %1\n\t"
                   "v_accvgpr_read_b32 %0, a248\n\tv_mul_f32 %0, %3, %0\n\tv_accvgpr_write_b32 a248, %0\n\t"
                   "v_accvgpr_read_b32 %1, a249\n\tv_mul_f32 %1, %3, %1\n\tv_accvgpr_write_b32 a249, %1\n\t"
                   "v_accvgpr_read_b32 %0, a250\n\tv_mul_f32 %0, %3, %0\n\tv_accvgpr_write_b32 a250, %0\n\t"
                   "v_accvgpr_read_b32 %1, a251\n\tv_mul_f32 %1, %3, %1\n\tv_accvgpr_write_b32 a251, %1\n\t"
                   "v_accvgpr_read_b32 %0, a252\n\tv_mul_f32 %0, %3, %0\n\tv_accvgpr_write_b32 a252, %0\n\t"
                   "v_accvgpr_read_b32 %1, a253\n\tv_mul_f32 %1, %3, %1\n\tv_accvgpr_write_b32 a253, %1\n\t"
                   "v_accvgpr_read_b32 %0, a254\n\tv_mul_f32 %0, %3, %0\n\tv_accvgpr_write_b32 a254, %0\n\t"
                   "v_accvgpr_read_b32 %1, a255\n\tv_mul_f32 %1, %3, %1\n\tv_accvgpr_write_b32 a255, %1\n\t"
                   "s_nop 1"
                   : "=&v"(t0), "=&v"(t1) : "v"(c.fct[0]), "v"(c.fct[1]) : ACLOB);
      c.pend = false;
    }
  }
  asm volatile("s_nop 1" ::: "memory");
  GAP();
}
template <int T, bool HASPV, bool HASSM> DI void phaseB5(Ctx4& c) {
  phaseB5_<T, HASPV, HASSM>(c, std::make_integer_sequence<int, 32>{});
}

__global__ __launch_bounds__(256, 1) void attn5_kernel(const u16* __restrict__ qfrag, const u16* __restrict__ kc,
                                                       const u16* __restrict__ vc, const u16* __restrict__ wo,
                                                       u16* __restrict__ o, u16* __restrict__ part,
                                                       float* __restrict__ mlg, unsigned* __restrict__ cnt DBGARG) {
  extern __shared__ __attribute__((aligned(16))) char lds[];
  STAMP(0);
  const int tid = threadIdx.x, lane = tid & 63;
  const int w = __builtin_amdgcn_readfirstlane(tid >> 6);
  const int r = lane & 31, h = lane >> 5;
  const int bid = blockIdx.x;
  const int xcd = bid & 7, j = bid >> 3;
  const int grp = (j >> 2) * 8 + xcd, sp = j & 3;
  const int b = grp >> 3, kvh = grp & 7;

  const u16* Kw = kc + ((size_t)(b * HKV + kvh) * SK + 256 * sp) * DH;
  const u16* Vw = vc + ((size_t)(b * HKV + kvh) * SK + 256 * sp) * DH;

  Ctx4 c;
  c.pfd = 0;
  c.ring = lds + w * 2048;
  c.rk = __builtin_amdgcn_make_buffer_rsrc((void*)Kw, 0, 256 * DH * 2, 0x00020000);
  c.rv = __builtin_amdgcn_make_buffer_rsrc((void*)Vw, 0, 256 * DH * 2, 0x00020000);
#pragma unroll
  for (int pi = 0; pi < 2; ++pi) {
    const int krow = 4 * (2 * w + pi) + (lane >> 4);
    c.koff[pi] = krow * 512 + (((lane & 15) ^ (krow & 15)) * 16);
    const int vrow = 2 * (2 * w + pi) + (lane >> 5);
    c.voff[pi] = vrow * 512 + (((lane & 31) ^ ((vrow & 3) << 2)) * 16);
  }
  {
    const u16* qp = qfrag + ((size_t)(grp * 8 + 2 * w) * 16 * 64 + lane) * 8;
#pragma unroll
    for (int qb = 0; qb < 2; ++qb)
#pragma unroll
      for (int s = 0; s < 16; ++s) c.qf[qb][s] = *(const bf16x8*)(qp + (size_t)(qb * 16 + s) * 512);
  }
  GAP();
  dma5_unit<0>(c); dma5_unit<1>(c); dma5_unit<2>(c); dma5_unit<3>(c); dma5_unit<4>(c); dma5_unit<5>(c); dma5_unit<6>(c);
  dma5_unit<7>(c); dma5_unit<8>(c); dma5_unit<9>(c); dma5_unit<10>(c); dma5_unit<11>(c); dma5_unit<12>(c); dma5_unit<13>(c);
#pragma unroll
  for (int qb = 0; qb < 2; ++qb)
#pragma unroll
    for (int s = 0; s < 16; ++s) asm volatile("" : "+v"(c.qf[qb][s]));
  if (w == 3) {
#pragma unroll
    for (int s = 0; s < 16; ++s) c.qf[1][s] = bf16x8{0, 0, 0, 0, 0, 0, 0, 0};
  }
  STAMP(1);
  {
    const unsigned rb = (unsigned)(size_t)lds;
#pragma unroll
    for (int ss = 0; ss < 8; ++ss) c.kaddr[ss] = rb + r * 256 + ((((2 * ss + h) ^ (r & 15))) * 16);
    const int g = lane >> 4, qq = (lane >> 2) & 3, pp = lane & 3;
#pragma unroll
    for (int d = 0; d < 4; ++d)
      c.vaddr[d] = rb + (4 * (g >> 1) + qq) * 512 + ((64 * d + 32 * (g & 1) + 8 * pp) ^ (qq << 6));
  }
  c.pend = false;
  c.fct[0] = 1.f; c.fct[1] = 1.f;

  phaseA5<0>(c); phaseB5<0, false, true>(c); STAMP(2);
  phaseA5<1>(c); phaseB5<1, true, true>(c); STAMP(3);
  phaseA5<2>(c); phaseB5<2, true, true>(c); STAMP(4);
  phaseA5<3>(c); phaseB5<3, true, true>(c); STAMP(5);
  phaseA5<4>(c); phaseB5<4, true, true>(c); STAMP(6);
  phaseA5<5>(c); phaseB5<5, true, true>(c); STAMP(7);
  phaseA5<6>(c); phaseB5<6, true, true>(c); STAMP(8);
  {
    int li = (bid * 4 + w) * 28 + (lane < 28 ? lane : 27);
    asm volatile("global_load_dword %0, %1, off" : "+v"(c.pfd) : "v"(wo + (size_t)li * 64) : "memory");
  }
  phaseA5<7>(c); phaseB5<7, true, true>(c); STAMP(9);
  phaseB5<8, true, false>(c);
  asm volatile("s_nop 7\n\ts_nop 7" :: "v"(c.pfd) : "memory");
  STAMP(13);

  float lt[2], inv[2];
#pragma unroll
  for (int qb = 0; qb < 2; ++qb) {
    auto rr = __builtin_amdgcn_permlane32_swap(__float_as_uint(c.l[qb]), __float_as_uint(c.l[qb]), false, false);
    lt[qb] = __uint_as_float(rr[0]) + __uint_as_float(rr[1]);
    inv[qb] = 1.f / lt[qb];
  }
  __amdgpu_buffer_rsrc_t rml = __builtin_amdgcn_make_buffer_rsrc((void*)(mlg + (size_t)grp * 4 * 512), 0, 4 * 512 * 4, 0x00020000);
  __amdgpu_buffer_rsrc_t rpt = __builtin_amdgcn_make_buffer_rsrc((void*)(part + (size_t)grp * 16 * 16384), 0, 16 * 32768, 0x00020000);
  if (h == 0) {
#pragma unroll
    for (int qb = 0; qb < 2; ++qb) {
      u32x2 v = {__float_as_uint(c.m[qb]), __float_as_uint(lt[qb])};
      __builtin_amdgcn_raw_buffer_store_b64(v, rml, (sp * 256 + 64 * w + 32 * qb + r) * 8, 0, 16);
    }
  }
  switch (sp) {
    case 0: {
      { f32x4 a = acc_read4<32>(), b2 = acc_read4<36>(); a *= inv[0]; b2 *= inv[0];
        u32x4 v = {cvtpk(a[0], a[1]), cvtpk(a[2], a[3]), cvtpk(b2[0], b2[1]), cvtpk(b2[2], b2[3])};
        __builtin_amdgcn_raw_buffer_store_b128(v, rpt, 32768 + w * 8192 + 0 + lane * 16, 0, 16); }
      { f32x4 a = acc_read4<40>(), b2 = acc_read4<44>(); a *= inv[0]; b2 *= inv[0];
        u32x4 v = {cvtpk(a[0], a[1]), cvtpk(a[2], a[3]), cvtpk(b2[0], b2[1]), cvtpk(b2[2], b2[3])};
        __builtin_amdgcn_raw_buffer_store_b128(v, rpt, 32768 + w * 8192 + 1024 + lane * 16, 0, 16); }
      { f32x4 a = acc_read4<48>(), b2 = acc_read4<52>(); a *= inv[0]; b2 *= inv[0];
        u32x4 v = {cvtpk(a[0], a[1]), cvtpk(a[2], a[3]), cvtpk(b2[0], b2[1]), cvtpk(b2[2], b2[3])};
        __builtin_amdgcn_raw_buffer_store_b128(v, rpt, 32768 + w * 8192 + 2048 + lane * 16, 0, 16); }
      { f32x4 a = acc_read4<56>(), b2 = acc_read4<60>(); a *= inv[0]; b2 *= inv[0];
        u32x4 v = {cvtpk(a[0], a[1]), cvtpk(a[2], a[3]), cvtpk(b2[0], b2[1]), cvtpk(b2[2], b2[3])};
        __builtin_amdgcn_raw_buffer_store_b128(v, rpt, 32768 + w * 8192 + 3072 + lane * 16, 0, 16); }
      { f32x4 a = acc_read4<160>(), b2 = acc_read4<164>(); a *= inv[1]; b2 *= inv[1];
        u32x4 v = {cvtpk(a[0], a[1]), cvtpk(a[2], a[3]), cvtpk(b2[0], b2[1]), cvtpk(b2[2], b2[3])};
        __builtin_amdgcn_raw_buffer_store_b128(v, rpt, 32768 + w * 8192 + 4096 + lane * 16, 0, 16); }
      { f32x4 a = acc_read4<168>(), b2 = acc_read4<172>(); a *= inv[1]; b2 *= inv[1];
        u32x4 v = {cvtpk(a[0], a[1]), cvtpk(a[2], a[3]), cvtpk(b2[0], b2[1]), cvtpk(b2[2], b2[3])};
        __builtin_amdgcn_raw_buffer_store_b128(v, rpt, 32768 + w * 8192 + 5120 + lane * 16, 0, 16); }
      { f32x4 a = acc_read4<176>(), b2 = acc_read4<180>(); a *= inv[1]; b2 *= inv[1];
        u32x4 v = {cvtpk(a[0], a[1]), cvtpk(a[2], a[3]), cvtpk(b2[0], b2[1]), cvtpk(b2[2], b2[3])};
        __builtin_amdgcn_raw_buffer_store_b128(v, rpt, 32768 + w * 8192 + 6144 + lane * 16, 0, 16); }
      { f32x4 a = acc_read4<184>(), b2 = acc_read4<188>(); a *= inv[1]; b2 *= inv[1];
        u32x4 v = {cvtpk(a[0], a[1]), cvtpk(a[2], a[3]), cvtpk(b2[0], b2[1]), cvtpk(b2[2], b2[3])};
        __builtin_amdgcn_raw_buffer_store_b128(v, rpt, 32768 + w * 8192 + 7168 + lane * 16, 0, 16); }
      { f32x4 a = acc_read4<64>(), b2 = acc_read4<68>(); a *= inv[0]; b2 *= inv[0];
        u32x4 v = {cvtpk(a[0], a[1]), cvtpk(a[2], a[3]), cvtpk(b2[0], b2[1]), cvtpk(b2[2], b2[3])};
        __builtin_amdgcn_raw_buffer_store_b128(v, rpt, 65536 + w * 8192 + 0 + lane * 16, 0, 16); }
      { f32x4 a = acc_read4<72>(), b2 = acc_read4<76>(); a *= inv[0]; b2 *= inv[0];
        u32x4 v = {cvtpk(a[0], a[1]), cvtpk(a[2], a[3]), cvtpk(b2[0], b2[1]), cvtpk(b2[2], b2[3])};
        __builtin_amdgcn_raw_buffer_store_b128(v, rpt, 65536 + w * 8192 + 1024 + lane * 16, 0, 16); }
      { f32x4 a = acc_read4<80>(), b2 = acc_read4<84>(); a *= inv[0]; b2 *= inv[0];
        u32x4 v = {cvtpk(a[0], a[1]), cvtpk(a[2], a[3]), cvtpk(b2[0], b2[1]), cvtpk(b2[2], b2[3])};
        __builtin_amdgcn_raw_buffer_store_b128(v, rpt, 65536 + w * 8192 + 2048 + lane * 16, 0, 16); }
      { f32x4 a = acc_read4<88>(), b2 = acc_read4<92>(); a *= inv[0]; b2 *= inv[0];
        u32x4 v = {cvtpk(a[0], a[1]), cvtpk(a[2], a[3]), cvtpk(b2[0], b2[1]), cvtpk(b2[2], b2[3])};
        __builtin_amdgcn_raw_buffer_store_b128(v, rpt, 65536 + w * 8192 + 3072 + lane * 16, 0, 16); }
      { f32x4 a = acc_read4<192>(), b2 = acc_read4<196>(); a *= inv[1]; b2 *= inv[1];
        u32x4 v = {cvtpk(a[0], a[1]), cvtpk(a[2], a[3]), cvtpk(b2[0], b2[1]), cvtpk(b2[2], b2[3])};
        __builtin_amdgcn_raw_buffer_store_b128(v, rpt, 65536 + w * 8192 + 4096 + lane * 16, 0, 16); }
      { f32x4 a = acc_read4<200>(), b2 = acc_read4<204>(); a *= inv[1]; b2 *= inv[1];
        u32x4 v = {cvtpk(a[0], a[1]), cvtpk(a[2], a[3]), cvtpk(b2[0], b2[1]), cvtpk(b2[2], b2[3])};
        __builtin_amdgcn_raw_buffer_store_b128(v, rpt, 65536 + w * 8192 + 5120 + lane * 16, 0, 16); }
      { f32x4 a = acc_read4<208>(), b2 = acc_read4<212>(); a *= inv[1]; b2 *= inv[1];
        u32x4 v = {cvtpk(a[0], a[1]), cvtpk(a[2], a[3]), cvtpk(b2[0], b2[1]), cvtpk(b2[2], b2[3])};
        __builtin_amdgcn_raw_buffer_store_b128(v, rpt, 65536 + w * 8192 + 6144 + lane * 16, 0, 16); }
      { f32x4 a = acc_read4<216>(), b2 = acc_read4<220>(); a *= inv[1]; b2 *= inv[1];
        u32x4 v = {cvtpk(a[0], a[1]), cvtpk(a[2], a[3]), cvtpk(b2[0], b2[1]), cvtpk(b2[2], b2[3])};
        __builtin_amdgcn_raw_buffer_store_b128(v, rpt, 65536 + w * 8192 + 7168 + lane * 16, 0, 16); }
      { f32x4 a = acc_read4<96>(), b2 = acc_read4<100>(); a *= inv[0]; b2 *= inv[0];
        u32x4 v = {cvtpk(a[0], a[1]), cvtpk(a[2], a[3]), cvtpk(b2[0], b2[1]), cvtpk(b2[2], b2[3])};
        __builtin_amdgcn_raw_buffer_store_b128(v, rpt, 98304 + w * 8192 + 0 + lane * 16, 0, 16); }
      { f32x4 a = acc_read4<104>(), b2 = acc_read4<108>(); a *= inv[0]; b2 *= inv[0];
        u32x4 v = {cvtpk(a[0], a[1]), cvtpk(a[2], a[3]), cvtpk(b2[0], b2[1]), cvtpk(b2[2], b2[3])};
        __builtin_amdgcn_raw_buffer_store_b128(v, rpt, 98304 + w * 8192 + 1024 + lane * 16, 0, 16); }
      { f32x4 a = acc_read4<112>(), b2 = acc_read4<116>(); a *= inv[0]; b2 *= inv[0];
        u32x4 v = {cvtpk(a[0], a[1]), cvtpk(a[2], a[3]), cvtpk(b2[0], b2[1]), cvtpk(b2[2], b2[3])};
        __builtin_amdgcn_raw_buffer_store_b128(v, rpt, 98304 + w * 8192 + 2048 + lane * 16, 0, 16); }
      { f32x4 a = acc_read4<120>(), b2 = acc_read4<124>(); a *= inv[0]; b2 *= inv[0];
        u32x4 v = {cvtpk(a[0], a[1]), cvtpk(a[2], a[3]), cvtpk(b2[0], b2[1]), cvtpk(b2[2], b2[3])};
        __builtin_amdgcn_raw_buffer_store_b128(v, rpt, 98304 + w * 8192 + 3072 + lane * 16, 0, 16); }
      { f32x4 a = acc_read4<224>(), b2 = acc_read4<228>(); a *= inv[1]; b2 *= inv[1];
        u32x4 v = {cvtpk(a[0], a[1]), cvtpk(a[2], a[3]), cvtpk(b2[0], b2[1]), cvtpk(b2[2], b2[3])};
        __builtin_amdgcn_raw_buffer_store_b128(v, rpt, 98304 + w * 8192 + 4096 + lane * 16, 0, 16); }
      { f32x4 a = acc_read4<232>(), b2 = acc_read4<236>(); a *= inv[1]; b2 *= inv[1];
        u32x4 v = {cvtpk(a[0], a[1]), cvtpk(a[2], a[3]), cvtpk(b2[0], b2[1]), cvtpk(b2[2], b2[3])};
        __builtin_amdgcn_raw_buffer_store_b128(v, rpt, 98304 + w * 8192 + 5120 + lane * 16, 0, 16); }
      { f32x4 a = acc_read4<240>(), b2 = acc_read4<244>(); a *= inv[1]; b2 *= inv[1];
        u32x4 v = {cvtpk(a[0], a[1]), cvtpk(a[2], a[3]), cvtpk(b2[0], b2[1]), cvtpk(b2[2], b2[3])};
        __builtin_amdgcn_raw_buffer_store_b128(v, rpt, 98304 + w * 8192 + 6144 + lane * 16, 0, 16); }
      { f32x4 a = acc_read4<248>(), b2 = acc_read4<252>(); a *= inv[1]; b2 *= inv[1];
        u32x4 v = {cvtpk(a[0], a[1]), cvtpk(a[2], a[3]), cvtpk(b2[0], b2[1]), cvtpk(b2[2], b2[3])};
        __builtin_amdgcn_raw_buffer_store_b128(v, rpt, 98304 + w * 8192 + 7168 + lane * 16, 0, 16); }
    } break;
    case 1: {
      { f32x4 a = acc_read4<0>(), b2 = acc_read4<4>(); a *= inv[0]; b2 *= inv[0];
        u32x4 v = {cvtpk(a[0], a[1]), cvtpk(a[2], a[3]), cvtpk(b2[0], b2[1]), cvtpk(b2[2], b2[3])};
        __builtin_amdgcn_raw_buffer_store_b128(v, rpt, 131072 + w * 8192 + 0 + lane * 16, 0, 16); }
      { f32x4 a = acc_read4<8>(), b2 = acc_read4<12>(); a *= inv[0]; b2 *= inv[0];
        u32x4 v = {cvtpk(a[0], a[1]), cvtpk(a[2], a[3]), cvtpk(b2[0], b2[1]), cvtpk(b2[2], b2[3])};
        __builtin_amdgcn_raw_buffer_store_b128(v, rpt, 131072 + w * 8192 + 1024 + lane * 16, 0, 16); }
      { f32x4 a = acc_read4<16>(), b2 = acc_read4<20>(); a *= inv[0]; b2 *= inv[0];
        u32x4 v = {cvtpk(a[0], a[1]), cvtpk(a[2], a[3]), cvtpk(b2[0], b2[1]), cvtpk(b2[2], b2[3])};
        __builtin_amdgcn_raw_buffer_store_b128(v, rpt, 131072 + w * 8192 + 2048 + lane * 16, 0, 16); }
      { f32x4 a = acc_read4<24>(), b2 = acc_read4<28>(); a *= inv[0]; b2 *= inv[0];
        u32x4 v = {cvtpk(a[0], a[1]), cvtpk(a[2], a[3]), cvtpk(b2[0], b2[1]), cvtpk(b2[2], b2[3])};
        __builtin_amdgcn_raw_buffer_store_b128(v, rpt, 131072 + w * 8192 + 3072 + lane * 16, 0, 16); }
      { f32x4 a = acc_read4<128>(), b2 = acc_read4<132>(); a *= inv[1]; b2 *= inv[1];
        u32x4 v = {cvtpk(a[0], a[1]), cvtpk(a[2], a[3]), cvtpk(b2[0], b2[1]), cvtpk(b2[2], b2[3])};
        __builtin_amdgcn_raw_buffer_store_b128(v, rpt, 131072 + w * 8192 + 4096 + lane * 16, 0, 16); }
      { f32x4 a = acc_read4<136>(), b2 = acc_read4<140>(); a *= inv[1]; b2 *= inv[1];
        u32x4 v = {cvtpk(a[0], a[1]), cvtpk(a[2], a[3]), cvtpk(b2[0], b2[1]), cvtpk(b2[2], b2[3])};
        __builtin_amdgcn_raw_buffer_store_b128(v, rpt, 131072 + w * 8192 + 5120 + lane * 16, 0, 16); }
      { f32x4 a = acc_read4<144>(), b2 = acc_read4<148>(); a *= inv[1]; b2 *= inv[1];
        u32x4 v = {cvtpk(a[0], a[1]), cvtpk(a[2], a[3]), cvtpk(b2[0], b2[1]), cvtpk(b2[2], b2[3])};
        __builtin_amdgcn_raw_buffer_store_b128(v, rpt, 131072 + w * 8192 + 6144 + lane * 16, 0, 16); }
      { f32x4 a = acc_read4<152>(), b2 = acc_read4<156>(); a *= inv[1]; b2 *= inv[1];
        u32x4 v = {cvtpk(a[0], a[1]), cvtpk(a[2], a[3]), cvtpk(b2[0], b2[1]), cvtpk(b2[2], b2[3])};
        __builtin_amdgcn_raw_buffer_store_b128(v, rpt, 131072 + w * 8192 + 7168 + lane * 16, 0, 16); }
      { f32x4 a = acc_read4<64>(), b2 = acc_read4<68>(); a *= inv[0]; b2 *= inv[0];
        u32x4 v = {cvtpk(a[0], a[1]), cvtpk(a[2], a[3]), cvtpk(b2[0], b2[1]), cvtpk(b2[2], b2[3])};
        __builtin_amdgcn_raw_buffer_store_b128(v, rpt, 196608 + w * 8192 + 0 + lane * 16, 0, 16); }
      { f32x4 a = acc_read4<72>(), b2 = acc_read4<76>(); a *= inv[0]; b2 *= inv[0];
        u32x4 v = {cvtpk(a[0], a[1]), cvtpk(a[2], a[3]), cvtpk(b2[0], b2[1]), cvtpk(b2[2], b2[3])};
        __builtin_amdgcn_raw_buffer_store_b128(v, rpt, 196608 + w * 8192 + 1024 + lane * 16, 0, 16); }
      { f32x4 a = acc_read4<80>(), b2 = acc_read4<84>(); a *= inv[0]; b2 *= inv[0];
        u32x4 v = {cvtpk(a[0], a[1]), cvtpk(a[2], a[3]), cvtpk(b2[0], b2[1]), cvtpk(b2[2], b2[3])};
        __builtin_amdgcn_raw_buffer_store_b128(v, rpt, 196608 + w * 8192 + 2048 + lane * 16, 0, 16); }
      { f32x4 a = acc_read4<88>(), b2 = acc_read4<92>(); a *= inv[0]; b2 *= inv[0];
        u32x4 v = {cvtpk(a[0], a[1]), cvtpk(a[2], a[3]), cvtpk(b2[0], b2[1]), cvtpk(b2[2], b2[3])};
        __builtin_amdgcn_raw_buffer_store_b128(v, rpt, 196608 + w * 8192 + 3072 + lane * 16, 0, 16); }
      { f32x4 a = acc_read4<192>(), b2 = acc_read4<196>(); a *= inv[1]; b2 *= inv[1];
        u32x4 v = {cvtpk(a[0], a[1]), cvtpk(a[2], a[3]), cvtpk(b2[0], b2[1]), cvtpk(b2[2], b2[3])};
        __builtin_amdgcn_raw_buffer_store_b128(v, rpt, 196608 + w * 8192 + 4096 + lane * 16, 0, 16); }
      { f32x4 a = acc_read4<200>(), b2 = acc_read4<204>(); a *= inv[1]; b2 *= inv[1];
        u32x4 v = {cvtpk(a[0], a[1]), cvtpk(a[2], a[3]), cvtpk(b2[0], b2[1]), cvtpk(b2[2], b2[3])};
        __builtin_amdgcn_raw_buffer_store_b128(v, rpt, 196608 + w * 8192 + 5120 + lane * 16, 0, 16); }
      { f32x4 a = acc_read4<208>(), b2 = acc_read4<212>(); a *= inv[1]; b2 *= inv[1];
        u32x4 v = {cvtpk(a[0], a[1]), cvtpk(a[2], a[3]), cvtpk(b2[0], b2[1]), cvtpk(b2[2], b2[3])};
        __builtin_amdgcn_raw_buffer_store_b128(v, rpt, 196608 + w * 8192 + 6144 + lane * 16, 0, 16); }
      { f32x4 a = acc_read4<216>(), b2 = acc_read4<220>(); a *= inv[1]; b2 *= inv[1];
        u32x4 v = {cvtpk(a[0], a[1]), cvtpk(a[2], a[3]), cvtpk(b2[0], b2[1]), cvtpk(b2[2], b2[3])};
        __builtin_amdgcn_raw_buffer_store_b128(v, rpt, 196608 + w * 8192 + 7168 + lane * 16, 0, 16); }
      { f32x4 a = acc_read4<96>(), b2 = acc_read4<100>(); a *= inv[0]; b2 *= inv[0];
        u32x4 v = {cvtpk(a[0], a[1]), cvtpk(a[2], a[3]), cvtpk(b2[0], b2[1]), cvtpk(b2[2], b2[3])};
        __builtin_amdgcn_raw_buffer_store_b128(v, rpt, 229376 + w * 8192 + 0 + lane * 16, 0, 16); }
      { f32x4 a = acc_read4<104>(), b2 = acc_read4<108>(); a *= inv[0]; b2 *= inv[0];
        u32x4 v = {cvtpk(a[0], a[1]), cvtpk(a[2], a[3]), cvtpk(b2[0], b2[1]), cvtpk(b2[2], b2[3])};
        __builtin_amdgcn_raw_buffer_store_b128(v, rpt, 229376 + w * 8192 + 1024 + lane * 16, 0, 16); }
      { f32x4 a = acc_read4<112>(), b2 = acc_read4<116>(); a *= inv[0]; b2 *= inv[0];
        u32x4 v = {cvtpk(a[0], a[1]), cvtpk(a[2], a[3]), cvtpk(b2[0], b2[1]), cvtpk(b2[2], b2[3])};
        __builtin_amdgcn_raw_buffer_store_b128(v, rpt, 229376 + w * 8192 + 2048 + lane * 16, 0, 16); }
      { f32x4 a = acc_read4<120>(), b2 = acc_read4<124>(); a *= inv[0]; b2 *= inv[0];
        u32x4 v = {cvtpk(a[0], a[1]), cvtpk(a[2], a[3]), cvtpk(b2[0], b2[1]), cvtpk(b2[2], b2[3])};
        __builtin_amdgcn_raw_buffer_store_b128(v, rpt, 229376 + w * 8192 + 3072 + lane * 16, 0, 16); }
      { f32x4 a = acc_read4<224>(), b2 = acc_read4<228>(); a *= inv[1]; b2 *= inv[1];
        u32x4 v = {cvtpk(a[0], a[1]), cvtpk(a[2], a[3]), cvtpk(b2[0], b2[1]), cvtpk(b2[2], b2[3])};
        __builtin_amdgcn_raw_buffer_store_b128(v, rpt, 229376 + w * 8192 + 4096 + lane * 16, 0, 16); }
      { f32x4 a = acc_read4<232>(), b2 = acc_read4<236>(); a *= inv[1]; b2 *= inv[1];
        u32x4 v = {cvtpk(a[0], a[1]), cvtpk(a[2], a[3]), cvtpk(b2[0], b2[1]), cvtpk(b2[2], b2[3])};
        __builtin_amdgcn_raw_buffer_store_b128(v, rpt, 229376 + w * 8192 + 5120 + lane * 16, 0, 16); }
      { f32x4 a = acc_read4<240>(), b2 = acc_read4<244>(); a *= inv[1]; b2 *= inv[1];
        u32x4 v = {cvtpk(a[0], a[1]), cvtpk(a[2], a[3]), cvtpk(b2[0], b2[1]), cvtpk(b2[2], b2[3])};
        __builtin_amdgcn_raw_buffer_store_b128(v, rpt, 229376 + w * 8192 + 6144 + lane * 16, 0, 16); }
      { f32x4 a = acc_read4<248>(), b2 = acc_read4<252>(); a *= inv[1]; b2 *= inv[1];
        u32x4 v = {cvtpk(a[0], a[1]), cvtpk(a[2], a[3]), cvtpk(b2[0], b2[1]), cvtpk(b2[2], b2[3])};
        __builtin_amdgcn_raw_buffer_store_b128(v, rpt, 229376 + w * 8192 + 7168 + lane * 16, 0, 16); }
    } break;
    case 2: {
      { f32x4 a = acc_read4<0>(), b2 = acc_read4<4>(); a *= inv[0]; b2 *= inv[0];
        u32x4 v = {cvtpk(a[0], a[1]), cvtpk(a[2], a[3]), cvtpk(b2[0], b2[1]), cvtpk(b2[2], b2[3])};
        __builtin_amdgcn_raw_buffer_store_b128(v, rpt, 262144 + w * 8192 + 0 + lane * 16, 0, 16); }
      { f32x4 a = acc_read4<8>(), b2 = acc_read4<12>(); a *= inv[0]; b2 *= inv[0];
        u32x4 v = {cvtpk(a[0], a[1]), cvtpk(a[2], a[3]), cvtpk(b2[0], b2[1]), cvtpk(b2[2], b2[3])};
        __builtin_amdgcn_raw_buffer_store_b128(v, rpt, 262144 + w * 8192 + 1024 + lane * 16, 0, 16); }
      { f32x4 a = acc_read4<16>(), b2 = acc_read4<20>(); a *= inv[0]; b2 *= inv[0];
        u32x4 v = {cvtpk(a[0], a[1]), cvtpk(a[2], a[3]), cvtpk(b2[0], b2[1]), cvtpk(b2[2], b2[3])};
        __builtin_amdgcn_raw_buffer_store_b128(v, rpt, 262144 + w * 8192 + 2048 + lane * 16, 0, 16); }
      { f32x4 a = acc_read4<24>(), b2 = acc_read4<28>(); a *= inv[0]; b2 *= inv[0];
        u32x4 v = {cvtpk(a[0], a[1]), cvtpk(a[2], a[3]), cvtpk(b2[0], b2[1]), cvtpk(b2[2], b2[3])};
        __builtin_amdgcn_raw_buffer_store_b128(v, rpt, 262144 + w * 8192 + 3072 + lane * 16, 0, 16); }
      { f32x4 a = acc_read4<128>(), b2 = acc_read4<132>(); a *= inv[1]; b2 *= inv[1];
        u32x4 v = {cvtpk(a[0], a[1]), cvtpk(a[2], a[3]), cvtpk(b2[0], b2[1]), cvtpk(b2[2], b2[3])};
        __builtin_amdgcn_raw_buffer_store_b128(v, rpt, 262144 + w * 8192 + 4096 + lane * 16, 0, 16); }
      { f32x4 a = acc_read4<136>(), b2 = acc_read4<140>(); a *= inv[1]; b2 *= inv[1];
        u32x4 v = {cvtpk(a[0], a[1]), cvtpk(a[2], a[3]), cvtpk(b2[0], b2[1]), cvtpk(b2[2], b2[3])};
        __builtin_amdgcn_raw_buffer_store_b128(v, rpt, 262144 + w * 8192 + 5120 + lane * 16, 0, 16); }
      { f32x4 a = acc_read4<144>(), b2 = acc_read4<148>(); a *= inv[1]; b2 *= inv[1];
        u32x4 v = {cvtpk(a[0], a[1]), cvtpk(a[2], a[3]), cvtpk(b2[0], b2[1]), cvtpk(b2[2], b2[3])};
        __builtin_amdgcn_raw_buffer_store_b128(v, rpt, 262144 + w * 8192 + 6144 + lane * 16, 0, 16); }
      { f32x4 a = acc_read4<152>(), b2 = acc_read4<156>(); a *= inv[1]; b2 *= inv[1];
        u32x4 v = {cvtpk(a[0], a[1]), cvtpk(a[2], a[3]), cvtpk(b2[0], b2[1]), cvtpk(b2[2], b2[3])};
        __builtin_amdgcn_raw_buffer_store_b128(v, rpt, 262144 + w * 8192 + 7168 + lane * 16, 0, 16); }
      { f32x4 a = acc_read4<32>(), b2 = acc_read4<36>(); a *= inv[0]; b2 *= inv[0];
        u32x4 v = {cvtpk(a[0], a[1]), cvtpk(a[2], a[3]), cvtpk(b2[0], b2[1]), cvtpk(b2[2], b2[3])};
        __builtin_amdgcn_raw_buffer_store_b128(v, rpt, 294912 + w * 8192 + 0 + lane * 16, 0, 16); }
      { f32x4 a = acc_read4<40>(), b2 = acc_read4<44>(); a *= inv[0]; b2 *= inv[0];
        u32x4 v = {cvtpk(a[0], a[1]), cvtpk(a[2], a[3]), cvtpk(b2[0], b2[1]), cvtpk(b2[2], b2[3])};
        __builtin_amdgcn_raw_buffer_store_b128(v, rpt, 294912 + w * 8192 + 1024 + lane * 16, 0, 16); }
      { f32x4 a = acc_read4<48>(), b2 = acc_read4<52>(); a *= inv[0]; b2 *= inv[0];
        u32x4 v = {cvtpk(a[0], a[1]), cvtpk(a[2], a[3]), cvtpk(b2[0], b2[1]), cvtpk(b2[2], b2[3])};
        __builtin_amdgcn_raw_buffer_store_b128(v, rpt, 294912 + w * 8192 + 2048 + lane * 16, 0, 16); }
      { f32x4 a = acc_read4<56>(), b2 = acc_read4<60>(); a *= inv[0]; b2 *= inv[0];
        u32x4 v = {cvtpk(a[0], a[1]), cvtpk(a[2], a[3]), cvtpk(b2[0], b2[1]), cvtpk(b2[2], b2[3])};
        __builtin_amdgcn_raw_buffer_store_b128(v, rpt, 294912 + w * 8192 + 3072 + lane * 16, 0, 16); }
      { f32x4 a = acc_read4<160>(), b2 = acc_read4<164>(); a *= inv[1]; b2 *= inv[1];
        u32x4 v = {cvtpk(a[0], a[1]), cvtpk(a[2], a[3]), cvtpk(b2[0], b2[1]), cvtpk(b2[2], b2[3])};
        __builtin_amdgcn_raw_buffer_store_b128(v, rpt, 294912 + w * 8192 + 4096 + lane * 16, 0, 16); }
      { f32x4 a = acc_read4<168>(), b2 = acc_read4<172>(); a *= inv[1]; b2 *= inv[1];
        u32x4 v = {cvtpk(a[0], a[1]), cvtpk(a[2], a[3]), cvtpk(b2[0], b2[1]), cvtpk(b2[2], b2[3])};
        __builtin_amdgcn_raw_buffer_store_b128(v, rpt, 294912 + w * 8192 + 5120 + lane * 16, 0, 16); }
      { f32x4 a = acc_read4<176>(), b2 = acc_read4<180>(); a *= inv[1]; b2 *= inv[1];
        u32x4 v = {cvtpk(a[0], a[1]), cvtpk(a[2], a[3]), cvtpk(b2[0], b2[1]), cvtpk(b2[2], b2[3])};
        __builtin_amdgcn_raw_buffer_store_b128(v, rpt, 294912 + w * 8192 + 6144 + lane * 16, 0, 16); }
      { f32x4 a = acc_read4<184>(), b2 = acc_read4<188>(); a *= inv[1]; b2 *= inv[1];
        u32x4 v = {cvtpk(a[0], a[1]), cvtpk(a[2], a[3]), cvtpk(b2[0], b2[1]), cvtpk(b2[2], b2[3])};
        __builtin_amdgcn_raw_buffer_store_b128(v, rpt, 294912 + w * 8192 + 7168 + lane * 16, 0, 16); }
      { f32x4 a = acc_read4<96>(), b2 = acc_read4<100>(); a *= inv[0]; b2 *= inv[0];
        u32x4 v = {cvtpk(a[0], a[1]), cvtpk(a[2], a[3]), cvtpk(b2[0], b2[1]), cvtpk(b2[2], b2[3])};
        __builtin_amdgcn_raw_buffer_store_b128(v, rpt, 360448 + w * 8192 + 0 + lane * 16, 0, 16); }
      { f32x4 a = acc_read4<104>(), b2 = acc_read4<108>(); a *= inv[0]; b2 *= inv[0];
        u32x4 v = {cvtpk(a[0], a[1]), cvtpk(a[2], a[3]), cvtpk(b2[0], b2[1]), cvtpk(b2[2], b2[3])};
        __builtin_amdgcn_raw_buffer_store_b128(v, rpt, 360448 + w * 8192 + 1024 + lane * 16, 0, 16); }
      { f32x4 a = acc_read4<112>(), b2 = acc_read4<116>(); a *= inv[0]; b2 *= inv[0];
        u32x4 v = {cvtpk(a[0], a[1]), cvtpk(a[2], a[3]), cvtpk(b2[0], b2[1]), cvtpk(b2[2], b2[3])};
        __builtin_amdgcn_raw_buffer_store_b128(v, rpt, 360448 + w * 8192 + 2048 + lane * 16, 0, 16); }
      { f32x4 a = acc_read4<120>(), b2 = acc_read4<124>(); a *= inv[0]; b2 *= inv[0];
        u32x4 v = {cvtpk(a[0], a[1]), cvtpk(a[2], a[3]), cvtpk(b2[0], b2[1]), cvtpk(b2[2], b2[3])};
        __builtin_amdgcn_raw_buffer_store_b128(v, rpt, 360448 + w * 8192 + 3072 + lane * 16, 0, 16); }
      { f32x4 a = acc_read4<224>(), b2 = acc_read4<228>(); a *= inv[1]; b2 *= inv[1];
        u32x4 v = {cvtpk(a[0], a[1]), cvtpk(a[2], a[3]), cvtpk(b2[0], b2[1]), cvtpk(b2[2], b2[3])};
        __builtin_amdgcn_raw_buffer_store_b128(v, rpt, 360448 + w * 8192 + 4096 + lane * 16, 0, 16); }
      { f32x4 a = acc_read4<232>(), b2 = acc_read4<236>(); a *= inv[1]; b2 *= inv[1];
        u32x4 v = {cvtpk(a[0], a[1]), cvtpk(a[2], a[3]), cvtpk(b2[0], b2[1]), cvtpk(b2[2], b2[3])};
        __builtin_amdgcn_raw_buffer_store_b128(v, rpt, 360448 + w * 8192 + 5120 + lane * 16, 0, 16); }
      { f32x4 a = acc_read4<240>(), b2 = acc_read4<244>(); a *= inv[1]; b2 *= inv[1];
        u32x4 v = {cvtpk(a[0], a[1]), cvtpk(a[2], a[3]), cvtpk(b2[0], b2[1]), cvtpk(b2[2], b2[3])};
        __builtin_amdgcn_raw_buffer_store_b128(v, rpt, 360448 + w * 8192 + 6144 + lane * 16, 0, 16); }
      { f32x4 a = acc_read4<248>(), b2 = acc_read4<252>(); a *= inv[1]; b2 *= inv[1];
        u32x4 v = {cvtpk(a[0], a[1]), cvtpk(a[2], a[3]), cvtpk(b2[0], b2[1]), cvtpk(b2[2], b2[3])};
        __builtin_amdgcn_raw_buffer_store_b128(v, rpt, 360448 + w * 8192 + 7168 + lane * 16, 0, 16); }
    } break;
    case 3: {
      { f32x4 a = acc_read4<0>(), b2 = acc_read4<4>(); a *= inv[0]; b2 *= inv[0];
        u32x4 v = {cvtpk(a[0], a[1]), cvtpk(a[2], a[3]), cvtpk(b2[0], b2[1]), cvtpk(b2[2], b2[3])};
        __builtin_amdgcn_raw_buffer_store_b128(v, rpt, 393216 + w * 8192 + 0 + lane * 16, 0, 16); }
      { f32x4 a = acc_read4<8>(), b2 = acc_read4<12>(); a *= inv[0]; b2 *= inv[0];
        u32x4 v = {cvtpk(a[0], a[1]), cvtpk(a[2], a[3]), cvtpk(b2[0], b2[1]), cvtpk(b2[2], b2[3])};
        __builtin_amdgcn_raw_buffer_store_b128(v, rpt, 393216 + w * 8192 + 1024 + lane * 16, 0, 16); }
      { f32x4 a = acc_read4<16>(), b2 = acc_read4<20>(); a *= inv[0]; b2 *= inv[0];
        u32x4 v = {cvtpk(a[0], a[1]), cvtpk(a[2], a[3]), cvtpk(b2[0], b2[1]), cvtpk(b2[2], b2[3])};
        __builtin_amdgcn_raw_buffer_store_b128(v, rpt, 393216 + w * 8192 + 2048 + lane * 16, 0, 16); }
      { f32x4 a = acc_read4<24>(), b2 = acc_read4<28>(); a *= inv[0]; b2 *= inv[0];
        u32x4 v = {cvtpk(a[0], a[1]), cvtpk(a[2], a[3]), cvtpk(b2[0], b2[1]), cvtpk(b2[2], b2[3])};
        __builtin_amdgcn_raw_buffer_store_b128(v, rpt, 393216 + w * 8192 + 3072 + lane * 16, 0, 16); }
      { f32x4 a = acc_read4<128>(), b2 = acc_read4<132>(); a *= inv[1]; b2 *= inv[1];
        u32x4 v = {cvtpk(a[0], a[1]), cvtpk(a[2], a[3]), cvtpk(b2[0], b2[1]), cvtpk(b2[2], b2[3])};
        __builtin_amdgcn_raw_buffer_store_b128(v, rpt, 393216 + w * 8192 + 4096 + lane * 16, 0, 16); }
      { f32x4 a = acc_read4<136>(), b2 = acc_read4<140>(); a *= inv[1]; b2 *= inv[1];
        u32x4 v = {cvtpk(a[0], a[1]), cvtpk(a[2], a[3]), cvtpk(b2[0], b2[1]), cvtpk(b2[2], b2[3])};
        __builtin_amdgcn_raw_buffer_store_b128(v, rpt, 393216 + w * 8192 + 5120 + lane * 16, 0, 16); }
      { f32x4 a = acc_read4<144>(), b2 = acc_read4<148>(); a *= inv[1]; b2 *= inv[1];
        u32x4 v = {cvtpk(a[0], a[1]), cvtpk(a[2], a[3]), cvtpk(b2[0], b2[1]), cvtpk(b2[2], b2[3])};
        __builtin_amdgcn_raw_buffer_store_b128(v, rpt, 393216 + w * 8192 + 6144 + lane * 16, 0, 16); }
      { f32x4 a = acc_read4<152>(), b2 = acc_read4<156>(); a *= inv[1]; b2 *= inv[1];
        u32x4 v = {cvtpk(a[0], a[1]), cvtpk(a[2], a[3]), cvtpk(b2[0], b2[1]), cvtpk(b2[2], b2[3])};
        __builtin_amdgcn_raw_buffer_store_b128(v, rpt, 393216 + w * 8192 + 7168 + lane * 16, 0, 16); }
      { f32x4 a = acc_read4<32>(), b2 = acc_read4<36>(); a *= inv[0]; b2 *= inv[0];
        u32x4 v = {cvtpk(a[0], a[1]), cvtpk(a[2], a[3]), cvtpk(b2[0], b2[1]), cvtpk(b2[2], b2[3])};
        __builtin_amdgcn_raw_buffer_store_b128(v, rpt, 425984 + w * 8192 + 0 + lane * 16, 0, 16); }
      { f32x4 a = acc_read4<40>(), b2 = acc_read4<44>(); a *= inv[0]; b2 *= inv[0];
        u32x4 v = {cvtpk(a[0], a[1]), cvtpk(a[2], a[3]), cvtpk(b2[0], b2[1]), cvtpk(b2[2], b2[3])};
        __builtin_amdgcn_raw_buffer_store_b128(v, rpt, 425984 + w * 8192 + 1024 + lane * 16, 0, 16); }
      { f32x4 a = acc_read4<48>(), b2 = acc_read4<52>(); a *= inv[0]; b2 *= inv[0];
        u32x4 v = {cvtpk(a[0], a[1]), cvtpk(a[2], a[3]), cvtpk(b2[0], b2[1]), cvtpk(b2[2], b2[3])};
        __builtin_amdgcn_raw_buffer_store_b128(v, rpt, 425984 + w * 8192 + 2048 + lane * 16, 0, 16); }
      { f32x4 a = acc_read4<56>(), b2 = acc_read4<60>(); a *= inv[0]; b2 *= inv[0];
        u32x4 v = {cvtpk(a[0], a[1]), cvtpk(a[2], a[3]), cvtpk(b2[0], b2[1]), cvtpk(b2[2], b2[3])};
        __builtin_amdgcn_raw_buffer_store_b128(v, rpt, 425984 + w * 8192 + 3072 + lane * 16, 0, 16); }
      { f32x4 a = acc_read4<160>(), b2 = acc_read4<164>(); a *= inv[1]; b2 *= inv[1];
        u32x4 v = {cvtpk(a[0], a[1]), cvtpk(a[2], a[3]), cvtpk(b2[0], b2[1]), cvtpk(b2[2], b2[3])};
        __builtin_amdgcn_raw_buffer_store_b128(v, rpt, 425984 + w * 8192 + 4096 + lane * 16, 0, 16); }
      { f32x4 a = acc_read4<168>(), b2 = acc_read4<172>(); a *= inv[1]; b2 *= inv[1];
        u32x4 v = {cvtpk(a[0], a[1]), cvtpk(a[2], a[3]), cvtpk(b2[0], b2[1]), cvtpk(b2[2], b2[3])};
        __builtin_amdgcn_raw_buffer_store_b128(v, rpt, 425984 + w * 8192 + 5120 + lane * 16, 0, 16); }
      { f32x4 a = acc_read4<176>(), b2 = acc_read4<180>(); a *= inv[1]; b2 *= inv[1];
        u32x4 v = {cvtpk(a[0], a[1]), cvtpk(a[2], a[3]), cvtpk(b2[0], b2[1]), cvtpk(b2[2], b2[3])};
        __builtin_amdgcn_raw_buffer_store_b128(v, rpt, 425984 + w * 8192 + 6144 + lane * 16, 0, 16); }
      { f32x4 a = acc_read4<184>(), b2 = acc_read4<188>(); a *= inv[1]; b2 *= inv[1];
        u32x4 v = {cvtpk(a[0], a[1]), cvtpk(a[2], a[3]), cvtpk(b2[0], b2[1]), cvtpk(b2[2], b2[3])};
        __builtin_amdgcn_raw_buffer_store_b128(v, rpt, 425984 + w * 8192 + 7168 + lane * 16, 0, 16); }
      { f32x4 a = acc_read4<64>(), b2 = acc_read4<68>(); a *= inv[0]; b2 *= inv[0];
        u32x4 v = {cvtpk(a[0], a[1]), cvtpk(a[2], a[3]), cvtpk(b2[0], b2[1]), cvtpk(b2[2], b2[3])};
        __builtin_amdgcn_raw_buffer_store_b128(v, rpt, 458752 + w * 8192 + 0 + lane * 16, 0, 16); }
      { f32x4 a = acc_read4<72>(), b2 = acc_read4<76>(); a *= inv[0]; b2 *= inv[0];
        u32x4 v = {cvtpk(a[0], a[1]), cvtpk(a[2], a[3]), cvtpk(b2[0], b2[1]), cvtpk(b2[2], b2[3])};
        __builtin_amdgcn_raw_buffer_store_b128(v, rpt, 458752 + w * 8192 + 1024 + lane * 16, 0, 16); }
      { f32x4 a = acc_read4<80>(), b2 = acc_read4<84>(); a *= inv[0]; b2 *= inv[0];
        u32x4 v = {cvtpk(a[0], a[1]), cvtpk(a[2], a[3]), cvtpk(b2[0], b2[1]), cvtpk(b2[2], b2[3])};
        __builtin_amdgcn_raw_buffer_store_b128(v, rpt, 458752 + w * 8192 + 2048 + lane * 16, 0, 16); }
      { f32x4 a = acc_read4<88>(), b2 = acc_read4<92>(); a *= inv[0]; b2 *= inv[0];
        u32x4 v = {cvtpk(a[0], a[1]), cvtpk(a[2], a[3]), cvtpk(b2[0], b2[1]), cvtpk(b2[2], b2[3])};
        __builtin_amdgcn_raw_buffer_store_b128(v, rpt, 458752 + w * 8192 + 3072 + lane * 16, 0, 16); }
      { f32x4 a = acc_read4<192>(), b2 = acc_read4<196>(); a *= inv[1]; b2 *= inv[1];
        u32x4 v = {cvtpk(a[0], a[1]), cvtpk(a[2], a[3]), cvtpk(b2[0], b2[1]), cvtpk(b2[2], b2[3])};
        __builtin_amdgcn_raw_buffer_store_b128(v, rpt, 458752 + w * 8192 + 4096 + lane * 16, 0, 16); }
      { f32x4 a = acc_read4<200>(), b2 = acc_read4<204>(); a *= inv[1]; b2 *= inv[1];
        u32x4 v = {cvtpk(a[0], a[1]), cvtpk(a[2], a[3]), cvtpk(b2[0], b2[1]), cvtpk(b2[2], b2[3])};
        __builtin_amdgcn_raw_buffer_store_b128(v, rpt, 458752 + w * 8192 + 5120 + lane * 16, 0, 16); }
      { f32x4 a = acc_read4<208>(), b2 = acc_read4<212>(); a *= inv[1]; b2 *= inv[1];
        u32x4 v = {cvtpk(a[0], a[1]), cvtpk(a[2], a[3]), cvtpk(b2[0], b2[1]), cvtpk(b2[2], b2[3])};
        __builtin_amdgcn_raw_buffer_store_b128(v, rpt, 458752 + w * 8192 + 6144 + lane * 16, 0, 16); }
      { f32x4 a = acc_read4<216>(), b2 = acc_read4<220>(); a *= inv[1]; b2 *= inv[1];
        u32x4 v = {cvtpk(a[0], a[1]), cvtpk(a[2], a[3]), cvtpk(b2[0], b2[1]), cvtpk(b2[2], b2[3])};
        __builtin_amdgcn_raw_buffer_store_b128(v, rpt, 458752 + w * 8192 + 7168 + lane * 16, 0, 16); }
    } break;
  }
  asm volatile("s_waitcnt vmcnt(0)" ::: "memory");
  __syncthreads();
  STAMP(14);
  if (tid == 0) {
    __hip_atomic_fetch_add(cnt + grp, 1u, __ATOMIC_RELAXED, __HIP_MEMORY_SCOPE_AGENT);
    for (int it = 0; it < (1 << 24); ++it) {
      if (__hip_atomic_load(cnt + grp, __ATOMIC_RELAXED, __HIP_MEMORY_SCOPE_AGENT) >= 4u) break;
      __builtin_amdgcn_s_sleep(1);
    }
  }
  __syncthreads();
  STAMP(15);
  float fw[2][4];
#pragma unroll
  for (int qb = 0; qb < 2; ++qb) {
    float mm[4], ll[4];
#pragma unroll
    for (int s4 = 0; s4 < 4; ++s4) {
      u32x2 v = __builtin_amdgcn_raw_buffer_load_b64(rml, (s4 * 256 + 64 * w + 32 * qb + r) * 8, 0, 16);
      mm[s4] = __uint_as_float(v[0]); ll[s4] = __uint_as_float(v[1]);
    }
    const float M = fmaxf(fmaxf(mm[0], mm[1]), fmaxf(mm[2], mm[3]));
    float e[4], L = 0.f;
#pragma unroll
    for (int s4 = 0; s4 < 4; ++s4) { e[s4] = __builtin_amdgcn_exp2f(mm[s4] - M) * ll[s4]; L += e[s4]; }
    const float iL = 1.f / L;
#pragma unroll
    for (int s4 = 0; s4 < 4; ++s4) fw[qb][s4] = e[s4] * iL;
  }
  switch (sp) {
    case 0: {
      { const int R = 64 * w + 0 + r; const int hl = R >= 112 ? 1 : 0; const int orow = b * SQ + R - 112 * hl;
        { f32x4 a = acc_read4<0>(), b2 = acc_read4<4>(); const float fo = fw[0][0] * inv[0]; a *= fo; b2 *= fo;
          { u32x4 v = __builtin_amdgcn_raw_buffer_load_b128(rpt, 131072 + w * 8192 + 0 + lane * 16, 0, 16); const float f = fw[0][1];
            a[0] += f * __uint_as_float(v[0] << 16); a[1] += f * __uint_as_float(v[0] & 0xffff0000u); a[2] += f * __uint_as_float(v[1] << 16); a[3] += f * __uint_as_float(v[1] & 0xffff0000u);
            b2[0] += f * __uint_as_float(v[2] << 16); b2[1] += f * __uint_as_float(v[2] & 0xffff0000u); b2[2] += f * __uint_as_float(v[3] << 16); b2[3] += f * __uint_as_float(v[3] & 0xffff0000u); }
          { u32x4 v = __builtin_amdgcn_raw_buffer_load_b128(rpt, 262144 + w * 8192 + 0 + lane * 16, 0, 16); const float f = fw[0][2];
            a[0] += f * __uint_as_float(v[0] << 16); a[1] += f * __uint_as_float(v[0] & 0xffff0000u); a[2] += f * __uint_as_float(v[1] << 16); a[3] += f * __uint_as_float(v[1] & 0xffff0000u);
            b2[0] += f * __uint_as_float(v[2] << 16); b2[1] += f * __uint_as_float(v[2] & 0xffff0000u); b2[2] += f * __uint_as_float(v[3] << 16); b2[3] += f * __uint_as_float(v[3] & 0xffff0000u); }
          { u32x4 v = __builtin_amdgcn_raw_buffer_load_b128(rpt, 393216 + w * 8192 + 0 + lane * 16, 0, 16); const float f = fw[0][3];
            a[0] += f * __uint_as_float(v[0] << 16); a[1] += f * __uint_as_float(v[0] & 0xffff0000u); a[2] += f * __uint_as_float(v[1] << 16); a[3] += f * __uint_as_float(v[1] & 0xffff0000u);
            b2[0] += f * __uint_as_float(v[2] << 16); b2[1] += f * __uint_as_float(v[2] & 0xffff0000u); b2[2] += f * __uint_as_float(v[3] << 16); b2[3] += f * __uint_as_float(v[3] & 0xffff0000u); }
          if (R < 224) { u16* dst = o + ((size_t)((kvh * 2 + hl) * 8 + 0) * NROW + orow) * 32 + 0 + 4 * h;
            u32x2 v0 = {cvtpk(a[0], a[1]), cvtpk(a[2], a[3])}; u32x2 v1 = {cvtpk(b2[0], b2[1]), cvtpk(b2[2], b2[3])};
            *(u32x2*)dst = v0; *(u32x2*)(dst + 8) = v1; } }
        { f32x4 a = acc_read4<8>(), b2 = acc_read4<12>(); const float fo = fw[0][0] * inv[0]; a *= fo; b2 *= fo;
          { u32x4 v = __builtin_amdgcn_raw_buffer_load_b128(rpt, 131072 + w * 8192 + 1024 + lane * 16, 0, 16); const float f = fw[0][1];
            a[0] += f * __uint_as_float(v[0] << 16); a[1] += f * __uint_as_float(v[0] & 0xffff0000u); a[2] += f * __uint_as_float(v[1] << 16); a[3] += f * __uint_as_float(v[1] & 0xffff0000u);
            b2[0] += f * __uint_as_float(v[2] << 16); b2[1] += f * __uint_as_float(v[2] & 0xffff0000u); b2[2] += f * __uint_as_float(v[3] << 16); b2[3] += f * __uint_as_float(v[3] & 0xffff0000u); }
          { u32x4 v = __builtin_amdgcn_raw_buffer_load_b128(rpt, 262144 + w * 8192 + 1024 + lane * 16, 0, 16); const float f = fw[0][2];
            a[0] += f * __uint_as_float(v[0] << 16); a[1] += f * __uint_as_float(v[0] & 0xffff0000u); a[2] += f * __uint_as_float(v[1] << 16); a[3] += f * __uint_as_float(v[1] & 0xffff0000u);
            b2[0] += f * __uint_as_float(v[2] << 16); b2[1] += f * __uint_as_float(v[2] & 0xffff0000u); b2[2] += f * __uint_as_float(v[3] << 16); b2[3] += f * __uint_as_float(v[3] & 0xffff0000u); }
          { u32x4 v = __builtin_amdgcn_raw_buffer_load_b128(rpt, 393216 + w * 8192 + 1024 + lane * 16, 0, 16); const float f = fw[0][3];
            a[0] += f * __uint_as_float(v[0] << 16); a[1] += f * __uint_as_float(v[0] & 0xffff0000u); a[2] += f * __uint_as_float(v[1] << 16); a[3] += f * __uint_as_float(v[1] & 0xffff0000u);
            b2[0] += f * __uint_as_float(v[2] << 16); b2[1] += f * __uint_as_float(v[2] & 0xffff0000u); b2[2] += f * __uint_as_float(v[3] << 16); b2[3] += f * __uint_as_float(v[3] & 0xffff0000u); }
          if (R < 224) { u16* dst = o + ((size_t)((kvh * 2 + hl) * 8 + 0) * NROW + orow) * 32 + 16 + 4 * h;
            u32x2 v0 = {cvtpk(a[0], a[1]), cvtpk(a[2], a[3])}; u32x2 v1 = {cvtpk(b2[0], b2[1]), cvtpk(b2[2], b2[3])};
            *(u32x2*)dst = v0; *(u32x2*)(dst + 8) = v1; } }
        { f32x4 a = acc_read4<16>(), b2 = acc_read4<20>(); const float fo = fw[0][0] * inv[0]; a *= fo; b2 *= fo;
          { u32x4 v = __builtin_amdgcn_raw_buffer_load_b128(rpt, 131072 + w * 8192 + 2048 + lane * 16, 0, 16); const float f = fw[0][1];
            a[0] += f * __uint_as_float(v[0] << 16); a[1] += f * __uint_as_float(v[0] & 0xffff0000u); a[2] += f * __uint_as_float(v[1] << 16); a[3] += f * __uint_as_float(v[1] & 0xffff0000u);
            b2[0] += f * __uint_as_float(v[2] << 16); b2[1] += f * __uint_as_float(v[2] & 0xffff0000u); b2[2] += f * __uint_as_float(v[3] << 16); b2[3] += f * __uint_as_float(v[3] & 0xffff0000u); }
          { u32x4 v = __builtin_amdgcn_raw_buffer_load_b128(rpt, 262144 + w * 8192 + 2048 + lane * 16, 0, 16); const float f = fw[0][2];
            a[0] += f * __uint_as_float(v[0] << 16); a[1] += f * __uint_as_float(v[0] & 0xffff0000u); a[2] += f * __uint_as_float(v[1] << 16); a[3] += f * __uint_as_float(v[1] & 0xffff0000u);
            b2[0] += f * __uint_as_float(v[2] << 16); b2[1] += f * __uint_as_float(v[2] & 0xffff0000u); b2[2] += f * __uint_as_float(v[3] << 16); b2[3] += f * __uint_as_float(v[3] & 0xffff0000u); }
          { u32x4 v = __builtin_amdgcn_raw_buffer_load_b128(rpt, 393216 + w * 8192 + 2048 + lane * 16, 0, 16); const float f = fw[0][3];
            a[0] += f * __uint_as_float(v[0] << 16); a[1] += f * __uint_as_float(v[0] & 0xffff0000u); a[2] += f * __uint_as_float(v[1] << 16); a[3] += f * __uint_as_float(v[1] & 0xffff0000u);
            b2[0] += f * __uint_as_float(v[2] << 16); b2[1] += f * __uint_as_float(v[2] & 0xffff0000u); b2[2] += f * __uint_as_float(v[3] << 16); b2[3] += f * __uint_as_float(v[3] & 0xffff0000u); }
          if (R < 224) { u16* dst = o + ((size_t)((kvh * 2 + hl) * 8 + 1) * NROW + orow) * 32 + 0 + 4 * h;
            u32x2 v0 = {cvtpk(a[0], a[1]), cvtpk(a[2], a[3])}; u32x2 v1 = {cvtpk(b2[0], b2[1]), cvtpk(b2[2], b2[3])};
            *(u32x2*)dst = v0; *(u32x2*)(dst + 8) = v1; } }
        { f32x4 a = acc_read4<24>(), b2 = acc_read4<28>(); const float fo = fw[0][0] * inv[0]; a *= fo; b2 *= fo;
          { u32x4 v = __builtin_amdgcn_raw_buffer_load_b128(rpt, 131072 + w * 8192 + 3072 + lane * 16, 0, 16); const float f = fw[0][1];
            a[0] += f * __uint_as_float(v[0] << 16); a[1] += f * __uint_as_float(v[0] & 0xffff0000u); a[2] += f * __uint_as_float(v[1] << 16); a[3] += f * __uint_as_float(v[1] & 0xffff0000u);
            b2[0] += f * __uint_as_float(v[2] << 16); b2[1] += f * __uint_as_float(v[2] & 0xffff0000u); b2[2] += f * __uint_as_float(v[3] << 16); b2[3] += f * __uint_as_float(v[3] & 0xffff0000u); }
          { u32x4 v = __builtin_amdgcn_raw_buffer_load_b128(rpt, 262144 + w * 8192 + 3072 + lane * 16, 0, 16); const float f = fw[0][2];
            a[0] += f * __uint_as_float(v[0] << 16); a[1] += f * __uint_as_float(v[0] & 0xffff0000u); a[2] += f * __uint_as_float(v[1] << 16); a[3] += f * __uint_as_float(v[1] & 0xffff0000u);
            b2[0] += f * __uint_as_float(v[2] << 16); b2[1] += f * __uint_as_float(v[2] & 0xffff0000u); b2[2] += f * __uint_as_float(v[3] << 16); b2[3] += f * __uint_as_float(v[3] & 0xffff0000u); }
          { u32x4 v = __builtin_amdgcn_raw_buffer_load_b128(rpt, 393216 + w * 8192 + 3072 + lane * 16, 0, 16); const float f = fw[0][3];
            a[0] += f * __uint_as_float(v[0] << 16); a[1] += f * __uint_as_float(v[0] & 0xffff0000u); a[2] += f * __uint_as_float(v[1] << 16); a[3] += f * __uint_as_float(v[1] & 0xffff0000u);
            b2[0] += f * __uint_as_float(v[2] << 16); b2[1] += f * __uint_as_float(v[2] & 0xffff0000u); b2[2] += f * __uint_as_float(v[3] << 16); b2[3] += f * __uint_as_float(v[3] & 0xffff0000u); }
          if (R < 224) { u16* dst = o + ((size_t)((kvh * 2 + hl) * 8 + 1) * NROW + orow) * 32 + 16 + 4 * h;
            u32x2 v0 = {cvtpk(a[0], a[1]), cvtpk(a[2], a[3])}; u32x2 v1 = {cvtpk(b2[0], b2[1]), cvtpk(b2[2], b2[3])};
            *(u32x2*)dst = v0; *(u32x2*)(dst + 8) = v1; } }
      }
      { const int R = 64 * w + 32 + r; const int hl = R >= 112 ? 1 : 0; const int orow = b * SQ + R - 112 * hl;
        { f32x4 a = acc_read4<128>(), b2 = acc_read4<132>(); const float fo = fw[1][0] * inv[1]; a *= fo; b2 *= fo;
          { u32x4 v = __builtin_amdgcn_raw_buffer_load_b128(rpt, 131072 + w * 8192 + 4096 + lane * 16, 0, 16); const float f = fw[1][1];
            a[0] += f * __uint_as_float(v[0] << 16); a[1] += f * __uint_as_float(v[0] & 0xffff0000u); a[2] += f * __uint_as_float(v[1] << 16); a[3] += f * __uint_as_float(v[1] & 0xffff0000u);
            b2[0] += f * __uint_as_float(v[2] << 16); b2[1] += f * __uint_as_float(v[2] & 0xffff0000u); b2[2] += f * __uint_as_float(v[3] << 16); b2[3] += f * __uint_as_float(v[3] & 0xffff0000u); }
          { u32x4 v = __builtin_amdgcn_raw_buffer_load_b128(rpt, 262144 + w * 8192 + 4096 + lane * 16, 0, 16); const float f = fw[1][2];
            a[0] += f * __uint_as_float(v[0] << 16); a[1] += f * __uint_as_float(v[0] & 0xffff0000u); a[2] += f * __uint_as_float(v[1] << 16); a[3] += f * __uint_as_float(v[1] & 0xffff0000u);
            b2[0] += f * __uint_as_float(v[2] << 16); b2[1] += f * __uint_as_float(v[2] & 0xffff0000u); b2[2] += f * __uint_as_float(v[3] << 16); b2[3] += f * __uint_as_float(v[3] & 0xffff0000u); }
          { u32x4 v = __builtin_amdgcn_raw_buffer_load_b128(rpt, 393216 + w * 8192 + 4096 + lane * 16, 0, 16); const float f = fw[1][3];
            a[0] += f * __uint_as_float(v[0] << 16); a[1] += f * __uint_as_float(v[0] & 0xffff0000u); a[2] += f * __uint_as_float(v[1] << 16); a[3] += f * __uint_as_float(v[1] & 0xffff0000u);
            b2[0] += f * __uint_as_float(v[2] << 16); b2[1] += f * __uint_as_float(v[2] & 0xffff0000u); b2[2] += f * __uint_as_float(v[3] << 16); b2[3] += f * __uint_as_float(v[3] & 0xffff0000u); }
          if (R < 224) { u16* dst = o + ((size_t)((kvh * 2 + hl) * 8 + 0) * NROW + orow) * 32 + 0 + 4 * h;
            u32x2 v0 = {cvtpk(a[0], a[1]), cvtpk(a[2], a[3])}; u32x2 v1 = {cvtpk(b2[0], b2[1]), cvtpk(b2[2], b2[3])};
            *(u32x2*)dst = v0; *(u32x2*)(dst + 8) = v1; } }
        { f32x4 a = acc_read4<136>(), b2 = acc_read4<140>(); const float fo = fw[1][0] * inv[1]; a *= fo; b2 *= fo;
          { u32x4 v = __builtin_amdgcn_raw_buffer_load_b128(rpt, 131072 + w * 8192 + 5120 + lane * 16, 0, 16); const float f = fw[1][1];
            a[0] += f * __uint_as_float(v[0] << 16); a[1] += f * __uint_as_float(v[0] & 0xffff0000u); a[2] += f * __uint_as_float(v[1] << 16); a[3] += f * __uint_as_float(v[1] & 0xffff0000u);
            b2[0] += f * __uint_as_float(v[2] << 16); b2[1] += f * __uint_as_float(v[2] & 0xffff0000u); b2[2] += f * __uint_as_float(v[3] << 16); b2[3] += f * __uint_as_float(v[3] & 0xffff0000u); }
          { u32x4 v = __builtin_amdgcn_raw_buffer_load_b128(rpt, 262144 + w * 8192 + 5120 + lane * 16, 0, 16); const float f = fw[1][2];
            a[0] += f * __uint_as_float(v[0] << 16); a[1] += f * __uint_as_float(v[0] & 0xffff0000u); a[2] += f * __uint_as_float(v[1] << 16); a[3] += f * __uint_as_float(v[1] & 0xffff0000u);
            b2[0] += f * __uint_as_float(v[2] << 16); b2[1] += f * __uint_as_float(v[2] & 0xffff0000u); b2[2] += f * __uint_as_float(v[3] << 16); b2[3] += f * __uint_as_float(v[3] & 0xffff0000u); }
          { u32x4 v = __builtin_amdgcn_raw_buffer_load_b128(rpt, 393216 + w * 8192 + 5120 + lane * 16, 0, 16); const float f = fw[1][3];
            a[0] += f * __uint_as_float(v[0] << 16); a[1] += f * __uint_as_float(v[0] & 0xffff0000u); a[2] += f * __uint_as_float(v[1] << 16); a[3] += f * __uint_as_float(v[1] & 0xffff0000u);
            b2[0] += f * __uint_as_float(v[2] << 16); b2[1] += f * __uint_as_float(v[2] & 0xffff0000u); b2[2] += f * __uint_as_float(v[3] << 16); b2[3] += f * __uint_as_float(v[3] & 0xffff0000u); }
          if (R < 224) { u16* dst = o + ((size_t)((kvh * 2 + hl) * 8 + 0) * NROW + orow) * 32 + 16 + 4 * h;
            u32x2 v0 = {cvtpk(a[0], a[1]), cvtpk(a[2], a[3])}; u32x2 v1 = {cvtpk(b2[0], b2[1]), cvtpk(b2[2], b2[3])};
            *(u32x2*)dst = v0; *(u32x2*)(dst + 8) = v1; } }
        { f32x4 a = acc_read4<144>(), b2 = acc_read4<148>(); const float fo = fw[1][0] * inv[1]; a *= fo; b2 *= fo;
          { u32x4 v = __builtin_amdgcn_raw_buffer_load_b128(rpt, 131072 + w * 8192 + 6144 + lane * 16, 0, 16); const float f = fw[1][1];
            a[0] += f * __uint_as_float(v[0] << 16); a[1] += f * __uint_as_float(v[0] & 0xffff0000u); a[2] += f * __uint_as_float(v[1] << 16); a[3] += f * __uint_as_float(v[1] & 0xffff0000u);
            b2[0] += f * __uint_as_float(v[2] << 16); b2[1] += f * __uint_as_float(v[2] & 0xffff0000u); b2[2] += f * __uint_as_float(v[3] << 16); b2[3] += f * __uint_as_float(v[3] & 0xffff0000u); }
          { u32x4 v = __builtin_amdgcn_raw_buffer_load_b128(rpt, 262144 + w * 8192 + 6144 + lane * 16, 0, 16); const float f = fw[1][2];
            a[0] += f * __uint_as_float(v[0] << 16); a[1] += f * __uint_as_float(v[0] & 0xffff0000u); a[2] += f * __uint_as_float(v[1] << 16); a[3] += f * __uint_as_float(v[1] & 0xffff0000u);
            b2[0] += f * __uint_as_float(v[2] << 16); b2[1] += f * __uint_as_float(v[2] & 0xffff0000u); b2[2] += f * __uint_as_float(v[3] << 16); b2[3] += f * __uint_as_float(v[3] & 0xffff0000u); }
          { u32x4 v = __builtin_amdgcn_raw_buffer_load_b128(rpt, 393216 + w * 8192 + 6144 + lane * 16, 0, 16); const float f = fw[1][3];
            a[0] += f * __uint_as_float(v[0] << 16); a[1] += f * __uint_as_float(v[0] & 0xffff0000u); a[2] += f * __uint_as_float(v[1] << 16); a[3] += f * __uint_as_float(v[1] & 0xffff0000u);
            b2[0] += f * __uint_as_float(v[2] << 16); b2[1] += f * __uint_as_float(v[2] & 0xffff0000u); b2[2] += f * __uint_as_float(v[3] << 16); b2[3] += f * __uint_as_float(v[3] & 0xffff0000u); }
          if (R < 224) { u16* dst = o + ((size_t)((kvh * 2 + hl) * 8 + 1) * NROW + orow) * 32 + 0 + 4 * h;
            u32x2 v0 = {cvtpk(a[0], a[1]), cvtpk(a[2], a[3])}; u32x2 v1 = {cvtpk(b2[0], b2[1]), cvtpk(b2[2], b2[3])};
            *(u32x2*)dst = v0; *(u32x2*)(dst + 8) = v1; } }
        { f32x4 a = acc_read4<152>(), b2 = acc_read4<156>(); const float fo = fw[1][0] * inv[1]; a *= fo; b2 *= fo;
          { u32x4 v = __builtin_amdgcn_raw_buffer_load_b128(rpt, 131072 + w * 8192 + 7168 + lane * 16, 0, 16); const float f = fw[1][1];
            a[0] += f * __uint_as_float(v[0] << 16); a[1] += f * __uint_as_float(v[0] & 0xffff0000u); a[2] += f * __uint_as_float(v[1] << 16); a[3] += f * __uint_as_float(v[1] & 0xffff0000u);
            b2[0] += f * __uint_as_float(v[2] << 16); b2[1] += f * __uint_as_float(v[2] & 0xffff0000u); b2[2] += f * __uint_as_float(v[3] << 16); b2[3] += f * __uint_as_float(v[3] & 0xffff0000u); }
          { u32x4 v = __builtin_amdgcn_raw_buffer_load_b128(rpt, 262144 + w * 8192 + 7168 + lane * 16, 0, 16); const float f = fw[1][2];
            a[0] += f * __uint_as_float(v[0] << 16); a[1] += f * __uint_as_float(v[0] & 0xffff0000u); a[2] += f * __uint_as_float(v[1] << 16); a[3] += f * __uint_as_float(v[1] & 0xffff0000u);
            b2[0] += f * __uint_as_float(v[2] << 16); b2[1] += f * __uint_as_float(v[2] & 0xffff0000u); b2[2] += f * __uint_as_float(v[3] << 16); b2[3] += f * __uint_as_float(v[3] & 0xffff0000u); }
          { u32x4 v = __builtin_amdgcn_raw_buffer_load_b128(rpt, 393216 + w * 8192 + 7168 + lane * 16, 0, 16); const float f = fw[1][3];
            a[0] += f * __uint_as_float(v[0] << 16); a[1] += f * __uint_as_float(v[0] & 0xffff0000u); a[2] += f * __uint_as_float(v[1] << 16); a[3] += f * __uint_as_float(v[1] & 0xffff0000u);
            b2[0] += f * __uint_as_float(v[2] << 16); b2[1] += f * __uint_as_float(v[2] & 0xffff0000u); b2[2] += f * __uint_as_float(v[3] << 16); b2[3] += f * __uint_as_float(v[3] & 0xffff0000u); }
          if (R < 224) { u16* dst = o + ((size_t)((kvh * 2 + hl) * 8 + 1) * NROW + orow) * 32 + 16 + 4 * h;
            u32x2 v0 = {cvtpk(a[0], a[1]), cvtpk(a[2], a[3])}; u32x2 v1 = {cvtpk(b2[0], b2[1]), cvtpk(b2[2], b2[3])};
            *(u32x2*)dst = v0; *(u32x2*)(dst + 8) = v1; } }
      }
    } break;
    case 1: {
      { const int R = 64 * w + 0 + r; const int hl = R >= 112 ? 1 : 0; const int orow = b * SQ + R - 112 * hl;
        { f32x4 a = acc_read4<32>(), b2 = acc_read4<36>(); const float fo = fw[0][1] * inv[0]; a *= fo; b2 *= fo;
          { u32x4 v = __builtin_amdgcn_raw_buffer_load_b128(rpt, 32768 + w * 8192 + 0 + lane * 16, 0, 16); const float f = fw[0][0];
            a[0] += f * __uint_as_float(v[0] << 16); a[1] += f * __uint_as_float(v[0] & 0xffff0000u); a[2] += f * __uint_as_float(v[1] << 16); a[3] += f * __uint_as_float(v[1] & 0xffff0000u);
            b2[0] += f * __uint_as_float(v[2] << 16); b2[1] += f * __uint_as_float(v[2] & 0xffff0000u); b2[2] += f * __uint_as_float(v[3] << 16); b2[3] += f * __uint_as_float(v[3] & 0xffff0000u); }
          { u32x4 v = __builtin_amdgcn_raw_buffer_load_b128(rpt, 294912 + w * 8192 + 0 + lane * 16, 0, 16); const float f = fw[0][2];
            a[0] += f * __uint_as_float(v[0] << 16); a[1] += f * __uint_as_float(v[0] & 0xffff0000u); a[2] += f * __uint_as_float(v[1] << 16); a[3] += f * __uint_as_float(v[1] & 0xffff0000u);
            b2[0] += f * __uint_as_float(v[2] << 16); b2[1] += f * __uint_as_float(v[2] & 0xffff0000u); b2[2] += f * __uint_as_float(v[3] << 16); b2[3] += f * __uint_as_float(v[3] & 0xffff0000u); }
          { u32x4 v = __builtin_amdgcn_raw_buffer_load_b128(rpt, 425984 + w * 8192 + 0 + lane * 16, 0, 16); const float f = fw[0][3];
            a[0] += f * __uint_as_float(v[0] << 16); a[1] += f * __uint_as_float(v[0] & 0xffff0000u); a[2] += f * __uint_as_float(v[1] << 16); a[3] += f * __uint_as_float(v[1] & 0xffff0000u);
            b2[0] += f * __uint_as_float(v[2] << 16); b2[1] += f * __uint_as_float(v[2] & 0xffff0000u); b2[2] += f * __uint_as_float(v[3] << 16); b2[3] += f * __uint_as_float(v[3] & 0xffff0000u); }
          if (R < 224) { u16* dst = o + ((size_t)((kvh * 2 + hl) * 8 + 2) * NROW + orow) * 32 + 0 + 4 * h;
            u32x2 v0 = {cvtpk(a[0], a[1]), cvtpk(a[2], a[3])}; u32x2 v1 = {cvtpk(b2[0], b2[1]), cvtpk(b2[2], b2[3])};
            *(u32x2*)dst = v0; *(u32x2*)(dst + 8) = v1; } }
        { f32x4 a = acc_read4<40>(), b2 = acc_read4<44>(); const float fo = fw[0][1] * inv[0]; a *= fo; b2 *= fo;
          { u32x4 v = __builtin_amdgcn_raw_buffer_load_b128(rpt, 32768 + w * 8192 + 1024 + lane * 16, 0, 16); const float f = fw[0][0];
            a[0] += f * __uint_as_float(v[0] << 16); a[1] += f * __uint_as_float(v[0] & 0xffff0000u); a[2] += f * __uint_as_float(v[1] << 16); a[3] += f * __uint_as_float(v[1] & 0xffff0000u);
            b2[0] += f * __uint_as_float(v[2] << 16); b2[1] += f * __uint_as_float(v[2] & 0xffff0000u); b2[2] += f * __uint_as_float(v[3] << 16); b2[3] += f * __uint_as_float(v[3] & 0xffff0000u); }
          { u32x4 v = __builtin_amdgcn_raw_buffer_load_b128(rpt, 294912 + w * 8192 + 1024 + lane * 16, 0, 16); const float f = fw[0][2];
            a[0] += f * __uint_as_float(v[0] << 16); a[1] += f * __uint_as_float(v[0] & 0xffff0000u); a[2] += f * __uint_as_float(v[1] << 16); a[3] += f * __uint_as_float(v[1] & 0xffff0000u);
            b2[0] += f * __uint_as_float(v[2] << 16); b2[1] += f * __uint_as_float(v[2] & 0xffff0000u); b2[2] += f * __uint_as_float(v[3] << 16); b2[3] += f * __uint_as_float(v[3] & 0xffff0000u); }
          { u32x4 v = __builtin_amdgcn_raw_buffer_load_b128(rpt, 425984 + w * 8192 + 1024 + lane * 16, 0, 16); const float f = fw[0][3];
            a[0] += f * __uint_as_float(v[0] << 16); a[1] += f * __uint_as_float(v[0] & 0xffff0000u); a[2] += f * __uint_as_float(v[1] << 16); a[3] += f * __uint_as_float(v[1] & 0xffff0000u);
            b2[0] += f * __uint_as_float(v[2] << 16); b2[1] += f * __uint_as_float(v[2] & 0xffff0000u); b2[2] += f * __uint_as_float(v[3] << 16); b2[3] += f * __uint_as_float(v[3] & 0xffff0000u); }
          if (R < 224) { u16* dst = o + ((size_t)((kvh * 2 + hl) * 8 + 2) * NROW + orow) * 32 + 16 + 4 * h;
            u32x2 v0 = {cvtpk(a[0], a[1]), cvtpk(a[2], a[3])}; u32x2 v1 = {cvtpk(b2[0], b2[1]), cvtpk(b2[2], b2[3])};
            *(u32x2*)dst = v0; *(u32x2*)(dst + 8) = v1; } }
        { f32x4 a = acc_read4<48>(), b2 = acc_read4<52>(); const float fo = fw[0][1] * inv[0]; a *= fo; b2 *= fo;
          { u32x4 v = __builtin_amdgcn_raw_buffer_load_b128(rpt, 32768 + w * 8192 + 2048 + lane * 16, 0, 16); const float f = fw[0][0];
            a[0] += f * __uint_as_float(v[0] << 16); a[1] += f * __uint_as_float(v[0] & 0xffff0000u); a[2] += f * __uint_as_float(v[1] << 16); a[3] += f * __uint_as_float(v[1] & 0xffff0000u);
            b2[0] += f * __uint_as_float(v[2] << 16); b2[1] += f * __uint_as_float(v[2] & 0xffff0000u); b2[2] += f * __uint_as_float(v[3] << 16); b2[3] += f * __uint_as_float(v[3] & 0xffff0000u); }
          { u32x4 v = __builtin_amdgcn_raw_buffer_load_b128(rpt, 294912 + w * 8192 + 2048 + lane * 16, 0, 16); const float f = fw[0][2];
            a[0] += f * __uint_as_float(v[0] << 16); a[1] += f * __uint_as_float(v[0] & 0xffff0000u); a[2] += f * __uint_as_float(v[1] << 16); a[3] += f * __uint_as_float(v[1] & 0xffff0000u);
            b2[0] += f * __uint_as_float(v[2] << 16); b2[1] += f * __uint_as_float(v[2] & 0xffff0000u); b2[2] += f * __uint_as_float(v[3] << 16); b2[3] += f * __uint_as_float(v[3] & 0xffff0000u); }
          { u32x4 v = __builtin_amdgcn_raw_buffer_load_b128(rpt, 425984 + w * 8192 + 2048 + lane * 16, 0, 16); const float f = fw[0][3];
            a[0] += f * __uint_as_float(v[0] << 16); a[1] += f * __uint_as_float(v[0] & 0xffff0000u); a[2] += f * __uint_as_float(v[1] << 16); a[3] += f * __uint_as_float(v[1] & 0xffff0000u);
            b2[0] += f * __uint_as_float(v[2] << 16); b2[1] += f * __uint_as_float(v[2] & 0xffff0000u); b2[2] += f * __uint_as_float(v[3] << 16); b2[3] += f * __uint_as_float(v[3] & 0xffff0000u); }
          if (R < 224) { u16* dst = o + ((size_t)((kvh * 2 + hl) * 8 + 3) * NROW + orow) * 32 + 0 + 4 * h;
            u32x2 v0 = {cvtpk(a[0], a[1]), cvtpk(a[2], a[3])}; u32x2 v1 = {cvtpk(b2[0], b2[1]), cvtpk(b2[2], b2[3])};
            *(u32x2*)dst = v0; *(u32x2*)(dst + 8) = v1; } }
        { f32x4 a = acc_read4<56>(), b2 = acc_read4<60>(); const float fo = fw[0][1] * inv[0]; a *= fo; b2 *= fo;
          { u32x4 v = __builtin_amdgcn_raw_buffer_load_b128(rpt, 32768 + w * 8192 + 3072 + lane * 16, 0, 16); const float f = fw[0][0];
            a[0] += f * __uint_as_float(v[0] << 16); a[1] += f * __uint_as_float(v[0] & 0xffff0000u); a[2] += f * __uint_as_float(v[1] << 16); a[3] += f * __uint_as_float(v[1] & 0xffff0000u);
            b2[0] += f * __uint_as_float(v[2] << 16); b2[1] += f * __uint_as_float(v[2] & 0xffff0000u); b2[2] += f * __uint_as_float(v[3] << 16); b2[3] += f * __uint_as_float(v[3] & 0xffff0000u); }
          { u32x4 v = __builtin_amdgcn_raw_buffer_load_b128(rpt, 294912 + w * 8192 + 3072 + lane * 16, 0, 16); const float f = fw[0][2];
            a[0] += f * __uint_as_float(v[0] << 16); a[1] += f * __uint_as_float(v[0] & 0xffff0000u); a[2] += f * __uint_as_float(v[1] << 16); a[3] += f * __uint_as_float(v[1] & 0xffff0000u);
            b2[0] += f * __uint_as_float(v[2] << 16); b2[1] += f * __uint_as_float(v[2] & 0xffff0000u); b2[2] += f * __uint_as_float(v[3] << 16); b2[3] += f * __uint_as_float(v[3] & 0xffff0000u); }
          { u32x4 v = __builtin_amdgcn_raw_buffer_load_b128(rpt, 425984 + w * 8192 + 3072 + lane * 16, 0, 16); const float f = fw[0][3];
            a[0] += f * __uint_as_float(v[0] << 16); a[1] += f * __uint_as_float(v[0] & 0xffff0000u); a[2] += f * __uint_as_float(v[1] << 16); a[3] += f * __uint_as_float(v[1] & 0xffff0000u);
            b2[0] += f * __uint_as_float(v[2] << 16); b2[1] += f * __uint_as_float(v[2] & 0xffff0000u); b2[2] += f * __uint_as_float(v[3] << 16); b2[3] += f * __uint_as_float(v[3] & 0xffff0000u); }
          if (R < 224) { u16* dst = o + ((size_t)((kvh * 2 + hl) * 8 + 3) * NROW + orow) * 32 + 16 + 4 * h;
            u32x2 v0 = {cvtpk(a[0], a[1]), cvtpk(a[2], a[3])}; u32x2 v1 = {cvtpk(b2[0], b2[1]), cvtpk(b2[2], b2[3])};
            *(u32x2*)dst = v0; *(u32x2*)(dst + 8) = v1; } }
      }
      { const int R = 64 * w + 32 + r; const int hl = R >= 112 ? 1 : 0; const int orow = b * SQ + R - 112 * hl;
        { f32x4 a = acc_read4<160>(), b2 = acc_read4<164>(); const float fo = fw[1][1] * inv[1]; a *= fo; b2 *= fo;
          { u32x4 v = __builtin_amdgcn_raw_buffer_load_b128(rpt, 32768 + w * 8192 + 4096 + lane * 16, 0, 16); const float f = fw[1][0];
            a[0] += f * __uint_as_float(v[0] << 16); a[1] += f * __uint_as_float(v[0] & 0xffff0000u); a[2] += f * __uint_as_float(v[1] << 16); a[3] += f * __uint_as_float(v[1] & 0xffff0000u);
            b2[0] += f * __uint_as_float(v[2] << 16); b2[1] += f * __uint_as_float(v[2] & 0xffff0000u); b2[2] += f * __uint_as_float(v[3] << 16); b2[3] += f * __uint_as_float(v[3] & 0xffff0000u); }
          { u32x4 v = __builtin_amdgcn_raw_buffer_load_b128(rpt, 294912 + w * 8192 + 4096 + lane * 16, 0, 16); const float f = fw[1][2];
            a[0] += f * __uint_as_float(v[0] << 16); a[1] += f * __uint_as_float(v[0] & 0xffff0000u); a[2] += f * __uint_as_float(v[1] << 16); a[3] += f * __uint_as_float(v[1] & 0xffff0000u);
            b2[0] += f * __uint_as_float(v[2] << 16); b2[1] += f * __uint_as_float(v[2] & 0xffff0000u); b2[2] += f * __uint_as_float(v[3] << 16); b2[3] += f * __uint_as_float(v[3] & 0xffff0000u); }
          { u32x4 v = __builtin_amdgcn_raw_buffer_load_b128(rpt, 425984 + w * 8192 + 4096 + lane * 16, 0, 16); const float f = fw[1][3];
            a[0] += f * __uint_as_float(v[0] << 16); a[1] += f * __uint_as_float(v[0] & 0xffff0000u); a[2] += f * __uint_as_float(v[1] << 16); a[3] += f * __uint_as_float(v[1] & 0xffff0000u);
            b2[0] += f * __uint_as_float(v[2] << 16); b2[1] += f * __uint_as_float(v[2] & 0xffff0000u); b2[2] += f * __uint_as_float(v[3] << 16); b2[3] += f * __uint_as_float(v[3] & 0xffff0000u); }
          if (R < 224) { u16* dst = o + ((size_t)((kvh * 2 + hl) * 8 + 2) * NROW + orow) * 32 + 0 + 4 * h;
            u32x2 v0 = {cvtpk(a[0], a[1]), cvtpk(a[2], a[3])}; u32x2 v1 = {cvtpk(b2[0], b2[1]), cvtpk(b2[2], b2[3])};
            *(u32x2*)dst = v0; *(u32x2*)(dst + 8) = v1; } }
        { f32x4 a = acc_read4<168>(), b2 = acc_read4<172>(); const float fo = fw[1][1] * inv[1]; a *= fo; b2 *= fo;
          { u32x4 v = __builtin_amdgcn_raw_buffer_load_b128(rpt, 32768 + w * 8192 + 5120 + lane * 16, 0, 16); const float f = fw[1][0];
            a[0] += f * __uint_as_float(v[0] << 16); a[1] += f * __uint_as_float(v[0] & 0xffff0000u); a[2] += f * __uint_as_float(v[1] << 16); a[3] += f * __uint_as_float(v[1] & 0xffff0000u);
            b2[0] += f * __uint_as_float(v[2] << 16); b2[1] += f * __uint_as_float(v[2] & 0xffff0000u); b2[2] += f * __uint_as_float(v[3] << 16); b2[3] += f * __uint_as_float(v[3] & 0xffff0000u); }
          { u32x4 v = __builtin_amdgcn_raw_buffer_load_b128(rpt, 294912 + w * 8192 + 5120 + lane * 16, 0, 16); const float f = fw[1][2];
            a[0] += f * __uint_as_float(v[0] << 16); a[1] += f * __uint_as_float(v[0] & 0xffff0000u); a[2] += f * __uint_as_float(v[1] << 16); a[3] += f * __uint_as_float(v[1] & 0xffff0000u);
            b2[0] += f * __uint_as_float(v[2] << 16); b2[1] += f * __uint_as_float(v[2] & 0xffff0000u); b2[2] += f * __uint_as_float(v[3] << 16); b2[3] += f * __uint_as_float(v[3] & 0xffff0000u); }
          { u32x4 v = __builtin_amdgcn_raw_buffer_load_b128(rpt, 425984 + w * 8192 + 5120 + lane * 16, 0, 16); const float f = fw[1][3];
            a[0] += f * __uint_as_float(v[0] << 16); a[1] += f * __uint_as_float(v[0] & 0xffff0000u); a[2] += f * __uint_as_float(v[1] << 16); a[3] += f * __uint_as_float(v[1] & 0xffff0000u);
            b2[0] += f * __uint_as_float(v[2] << 16); b2[1] += f * __uint_as_float(v[2] & 0xffff0000u); b2[2] += f * __uint_as_float(v[3] << 16); b2[3] += f * __uint_as_float(v[3] & 0xffff0000u); }
          if (R < 224) { u16* dst = o + ((size_t)((kvh * 2 + hl) * 8 + 2) * NROW + orow) * 32 + 16 + 4 * h;
            u32x2 v0 = {cvtpk(a[0], a[1]), cvtpk(a[2], a[3])}; u32x2 v1 = {cvtpk(b2[0], b2[1]), cvtpk(b2[2], b2[3])};
            *(u32x2*)dst = v0; *(u32x2*)(dst + 8) = v1; } }
        { f32x4 a = acc_read4<176>(), b2 = acc_read4<180>(); const float fo = fw[1][1] * inv[1]; a *= fo; b2 *= fo;
          { u32x4 v = __builtin_amdgcn_raw_buffer_load_b128(rpt, 32768 + w * 8192 + 6144 + lane * 16, 0, 16); const float f = fw[1][0];
            a[0] += f * __uint_as_float(v[0] << 16); a[1] += f * __uint_as_float(v[0] & 0xffff0000u); a[2] += f * __uint_as_float(v[1] << 16); a[3] += f * __uint_as_float(v[1] & 0xffff0000u);
            b2[0] += f * __uint_as_float(v[2] << 16); b2[1] += f * __uint_as_float(v[2] & 0xffff0000u); b2[2] += f * __uint_as_float(v[3] << 16); b2[3] += f * __uint_as_float(v[3] & 0xffff0000u); }
          { u32x4 v = __builtin_amdgcn_raw_buffer_load_b128(rpt, 294912 + w * 8192 + 6144 + lane * 16, 0, 16); const float f = fw[1][2];
            a[0] += f * __uint_as_float(v[0] << 16); a[1] += f * __uint_as_float(v[0] & 0xffff0000u); a[2] += f * __uint_as_float(v[1] << 16); a[3] += f * __uint_as_float(v[1] & 0xffff0000u);
            b2[0] += f * __uint_as_float(v[2] << 16); b2[1] += f * __uint_as_float(v[2] & 0xffff0000u); b2[2] += f * __uint_as_float(v[3] << 16); b2[3] += f * __uint_as_float(v[3] & 0xffff0000u); }
          { u32x4 v = __builtin_amdgcn_raw_buffer_load_b128(rpt, 425984 + w * 8192 + 6144 + lane * 16, 0, 16); const float f = fw[1][3];
            a[0] += f * __uint_as_float(v[0] << 16); a[1] += f * __uint_as_float(v[0] & 0xffff0000u); a[2] += f * __uint_as_float(v[1] << 16); a[3] += f * __uint_as_float(v[1] & 0xffff0000u);
            b2[0] += f * __uint_as_float(v[2] << 16); b2[1] += f * __uint_as_float(v[2] & 0xffff0000u); b2[2] += f * __uint_as_float(v[3] << 16); b2[3] += f * __uint_as_float(v[3] & 0xffff0000u); }
          if (R < 224) { u16* dst = o + ((size_t)((kvh * 2 + hl) * 8 + 3) * NROW + orow) * 32 + 0 + 4 * h;
            u32x2 v0 = {cvtpk(a[0], a[1]), cvtpk(a[2], a[3])}; u32x2 v1 = {cvtpk(b2[0], b2[1]), cvtpk(b2[2], b2[3])};
            *(u32x2*)dst = v0; *(u32x2*)(dst + 8) = v1; } }
        { f32x4 a = acc_read4<184>(), b2 = acc_read4<188>(); const float fo = fw[1][1] * inv[1]; a *= fo; b2 *= fo;
          { u32x4 v = __builtin_amdgcn_raw_buffer_load_b128(rpt, 32768 + w * 8192 + 7168 + lane * 16, 0, 16); const float f = fw[1][0];
            a[0] += f * __uint_as_float(v[0] << 16); a[1] += f * __uint_as_float(v[0] & 0xffff0000u); a[2] += f * __uint_as_float(v[1] << 16); a[3] += f * __uint_as_float(v[1] & 0xffff0000u);
            b2[0] += f * __uint_as_float(v[2] << 16); b2[1] += f * __uint_as_float(v[2] & 0xffff0000u); b2[2] += f * __uint_as_float(v[3] << 16); b2[3] += f * __uint_as_float(v[3] & 0xffff0000u); }
          { u32x4 v = __builtin_amdgcn_raw_buffer_load_b128(rpt, 294912 + w * 8192 + 7168 + lane * 16, 0, 16); const float f = fw[1][2];
            a[0] += f * __uint_as_float(v[0] << 16); a[1] += f * __uint_as_float(v[0] & 0xffff0000u); a[2] += f * __uint_as_float(v[1] << 16); a[3] += f * __uint_as_float(v[1] & 0xffff0000u);
            b2[0] += f * __uint_as_float(v[2] << 16); b2[1] += f * __uint_as_float(v[2] & 0xffff0000u); b2[2] += f * __uint_as_float(v[3] << 16); b2[3] += f * __uint_as_float(v[3] & 0xffff0000u); }
          { u32x4 v = __builtin_amdgcn_raw_buffer_load_b128(rpt, 425984 + w * 8192 + 7168 + lane * 16, 0, 16); const float f = fw[1][3];
            a[0] += f * __uint_as_float(v[0] << 16); a[1] += f * __uint_as_float(v[0] & 0xffff0000u); a[2] += f * __uint_as_float(v[1] << 16); a[3] += f * __uint_as_float(v[1] & 0xffff0000u);
            b2[0] += f * __uint_as_float(v[2] << 16); b2[1] += f * __uint_as_float(v[2] & 0xffff0000u); b2[2] += f * __uint_as_float(v[3] << 16); b2[3] += f * __uint_as_float(v[3] & 0xffff0000u); }
          if (R < 224) { u16* dst = o + ((size_t)((kvh * 2 + hl) * 8 + 3) * NROW + orow) * 32 + 16 + 4 * h;
            u32x2 v0 = {cvtpk(a[0], a[1]), cvtpk(a[2], a[3])}; u32x2 v1 = {cvtpk(b2[0], b2[1]), cvtpk(b2[2], b2[3])};
            *(u32x2*)dst = v0; *(u32x2*)(dst + 8) = v1; } }
      }
    } break;
    case 2: {
      { const int R = 64 * w + 0 + r; const int hl = R >= 112 ? 1 : 0; const int orow = b * SQ + R - 112 * hl;
        { f32x4 a = acc_read4<64>(), b2 = acc_read4<68>(); const float fo = fw[0][2] * inv[0]; a *= fo; b2 *= fo;
          { u32x4 v = __builtin_amdgcn_raw_buffer_load_b128(rpt, 65536 + w * 8192 + 0 + lane * 16, 0, 16); const float f = fw[0][0];
            a[0] += f * __uint_as_float(v[0] << 16); a[1] += f * __uint_as_float(v[0] & 0xffff0000u); a[2] += f * __uint_as_float(v[1] << 16); a[3] += f * __uint_as_float(v[1] & 0xffff0000u);
            b2[0] += f * __uint_as_float(v[2] << 16); b2[1] += f * __uint_as_float(v[2] & 0xffff0000u); b2[2] += f * __uint_as_float(v[3] << 16); b2[3] += f * __uint_as_float(v[3] & 0xffff0000u); }
          { u32x4 v = __builtin_amdgcn_raw_buffer_load_b128(rpt, 196608 + w * 8192 + 0 + lane * 16, 0, 16); const float f = fw[0][1];
            a[0] += f * __uint_as_float(v[0] << 16); a[1] += f * __uint_as_float(v[0] & 0xffff0000u); a[2] += f * __uint_as_float(v[1] << 16); a[3] += f * __uint_as_float(v[1] & 0xffff0000u);
            b2[0] += f * __uint_as_float(v[2] << 16); b2[1] += f * __uint_as_float(v[2] & 0xffff0000u); b2[2] += f * __uint_as_float(v[3] << 16); b2[3] += f * __uint_as_float(v[3] & 0xffff0000u); }
          { u32x4 v = __builtin_amdgcn_raw_buffer_load_b128(rpt, 458752 + w * 8192 + 0 + lane * 16, 0, 16); const float f = fw[0][3];
            a[0] += f * __uint_as_float(v[0] << 16); a[1] += f * __uint_as_float(v[0] & 0xffff0000u); a[2] += f * __uint_as_float(v[1] << 16); a[3] += f * __uint_as_float(v[1] & 0xffff0000u);
            b2[0] += f * __uint_as_float(v[2] << 16); b2[1] += f * __uint_as_float(v[2] & 0xffff0000u); b2[2] += f * __uint_as_float(v[3] << 16); b2[3] += f * __uint_as_float(v[3] & 0xffff0000u); }
          if (R < 224) { u16* dst = o + ((size_t)((kvh * 2 + hl) * 8 + 4) * NROW + orow) * 32 + 0 + 4 * h;
            u32x2 v0 = {cvtpk(a[0], a[1]), cvtpk(a[2], a[3])}; u32x2 v1 = {cvtpk(b2[0], b2[1]), cvtpk(b2[2], b2[3])};
            *(u32x2*)dst = v0; *(u32x2*)(dst + 8) = v1; } }
        { f32x4 a = acc_read4<72>(), b2 = acc_read4<76>(); const float fo = fw[0][2] * inv[0]; a *= fo; b2 *= fo;
          { u32x4 v = __builtin_amdgcn_raw_buffer_load_b128(rpt, 65536 + w * 8192 + 1024 + lane * 16, 0, 16); const float f = fw[0][0];
            a[0] += f * __uint_as_float(v[0] << 16); a[1] += f * __uint_as_float(v[0] & 0xffff0000u); a[2] += f * __uint_as_float(v[1] << 16); a[3] += f * __uint_as_float(v[1] & 0xffff0000u);
            b2[0] += f * __uint_as_float(v[2] << 16); b2[1] += f * __uint_as_float(v[2] & 0xffff0000u); b2[2] += f * __uint_as_float(v[3] << 16); b2[3] += f * __uint_as_float(v[3] & 0xffff0000u); }
          { u32x4 v = __builtin_amdgcn_raw_buffer_load_b128(rpt, 196608 + w * 8192 + 1024 + lane * 16, 0, 16); const float f = fw[0][1];
            a[0] += f * __uint_as_float(v[0] << 16); a[1] += f * __uint_as_float(v[0] & 0xffff0000u); a[2] += f * __uint_as_float(v[1] << 16); a[3] += f * __uint_as_float(v[1] & 0xffff0000u);
            b2[0] += f * __uint_as_float(v[2] << 16); b2[1] += f * __uint_as_float(v[2] & 0xffff0000u); b2[2] += f * __uint_as_float(v[3] << 16); b2[3] += f * __uint_as_float(v[3] & 0xffff0000u); }
          { u32x4 v = __builtin_amdgcn_raw_buffer_load_b128(rpt, 458752 + w * 8192 + 1024 + lane * 16, 0, 16); const float f = fw[0][3];
            a[0] += f * __uint_as_float(v[0] << 16); a[1] += f * __uint_as_float(v[0] & 0xffff0000u); a[2] += f * __uint_as_float(v[1] << 16); a[3] += f * __uint_as_float(v[1] & 0xffff0000u);
            b2[0] += f * __uint_as_float(v[2] << 16); b2[1] += f * __uint_as_float(v[2] & 0xffff0000u); b2[2] += f * __uint_as_float(v[3] << 16); b2[3] += f * __uint_as_float(v[3] & 0xffff0000u); }
          if (R < 224) { u16* dst = o + ((size_t)((kvh * 2 + hl) * 8 + 4) * NROW + orow) * 32 + 16 + 4 * h;
            u32x2 v0 = {cvtpk(a[0], a[1]), cvtpk(a[2], a[3])}; u32x2 v1 = {cvtpk(b2[0], b2[1]), cvtpk(b2[2], b2[3])};
            *(u32x2*)dst = v0; *(u32x2*)(dst + 8) = v1; } }
        { f32x4 a = acc_read4<80>(), b2 = acc_read4<84>(); const float fo = fw[0][2] * inv[0]; a *= fo; b2 *= fo;
          { u32x4 v = __builtin_amdgcn_raw_buffer_load_b128(rpt, 65536 + w * 8192 + 2048 + lane * 16, 0, 16); const float f = fw[0][0];
            a[0] += f * __uint_as_float(v[0] << 16); a[1] += f * __uint_as_float(v[0] & 0xffff0000u); a[2] += f * __uint_as_float(v[1] << 16); a[3] += f * __uint_as_float(v[1] & 0xffff0000u);
            b2[0] += f * __uint_as_float(v[2] << 16); b2[1] += f * __uint_as_float(v[2] & 0xffff0000u); b2[2] += f * __uint_as_float(v[3] << 16); b2[3] += f * __uint_as_float(v[3] & 0xffff0000u); }
          { u32x4 v = __builtin_amdgcn_raw_buffer_load_b128(rpt, 196608 + w * 8192 + 2048 + lane * 16, 0, 16); const float f = fw[0][1];
            a[0] += f * __uint_as_float(v[0] << 16); a[1] += f * __uint_as_float(v[0] & 0xffff0000u); a[2] += f * __uint_as_float(v[1] << 16); a[3] += f * __uint_as_float(v[1] & 0xffff0000u);
            b2[0] += f * __uint_as_float(v[2] << 16); b2[1] += f * __uint_as_float(v[2] & 0xffff0000u); b2[2] += f * __uint_as_float(v[3] << 16); b2[3] += f * __uint_as_float(v[3] & 0xffff0000u); }
          { u32x4 v = __builtin_amdgcn_raw_buffer_load_b128(rpt, 458752 + w * 8192 + 2048 + lane * 16, 0, 16); const float f = fw[0][3];
            a[0] += f * __uint_as_float(v[0] << 16); a[1] += f * __uint_as_float(v[0] & 0xffff0000u); a[2] += f * __uint_as_float(v[1] << 16); a[3] += f * __uint_as_float(v[1] & 0xffff0000u);
            b2[0] += f * __uint_as_float(v[2] << 16); b2[1] += f * __uint_as_float(v[2] & 0xffff0000u); b2[2] += f * __uint_as_float(v[3] << 16); b2[3] += f * __uint_as_float(v[3] & 0xffff0000u); }
          if (R < 224) { u16* dst = o + ((size_t)((kvh * 2 + hl) * 8 + 5) * NROW + orow) * 32 + 0 + 4 * h;
            u32x2 v0 = {cvtpk(a[0], a[1]), cvtpk(a[2], a[3])}; u32x2 v1 = {cvtpk(b2[0], b2[1]), cvtpk(b2[2], b2[3])};
            *(u32x2*)dst = v0; *(u32x2*)(dst + 8) = v1; } }
        { f32x4 a = acc_read4<88>(), b2 = acc_read4<92>(); const float fo = fw[0][2] * inv[0]; a *= fo; b2 *= fo;
          { u32x4 v = __builtin_amdgcn_raw_buffer_load_b128(rpt, 65536 + w * 8192 + 3072 + lane * 16, 0, 16); const float f = fw[0][0];
            a[0] += f * __uint_as_float(v[0] << 16); a[1] += f * __uint_as_float(v[0] & 0xffff0000u); a[2] += f * __uint_as_float(v[1] << 16); a[3] += f * __uint_as_float(v[1] & 0xffff0000u);
            b2[0] += f * __uint_as_float(v[2] << 16); b2[1] += f * __uint_as_float(v[2] & 0xffff0000u); b2[2] += f * __uint_as_float(v[3] << 16); b2[3] += f * __uint_as_float(v[3] & 0xffff0000u); }
          { u32x4 v = __builtin_amdgcn_raw_buffer_load_b128(rpt, 196608 + w * 8192 + 3072 + lane * 16, 0, 16); const float f = fw[0][1];
            a[0] += f * __uint_as_float(v[0] << 16); a[1] += f * __uint_as_float(v[0] & 0xffff0000u); a[2] += f * __uint_as_float(v[1] << 16); a[3] += f * __uint_as_float(v[1] & 0xffff0000u);
            b2[0] += f * __uint_as_float(v[2] << 16); b2[1] += f * __uint_as_float(v[2] & 0xffff0000u); b2[2] += f * __uint_as_float(v[3] << 16); b2[3] += f * __uint_as_float(v[3] & 0xffff0000u); }
          { u32x4 v = __builtin_amdgcn_raw_buffer_load_b128(rpt, 458752 + w * 8192 + 3072 + lane * 16, 0, 16); const float f = fw[0][3];
            a[0] += f * __uint_as_float(v[0] << 16); a[1] += f * __uint_as_float(v[0] & 0xffff0000u); a[2] += f * __uint_as_float(v[1] << 16); a[3] += f * __uint_as_float(v[1] & 0xffff0000u);
            b2[0] += f * __uint_as_float(v[2] << 16); b2[1] += f * __uint_as_float(v[2] & 0xffff0000u); b2[2] += f * __uint_as_float(v[3] << 16); b2[3] += f * __uint_as_float(v[3] & 0xffff0000u); }
          if (R < 224) { u16* dst = o + ((size_t)((kvh * 2 + hl) * 8 + 5) * NROW + orow) * 32 + 16 + 4 * h;
            u32x2 v0 = {cvtpk(a[0], a[1]), cvtpk(a[2], a[3])}; u32x2 v1 = {cvtpk(b2[0], b2[1]), cvtpk(b2[2], b2[3])};
            *(u32x2*)dst = v0; *(u32x2*)(dst + 8) = v1; } }
      }
      { const int R = 64 * w + 32 + r; const int hl = R >= 112 ? 1 : 0; const int orow = b * SQ + R - 112 * hl;
        { f32x4 a = acc_read4<192>(), b2 = acc_read4<196>(); const float fo = fw[1][2] * inv[1]; a *= fo; b2 *= fo;
          { u32x4 v = __builtin_amdgcn_raw_buffer_load_b128(rpt, 65536 + w * 8192 + 4096 + lane * 16, 0, 16); const float f = fw[1][0];
            a[0] += f * __uint_as_float(v[0] << 16); a[1] += f * __uint_as_float(v[0] & 0xffff0000u); a[2] += f * __uint_as_float(v[1] << 16); a[3] += f * __uint_as_float(v[1] & 0xffff0000u);
            b2[0] += f * __uint_as_float(v[2] << 16); b2[1] += f * __uint_as_float(v[2] & 0xffff0000u); b2[2] += f * __uint_as_float(v[3] << 16); b2[3] += f * __uint_as_float(v[3] & 0xffff0000u); }
          { u32x4 v = __builtin_amdgcn_raw_buffer_load_b128(rpt, 196608 + w * 8192 + 4096 + lane * 16, 0, 16); const float f = fw[1][1];
            a[0] += f * __uint_as_float(v[0] << 16); a[1] += f * __uint_as_float(v[0] & 0xffff0000u); a[2] += f * __uint_as_float(v[1] << 16); a[3] += f * __uint_as_float(v[1] & 0xffff0000u);
            b2[0] += f * __uint_as_float(v[2] << 16); b2[1] += f * __uint_as_float(v[2] & 0xffff0000u); b2[2] += f * __uint_as_float(v[3] << 16); b2[3] += f * __uint_as_float(v[3] & 0xffff0000u); }
          { u32x4 v = __builtin_amdgcn_raw_buffer_load_b128(rpt, 458752 + w * 8192 + 4096 + lane * 16, 0, 16); const float f = fw[1][3];
            a[0] += f * __uint_as_float(v[0] << 16); a[1] += f * __uint_as_float(v[0] & 0xffff0000u); a[2] += f * __uint_as_float(v[1] << 16); a[3] += f * __uint_as_float(v[1] & 0xffff0000u);
            b2[0] += f * __uint_as_float(v[2] << 16); b2[1] += f * __uint_as_float(v[2] & 0xffff0000u); b2[2] += f * __uint_as_float(v[3] << 16); b2[3] += f * __uint_as_float(v[3] & 0xffff0000u); }
          if (R < 224) { u16* dst = o + ((size_t)((kvh * 2 + hl) * 8 + 4) * NROW + orow) * 32 + 0 + 4 * h;
            u32x2 v0 = {cvtpk(a[0], a[1]), cvtpk(a[2], a[3])}; u32x2 v1 = {cvtpk(b2[0], b2[1]), cvtpk(b2[2], b2[3])};
            *(u32x2*)dst = v0; *(u32x2*)(dst + 8) = v1; } }
        { f32x4 a = acc_read4<200>(), b2 = acc_read4<204>(); const float fo = fw[1][2] * inv[1]; a *= fo; b2 *= fo;
          { u32x4 v = __builtin_amdgcn_raw_buffer_load_b128(rpt, 65536 + w * 8192 + 5120 + lane * 16, 0, 16); const float f = fw[1][0];
            a[0] += f * __uint_as_float(v[0] << 16); a[1] += f * __uint_as_float(v[0] & 0xffff0000u); a[2] += f * __uint_as_float(v[1] << 16); a[3] += f * __uint_as_float(v[1] & 0xffff0000u);
            b2[0] += f * __uint_as_float(v[2] << 16); b2[1] += f * __uint_as_float(v[2] & 0xffff0000u); b2[2] += f * __uint_as_float(v[3] << 16); b2[3] += f * __uint_as_float(v[3] & 0xffff0000u); }
          { u32x4 v = __builtin_amdgcn_raw_buffer_load_b128(rpt, 196608 + w * 8192 + 5120 + lane * 16, 0, 16); const float f = fw[1][1];
            a[0] += f * __uint_as_float(v[0] << 16); a[1] += f * __uint_as_float(v[0] & 0xffff0000u); a[2] += f * __uint_as_float(v[1] << 16); a[3] += f * __uint_as_float(v[1] & 0xffff0000u);
            b2[0] += f * __uint_as_float(v[2] << 16); b2[1] += f * __uint_as_float(v[2] & 0xffff0000u); b2[2] += f * __uint_as_float(v[3] << 16); b2[3] += f * __uint_as_float(v[3] & 0xffff0000u); }
          { u32x4 v = __builtin_amdgcn_raw_buffer_load_b128(rpt, 458752 + w * 8192 + 5120 + lane * 16, 0, 16); const float f = fw[1][3];
            a[0] += f * __uint_as_float(v[0] << 16); a[1] += f * __uint_as_float(v[0] & 0xffff0000u); a[2] += f * __uint_as_float(v[1] << 16); a[3] += f * __uint_as_float(v[1] & 0xffff0000u);
            b2[0] += f * __uint_as_float(v[2] << 16); b2[1] += f * __uint_as_float(v[2] & 0xffff0000u); b2[2] += f * __uint_as_float(v[3] << 16); b2[3] += f * __uint_as_float(v[3] & 0xffff0000u); }
          if (R < 224) { u16* dst = o + ((size_t)((kvh * 2 + hl) * 8 + 4) * NROW + orow) * 32 + 16 + 4 * h;
            u32x2 v0 = {cvtpk(a[0], a[1]), cvtpk(a[2], a[3])}; u32x2 v1 = {cvtpk(b2[0], b2[1]), cvtpk(b2[2], b2[3])};
            *(u32x2*)dst = v0; *(u32x2*)(dst + 8) = v1; } }
        { f32x4 a = acc_read4<208>(), b2 = acc_read4<212>(); const float fo = fw[1][2] * inv[1]; a *= fo; b2 *= fo;
          { u32x4 v = __builtin_amdgcn_raw_buffer_load_b128(rpt, 65536 + w * 8192 + 6144 + lane * 16, 0, 16); const float f = fw[1][0];
            a[0] += f * __uint_as_float(v[0] << 16); a[1] += f * __uint_as_float(v[0] & 0xffff0000u); a[2] += f * __uint_as_float(v[1] << 16); a[3] += f * __uint_as_float(v[1] & 0xffff0000u);
            b2[0] += f * __uint_as_float(v[2] << 16); b2[1] += f * __uint_as_float(v[2] & 0xffff0000u); b2[2] += f * __uint_as_float(v[3] << 16); b2[3] += f * __uint_as_float(v[3] & 0xffff0000u); }
          { u32x4 v = __builtin_amdgcn_raw_buffer_load_b128(rpt, 196608 + w * 8192 + 6144 + lane * 16, 0, 16); const float f = fw[1][1];
            a[0] += f * __uint_as_float(v[0] << 16); a[1] += f * __uint_as_float(v[0] & 0xffff0000u); a[2] += f * __uint_as_float(v[1] << 16); a[3] += f * __uint_as_float(v[1] & 0xffff0000u);
            b2[0] += f * __uint_as_float(v[2] << 16); b2[1] += f * __uint_as_float(v[2] & 0xffff0000u); b2[2] += f * __uint_as_float(v[3] << 16); b2[3] += f * __uint_as_float(v[3] & 0xffff0000u); }
          { u32x4 v = __builtin_amdgcn_raw_buffer_load_b128(rpt, 458752 + w * 8192 + 6144 + lane * 16, 0, 16); const float f = fw[1][3];
            a[0] += f * __uint_as_float(v[0] << 16); a[1] += f * __uint_as_float(v[0] & 0xffff0000u); a[2] += f * __uint_as_float(v[1] << 16); a[3] += f * __uint_as_float(v[1] & 0xffff0000u);
            b2[0] += f * __uint_as_float(v[2] << 16); b2[1] += f * __uint_as_float(v[2] & 0xffff0000u); b2[2] += f * __uint_as_float(v[3] << 16); b2[3] += f * __uint_as_float(v[3] & 0xffff0000u); }
          if (R < 224) { u16* dst = o + ((size_t)((kvh * 2 + hl) * 8 + 5) * NROW + orow) * 32 + 0 + 4 * h;
            u32x2 v0 = {cvtpk(a[0], a[1]), cvtpk(a[2], a[3])}; u32x2 v1 = {cvtpk(b2[0], b2[1]), cvtpk(b2[2], b2[3])};
            *(u32x2*)dst = v0; *(u32x2*)(dst + 8) = v1; } }
        { f32x4 a = acc_read4<216>(), b2 = acc_read4<220>(); const float fo = fw[1][2] * inv[1]; a *= fo; b2 *= fo;
          { u32x4 v = __builtin_amdgcn_raw_buffer_load_b128(rpt, 65536 + w * 8192 + 7168 + lane * 16, 0, 16); const float f = fw[1][0];
            a[0] += f * __uint_as_float(v[0] << 16); a[1] += f * __uint_as_float(v[0] & 0xffff0000u); a[2] += f * __uint_as_float(v[1] << 16); a[3] += f * __uint_as_float(v[1] & 0xffff0000u);
            b2[0] += f * __uint_as_float(v[2] << 16); b2[1] += f * __uint_as_float(v[2] & 0xffff0000u); b2[2] += f * __uint_as_float(v[3] << 16); b2[3] += f * __uint_as_float(v[3] & 0xffff0000u); }
          { u32x4 v = __builtin_amdgcn_raw_buffer_load_b128(rpt, 196608 + w * 8192 + 7168 + lane * 16, 0, 16); const float f = fw[1][1];
            a[0] += f * __uint_as_float(v[0] << 16); a[1] += f * __uint_as_float(v[0] & 0xffff0000u); a[2] += f * __uint_as_float(v[1] << 16); a[3] += f * __uint_as_float(v[1] & 0xffff0000u);
            b2[0] += f * __uint_as_float(v[2] << 16); b2[1] += f * __uint_as_float(v[2] & 0xffff0000u); b2[2] += f * __uint_as_float(v[3] << 16); b2[3] += f * __uint_as_float(v[3] & 0xffff0000u); }
          { u32x4 v = __builtin_amdgcn_raw_buffer_load_b128(rpt, 458752 + w * 8192 + 7168 + lane * 16, 0, 16); const float f = fw[1][3];
            a[0] += f * __uint_as_float(v[0] << 16); a[1] += f * __uint_as_float(v[0] & 0xffff0000u); a[2] += f * __uint_as_float(v[1] << 16); a[3] += f * __uint_as_float(v[1] & 0xffff0000u);
            b2[0] += f * __uint_as_float(v[2] << 16); b2[1] += f * __uint_as_float(v[2] & 0xffff0000u); b2[2] += f * __uint_as_float(v[3] << 16); b2[3] += f * __uint_as_float(v[3] & 0xffff0000u); }
          if (R < 224) { u16* dst = o + ((size_t)((kvh * 2 + hl) * 8 + 5) * NROW + orow) * 32 + 16 + 4 * h;
            u32x2 v0 = {cvtpk(a[0], a[1]), cvtpk(a[2], a[3])}; u32x2 v1 = {cvtpk(b2[0], b2[1]), cvtpk(b2[2], b2[3])};
            *(u32x2*)dst = v0; *(u32x2*)(dst + 8) = v1; } }
      }
    } break;
    case 3: {
      { const int R = 64 * w + 0 + r; const int hl = R >= 112 ? 1 : 0; const int orow = b * SQ + R - 112 * hl;
        { f32x4 a = acc_read4<96>(), b2 = acc_read4<100>(); const float fo = fw[0][3] * inv[0]; a *= fo; b2 *= fo;
          { u32x4 v = __builtin_amdgcn_raw_buffer_load_b128(rpt, 98304 + w * 8192 + 0 + lane * 16, 0, 16); const float f = fw[0][0];
            a[0] += f * __uint_as_float(v[0] << 16); a[1] += f * __uint_as_float(v[0] & 0xffff0000u); a[2] += f * __uint_as_float(v[1] << 16); a[3] += f * __uint_as_float(v[1] & 0xffff0000u);
            b2[0] += f * __uint_as_float(v[2] << 16); b2[1] += f * __uint_as_float(v[2] & 0xffff0000u); b2[2] += f * __uint_as_float(v[3] << 16); b2[3] += f * __uint_as_float(v[3] & 0xffff0000u); }
          { u32x4 v = __builtin_amdgcn_raw_buffer_load_b128(rpt, 229376 + w * 8192 + 0 + lane * 16, 0, 16); const float f = fw[0][1];
            a[0] += f * __uint_as_float(v[0] << 16); a[1] += f * __uint_as_float(v[0] & 0xffff0000u); a[2] += f * __uint_as_float(v[1] << 16); a[3] += f * __uint_as_float(v[1] & 0xffff0000u);
            b2[0] += f * __uint_as_float(v[2] << 16); b2[1] += f * __uint_as_float(v[2] & 0xffff0000u); b2[2] += f * __uint_as_float(v[3] << 16); b2[3] += f * __uint_as_float(v[3] & 0xffff0000u); }
          { u32x4 v = __builtin_amdgcn_raw_buffer_load_b128(rpt, 360448 + w * 8192 + 0 + lane * 16, 0, 16); const float f = fw[0][2];
            a[0] += f * __uint_as_float(v[0] << 16); a[1] += f * __uint_as_float(v[0] & 0xffff0000u); a[2] += f * __uint_as_float(v[1] << 16); a[3] += f * __uint_as_float(v[1] & 0xffff0000u);
            b2[0] += f * __uint_as_float(v[2] << 16); b2[1] += f * __uint_as_float(v[2] & 0xffff0000u); b2[2] += f * __uint_as_float(v[3] << 16); b2[3] += f * __uint_as_float(v[3] & 0xffff0000u); }
          if (R < 224) { u16* dst = o + ((size_t)((kvh * 2 + hl) * 8 + 6) * NROW + orow) * 32 + 0 + 4 * h;
            u32x2 v0 = {cvtpk(a[0], a[1]), cvtpk(a[2], a[3])}; u32x2 v1 = {cvtpk(b2[0], b2[1]), cvtpk(b2[2], b2[3])};
            *(u32x2*)dst = v0; *(u32x2*)(dst + 8) = v1; } }
        { f32x4 a = acc_read4<104>(), b2 = acc_read4<108>(); const float fo = fw[0][3] * inv[0]; a *= fo; b2 *= fo;
          { u32x4 v = __builtin_amdgcn_raw_buffer_load_b128(rpt, 98304 + w * 8192 + 1024 + lane * 16, 0, 16); const float f = fw[0][0];
            a[0] += f * __uint_as_float(v[0] << 16); a[1] += f * __uint_as_float(v[0] & 0xffff0000u); a[2] += f * __uint_as_float(v[1] << 16); a[3] += f * __uint_as_float(v[1] & 0xffff0000u);
            b2[0] += f * __uint_as_float(v[2] << 16); b2[1] += f * __uint_as_float(v[2] & 0xffff0000u); b2[2] += f * __uint_as_float(v[3] << 16); b2[3] += f * __uint_as_float(v[3] & 0xffff0000u); }
          { u32x4 v = __builtin_amdgcn_raw_buffer_load_b128(rpt, 229376 + w * 8192 + 1024 + lane * 16, 0, 16); const float f = fw[0][1];
            a[0] += f * __uint_as_float(v[0] << 16); a[1] += f * __uint_as_float(v[0] & 0xffff0000u); a[2] += f * __uint_as_float(v[1] << 16); a[3] += f * __uint_as_float(v[1] & 0xffff0000u);
            b2[0] += f * __uint_as_float(v[2] << 16); b2[1] += f * __uint_as_float(v[2] & 0xffff0000u); b2[2] += f * __uint_as_float(v[3] << 16); b2[3] += f * __uint_as_float(v[3] & 0xffff0000u); }
          { u32x4 v = __builtin_amdgcn_raw_buffer_load_b128(rpt, 360448 + w * 8192 + 1024 + lane * 16, 0, 16); const float f = fw[0][2];
            a[0] += f * __uint_as_float(v[0] << 16); a[1] += f * __uint_as_float(v[0] & 0xffff0000u); a[2] += f * __uint_as_float(v[1] << 16); a[3] += f * __uint_as_float(v[1] & 0xffff0000u);
            b2[0] += f * __uint_as_float(v[2] << 16); b2[1] += f * __uint_as_float(v[2] & 0xffff0000u); b2[2] += f * __uint_as_float(v[3] << 16); b2[3] += f * __uint_as_float(v[3] & 0xffff0000u); }
          if (R < 224) { u16* dst = o + ((size_t)((kvh * 2 + hl) * 8 + 6) * NROW + orow) * 32 + 16 + 4 * h;
            u32x2 v0 = {cvtpk(a[0], a[1]), cvtpk(a[2], a[3])}; u32x2 v1 = {cvtpk(b2[0], b2[1]), cvtpk(b2[2], b2[3])};
            *(u32x2*)dst = v0; *(u32x2*)(dst + 8) = v1; } }
        { f32x4 a = acc_read4<112>(), b2 = acc_read4<116>(); const float fo = fw[0][3] * inv[0]; a *= fo; b2 *= fo;
          { u32x4 v = __builtin_amdgcn_raw_buffer_load_b128(rpt, 98304 + w * 8192 + 2048 + lane * 16, 0, 16); const float f = fw[0][0];
            a[0] += f * __uint_as_float(v[0] << 16); a[1] += f * __uint_as_float(v[0] & 0xffff0000u); a[2] += f * __uint_as_float(v[1] << 16); a[3] += f * __uint_as_float(v[1] & 0xffff0000u);
            b2[0] += f * __uint_as_float(v[2] << 16); b2[1] += f * __uint_as_float(v[2] & 0xffff0000u); b2[2] += f * __uint_as_float(v[3] << 16); b2[3] += f * __uint_as_float(v[3] & 0xffff0000u); }
          { u32x4 v = __builtin_amdgcn_raw_buffer_load_b128(rpt, 229376 + w * 8192 + 2048 + lane * 16, 0, 16); const float f = fw[0][1];
            a[0] += f * __uint_as_float(v[0] << 16); a[1] += f * __uint_as_float(v[0] & 0xffff0000u); a[2] += f * __uint_as_float(v[1] << 16); a[3] += f * __uint_as_float(v[1] & 0xffff0000u);
            b2[0] += f * __uint_as_float(v[2] << 16); b2[1] += f * __uint_as_float(v[2] & 0xffff0000u); b2[2] += f * __uint_as_float(v[3] << 16); b2[3] += f * __uint_as_float(v[3] & 0xffff0000u); }
          { u32x4 v = __builtin_amdgcn_raw_buffer_load_b128(rpt, 360448 + w * 8192 + 2048 + lane * 16, 0, 16); const float f = fw[0][2];
            a[0] += f * __uint_as_float(v[0] << 16); a[1] += f * __uint_as_float(v[0] & 0xffff0000u); a[2] += f * __uint_as_float(v[1] << 16); a[3] += f * __uint_as_float(v[1] & 0xffff0000u);
            b2[0] += f * __uint_as_float(v[2] << 16); b2[1] += f * __uint_as_float(v[2] & 0xffff0000u); b2[2] += f * __uint_as_float(v[3] << 16); b2[3] += f * __uint_as_float(v[3] & 0xffff0000u); }
          if (R < 224) { u16* dst = o + ((size_t)((kvh * 2 + hl) * 8 + 7) * NROW + orow) * 32 + 0 + 4 * h;
            u32x2 v0 = {cvtpk(a[0], a[1]), cvtpk(a[2], a[3])}; u32x2 v1 = {cvtpk(b2[0], b2[1]), cvtpk(b2[2], b2[3])};
            *(u32x2*)dst = v0; *(u32x2*)(dst + 8) = v1; } }
        { f32x4 a = acc_read4<120>(), b2 = acc_read4<124>(); const float fo = fw[0][3] * inv[0]; a *= fo; b2 *= fo;
          { u32x4 v = __builtin_amdgcn_raw_buffer_load_b128(rpt, 98304 + w * 8192 + 3072 + lane * 16, 0, 16); const float f = fw[0][0];
            a[0] += f * __uint_as_float(v[0] << 16); a[1] += f * __uint_as_float(v[0] & 0xffff0000u); a[2] += f * __uint_as_float(v[1] << 16); a[3] += f * __uint_as_float(v[1] & 0xffff0000u);
            b2[0] += f * __uint_as_float(v[2] << 16); b2[1] += f * __uint_as_float(v[2] & 0xffff0000u); b2[2] += f * __uint_as_float(v[3] << 16); b2[3] += f * __uint_as_float(v[3] & 0xffff0000u); }
          { u32x4 v = __builtin_amdgcn_raw_buffer_load_b128(rpt, 229376 + w * 8192 + 3072 + lane * 16, 0, 16); const float f = fw[0][1];
            a[0] += f * __uint_as_float(v[0] << 16); a[1] += f * __uint_as_float(v[0] & 0xffff0000u); a[2] += f * __uint_as_float(v[1] << 16); a[3] += f * __uint_as_float(v[1] & 0xffff0000u);
            b2[0] += f * __uint_as_float(v[2] << 16); b2[1] += f * __uint_as_float(v[2] & 0xffff0000u); b2[2] += f * __uint_as_float(v[3] << 16); b2[3] += f * __uint_as_float(v[3] & 0xffff0000u); }
          { u32x4 v = __builtin_amdgcn_raw_buffer_load_b128(rpt, 360448 + w * 8192 + 3072 + lane * 16, 0, 16); const float f = fw[0][2];
            a[0] += f * __uint_as_float(v[0] << 16); a[1] += f * __uint_as_float(v[0] & 0xffff0000u); a[2] += f * __uint_as_float(v[1] << 16); a[3] += f * __uint_as_float(v[1] & 0xffff0000u);
            b2[0] += f * __uint_as_float(v[2] << 16); b2[1] += f * __uint_as_float(v[2] & 0xffff0000u); b2[2] += f * __uint_as_float(v[3] << 16); b2[3] += f * __uint_as_float(v[3] & 0xffff0000u); }
          if (R < 224) { u16* dst = o + ((size_t)((kvh * 2 + hl) * 8 + 7) * NROW + orow) * 32 + 16 + 4 * h;
            u32x2 v0 = {cvtpk(a[0], a[1]), cvtpk(a[2], a[3])}; u32x2 v1 = {cvtpk(b2[0], b2[1]), cvtpk(b2[2], b2[3])};
            *(u32x2*)dst = v0; *(u32x2*)(dst + 8) = v1; } }
      }
      { const int R = 64 * w + 32 + r; const int hl = R >= 112 ? 1 : 0; const int orow = b * SQ + R - 112 * hl;
        { f32x4 a = acc_read4<224>(), b2 = acc_read4<228>(); const float fo = fw[1][3] * inv[1]; a *= fo; b2 *= fo;
          { u32x4 v = __builtin_amdgcn_raw_buffer_load_b128(rpt, 98304 + w * 8192 + 4096 + lane * 16, 0, 16); const float f = fw[1][0];
            a[0] += f * __uint_as_float(v[0] << 16); a[1] += f * __uint_as_float(v[0] & 0xffff0000u); a[2] += f * __uint_as_float(v[1] << 16); a[3] += f * __uint_as_float(v[1] & 0xffff0000u);
            b2[0] += f * __uint_as_float(v[2] << 16); b2[1] += f * __uint_as_float(v[2] & 0xffff0000u); b2[2] += f * __uint_as_float(v[3] << 16); b2[3] += f * __uint_as_float(v[3] & 0xffff0000u); }
          { u32x4 v = __builtin_amdgcn_raw_buffer_load_b128(rpt, 229376 + w * 8192 + 4096 + lane * 16, 0, 16); const float f = fw[1][1];
            a[0] += f * __uint_as_float(v[0] << 16); a[1] += f * __uint_as_float(v[0] & 0xffff0000u); a[2] += f * __uint_as_float(v[1] << 16); a[3] += f * __uint_as_float(v[1] & 0xffff0000u);
            b2[0] += f * __uint_as_float(v[2] << 16); b2[1] += f * __uint_as_float(v[2] & 0xffff0000u); b2[2] += f * __uint_as_float(v[3] << 16); b2[3] += f * __uint_as_float(v[3] & 0xffff0000u); }
          { u32x4 v = __builtin_amdgcn_raw_buffer_load_b128(rpt, 360448 + w * 8192 + 4096 + lane * 16, 0, 16); const float f = fw[1][2];
            a[0] += f * __uint_as_float(v[0] << 16); a[1] += f * __uint_as_float(v[0] & 0xffff0000u); a[2] += f * __uint_as_float(v[1] << 16); a[3] += f * __uint_as_float(v[1] & 0xffff0000u);
            b2[0] += f * __uint_as_float(v[2] << 16); b2[1] += f * __uint_as_float(v[2] & 0xffff0000u); b2[2] += f * __uint_as_float(v[3] << 16); b2[3] += f * __uint_as_float(v[3] & 0xffff0000u); }
          if (R < 224) { u16* dst = o + ((size_t)((kvh * 2 + hl) * 8 + 6) * NROW + orow) * 32 + 0 + 4 * h;
            u32x2 v0 = {cvtpk(a[0], a[1]), cvtpk(a[2], a[3])}; u32x2 v1 = {cvtpk(b2[0], b2[1]), cvtpk(b2[2], b2[3])};
            *(u32x2*)dst = v0; *(u32x2*)(dst + 8) = v1; } }
        { f32x4 a = acc_read4<232>(), b2 = acc_read4<236>(); const float fo = fw[1][3] * inv[1]; a *= fo; b2 *= fo;
          { u32x4 v = __builtin_amdgcn_raw_buffer_load_b128(rpt, 98304 + w * 8192 + 5120 + lane * 16, 0, 16); const float f = fw[1][0];
            a[0] += f * __uint_as_float(v[0] << 16); a[1] += f * __uint_as_float(v[0] & 0xffff0000u); a[2] += f * __uint_as_float(v[1] << 16); a[3] += f * __uint_as_float(v[1] & 0xffff0000u);
            b2[0] += f * __uint_as_float(v[2] << 16); b2[1] += f * __uint_as_float(v[2] & 0xffff0000u); b2[2] += f * __uint_as_float(v[3] << 16); b2[3] += f * __uint_as_float(v[3] & 0xffff0000u); }
          { u32x4 v = __builtin_amdgcn_raw_buffer_load_b128(rpt, 229376 + w * 8192 + 5120 + lane * 16, 0, 16); const float f = fw[1][1];
            a[0] += f * __uint_as_float(v[0] << 16); a[1] += f * __uint_as_float(v[0] & 0xffff0000u); a[2] += f * __uint_as_float(v[1] << 16); a[3] += f * __uint_as_float(v[1] & 0xffff0000u);
            b2[0] += f * __uint_as_float(v[2] << 16); b2[1] += f * __uint_as_float(v[2] & 0xffff0000u); b2[2] += f * __uint_as_float(v[3] << 16); b2[3] += f * __uint_as_float(v[3] & 0xffff0000u); }
          { u32x4 v = __builtin_amdgcn_raw_buffer_load_b128(rpt, 360448 + w * 8192 + 5120 + lane * 16, 0, 16); const float f = fw[1][2];
            a[0] += f * __uint_as_float(v[0] << 16); a[1] += f * __uint_as_float(v[0] & 0xffff0000u); a[2] += f * __uint_as_float(v[1] << 16); a[3] += f * __uint_as_float(v[1] & 0xffff0000u);
            b2[0] += f * __uint_as_float(v[2] << 16); b2[1] += f * __uint_as_float(v[2] & 0xffff0000u); b2[2] += f * __uint_as_float(v[3] << 16); b2[3] += f * __uint_as_float(v[3] & 0xffff0000u); }
          if (R < 224) { u16* dst = o + ((size_t)((kvh * 2 + hl) * 8 + 6) * NROW + orow) * 32 + 16 + 4 * h;
            u32x2 v0 = {cvtpk(a[0], a[1]), cvtpk(a[2], a[3])}; u32x2 v1 = {cvtpk(b2[0], b2[1]), cvtpk(b2[2], b2[3])};
            *(u32x2*)dst = v0; *(u32x2*)(dst + 8) = v1; } }
        { f32x4 a = acc_read4<240>(), b2 = acc_read4<244>(); const float fo = fw[1][3] * inv[1]; a *= fo; b2 *= fo;
          { u32x4 v = __builtin_amdgcn_raw_buffer_load_b128(rpt, 98304 + w * 8192 + 6144 + lane * 16, 0, 16); const float f = fw[1][0];
            a[0] += f * __uint_as_float(v[0] << 16); a[1] += f * __uint_as_float(v[0] & 0xffff0000u); a[2] += f * __uint_as_float(v[1] << 16); a[3] += f * __uint_as_float(v[1] & 0xffff0000u);
            b2[0] += f * __uint_as_float(v[2] << 16); b2[1] += f * __uint_as_float(v[2] & 0xffff0000u); b2[2] += f * __uint_as_float(v[3] << 16); b2[3] += f * __uint_as_float(v[3] & 0xffff0000u); }
          { u32x4 v = __builtin_amdgcn_raw_buffer_load_b128(rpt, 229376 + w * 8192 + 6144 + lane * 16, 0, 16); const float f = fw[1][1];
            a[0] += f * __uint_as_float(v[0] << 16); a[1] += f * __uint_as_float(v[0] & 0xffff0000u); a[2] += f * __uint_as_float(v[1] << 16); a[3] += f * __uint_as_float(v[1] & 0xffff0000u);
            b2[0] += f * __uint_as_float(v[2] << 16); b2[1] += f * __uint_as_float(v[2] & 0xffff0000u); b2[2] += f * __uint_as_float(v[3] << 16); b2[3] += f * __uint_as_float(v[3] & 0xffff0000u); }
          { u32x4 v = __builtin_amdgcn_raw_buffer_load_b128(rpt, 360448 + w * 8192 + 6144 + lane * 16, 0, 16); const float f = fw[1][2];
            a[0] += f * __uint_as_float(v[0] << 16); a[1] += f * __uint_as_float(v[0] & 0xffff0000u); a[2] += f * __uint_as_float(v[1] << 16); a[3] += f * __uint_as_float(v[1] & 0xffff0000u);
            b2[0] += f * __uint_as_float(v[2] << 16); b2[1] += f * __uint_as_float(v[2] & 0xffff0000u); b2[2] += f * __uint_as_float(v[3] << 16); b2[3] += f * __uint_as_float(v[3] & 0xffff0000u); }
          if (R < 224) { u16* dst = o + ((size_t)((kvh * 2 + hl) * 8 + 7) * NROW + orow) * 32 + 0 + 4 * h;
            u32x2 v0 = {cvtpk(a[0], a[1]), cvtpk(a[2], a[3])}; u32x2 v1 = {cvtpk(b2[0], b2[1]), cvtpk(b2[2], b2[3])};
            *(u32x2*)dst = v0; *(u32x2*)(dst + 8) = v1; } }
        { f32x4 a = acc_read4<248>(), b2 = acc_read4<252>(); const float fo = fw[1][3] * inv[1]; a *= fo; b2 *= fo;
          { u32x4 v = __builtin_amdgcn_raw_buffer_load_b128(rpt, 98304 + w * 8192 + 7168 + lane * 16, 0, 16); const float f = fw[1][0];
            a[0] += f * __uint_as_float(v[0] << 16); a[1] += f * __uint_as_float(v[0] & 0xffff0000u); a[2] += f * __uint_as_float(v[1] << 16); a[3] += f * __uint_as_float(v[1] & 0xffff0000u);
            b2[0] += f * __uint_as_float(v[2] << 16); b2[1] += f * __uint_as_float(v[2] & 0xffff0000u); b2[2] += f * __uint_as_float(v[3] << 16); b2[3] += f * __uint_as_float(v[3] & 0xffff0000u); }
          { u32x4 v = __builtin_amdgcn_raw_buffer_load_b128(rpt, 229376 + w * 8192 + 7168 + lane * 16, 0, 16); const float f = fw[1][1];
            a[0] += f * __uint_as_float(v[0] << 16); a[1] += f * __uint_as_float(v[0] & 0xffff0000u); a[2] += f * __uint_as_float(v[1] << 16); a[3] += f * __uint_as_float(v[1] & 0xffff0000u);
            b2[0] += f * __uint_as_float(v[2] << 16); b2[1] += f * __uint_as_float(v[2] & 0xffff0000u); b2[2] += f * __uint_as_float(v[3] << 16); b2[3] += f * __uint_as_float(v[3] & 0xffff0000u); }
          { u32x4 v = __builtin_amdgcn_raw_buffer_load_b128(rpt, 360448 + w * 8192 + 7168 + lane * 16, 0, 16); const float f = fw[1][2];
            a[0] += f * __uint_as_float(v[0] << 16); a[1] += f * __uint_as_float(v[0] & 0xffff0000u); a[2] += f * __uint_as_float(v[1] << 16); a[3] += f * __uint_as_float(v[1] & 0xffff0000u);
            b2[0] += f * __uint_as_float(v[2] << 16); b2[1] += f * __uint_as_float(v[2] & 0xffff0000u); b2[2] += f * __uint_as_float(v[3] << 16); b2[3] += f * __uint_as_float(v[3] & 0xffff0000u); }
          if (R < 224) { u16* dst = o + ((size_t)((kvh * 2 + hl) * 8 + 7) * NROW + orow) * 32 + 16 + 4 * h;
            u32x2 v0 = {cvtpk(a[0], a[1]), cvtpk(a[2], a[3])}; u32x2 v1 = {cvtpk(b2[0], b2[1]), cvtpk(b2[2], b2[3])};
            *(u32x2*)dst = v0; *(u32x2*)(dst + 8) = v1; } }
      }
    } break;
  }
  STAMP(10);
}
__global__ __launch_bounds__(512, 2) void oproj2_kernel(const u16* __restrict__ o, const u16* __restrict__ wo,
                                                        u16* __restrict__ out) {
  extern __shared__ __attribute__((aligned(16))) char lds[];
  const int tid = threadIdx.x, lane = tid & 63;
  const int w = __builtin_amdgcn_readfirstlane(tid >> 6);
  const int r = lane & 31, h = lane >> 5;
  const int xcd = blockIdx.x & 7, idx = blockIdx.x >> 3;
  const int rt = xcd + 8 * (idx / 7), ct = idx % 7;
  if (rt >= 28) return;
  const int col0 = ct * 64;
  char* ring = lds + w * 18432;
  __amdgpu_buffer_rsrc_t rw = __builtin_amdgcn_make_buffer_rsrc((void*)(wo + (size_t)(512 * w) * DM + col0), 0,
                                                                 (512 * DM - col0) * 2, 0x00020000);
  __amdgpu_buffer_rsrc_t ra = __builtin_amdgcn_make_buffer_rsrc((void*)(o + ((size_t)(16 * w) * NROW + rt * 32) * 32), 0,
                                                                 (16 * NROW - rt * 32) * 64, 0x00020000);
  const int drow = lane >> 3;
  const int wvoff = drow * (DM * 2) + (((lane & 7) ^ (((drow >> 1) & 1) << 2)) * 16);
  const int arow = lane >> 2;
  const int avoff = arow * 64 + (((lane & 3) ^ ((arow >> 2) & 3)) * 16);
  auto issue = [&](int u) {
    char* dst = ring + (u % 3) * 6144;
#pragma unroll
    for (int p = 0; p < 4; ++p)
      __builtin_amdgcn_raw_ptr_buffer_load_lds(rw, (lds_vp)(dst + p * 1024), 16, wvoff, (32 * u + 8 * p) * (DM * 2), 0, 0);
#pragma unroll
    for (int p = 0; p < 2; ++p)
      __builtin_amdgcn_raw_ptr_buffer_load_lds(ra, (lds_vp)(dst + 4096 + p * 1024), 16, avoff, 1024 * p + u * (NROW * 64), 0, 0);
  };
  issue(0); issue(1); issue(2);
  const int g = lane >> 4, qq = (lane >> 2) & 3, pp = lane & 3;
  const unsigned rb = (unsigned)(size_t)ring;
  const unsigned trb = rb + (8 * (g >> 1) + qq) * 128 + ((32 * (g & 1) + 8 * pp) ^ ((qq >> 1) << 6));
  const unsigned ab0 = rb + 4096 + r * 64 + (((0 + h) ^ ((r >> 2) & 3)) * 16);
  const unsigned ab1 = rb + 4096 + r * 64 + (((2 + h) ^ ((r >> 2) & 3)) * 16);
  f32x16 acc[2];
#pragma unroll
  for (int b2 = 0; b2 < 2; ++b2)
#pragma unroll
    for (int i = 0; i < 16; ++i) acc[b2][i] = 0.f;
#pragma unroll
  for (int u = 0; u < 16; ++u) {
    if (u <= 13) WAITV(12); else if (u == 14) WAITV(6); else WAITV(0);
    const unsigned so = (u % 3) * 6144;
    s16x4 a[2][4];
    bf16x8 xb[2];
#pragma unroll
    for (int s = 0; s < 2; ++s) {
      a[s][0] = trread(trb + so + s * 2048);
      a[s][1] = trread(trb + so + s * 2048 + 512);
      a[s][2] = trread((trb + so + s * 2048) ^ 64);
      a[s][3] = trread((trb + so + s * 2048 + 512) ^ 64);
    }
    asm volatile("ds_read_b128 %0, %1" : "=v"(xb[0]) : "v"(ab0 + so) : "memory");
    asm volatile("ds_read_b128 %0, %1" : "=v"(xb[1]) : "v"(ab1 + so) : "memory");
    asm volatile("s_waitcnt lgkmcnt(0)"
                 : "+v"(a[0][0]), "+v"(a[0][1]), "+v"(a[0][2]), "+v"(a[0][3]), "+v"(a[1][0]), "+v"(a[1][1]),
                   "+v"(a[1][2]), "+v"(a[1][3]), "+v"(xb[0]), "+v"(xb[1])::"memory");
    if (u + 3 < 16) issue(u + 3);
#pragma unroll
    for (int s = 0; s < 2; ++s) {
      acc[0] = __builtin_amdgcn_mfma_f32_32x32x16_bf16(cat8(a[s][0], a[s][1]), xb[s], acc[0], 0, 0, 0);
      acc[1] = __builtin_amdgcn_mfma_f32_32x32x16_bf16(cat8(a[s][2], a[s][3]), xb[s], acc[1], 0, 0, 0);
    }
  }
  __syncthreads();
  float* red = (float*)lds;
#pragma unroll
  for (int cb = 0; cb < 2; ++cb)
#pragma unroll
    for (int gg = 0; gg < 4; ++gg) {
      f32x4 v = {acc[cb][4 * gg], acc[cb][4 * gg + 1], acc[cb][4 * gg + 2], acc[cb][4 * gg + 3]};
      *(f32x4*)(red + ((w * 8 + cb * 4 + gg) * 64 + lane) * 4) = v;
    }
  __syncthreads();
  {
    const int cg = w, cb = cg >> 2, gg = cg & 3;
    f32x4 s = {0.f, 0.f, 0.f, 0.f};
#pragma unroll
    for (int ww = 0; ww < 8; ++ww) s += *(const f32x4*)(red + ((ww * 8 + cg) * 64 + lane) * 4);
    u32x2 v = {cvtpk(s[0], s[1]), cvtpk(s[2], s[3])};
    *(u32x2*)(out + (size_t)(rt * 32 + r) * DM + col0 + 32 * cb + 8 * gg + 4 * h) = v;
  }
}
__global__ __launch_bounds__(512, 2) void oproj3_kernel(const u16* __restrict__ o, const u16* __restrict__ wo,
                                                        u16* __restrict__ out, float* __restrict__ slab,
                                                        unsigned* __restrict__ cnt DBGARG) {
  extern __shared__ __attribute__((aligned(16))) char lds[];
  STAMP(0);
  const int tid = threadIdx.x, lane = tid & 63;
  const int w = __builtin_amdgcn_readfirstlane(tid >> 6);
  const int r = lane & 31, h = lane >> 5;
  const int xcd = blockIdx.x & 7, idx = blockIdx.x >> 3;
  const int tile = (idx >> 1) * 8 + xcd, kh = idx & 1;
  if (tile >= 98) return;
  const int rt = tile / 7, ct = tile % 7;
  const int col0 = ct * 64, k0 = kh * 2048 + 256 * w;
  char* ring = lds + w * 16384;
  __amdgpu_buffer_rsrc_t rw = __builtin_amdgcn_make_buffer_rsrc((void*)(wo + (size_t)k0 * DM + col0), 0,
                                                                 (256 * DM - col0) * 2, 0x00020000);
  __amdgpu_buffer_rsrc_t ra = __builtin_amdgcn_make_buffer_rsrc((void*)(o + (size_t)(rt * 64) * NQ + k0), 0,
                                                                 (64 * NQ - k0) * 2, 0x00020000);
  const int drow = lane >> 3;
  const int wvoff = drow * (DM * 2) + (((lane & 7) ^ (((drow >> 1) & 1) << 2)) * 16);
  const int arow = lane >> 2;
  const int avoff = arow * (NQ * 2) + (((lane & 3) ^ ((arow >> 2) & 3)) * 16);
  auto issue = [&](int u) {
    char* dst = ring + (u & 1) * 8192;
#pragma unroll
    for (int p = 0; p < 4; ++p)
      __builtin_amdgcn_raw_ptr_buffer_load_lds(rw, (lds_vp)(dst + p * 1024), 16, wvoff, (32 * u + 8 * p) * (DM * 2), 0, 0);
#pragma unroll
    for (int p = 0; p < 4; ++p)
      __builtin_amdgcn_raw_ptr_buffer_load_lds(ra, (lds_vp)(dst + 4096 + p * 1024), 16, avoff, 16 * p * (NQ * 2) + 64 * u, 0, 0);
  };
  issue(0); issue(1);
  const int g = lane >> 4, qq = (lane >> 2) & 3, pp = lane & 3;
  const unsigned rb = (unsigned)(size_t)ring;
  const unsigned trb = rb + (8 * (g >> 1) + qq) * 128 + ((32 * (g & 1) + 8 * pp) ^ ((qq >> 1) << 6));
  const unsigned ab0 = rb + 4096 + r * 64 + (((0 + h) ^ ((r >> 2) & 3)) * 16);
  const unsigned ab1 = rb + 4096 + r * 64 + (((2 + h) ^ ((r >> 2) & 3)) * 16);
  f32x16 acc[2][2];
#pragma unroll
  for (int a2 = 0; a2 < 2; ++a2)
#pragma unroll
    for (int b2 = 0; b2 < 2; ++b2)
#pragma unroll
      for (int i = 0; i < 16; ++i) acc[a2][b2][i] = 0.f;
#pragma unroll
  for (int u = 0; u < 8; ++u) {
    if (u <= 6) WAITV(8); else WAITV(0);
    const unsigned so = (u & 1) * 8192;
    s16x4 a[2][4];
    bf16x8 xb[2][2];
#pragma unroll
    for (int s = 0; s < 2; ++s) {
      a[s][0] = trread(trb + so + s * 2048);
      a[s][1] = trread(trb + so + s * 2048 + 512);
      a[s][2] = trread((trb + so + s * 2048) ^ 64);
      a[s][3] = trread((trb + so + s * 2048 + 512) ^ 64);
    }
    asm volatile("ds_read_b128 %0, %1" : "=v"(xb[0][0]) : "v"(ab0 + so) : "memory");
    asm volatile("ds_read_b128 %0, %1" : "=v"(xb[1][0]) : "v"(ab1 + so) : "memory");
    asm volatile("ds_read_b128 %0, %1 offset:2048" : "=v"(xb[0][1]) : "v"(ab0 + so) : "memory");
    asm volatile("ds_read_b128 %0, %1 offset:2048" : "=v"(xb[1][1]) : "v"(ab1 + so) : "memory");
    asm volatile("s_waitcnt lgkmcnt(0)"
                 : "+v"(a[0][0]), "+v"(a[0][1]), "+v"(a[0][2]), "+v"(a[0][3]), "+v"(a[1][0]), "+v"(a[1][1]),
                   "+v"(a[1][2]), "+v"(a[1][3]), "+v"(xb[0][0]), "+v"(xb[1][0]), "+v"(xb[0][1]), "+v"(xb[1][1])::"memory");
    if (u + 2 < 8) issue(u + 2);
#pragma unroll
    for (int s = 0; s < 2; ++s)
#pragma unroll
      for (int rb2 = 0; rb2 < 2; ++rb2) {
        acc[rb2][0] = __builtin_amdgcn_mfma_f32_32x32x16_bf16(cat8(a[s][0], a[s][1]), xb[s][rb2], acc[rb2][0], 0, 0, 0);
        acc[rb2][1] = __builtin_amdgcn_mfma_f32_32x32x16_bf16(cat8(a[s][2], a[s][3]), xb[s][rb2], acc[rb2][1], 0, 0, 0);
      }
  }
  STAMP(1);
  __syncthreads();
  float* red = (float*)lds;
#pragma unroll
  for (int rb2 = 0; rb2 < 2; ++rb2)
#pragma unroll
    for (int cb = 0; cb < 2; ++cb)
#pragma unroll
      for (int gg = 0; gg < 4; ++gg) {
        f32x4 v = {acc[rb2][cb][4 * gg], acc[rb2][cb][4 * gg + 1], acc[rb2][cb][4 * gg + 2], acc[rb2][cb][4 * gg + 3]};
        *(f32x4*)(red + ((w * 16 + rb2 * 8 + cb * 4 + gg) * 64 + lane) * 4) = v;
      }
  __syncthreads();
  f32x4 sum[2];
#pragma unroll
  for (int jj = 0; jj < 2; ++jj) {
    const int grp = 2 * w + jj;
    f32x4 s = {0.f, 0.f, 0.f, 0.f};
#pragma unroll
    for (int ww = 0; ww < 8; ++ww) s += *(const f32x4*)(red + ((ww * 16 + grp) * 64 + lane) * 4);
    sum[jj] = s;
  }
  STAMP(2);
  __amdgpu_buffer_rsrc_t rs = __builtin_amdgcn_make_buffer_rsrc((void*)(slab + (size_t)tile * 2 * 4096), 0, 2 * 4096 * 4, 0x00020000);
#pragma unroll
  for (int jj = 0; jj < 2; ++jj)
    __builtin_amdgcn_raw_buffer_store_b128(__builtin_bit_cast(u32x4, sum[jj]), rs, (kh * 16 + 2 * w + jj) * 1024 + lane * 16, 0, 16);
  asm volatile("s_waitcnt vmcnt(0)" ::: "memory");
  __syncthreads();
  unsigned* flag = (unsigned*)(lds + 131072);
  if (tid == 0) *flag = __hip_atomic_fetch_add(cnt + tile, 1u, __ATOMIC_RELAXED, __HIP_MEMORY_SCOPE_AGENT);
  __syncthreads();
  STAMP(3);
  if (*flag == 1u) {
#pragma unroll
    for (int jj = 0; jj < 2; ++jj) {
      u32x4 pv = __builtin_amdgcn_raw_buffer_load_b128(rs, ((kh ^ 1) * 16 + 2 * w + jj) * 1024 + lane * 16, 0, 16);
      f32x4 t = sum[jj] + __builtin_bit_cast(f32x4, pv);
      const int grp = 2 * w + jj, rb2 = grp >> 3, cb = (grp >> 2) & 1, gg = grp & 3;
      u32x2 v = {cvtpk(t[0], t[1]), cvtpk(t[2], t[3])};
      *(u32x2*)(out + (size_t)(rt * 64 + 32 * rb2 + r) * DM + col0 + 32 * cb + 8 * gg + 4 * h) = v;
    }
  }
  STAMP(4);
}
extern "C" void kernel_launch(void* const* d_in, const int* in_sizes, int n_in, void* d_out, int out_size,
                              void* d_ws, size_t ws_size, hipStream_t stream) {
  const u16* x = (const u16*)d_in[0];
  const u16* kc = (const u16*)d_in[1];
  const u16* vc = (const u16*)d_in[2];
  const u16* wq = (const u16*)d_in[3];
  const u16* wo = (const u16*)d_in[4];
  u16* out = (u16*)d_out;
  u16* qws = (u16*)d_ws;
  u16* ows = (u16*)((char*)d_ws + (8  __H0__ ));
  float* slab = (float*)((char*)d_ws + (16 << 20));
  unsigned* cnt = (unsigned*)((char*)d_ws + (24 << 20));
  (void)hipFuncSetAttribute((const void*)attn4_kernel, hipFuncAttributeMaxDynamicSharedMemorySize, 163840);
  hipLaunchKernelGGL(attn4_kernel, dim3(256), dim3(256), 163840, stream, x, wq, kc, vc, wo, ows, cnt
  );
  (void)hipFuncSetAttribute((const void*)oproj2_kernel, hipFuncAttributeMaxDynamicSharedMemorySize, 147456);
  hipLaunchKernelGGL(oproj2_kernel, dim3(8 * 28), dim3(512), 147456, stream, (const u16*)ows, wo, out);
  return;
  (void)hipFuncSetAttribute((const void*)oproj3_kernel, hipFuncAttributeMaxDynamicSharedMemorySize, 131328);
  hipLaunchKernelGGL(oproj3_kernel, dim3(8 * 26), dim3(512), 131328, stream, (const u16*)ows, wo, out, slab, cnt
  );
}
```

```cpp
#include <hip/hip_runtime.h>
#include <hip/hip_bf16.h>
#include <utility>

typedef unsigned short u16;
typedef __attribute__((ext_vector_type(8))) short bf16x8;
typedef __attribute__((ext_vector_type(4))) short s16x4;
typedef __attribute__((ext_vector_type(16))) float f32x16;
typedef __attribute__((ext_vector_type(4))) float f32x4;
typedef __attribute__((ext_vector_type(2))) float f32x2;
typedef __attribute__((ext_vector_type(2))) __bf16 bf16x2v;
typedef __attribute__((ext_vector_type(2))) unsigned u32x2;
typedef __attribute__((ext_vector_type(4))) unsigned u32x4;
#define DI __device__ __forceinline__
#define LDSP(p) ((__attribute__((address_space(3))) unsigned*)(p))

constexpr int NB = 8, SQ = 112, DM = 448, HQ = 16, HKV = 8, SK = 1024, DH = 256;
constexpr int NROW = NB * SQ;
constexpr int NQ = HQ * DH;

DI unsigned cvtpk(float lo, float hi) {
  f32x2 v = {lo, hi};
  bf16x2v b = __builtin_convertvector(v, bf16x2v);
  return __builtin_bit_cast(unsigned, b);
}
DI void glds16(const void* g, void* l) {
  __builtin_amdgcn_global_load_lds((const unsigned*)g, LDSP(l), 16, 0, 0);
}
#define WAITV(n) asm volatile("s_waitcnt vmcnt(" #n ")" ::: "memory")
#define WAITL0() asm volatile("s_waitcnt lgkmcnt(0)" ::: "memory")

DI s16x4 trread(unsigned addr) {
  s16x4 r;
  asm volatile("ds_read_b64_tr_b16 %0, %1" : "=v"(r) : "v"(addr) : "memory");
  return r;
}
template <int OFF> DI s16x4 trread_o(unsigned addr) {
  s16x4 r;
  asm volatile("ds_read_b64_tr_b16 %0, %1 offset:%2" : "=v"(r) : "v"(addr), "i"(OFF) : "memory");
  return r;
}
DI bf16x8 cat8(s16x4 a, s16x4 b) { return __builtin_shufflevector(a, b, 0, 1, 2, 3, 4, 5, 6, 7); }

template <int RB, int NCH>
DI void wave_gemm(const u16* __restrict__ act, int lda, const int (&arow)[RB], const u16* __restrict__ W, int ldw,
                  char* wl, f32x16 (&acc)[RB][2], int lane) {
  const int r = lane & 31, h = lane >> 5;
  const int g = lane >> 4, q = (lane >> 2) & 3, pp = lane & 3;
  const unsigned ldsb = (unsigned)(size_t)wl;
  const unsigned trb = ldsb + (8 * (g >> 1) + q) * 128 + ((32 * (g & 1) + 8 * pp) ^ ((q >> 1) << 6));
  const int drow = lane >> 3;
  const int dchk = (lane & 7) ^ (((drow >> 1) & 1) << 2);
  const u16* wsrc = W + (size_t)drow * ldw + dchk * 8;
  auto issue = [&](int c, int buf) {
#pragma unroll
    for (int p = 0; p < 8; ++p) glds16(wsrc + (size_t)(64 * c + 8 * p) * ldw, wl + buf * 8192 + p * 1024);
  };
  issue(0, 0);
#pragma unroll
  for (int c = 0; c < NCH; ++c) {
    const int buf = c & 1;
    bf16x8 bfr[4][RB];
#pragma unroll
    for (int s = 0; s < 4; ++s)
#pragma unroll
      for (int rb = 0; rb < RB; ++rb)
        bfr[s][rb] = *(const bf16x8*)(act + (size_t)arow[rb] * lda + 64 * c + 16 * s + 8 * h);
    WAITV(0);
    if (c + 1 < NCH) issue(c + 1, buf ^ 1);
#pragma unroll
    for (int s = 0; s < 4; ++s) {
      s16x4 a00 = trread(trb + buf * 8192 + s * 2048 + 0);
      s16x4 a01 = trread(trb + buf * 8192 + s * 2048 + 512);
      s16x4 a10 = trread((trb + buf * 8192 + s * 2048 + 0) ^ 64);
      s16x4 a11 = trread((trb + buf * 8192 + s * 2048 + 512) ^ 64);
      asm volatile("s_waitcnt lgkmcnt(0)" : "+v"(a00), "+v"(a01), "+v"(a10), "+v"(a11)::"memory");
      bf16x8 A0 = cat8(a00, a01), A1 = cat8(a10, a11);
#pragma unroll
      for (int rb = 0; rb < RB; ++rb) {
        acc[rb][0] = __builtin_amdgcn_mfma_f32_32x32x16_bf16(A0, bfr[s][rb], acc[rb][0], 0, 0, 0);
        acc[rb][1] = __builtin_amdgcn_mfma_f32_32x32x16_bf16(A1, bfr[s][rb], acc[rb][1], 0, 0, 0);
      }
    }
  }
  (void)r;
}

__global__ __launch_bounds__(256) void qproj_kernel(const u16* __restrict__ x, const u16* __restrict__ wq,
                                                    u16* __restrict__ qout) {
  extern __shared__ __attribute__((aligned(16))) char lds[];
  const int tid = threadIdx.x, lane = tid & 63;
  const int w = __builtin_amdgcn_readfirstlane(tid >> 6);
  const int r = lane & 31, h = lane >> 5;
  const int rt = blockIdx.x % 14, head = blockIdx.x / 14;
  const int col0 = head * 256 + 64 * w;
  f32x16 acc[2][2];
#pragma unroll
  for (int a = 0; a < 2; ++a)
#pragma unroll
    for (int b = 0; b < 2; ++b)
#pragma unroll
      for (int i = 0; i < 16; ++i) acc[a][b][i] = 0.f;
  const int arow[2] = {rt * 64 + r, rt * 64 + 32 + r};
  wave_gemm<2, 7>(x, DM, arow, wq + col0, NQ, lds + w * 16384, acc, lane);
#pragma unroll
  for (int rb = 0; rb < 2; ++rb)
#pragma unroll
    for (int cb = 0; cb < 2; ++cb)
#pragma unroll
      for (int g = 0; g < 4; ++g) {
        u32x2 v = {cvtpk(acc[rb][cb][4 * g + 0], acc[rb][cb][4 * g + 1]),
                   cvtpk(acc[rb][cb][4 * g + 2], acc[rb][cb][4 * g + 3])};
        *(u32x2*)(qout + (size_t)arow[rb] * NQ + col0 + 32 * cb + 8 * g + 4 * h) = v;
      }
}

__global__ __launch_bounds__(256) void oproj_kernel(const u16* __restrict__ o, const u16* __restrict__ wo,
                                                    u16* __restrict__ out) {
  extern __shared__ __attribute__((aligned(16))) char lds[];
  const int tid = threadIdx.x, lane = tid & 63;
  const int w = __builtin_amdgcn_readfirstlane(tid >> 6);
  const int r = lane & 31, h = lane >> 5;
  const int rt = blockIdx.x % 28, ct = blockIdx.x / 28;
  const int col0 = ct * 64;
  f32x16 acc[1][2];
#pragma unroll
  for (int b = 0; b < 2; ++b)
#pragma unroll
    for (int i = 0; i < 16; ++i) acc[0][b][i] = 0.f;
  const int arow[1] = {rt * 32 + r};
  wave_gemm<1, 16>(o + 1024 * w, NQ, arow, wo + (size_t)(1024 * w) * DM + col0, DM, lds + w * 16384, acc, lane);
  __syncthreads();
  float* red = (float*)lds;
#pragma unroll
  for (int cb = 0; cb < 2; ++cb)
#pragma unroll
    for (int g = 0; g < 4; ++g) {
      f32x4 v = {acc[0][cb][4 * g], acc[0][cb][4 * g + 1], acc[0][cb][4 * g + 2], acc[0][cb][4 * g + 3]};
      *(f32x4*)(red + ((w * 8 + cb * 4 + g) * 64 + lane) * 4) = v;
    }
  __syncthreads();
#pragma unroll
  for (int j = 0; j < 2; ++j) {
    const int cg = 2 * w + j, cb = cg >> 2, g = cg & 3;
    f32x4 s = {0.f, 0.f, 0.f, 0.f};
#pragma unroll
    for (int ww = 0; ww < 4; ++ww) {
      f32x4 v = *(const f32x4*)(red + ((ww * 8 + cg) * 64 + lane) * 4);
      s += v;
    }
    u32x2 v = {cvtpk(s[0], s[1]), cvtpk(s[2], s[3])};
    *(u32x2*)(out + (size_t)arow[0] * DM + col0 + 32 * cb + 8 * g + 4 * h) = v;
  }
}

constexpr float SCL = 0.0625f * 1.4426950408889634f;
constexpr float THR = 8.0f;

typedef __attribute__((address_space(3))) void* lds_vp;
struct DmaCtx {
  __amdgpu_buffer_rsrc_t rk, rv;
  int koff[4], voff[4];
  char* dst;
};
template <int U>
DI void issue_unit(const DmaCtx& c) {
  constexpr int t = U >> 2, kind = U & 3, slot = U % 5;
  if constexpr (kind < 2) {
#pragma unroll
    for (int pi = 0; pi < 4; ++pi)
      __builtin_amdgcn_raw_ptr_buffer_load_lds(c.rk, (lds_vp)(c.dst + slot * 8192 + pi * 1024), 16, c.koff[pi],
                                               32 * t * 512 + kind * 256, 0, 0);
  } else {
    constexpr int ks = kind - 2;
#pragma unroll
    for (int pi = 0; pi < 4; ++pi)
      __builtin_amdgcn_raw_ptr_buffer_load_lds(c.rv, (lds_vp)(c.dst + slot * 8192 + pi * 1024), 16, c.voff[pi],
                                               (32 * t + 16 * ks) * 512, 0, 0);
  }
}
template <int U> DI void wait_unit_bar() {
  constexpr int after = (U + 3 > 31 ? 31 : U + 3) - U;
  if constexpr (after == 3) WAITV(12);
  else if constexpr (after == 2) WAITV(8);
  else if constexpr (after == 1) WAITV(4);
  else WAITV(0);
  WAITL0();
  __builtin_amdgcn_s_barrier();
}

struct AttnState {
  bf16x8 qf[16];
  f32x16 oacc[8];
  float m, l;
};

template <int T, int DHF>
DI void qk_half(AttnState& st, f32x16& s, const DmaCtx& c, char* ring, unsigned kaddr) {
  constexpr int U = 4 * T + DHF, slot = U % 5;
  wait_unit_bar<U>();
  if constexpr (U + 4 < 32) issue_unit<U + 4>(c);
#pragma unroll
  for (int ss = 0; ss < 8; ++ss) {
    bf16x8 kf = *(const bf16x8*)(ring + slot * 8192 + (kaddr ^ (ss * 32)));
    s = __builtin_amdgcn_mfma_f32_32x32x16_bf16(kf, st.qf[DHF * 8 + ss], s, 0, 0, 0);
  }
}
template <int T, int KS, int HF>
DI void pv_quarter(AttnState& st, const bf16x8& pf, const unsigned (&vaddr)[4]) {
  constexpr int U = 4 * T + 2 + KS, slot = U % 5;
  constexpr int OFF = slot * 8192 + HF * 256;
  s16x4 v0[4], v1[4];
  v0[0] = trread_o<OFF>(vaddr[0]); v1[0] = trread_o<OFF + 4096>(vaddr[0]);
  v0[1] = trread_o<OFF>(vaddr[1]); v1[1] = trread_o<OFF + 4096>(vaddr[1]);
  v0[2] = trread_o<OFF>(vaddr[2]); v1[2] = trread_o<OFF + 4096>(vaddr[2]);
  v0[3] = trread_o<OFF>(vaddr[3]); v1[3] = trread_o<OFF + 4096>(vaddr[3]);
  asm volatile("s_waitcnt lgkmcnt(0)"
               : "+v"(v0[0]), "+v"(v0[1]), "+v"(v0[2]), "+v"(v0[3]), "+v"(v1[0]), "+v"(v1[1]), "+v"(v1[2]),
                 "+v"(v1[3])::"memory");
#pragma unroll
  for (int d = 0; d < 4; ++d) {
    bf16x8 vf = cat8(v0[d], v1[d]);
    st.oacc[HF * 4 + d] = __builtin_amdgcn_mfma_f32_32x32x16_bf16(vf, pf, st.oacc[HF * 4 + d], 0, 0, 0);
  }
}
template <int T, int KS>
DI void pv_half(AttnState& st, const bf16x8& pf, const DmaCtx& c, const unsigned (&vaddr)[4]) {
  constexpr int U = 4 * T + 2 + KS;
  wait_unit_bar<U>();
  if constexpr (U + 4 < 32) issue_unit<U + 4>(c);
  pv_quarter<T, KS, 0>(st, pf, vaddr);
  pv_quarter<T, KS, 1>(st, pf, vaddr);
}

template <int T>
DI void attn_tile(AttnState& st, const DmaCtx& c, char* ring, unsigned kaddr, const unsigned (&vaddr)[4]) {
  f32x16 s;
#pragma unroll
  for (int i = 0; i < 16; ++i) s[i] = 0.f;
  qk_half<T, 0>(st, s, c, ring, kaddr);
  qk_half<T, 1>(st, s, c, ring, kaddr);
  bf16x8 pf[2];
  {
    float a = s[0];
#pragma unroll
    for (int i = 1; i < 16; ++i) a = fmaxf(a, s[i]);
    auto rr = __builtin_amdgcn_permlane32_swap(__float_as_uint(a), __float_as_uint(a), false, false);
    const float mx = fmaxf(__uint_as_float(rr[0]), __uint_as_float(rr[1])) * SCL;
    const bool grew = (mx - st.m > THR);
    if (T == 0 || __any((int)grew)) {
      const float mn = fmaxf(st.m, mx);
      const float alpha = __builtin_amdgcn_exp2f(st.m - mn);
      st.m = mn;
      st.l *= alpha;
      if (T != 0) {
#pragma unroll
        for (int db = 0; db < 8; ++db)
#pragma unroll
          for (int i = 0; i < 16; ++i) st.oacc[db][i] *= alpha;
      }
    }
    const float nm = -st.m;
    float ps = 0.f;
#pragma unroll
    for (int i = 0; i < 16; ++i) {
      s[i] = __builtin_amdgcn_exp2f(fmaf(s[i], SCL, nm));
      ps += s[i];
    }
    st.l += ps;
#pragma unroll
    for (int ks = 0; ks < 2; ++ks) {
      u32x4 wv = {cvtpk(s[8 * ks + 0], s[8 * ks + 1]), cvtpk(s[8 * ks + 2], s[8 * ks + 3]),
                  cvtpk(s[8 * ks + 4], s[8 * ks + 5]), cvtpk(s[8 * ks + 6], s[8 * ks + 7])};
      pf[ks] = __builtin_bit_cast(bf16x8, wv);
    }
  }
  pv_half<T, 0>(st, pf[0], c, vaddr);
  pv_half<T, 1>(st, pf[1], c, vaddr);
}

#define STAMP(i)
#define DBGARG
__global__ __launch_bounds__(512, 2) void attn_kernel(const u16* __restrict__ q, const u16* __restrict__ kc,
                                                      const u16* __restrict__ vc, u16* __restrict__ o DBGARG) {
  extern __shared__ __attribute__((aligned(16))) char lds[];
  STAMP(0);
  const int tid = threadIdx.x, lane = tid & 63;
  const int w = __builtin_amdgcn_readfirstlane(tid >> 6);
  const int rb = w >> 2, kq = w & 3;
  const int r = lane & 31, h = lane >> 5;
  const int bid = blockIdx.x;
  const int xcd = bid & 7, j = bid >> 3;
  const int grp = (j >> 2) * 8 + xcd, sub = j & 3;
  const int b = grp >> 3, kvh = grp & 7;
  const int head = kvh * 2 + (sub >> 1), rhalf = sub & 1;
  const int row0 = b * SQ + rhalf * 56;

  const u16* Kw = kc + ((size_t)(b * HKV + kvh) * SK + 256 * kq) * DH;
  const u16* Vw = vc + ((size_t)(b * HKV + kvh) * SK + 256 * kq) * DH;
  char* ring = lds + kq * 40960;

  AttnState st;
  {
    int rr = 32 * rb + r;
    rr = rr > 55 ? 55 : rr;
    const u16* qrow = q + (size_t)(row0 + rr) * NQ + head * DH + 8 * h;
#pragma unroll
    for (int s = 0; s < 16; ++s) st.qf[s] = *(const bf16x8*)(qrow + 16 * s);
    st.m = -__builtin_inff();
    st.l = 0.f;
#pragma unroll
    for (int db = 0; db < 8; ++db)
#pragma unroll
      for (int i = 0; i < 16; ++i) st.oacc[db][i] = 0.f;
  }
#pragma unroll
  for (int s = 0; s < 16; ++s) asm volatile("" : "+v"(st.qf[s]));
  WAITV(0);

  STAMP(1);
  DmaCtx c;
  c.rk = __builtin_amdgcn_make_buffer_rsrc((void*)Kw, 0, 256 * DH * 2, 0x00020000);
  c.rv = __builtin_amdgcn_make_buffer_rsrc((void*)Vw, 0, 256 * DH * 2, 0x00020000);
  c.dst = ring + rb * 4096;
#pragma unroll
  for (int pi = 0; pi < 4; ++pi) {
    const int krow = 16 * rb + 4 * pi + (lane >> 4);
    c.koff[pi] = krow * 512 + (((lane & 15) ^ (krow & 15)) * 16);
    const int vrow = 8 * rb + 2 * pi + (lane >> 5);
    c.voff[pi] = vrow * 512 + (((lane & 31) ^ ((vrow & 3) << 2)) * 16);
  }
  issue_unit<0>(c);
  issue_unit<1>(c);
  issue_unit<2>(c);
  issue_unit<3>(c);

  const unsigned kaddr = r * 256 + ((h ^ (r & 15)) * 16);
  const int g = lane >> 4, qq = (lane >> 2) & 3, pp = lane & 3;
  unsigned vaddr[4];
#pragma unroll
  for (int d = 0; d < 4; ++d)
    vaddr[d] = (unsigned)(size_t)ring + (4 * (g >> 1) + qq) * 512 + ((64 * d + 32 * (g & 1) + 8 * pp) ^ (qq << 6));

  attn_tile<0>(st, c, ring, kaddr, vaddr);
  STAMP(2);
  attn_tile<1>(st, c, ring, kaddr, vaddr);
  STAMP(3);
  attn_tile<2>(st, c, ring, kaddr, vaddr);
  STAMP(4);
  attn_tile<3>(st, c, ring, kaddr, vaddr);
  STAMP(5);
  attn_tile<4>(st, c, ring, kaddr, vaddr);
  STAMP(6);
  attn_tile<5>(st, c, ring, kaddr, vaddr);
  STAMP(7);
  attn_tile<6>(st, c, ring, kaddr, vaddr);
  STAMP(8);
  attn_tile<7>(st, c, ring, kaddr, vaddr);
  STAMP(9);

  __syncthreads();
  float* ml = (float*)lds;
  {
    auto rr = __builtin_amdgcn_permlane32_swap(__float_as_uint(st.l), __float_as_uint(st.l), false, false);
    const float lt = __uint_as_float(rr[0]) + __uint_as_float(rr[1]);
    if (h == 0) {
      ml[(w * 32 + r) * 2 + 0] = st.m;
      ml[(w * 32 + r) * 2 + 1] = lt;
    }
  }
  __syncthreads();
  float f;
  {
    float mm[4], ll[4];
#pragma unroll
    for (int k = 0; k < 4; ++k) {
      mm[k] = ml[((rb * 4 + k) * 32 + r) * 2 + 0];
      ll[k] = ml[((rb * 4 + k) * 32 + r) * 2 + 1];
    }
    const float M = fmaxf(fmaxf(mm[0], mm[1]), fmaxf(mm[2], mm[3]));
    float L = 0.f;
#pragma unroll
    for (int k = 0; k < 4; ++k) L += __builtin_amdgcn_exp2f(mm[k] - M) * ll[k];
    f = __builtin_amdgcn_exp2f(st.m - M) / L;
  }
  float* xch = (float*)(lds + 4096);
#pragma unroll
  for (int rnd = 0; rnd < 2; ++rnd) {
#pragma unroll
    for (int db = 0; db < 8; ++db) {
      if ((db & 1) != rnd) continue;
      const int owner = db >> 1;
      if (owner == kq) continue;
      const int dslot = owner < kq ? owner : owner - 1;
#pragma unroll
      for (int gg = 0; gg < 4; ++gg) {
        f32x4 v = {st.oacc[db][4 * gg] * f, st.oacc[db][4 * gg + 1] * f, st.oacc[db][4 * gg + 2] * f,
                   st.oacc[db][4 * gg + 3] * f};
        *(f32x4*)(xch + (((w * 3 + dslot) * 4 + gg) * 64 + lane) * 4) = v;
      }
    }
    __syncthreads();
#pragma unroll
    for (int db = 0; db < 8; ++db) {
      if ((db & 1) != rnd) continue;
      if ((db >> 1) != kq) continue;
#pragma unroll
      for (int gg = 0; gg < 4; ++gg) {
        f32x4 sum = {st.oacc[db][4 * gg] * f, st.oacc[db][4 * gg + 1] * f, st.oacc[db][4 * gg + 2] * f,
                     st.oacc[db][4 * gg + 3] * f};
#pragma unroll
        for (int sk = 0; sk < 4; ++sk) {
          if (sk == kq) continue;
          const int dslot = kq < sk ? kq : kq - 1;
          f32x4 v = *(const f32x4*)(xch + ((((rb * 4 + sk) * 3 + dslot) * 4 + gg) * 64 + lane) * 4);
          sum += v;
        }
        const int rr = 32 * rb + r;
        if (rr < 56) {
          u32x2 v = {cvtpk(sum[0], sum[1]), cvtpk(sum[2], sum[3])};
          *(u32x2*)(o + (size_t)(row0 + rr) * NQ + head * DH + 32 * db + 8 * gg + 4 * h) = v;
        }
      }
    }
    __syncthreads();
  }
  STAMP(10);
}

#define GAP() __builtin_amdgcn_sched_barrier(0)
#define ACLOB "a0","a1","a2","a3","a4","a5","a6","a7","a8","a9","a10","a11","a12","a13","a14","a15","a16","a17","a18","a19","a20","a21","a22","a23","a24","a25","a26","a27","a28","a29","a30","a31","a32","a33","a34","a35","a36","a37","a38","a39","a40","a41","a42","a43","a44","a45","a46","a47","a48","a49","a50","a51","a52","a53","a54","a55","a56","a57","a58","a59","a60","a61","a62","a63","a64","a65","a66","a67","a68","a69","a70","a71","a72","a73","a74","a75","a76","a77","a78","a79","a80","a81","a82","a83","a84","a85","a86","a87","a88","a89","a90","a91","a92","a93","a94","a95","a96","a97","a98","a99","a100","a101","a102","a103","a104","a105","a106","a107","a108","a109","a110","a111","a112","a113","a114","a115","a116","a117","a118","a119","a120","a121","a122","a123","a124","a125","a126","a127","a128","a129","a130","a131","a132","a133","a134","a135","a136","a137","a138","a139","a140","a141","a142","a143","a144","a145","a146","a147","a148","a149","a150","a151","a152","a153","a154","a155","a156","a157","a158","a159","a160","a161","a162","a163","a164","a165","a166","a167","a168","a169","a170","a171","a172","a173","a174","a175","a176","a177","a178","a179","a180","a181","a182","a183","a184","a185","a186","a187","a188","a189","a190","a191","a192","a193","a194","a195","a196","a197","a198","a199","a200","a201","a202","a203","a204","a205","a206","a207","a208","a209","a210","a211","a212","a213","a214","a215","a216","a217","a218","a219","a220","a221","a222","a223","a224","a225","a226","a227","a228","a229","a230","a231","a232","a233","a234","a235","a236","a237","a238","a239","a240","a241","a242","a243","a244","a245","a246","a247","a248","a249","a250","a251","a252","a253","a254","a255"

template <int AO, bool ZERO> DI void pv_mfma(const bf16x8& vf, const bf16x8& pf) {
  if constexpr (ZERO)
    asm volatile("v_mfma_f32_32x32x16_bf16 a[%c2:%c3], %0, %1, 0" ::"v"(vf), "v"(pf), "i"(AO), "i"(AO + 15) : ACLOB);
  else
    asm volatile("v_mfma_f32_32x32x16_bf16 a[%c2:%c3], %0, %1, a[%c2:%c3]" ::"v"(vf), "v"(pf), "i"(AO), "i"(AO + 15)
                 : ACLOB);
}
template <bool FIRST> DI void qk_mfma(f32x16& s, const bf16x8& kf, const bf16x8& qf) {
  if constexpr (FIRST)
    asm volatile("v_mfma_f32_32x32x16_bf16 %0, %1, %2, 0" : "=&v"(s) : "v"(kf), "v"(qf) : ACLOB);
  else
    asm volatile("v_mfma_f32_32x32x16_bf16 %0, %1, %2, %0" : "+v"(s) : "v"(kf), "v"(qf) : ACLOB);
}
DI float max3f(float a, float b, float c) { float r; asm("v_max3_f32 %0, %1, %2, %3" : "=v"(r) : "v"(a), "v"(b), "v"(c)); return r; }
DI float max2f(float a, float b) { float r; asm("v_max_f32_e32 %0, %1, %2" : "=v"(r) : "v"(a), "v"(b)); return r; }
DI float fadd_s(float a, float b) { float r; asm("v_add_f32_e32 %0, %1, %2" : "=v"(r) : "v"(a), "v"(b)); return r; }
DI float ffma_s(float a, float b, float c) { float r; asm("v_fma_f32 %0, %1, %2, %3" : "=v"(r) : "v"(a), "v"(b), "v"(c)); return r; }

constexpr int unit_is_v(int n) { return n < 2 ? 0 : (n >= 30 ? 1 : (((n - 2) & 3) >= 2)); }
constexpr int unit_tile(int n) { return n < 2 ? 0 : (n >= 30 ? 7 : (((n - 2) & 3) >= 2 ? (n - 2) / 4 : (n - 2) / 4 + 1)); }
constexpr int unit_half(int n) { return n & 1; }
constexpr int unit_k(int t, int hf) { return t == 0 ? hf : 4 * (t - 1) + 2 + hf; }
constexpr int unit_v(int t, int hf) { return t == 7 ? 30 + hf : 4 * t + 4 + hf; }

struct Ctx4 {
  __amdgpu_buffer_rsrc_t rk, rv;
  int koff[4], voff[2];
  char* ring;
  unsigned kaddr[8];
  unsigned vaddr[4];
  bf16x8 qf[2][16];
  f32x16 s[2];
  bf16x8 pf[2][2];
  unsigned pn[2][8];
  float m[2], l[2], nm[2], mx[2], fct[2];
  float rs[2];
  bool pend;
  const u16* Kp; const u16* Vp;
  unsigned pfoff;
  int sub;
  unsigned pfd;
};
DI void prefetch_unit(Ctx4& c, int n) {
}

template <int NS, int N, int P> DI void dma_piece(const Ctx4& c) {
  if constexpr (N < 32) {
    constexpr int slot = N % NS, t = unit_tile(N), hf = unit_half(N);
    if constexpr (!unit_is_v(N))
      __builtin_amdgcn_raw_ptr_buffer_load_lds(c.rk, (lds_vp)(c.ring + slot * 8192 + P * 1024), 16, c.koff[P & 3],
                                               (32 * t + 16 * (P >> 2)) * 512 + hf * 256, 0, 0);
    else
      __builtin_amdgcn_raw_ptr_buffer_load_lds(c.rv, (lds_vp)(c.ring + slot * 8192 + P * 1024), 16, c.voff[P & 1],
                                               (32 * t + 16 * hf + 4 * (P >> 1)) * 512, 0, 0);
  }
}
template <int NS, int N> DI void dma_unit(const Ctx4& c) {
  dma_piece<NS, N, 0>(c); dma_piece<NS, N, 1>(c); dma_piece<NS, N, 2>(c); dma_piece<NS, N, 3>(c);
  dma_piece<NS, N, 4>(c); dma_piece<NS, N, 5>(c); dma_piece<NS, N, 6>(c); dma_piece<NS, N, 7>(c);
}
template <int N, int AHEAD> DI void wait_unit4() {
  constexpr int last = N + AHEAD > 31 ? 31 : N + AHEAD;
  constexpr int after = last - N;
  asm volatile("s_waitcnt vmcnt(%0)" ::"i"(8 * after) : "memory");
}

template <int NS, int T, int J> DI bf16x8 kread(const Ctx4& c) {
  constexpr int n = unit_k(T, J >> 3), slot = n % NS;
  bf16x8 r;
  asm volatile("ds_read_b128 %0, %1 offset:%2" : "=v"(r) : "v"(c.kaddr[J & 7]), "i"(slot * 8192) : "memory");
  return r;
}
template <int NS, int T, int J> DI void phaseA_step(Ctx4& c, bf16x8 (&kf)[4]) {
  constexpr int n0 = unit_k(T, 0), n1 = unit_k(T, 1);
  if constexpr (J + 3 < 16) asm volatile("s_waitcnt lgkmcnt(3)" : "+v"(kf[J & 3])::"memory");
  else if constexpr (J + 3 == 16) asm volatile("s_waitcnt lgkmcnt(2)" : "+v"(kf[J & 3])::"memory");
  else if constexpr (J + 3 == 17) asm volatile("s_waitcnt lgkmcnt(1)" : "+v"(kf[J & 3])::"memory");
  else asm volatile("s_waitcnt lgkmcnt(0)" : "+v"(kf[J & 3])::"memory");
  qk_mfma<J == 0>(c.s[0], kf[J & 3], c.qf[0][J]);
  if constexpr (J >= 8) { dma_piece<NS, n0 + NS, J - 8>(c); }
  GAP();
  qk_mfma<J == 0>(c.s[1], kf[J & 3], c.qf[1][J]);
  if constexpr (J + 4 == 8) wait_unit4<n1, NS - 2>();
  if constexpr (J + 4 < 16) kf[J & 3] = kread<NS, T, J + 4>(c);
  GAP();
}
template <int NS, int T, int... Js> DI void phaseA_(Ctx4& c, std::integer_sequence<int, Js...>) {
  bf16x8 kf[4];
  wait_unit4<unit_k(T, 0), NS - 1>();
  kf[0] = kread<NS, T, 0>(c); kf[1] = kread<NS, T, 1>(c); kf[2] = kread<NS, T, 2>(c); kf[3] = kread<NS, T, 3>(c);
  GAP();
  (phaseA_step<NS, T, Js>(c, kf), ...);
}
template <int NS, int T> DI void phaseA(Ctx4& c) { phaseA_<NS, T>(c, std::make_integer_sequence<int, 16>{}); }

constexpr int egap(int e) { return 6 + (e * 25) / 32; }
template <int T, int G> DI void sm_slice(Ctx4& c) {
  if constexpr (G < 4) {
    constexpr int qb = G >> 1;
    if constexpr ((G & 1) == 0) {
      float a = max3f(c.s[qb][0], c.s[qb][1], c.s[qb][2]);
      a = max3f(a, c.s[qb][3], c.s[qb][4]);
      a = max3f(a, c.s[qb][5], c.s[qb][6]);
      c.mx[qb] = max3f(a, c.s[qb][7], c.s[qb][8]);
    } else {
      float a = max3f(c.mx[qb], c.s[qb][9], c.s[qb][10]);
      a = max3f(a, c.s[qb][11], c.s[qb][12]);
      a = max3f(a, c.s[qb][13], c.s[qb][14]);
      c.mx[qb] = max2f(a, c.s[qb][15]);
    }
  } else if constexpr (G == 4) {
#pragma unroll
    for (int qb = 0; qb < 2; ++qb) {
      auto rr = __builtin_amdgcn_permlane32_swap(__float_as_uint(c.mx[qb]), __float_as_uint(c.mx[qb]), false, false);
      c.mx[qb] = max2f(__uint_as_float(rr[0]), __uint_as_float(rr[1])) * SCL;
    }
  } else if constexpr (G == 5) {
    if constexpr (T == 0) {
      c.m[0] = c.mx[0]; c.m[1] = c.mx[1];
      c.l[0] = 0.f; c.l[1] = 0.f;
    } else {
      const bool grew = (c.mx[0] - c.m[0] > THR) | (c.mx[1] - c.m[1] > THR);
      if (__builtin_expect(__any((int)grew), 0)) {
#pragma unroll
        for (int qb = 0; qb < 2; ++qb) {
          const float mn = fmaxf(c.m[qb], c.mx[qb]);
          const float al = __builtin_amdgcn_exp2f(c.m[qb] - mn);
          c.m[qb] = mn;
          c.l[qb] *= al;
          c.fct[qb] = al;
        }
        c.pend = true;
      }
    }
    c.nm[0] = -c.m[0]; c.nm[1] = -c.m[1];
    c.rs[0] = 0.f; c.rs[1] = 0.f;
  }
  if constexpr (G >= 6) {
#pragma unroll
    for (int e = 0; e < 32; ++e) {
      if (egap(e) == G) {
        const int qb = e >> 4, i = e & 15;
        c.s[qb][i] = __builtin_amdgcn_exp2f(ffma_s(c.s[qb][i], SCL, c.nm[qb]));
      }
    }
#pragma unroll
    for (int e = 0; e < 32; ++e) {
      const int ge = egap(e);
      if (ge == G - 1 || (G == 31 && ge == 31)) {
        const int qb = e >> 4, i = e & 15;
        c.rs[qb] += c.s[qb][i];
      }
    }
#pragma unroll
    for (int d = 0; d < 16; ++d) {
      const int ge = egap(2 * d + 1);
      if (ge == G - 1 || (G == 31 && ge == 31)) {
        const int qb = d >> 3, i = (2 * d) & 15;
        c.pn[qb][d & 7] = cvtpk(c.s[qb][i], c.s[qb][i + 1]);
      }
    }
  }
}

template <int NS, int TV, int S> DI void vread_set(const Ctx4& c, s16x4 (&v)[4]) {
  constexpr int ks = S >> 2, dbp = S & 3;
  constexpr int n = unit_v(TV, ks), slot = n % NS;
  constexpr int db0 = 2 * dbp, db1 = 2 * dbp + 1;
  constexpr int o0 = slot * 8192 + (db0 >> 2) * 256, o1 = slot * 8192 + (db1 >> 2) * 256;
  v[0] = trread_o<o0>(c.vaddr[db0 & 3]);
  v[1] = trread_o<o0 + 4096>(c.vaddr[db0 & 3]);
  v[2] = trread_o<o1>(c.vaddr[db1 & 3]);
  v[3] = trread_o<o1 + 4096>(c.vaddr[db1 & 3]);
}
template <int NS, int T, bool HASPV, bool HASSM, int M> DI void phaseB_gap(Ctx4& c, s16x4 (&va)[4], s16x4 (&vb)[4]) {
  constexpr int TV = T - 1;
  constexpr int S = M >> 2, ks = S >> 2, dbp = S & 3, dd = (M >> 1) & 1, qb = M & 1;
  if constexpr (HASPV) {
    if constexpr ((M & 3) == 0) {
      if constexpr (S + 1 < 8) {
        if constexpr (S & 1) asm volatile("s_waitcnt lgkmcnt(4)" : "+v"(vb[0]), "+v"(vb[1]), "+v"(vb[2]), "+v"(vb[3])::"memory");
        else asm volatile("s_waitcnt lgkmcnt(4)" : "+v"(va[0]), "+v"(va[1]), "+v"(va[2]), "+v"(va[3])::"memory");
      } else {
        asm volatile("s_waitcnt lgkmcnt(0)" : "+v"(vb[0]), "+v"(vb[1]), "+v"(vb[2]), "+v"(vb[3])::"memory");
      }
    }
    {
      constexpr int db = 2 * dbp + dd;
      const bf16x8 vf = (S & 1) ? cat8(vb[2 * dd], vb[2 * dd + 1]) : cat8(va[2 * dd], va[2 * dd + 1]);
      pv_mfma<(qb * 8 + db) * 16, (TV == 0 && ks == 0)>(vf, c.pf[qb][ks]);
    }
    if constexpr ((M & 3) == 3 && S + 2 < 8) {
      if constexpr (S + 2 == 4) wait_unit4<unit_v(TV, 1), NS - 2>();
      if constexpr (S & 1) vread_set<NS, TV, S + 2>(c, vb); else vread_set<NS, TV, S + 2>(c, va);
    }
    if constexpr (M >= 16 && M < 24) dma_piece<NS, unit_v(TV, 0) + NS, M - 16>(c);
  }
  if constexpr (HASSM) sm_slice<T, M>(c);
  GAP();
}
template <int NS, int T, bool HASPV, bool HASSM, int... Ms> DI void phaseB_(Ctx4& c, std::integer_sequence<int, Ms...>) {
  constexpr int TV = T - 1;
  s16x4 va[4], vb[4];
  if constexpr (HASPV) {
    wait_unit4<unit_v(TV, 0), NS - 2>();
    vread_set<NS, TV, 0>(c, va);
    vread_set<NS, TV, 1>(c, vb);
  }
  if constexpr (HASSM) dma_unit<NS, unit_k(T, 1) + NS>(c);
  if constexpr (HASSM) {
    asm volatile("s_nop 7\n\ts_nop 7" : "+v"(c.s[0]), "+v"(c.s[1]));
  }
  GAP();
  (phaseB_gap<NS, T, HASPV, HASSM, Ms>(c, va, vb), ...);
  if constexpr (HASPV) dma_unit<NS, unit_v(TV, 1) + NS>(c);
  if constexpr (HASSM) {
    c.l[0] += c.rs[0]; c.l[1] += c.rs[1];
#pragma unroll
    for (int qb = 0; qb < 2; ++qb)
#pragma unroll
      for (int ks = 0; ks < 2; ++ks) {
        u32x4 wv = {c.pn[qb][4 * ks], c.pn[qb][4 * ks + 1], c.pn[qb][4 * ks + 2], c.pn[qb][4 * ks + 3]};
        c.pf[qb][ks] = __builtin_bit_cast(bf16x8, wv);
      }
  }
  if constexpr (HASPV && HASSM) {
    if (__builtin_expect(c.pend, 0)) {
      float t0, t1;
      asm volatile("s_nop 7\n\ts_nop 7\n\t"
                   "v_accvgpr_read_b32 %0, a0\n\tv_mul_f32 %0, %2, %0\n\tv_accvgpr_write_b32 a0, %0\n\t"
                   "v_accvgpr_read_b32 %1, a1\n\tv_mul_f32 %1, %2, %1\n\tv_accvgpr_write_b32 a1, %1\n\t"
                   "v_accvgpr_read_b32 %0, a2\n\tv_mul_f32 %0, %2, %0\n\tv_accvgpr_write_b32 a2, %0\n\t"
                   "v_accvgpr_read_b32 %1, a3\n\tv_mul_f32 %1, %2, %1\n\tv_accvgpr_write_b32 a3, %1\n\t"
                   "v_accvgpr_read_b32 %0, a4\n\tv_mul_f32 %0, %2, %0\n\tv_accvgpr_write_b32 a4, %0\n\t"
                   "v_accvgpr_read_b32 %1, a5\n\tv_mul_f32 %1, %2, %1\n\tv_accvgpr_write_b32 a5, %1\n\t"
                   "v_accvgpr_read_b32 %0, a6\n\tv_mul_f32 %0, %2, %0\n\tv_accvgpr_write_b32 a6, %0\n\t"
                   "v_accvgpr_read_b32 %1, a7\n\tv_mul_f32 %1, %2, %1\n\tv_accvgpr_write_b32 a7, %1\n\t"
                   "v_accvgpr_read_b32 %0, a8\n\tv_mul_f32 %0, %2, %0\n\tv_accvgpr_write_b32 a8, %0\n\t"
                   "v_accvgpr_read_b32 %1, a9\n\tv_mul_f32 %1, %2, %1\n\tv_accvgpr_write_b32 a9, %1\n\t"
                   "v_accvgpr_read_b32 %0, a10\n\tv_mul_f32 %0, %2, %0\n\tv_accvgpr_write_b32 a10, %0\n\t"
                   "v_accvgpr_read_b32 %1, a11\n\tv_mul_f32 %1, %2, %1\n\tv_accvgpr_write_b32 a11, %1\n\t"
                   "v_accvgpr_read_b32 %0, a12\n\tv_mul_f32 %0, %2, %0\n\tv_accvgpr_write_b32 a12, %0\n\t"
                   "v_accvgpr_read_b32 %1, a13\n\tv_mul_f32 %1, %2, %1\n\tv_accvgpr_write_b32 a13, %1\n\t"
                   "v_accvgpr_read_b32 %0, a14\n\tv_mul_f32 %0, %2, %0\n\tv_accvgpr_write_b32 a14, %0\n\t"
                   "v_accvgpr_read_b32 %1, a15\n\tv_mul_f32 %1, %2, %1\n\tv_accvgpr_write_b32 a15, %1\n\t"
                   "v_accvgpr_read_b32 %0, a16\n\tv_mul_f32 %0, %2, %0\n\tv_accvgpr_write_b32 a16, %0\n\t"
                   "v_accvgpr_read_b32 %1, a17\n\tv_mul_f32 %1, %2, %1\n\tv_accvgpr_write_b32 a17, %1\n\t"
                   "v_accvgpr_read_b32 %0, a18\n\tv_mul_f32 %0, %2, %0\n\tv_accvgpr_write_b32 a18, %0\n\t"
                   "v_accvgpr_read_b32 %1, a19\n\tv_mul_f32 %1, %2, %1\n\tv_accvgpr_write_b32 a19, %1\n\t"
                   "v_accvgpr_read_b32 %0, a20\n\tv_mul_f32 %0, %2, %0\n\tv_accvgpr_write_b32 a20, %0\n\t"
                   "v_accvgpr_read_b32 %1, a21\n\tv_mul_f32 %1, %2, %1\n\tv_accvgpr_write_b32 a21, %1\n\t"
                   "v_accvgpr_read_b32 %0, a22\n\tv_mul_f32 %0, %2, %0\n\tv_accvgpr_write_b32 a22, %0\n\t"
                   "v_accvgpr_read_b32 %1, a23\n\tv_mul_f32 %1, %2, %1\n\tv_accvgpr_write_b32 a23, %1\n\t"
                   "v_accvgpr_read_b32 %0, a24\n\tv_mul_f32 %0, %2, %0\n\tv_accvgpr_write_b32 a24, %0\n\t"
                   "v_accvgpr_read_b32 %1, a25\n\tv_mul_f32 %1, %2, %1\n\tv_accvgpr_write_b32 a25, %1\n\t"
                   "v_accvgpr_read_b32 %0, a26\n\tv_mul_f32 %0, %2, %0\n\tv_accvgpr_write_b32 a26, %0\n\t"
                   "v_accvgpr_read_b32 %1, a27\n\tv_mul_f32 %1, %2, %1\n\tv_accvgpr_write_b32 a27, %1\n\t"
                   "v_accvgpr_read_b32 %0, a28\n\tv_mul_f32 %0, %2, %0\n\tv_accvgpr_write_b32 a28, %0\n\t"
                   "v_accvgpr_read_b32 %1, a29\n\tv_mul_f32 %1, %2, %1\n\tv_accvgpr_write_b32 a29, %1\n\t"
                   "v_accvgpr_read_b32 %0, a30\n\tv_mul_f32 %0, %2, %0\n\tv_accvgpr_write_b32 a30, %0\n\t"
                   "v_accvgpr_read_b32 %1, a31\n\tv_mul_f32 %1, %2, %1\n\tv_accvgpr_write_b32 a31, %1\n\t"
                   "v_accvgpr_read_b32 %0, a32\n\tv_mul_f32 %0, %2, %0\n\tv_accvgpr_write_b32 a32, %0\n\t"
                   "v_accvgpr_read_b32 %1, a33\n\tv_mul_f32 %1, %2, %1\n\tv_accvgpr_write_b32 a33, %1\n\t"
                   "v_accvgpr_read_b32 %0, a34\n\tv_mul_f32 %0, %2, %0\n\tv_accvgpr_write_b32 a34, %0\n\t"
                   "v_accvgpr_read_b32 %1, a35\n\tv_mul_f32 %1, %2, %1\n\tv_accvgpr_write_b32 a35, %1\n\t"
                   "v_accvgpr_read_b32 %0, a36\n\tv_mul_f32 %0, %2, %0\n\tv_accvgpr_write_b32 a36, %0\n\t"
                   "v_accvgpr_read_b32 %1, a37\n\tv_mul_f32 %1, %2, %1\n\tv_accvgpr_write_b32 a37, %1\n\t"
                   "v_accvgpr_read_b32 %0, a38\n\tv_mul_f32 %0, %2, %0\n\tv_accvgpr_write_b32 a38, %0\n\t"
                   "v_accvgpr_read_b32 %1, a39\n\tv_mul_f32 %1, %2, %1\n\tv_accvgpr_write_b32 a39, %1\n\t"
                   "v_accvgpr_read_b32 %0, a40\n\tv_mul_f32 %0, %2, %0\n\tv_accvgpr_write_b32 a40, %0\n\t"
                   "v_accvgpr_read_b32 %1, a41\n\tv_mul_f32 %1, %2, %1\n\tv_accvgpr_write_b32 a41, %1\n\t"
                   "v_accvgpr_read_b32 %0, a42\n\tv_mul_f32 %0, %2, %0\n\tv_accvgpr_write_b32 a42, %0\n\t"
                   "v_accvgpr_read_b32 %1, a43\n\tv_mul_f32 %1, %2, %1\n\tv_accvgpr_write_b32 a43, %1\n\t"
                   "v_accvgpr_read_b32 %0, a44\n\tv_mul_f32 %0, %2, %0\n\tv_accvgpr_write_b32 a44, %0\n\t"
                   "v_accvgpr_read_b32 %1, a45\n\tv_mul_f32 %1, %2, %1\n\tv_accvgpr_write_b32 a45, %1\n\t"
                   "v_accvgpr_read_b32 %0, a46\n\tv_mul_f32 %0, %2, %0\n\tv_accvgpr_write_b32 a46, %0\n\t"
                   "v_accvgpr_read_b32 %1, a47\n\tv_mul_f32 %1, %2, %1\n\tv_accvgpr_write_b32 a47, %1\n\t"
                   "v_accvgpr_read_b32 %0, a48\n\tv_mul_f32 %0, %2, %0\n\tv_accvgpr_write_b32 a48, %0\n\t"
                   "v_accvgpr_read_b32 %1, a49\n\tv_mul_f32 %1, %2, %1\n\tv_accvgpr_write_b32 a49, %1\n\t"
                   "v_accvgpr_read_b32 %0, a50\n\tv_mul_f32 %0, %2, %0\n\tv_accvgpr_write_b32 a50, %0\n\t"
                   "v_accvgpr_read_b32 %1, a51\n\tv_mul_f32 %1, %2, %1\n\tv_accvgpr_write_b32 a51, %1\n\t"
                   "v_accvgpr_read_b32 %0, a52\n\tv_mul_f32 %0, %2, %0\n\tv_accvgpr_write_b32 a52, %0\n\t"
                   "v_accvgpr_read_b32 %1, a53\n\tv_mul_f32 %1, %2, %1\n\tv_accvgpr_write_b32 a53, %1\n\t"
                   "v_accvgpr_read_b32 %0, a54\n\tv_mul_f32 %0, %2, %0\n\tv_accvgpr_write_b32 a54, %0\n\t"
                   "v_accvgpr_read_b32 %1, a55\n\tv_mul_f32 %1, %2, %1\n\tv_accvgpr_write_b32 a55, %1\n\t"
                   "v_accvgpr_read_b32 %0, a56\n\tv_mul_f32 %0, %2, %0\n\tv_accvgpr_write_b32 a56, %0\n\t"
                   "v_accvgpr_read_b32 %1, a57\n\tv_mul_f32 %1, %2, %1\n\tv_accvgpr_write_b32 a57, %1\n\t"
                   "v_accvgpr_read_b32 %0, a58\n\tv_mul_f32 %0, %2, %0\n\tv_accvgpr_write_b32 a58, %0\n\t"
                   "v_accvgpr_read_b32 %1, a59\n\tv_mul_f32 %1, %2, %1\n\tv_accvgpr_write_b32 a59, %1\n\t"
                   "v_accvgpr_read_b32 %0, a60\n\tv_mul_f32 %0, %2, %0\n\tv_accvgpr_write_b32 a60, %0\n\t"
                   "v_accvgpr_read_b32 %1, a61\n\tv_mul_f32 %1, %2, %1\n\tv_accvgpr_write_b32 a61, %1\n\t"
                   "v_accvgpr_read_b32 %0, a62\n\tv_mul_f32 %0, %2, %0\n\tv_accvgpr_write_b32 a62, %0\n\t"
                   "v_accvgpr_read_b32 %1, a63\n\tv_mul_f32 %1, %2, %1\n\tv_accvgpr_write_b32 a63, %1\n\t"
                   "v_accvgpr_read_b32 %0, a64\n\tv_mul_f32 %0, %2, %0\n\tv_accvgpr_write_b32 a64, %0\n\t"
                   "v_accvgpr_read_b32 %1, a65\n\tv_mul_f32 %1, %2, %1\n\tv_accvgpr_write_b32 a65, %1\n\t"
                   "v_accvgpr_read_b32 %0, a66\n\tv_mul_f32 %0, %2, %0\n\tv_accvgpr_write_b32 a66, %0\n\t"
                   "v_accvgpr_read_b32 %1, a67\n\tv_mul_f32 %1, %2, %1\n\tv_accvgpr_write_b32 a67, %1\n\t"
                   "v_accvgpr_read_b32 %0, a68\n\tv_mul_f32 %0, %2, %0\n\tv_accvgpr_write_b32 a68, %0\n\t"
                   "v_accvgpr_read_b32 %1, a69\n\tv_mul_f32 %1, %2, %1\n\tv_accvgpr_write_b32 a69, %1\n\t"
                   "v_accvgpr_read_b32 %0, a70\n\tv_mul_f32 %0, %2, %0\n\tv_accvgpr_write_b32 a70, %0\n\t"
                   "v_accvgpr_read_b32 %1, a71\n\tv_mul_f32 %1, %2, %1\n\tv_accvgpr_write_b32 a71, %1\n\t"
                   "v_accvgpr_read_b32 %0, a72\n\tv_mul_f32 %0, %2, %0\n\tv_accvgpr_write_b32 a72, %0\n\t"
                   "v_accvgpr_read_b32 %1, a73\n\tv_mul_f32 %1, %2, %1\n\tv_accvgpr_write_b32 a73, %1\n\t"
                   "v_accvgpr_read_b32 %0, a74\n\tv_mul_f32 %0, %2, %0\n\tv_accvgpr_write_b32 a74, %0\n\t"
                   "v_accvgpr_read_b32 %1, a75\n\tv_mul_f32 %1, %2, %1\n\tv_accvgpr_write_b32 a75, %1\n\t"
                   "v_accvgpr_read_b32 %0, a76\n\tv_mul_f32 %0, %2, %0\n\tv_accvgpr_write_b32 a76, %0\n\t"
                   "v_accvgpr_read_b32 %1, a77\n\tv_mul_f32 %1, %2, %1\n\tv_accvgpr_write_b32 a77, %1\n\t"
                   "v_accvgpr_read_b32 %0, a78\n\tv_mul_f32 %0, %2, %0\n\tv_accvgpr_write_b32 a78, %0\n\t"
                   "v_accvgpr_read_b32 %1, a79\n\tv_mul_f32 %1, %2, %1\n\tv_accvgpr_write_b32 a79, %1\n\t"
                   "v_accvgpr_read_b32 %0, a80\n\tv_mul_f32 %0, %2, %0\n\tv_accvgpr_write_b32 a80, %0\n\t"
                   "v_accvgpr_read_b32 %1, a81\n\tv_mul_f32 %1, %2, %1\n\tv_accvgpr_write_b32 a81, %1\n\t"
                   "v_accvgpr_read_b32 %0, a82\n\tv_mul_f32 %0, %2, %0\n\tv_accvgpr_write_b32 a82, %0\n\t"
                   "v_accvgpr_read_b32 %1, a83\n\tv_mul_f32 %1, %2, %1\n\tv_accvgpr_write_b32 a83, %1\n\t"
                   "v_accvgpr_read_b32 %0, a84\n\tv_mul_f32 %0, %2, %0\n\tv_accvgpr_write_b32 a84, %0\n\t"
                   "v_accvgpr_read_b32 %1, a85\n\tv_mul_f32 %1, %2, %1\n\tv_accvgpr_write_b32 a85, %1\n\t"
                   "v_accvgpr_read_b32 %0, a86\n\tv_mul_f32 %0, %2, %0\n\tv_accvgpr_write_b32 a86, %0\n\t"
                   "v_accvgpr_read_b32 %1, a87\n\tv_mul_f32 %1, %2, %1\n\tv_accvgpr_write_b32 a87, %1\n\t"
                   "v_accvgpr_read_b32 %0, a88\n\tv_mul_f32 %0, %2, %0\n\tv_accvgpr_write_b32 a88, %0\n\t"
                   "v_accvgpr_read_b32 %1, a89\n\tv_mul_f32 %1, %2, %1\n\tv_accvgpr_write_b32 a89, %1\n\t"
                   "v_accvgpr_read_b32 %0, a90\n\tv_mul_f32 %0, %2, %0\n\tv_accvgpr_write_b32 a90, %0\n\t"
                   "v_accvgpr_read_b32 %1, a91\n\tv_mul_f32 %1, %2, %1\n\tv_accvgpr_write_b32 a91, %1\n\t"
                   "v_accvgpr_read_b32 %0, a92\n\tv_mul_f32 %0, %2, %0\n\tv_accvgpr_write_b32 a92, %0\n\t"
                   "v_accvgpr_read_b32 %1, a93\n\tv_mul_f32 %1, %2, %1\n\tv_accvgpr_write_b32 a93, %1\n\t"
                   "v_accvgpr_read_b32 %0, a94\n\tv_mul_f32 %0, %2, %0\n\tv_accvgpr_write_b32 a94, %0\n\t"
                   "v_accvgpr_read_b32 %1, a95\n\tv_mul_f32 %1, %2, %1\n\tv_accvgpr_write_b32 a95, %1\n\t"
                   "v_accvgpr_read_b32 %0, a96\n\tv_mul_f32 %0, %2, %0\n\tv_accvgpr_write_b32 a96, %0\n\t"
                   "v_accvgpr_read_b32 %1, a97\n\tv_mul_f32 %1, %2, %1\n\tv_accvgpr_write_b32 a97, %1\n\t"
                   "v_accvgpr_read_b32 %0, a98\n\tv_mul_f32 %0, %2, %0\n\tv_accvgpr_write_b32 a98, %0\n\t"
                   "v_accvgpr_read_b32 %1, a99\n\tv_mul_f32 %1, %2, %1\n\tv_accvgpr_write_b32 a99, %1\n\t"
                   "v_accvgpr_read_b32 %0, a100\n\tv_mul_f32 %0, %2, %0\n\tv_accvgpr_write_b32 a100, %0\n\t"
                   "v_accvgpr_read_b32 %1, a101\n\tv_mul_f32 %1, %2, %1\n\tv_accvgpr_write_b32 a101, %1\n\t"
                   "v_accvgpr_read_b32 %0, a102\n\tv_mul_f32 %0, %2, %0\n\tv_accvgpr_write_b32 a102, %0\n\t"
                   "v_accvgpr_read_b32 %1, a103\n\tv_mul_f32 %1, %2, %1\n\tv_accvgpr_write_b32 a103, %1\n\t"
                   "v_accvgpr_read_b32 %0, a104\n\tv_mul_f32 %0, %2, %0\n\tv_accvgpr_write_b32 a104, %0\n\t"
                   "v_accvgpr_read_b32 %1, a105\n\tv_mul_f32 %1, %2, %1\n\tv_accvgpr_write_b32 a105, %1\n\t"
                   "v_accvgpr_read_b32 %0, a106\n\tv_mul_f32 %0, %2, %0\n\tv_accvgpr_write_b32 a106, %0\n\t"
                   "v_accvgpr_read_b32 %1, a107\n\tv_mul_f32 %1, %2, %1\n\tv_accvgpr_write_b32 a107, %1\n\t"
                   "v_accvgpr_read_b32 %0, a108\n\tv_mul_f32 %0, %2, %0\n\tv_accvgpr_write_b32 a108, %0\n\t"
                   "v_accvgpr_read_b32 %1, a109\n\tv_mul_f32 %1, %2, %1\n\tv_accvgpr_write_b32 a109, %1\n\t"
                   "v_accvgpr_read_b32 %0, a110\n\tv_mul_f32 %0, %2, %0\n\tv_accvgpr_write_b32 a110, %0\n\t"
                   "v_accvgpr_read_b32 %1, a111\n\tv_mul_f32 %1, %2, %1\n\tv_accvgpr_write_b32 a111, %1\n\t"
                   "v_accvgpr_read_b32 %0, a112\n\tv_mul_f32 %0, %2, %0\n\tv_accvgpr_write_b32 a112, %0\n\t"
                   "v_accvgpr_read_b32 %1, a113\n\tv_mul_f32 %1, %2, %1\n\tv_accvgpr_write_b32 a113, %1\n\t"
                   "v_accvgpr_read_b32 %0, a114\n\tv_mul_f32 %0, %2, %0\n\tv_accvgpr_write_b32 a114, %0\n\t"
                   "v_accvgpr_read_b32 %1, a115\n\tv_mul_f32 %1, %2, %1\n\tv_accvgpr_write_b32 a115, %1\n\t"
                   "v_accvgpr_read_b32 %0, a116\n\tv_mul_f32 %0, %2, %0\n\tv_accvgpr_write_b32 a116, %0\n\t"
                   "v_accvgpr_read_b32 %1, a117\n\tv_mul_f32 %1, %2, %1\n\tv_accvgpr_write_b32 a117, %1\n\t"
                   "v_accvgpr_read_b32 %0, a118\n\tv_mul_f32 %0, %2, %0\n\tv_accvgpr_write_b32 a118, %0\n\t"
                   "v_accvgpr_read_b32 %1, a119\n\tv_mul_f32 %1, %2, %1\n\tv_accvgpr_write_b32 a119, %1\n\t"
                   "v_accvgpr_read_b32 %0, a120\n\tv_mul_f32 %0, %2, %0\n\tv_accvgpr_write_b32 a120, %0\n\t"
                   "v_accvgpr_read_b32 %1, a121\n\tv_mul_f32 %1, %2, %1\n\tv_accvgpr_write_b32 a121, %1\n\t"
                   "v_accvgpr_read_b32 %0, a122\n\tv_mul_f32 %0, %2, %0\n\tv_accvgpr_write_b32 a122, %0\n\t"
                   "v_accvgpr_read_b32 %1, a123\n\tv_mul_f32 %1, %2, %1\n\tv_accvgpr_write_b32 a123, %1\n\t"
                   "v_accvgpr_read_b32 %0, a124\n\tv_mul_f32 %0, %2, %0\n\tv_accvgpr_write_b32 a124, %0\n\t"
                   "v_accvgpr_read_b32 %1, a125\n\tv_mul_f32 %1, %2, %1\n\tv_accvgpr_write_b32 a125, %1\n\t"
                   "v_accvgpr_read_b32 %0, a126\n\tv_mul_f32 %0, %2, %0\n\tv_accvgpr_write_b32 a126, %0\n\t"
                   "v_accvgpr_read_b32 %1, a127\n\tv_mul_f32 %1, %2, %1\n\tv_accvgpr_write_b32 a127, %1\n\t"
                   "v_accvgpr_read_b32 %0, a128\n\tv_mul_f32 %0, %3, %0\n\tv_accvgpr_write_b32 a128, %0\n\t"
                   "v_accvgpr_read_b32 %1, a129\n\tv_mul_f32 %1, %3, %1\n\tv_accvgpr_write_b32 a129, %1\n\t"
                   "v_accvgpr_read_b32 %0, a130\n\tv_mul_f32 %0, %3, %0\n\tv_accvgpr_write_b32 a130, %0\n\t"
                   "v_accvgpr_read_b32 %1, a131\n\tv_mul_f32 %1, %3, %1\n\tv_accvgpr_write_b32 a131, %1\n\t"
                   "v_accvgpr_read_b32 %0, a132\n\tv_mul_f32 %0, %3, %0\n\tv_accvgpr_write_b32 a132, %0\n\t"
                   "v_accvgpr_read_b32 %1, a133\n\tv_mul_f32 %1, %3, %1\n\tv_accvgpr_write_b32 a133, %1\n\t"
                   "v_accvgpr_read_b32 %0, a134\n\tv_mul_f32 %0, %3, %0\n\tv_accvgpr_write_b32 a134, %0\n\t"
                   "v_accvgpr_read_b32 %1, a135\n\tv_mul_f32 %1, %3, %1\n\tv_accvgpr_write_b32 a135, %1\n\t"
                   "v_accvgpr_read_b32 %0, a136\n\tv_mul_f32 %0, %3, %0\n\tv_accvgpr_write_b32 a136, %0\n\t"
                   "v_accvgpr_read_b32 %1, a137\n\tv_mul_f32 %1, %3, %1\n\tv_accvgpr_write_b32 a137, %1\n\t"
                   "v_accvgpr_read_b32 %0, a138\n\tv_mul_f32 %0, %3, %0\n\tv_accvgpr_write_b32 a138, %0\n\t"
                   "v_accvgpr_read_b32 %1, a139\n\tv_mul_f32 %1, %3, %1\n\tv_accvgpr_write_b32 a139, %1\n\t"
                   "v_accvgpr_read_b32 %0, a140\n\tv_mul_f32 %0, %3, %0\n\tv_accvgpr_write_b32 a140, %0\n\t"
                   "v_accvgpr_read_b32 %1, a141\n\tv_mul_f32 %1, %3, %1\n\tv_accvgpr_write_b32 a141, %1\n\t"
                   "v_accvgpr_read_b32 %0, a142\n\tv_mul_f32 %0, %3, %0\n\tv_accvgpr_write_b32 a142, %0\n\t"
                   "v_accvgpr_read_b32 %1, a143\n\tv_mul_f32 %1, %3, %1\n\tv_accvgpr_write_b32 a143, %1\n\t"
                   "v_accvgpr_read_b32 %0, a144\n\tv_mul_f32 %0, %3, %0\n\tv_accvgpr_write_b32 a144, %0\n\t"
                   "v_accvgpr_read_b32 %1, a145\n\tv_mul_f32 %1, %3, %1\n\tv_accvgpr_write_b32 a145, %1\n\t"
                   "v_accvgpr_read_b32 %0, a146\n\tv_mul_f32 %0, %3, %0\n\tv_accvgpr_write_b32 a146, %0\n\t"
                   "v_accvgpr_read_b32 %1, a147\n\tv_mul_f32 %1, %3, %1\n\tv_accvgpr_write_b32 a147, %1\n\t"
                   "v_accvgpr_read_b32 %0, a148\n\tv_mul_f32 %0, %3, %0\n\tv_accvgpr_write_b32 a148, %0\n\t"
                   "v_accvgpr_read_b32 %1, a149\n\tv_mul_f32 %1, %3, %1\n\tv_accvgpr_write_b32 a149, %1\n\t"
                   "v_accvgpr_read_b32 %0, a150\n\tv_mul_f32 %0, %3, %0\n\tv_accvgpr_write_b32 a150, %0\n\t"
                   "v_accvgpr_read_b32 %1, a151\n\tv_mul_f32 %1, %3, %1\n\tv_accvgpr_write_b32 a151, %1\n\t"
                   "v_accvgpr_read_b32 %0, a152\n\tv_mul_f32 %0, %3, %0\n\tv_accvgpr_write_b32 a152, %0\n\t"
                   "v_accvgpr_read_b32 %1, a153\n\tv_mul_f32 %1, %3, %1\n\tv_accvgpr_write_b32 a153, %1\n\t"
                   "v_accvgpr_read_b32 %0, a154\n\tv_mul_f32 %0, %3, %0\n\tv_accvgpr_write_b32 a154, %0\n\t"
                   "v_accvgpr_read_b32 %1, a155\n\tv_mul_f32 %1, %3, %1\n\tv_accvgpr_write_b32 a155, %1\n\t"
                   "v_accvgpr_read_b32 %0, a156\n\tv_mul_f32 %0, %3, %0\n\tv_accvgpr_write_b32 a156, %0\n\t"
                   "v_accvgpr_read_b32 %1, a157\n\tv_mul_f32 %1, %3, %1\n\tv_accvgpr_write_b32 a157, %1\n\t"
                   "v_accvgpr_read_b32 %0, a158\n\tv_mul_f32 %0, %3, %0\n\tv_accvgpr_write_b32 a158, %0\n\t"
                   "v_accvgpr_read_b32 %1, a159\n\tv_mul_f32 %1, %3, %1\n\tv_accvgpr_write_b32 a159, %1\n\t"
                   "v_accvgpr_read_b32 %0, a160\n\tv_mul_f32 %0, %3, %0\n\tv_accvgpr_write_b32 a160, %0\n\t"
                   "v_accvgpr_read_b32 %1, a161\n\tv_mul_f32 %1, %3, %1\n\tv_accvgpr_write_b32 a161, %1\n\t"
                   "v_accvgpr_read_b32 %0, a162\n\tv_mul_f32 %0, %3, %0\n\tv_accvgpr_write_b32 a162, %0\n\t"
                   "v_accvgpr_read_b32 %1, a163\n\tv_mul_f32 %1, %3, %1\n\tv_accvgpr_write_b32 a163, %1\n\t"
                   "v_accvgpr_read_b32 %0, a164\n\tv_mul_f32 %0, %3, %0\n\tv_accvgpr_write_b32 a164, %0\n\t"
                   "v_accvgpr_read_b32 %1, a165\n\tv_mul_f32 %1, %3, %1\n\tv_accvgpr_write_b32 a165, %1\n\t"
                   "v_accvgpr_read_b32 %0, a166\n\tv_mul_f32 %0, %3, %0\n\tv_accvgpr_write_b32 a166, %0\n\t"
                   "v_accvgpr_read_b32 %1, a167\n\tv_mul_f32 %1, %3, %1\n\tv_accvgpr_write_b32 a167, %1\n\t"
                   "v_accvgpr_read_b32 %0, a168\n\tv_mul_f32 %0, %3, %0\n\tv_accvgpr_write_b32 a168, %0\n\t"
                   "v_accvgpr_read_b32 %1, a169\n\tv_mul_f32 %1, %3, %1\n\tv_accvgpr_write_b32 a169, %1\n\t"
                   "v_accvgpr_read_b32 %0, a170\n\tv_mul_f32 %0, %3, %0\n\tv_accvgpr_write_b32 a170, %0\n\t"
                   "v_accvgpr_read_b32 %1, a171\n\tv_mul_f32 %1, %3, %1\n\tv_accvgpr_write_b32 a171, %1\n\t"
                   "v_accvgpr_read_b32 %0, a172\n\tv_mul_f32 %0, %3, %0\n\tv_accvgpr_write_b32 a172, %0\n\t"
                   "v_accvgpr_read_b32 %1, a173\n\tv_mul_f32 %1, %3, %1\n\tv_accvgpr_write_b32 a173, %1\n\t"
                   "v_accvgpr_read_b32 %0, a174\n\tv_mul_f32 %0, %3, %0\n\tv_accvgpr_write_b32 a174, %0\n\t"
                   "v_accvgpr_read_b32 %1, a175\n\tv_mul_f32 %1, %3, %1\n\tv_accvgpr_write_b32 a175, %1\n\t"
                   "v_accvgpr_read_b32 %0, a176\n\tv_mul_f32 %0, %3, %0\n\tv_accvgpr_write_b32 a176, %0\n\t"
                   "v_accvgpr_read_b32 %1, a177\n\tv_mul_f32 %1, %3, %1\n\tv_accvgpr_write_b32 a177, %1\n\t"
                   "v_accvgpr_read_b32 %0, a178\n\tv_mul_f32 %0, %3, %0\n\tv_accvgpr_write_b32 a178, %0\n\t"
                   "v_accvgpr_read_b32 %1, a179\n\tv_mul_f32 %1, %3, %1\n\tv_accvgpr_write_b32 a179, %1\n\t"
                   "v_accvgpr_read_b32 %0, a180\n\tv_mul_f32 %0, %3, %0\n\tv_accvgpr_write_b32 a180, %0\n\t"
                   "v_accvgpr_read_b32 %1, a181\n\tv_mul_f32 %1, %3, %1\n\tv_accvgpr_write_b32 a181, %1\n\t"
                   "v_accvgpr_read_b32 %0, a182\n\tv_mul_f32 %0, %3, %0\n\tv_accvgpr_write_b32 a182, %0\n\t"
                   "v_accvgpr_read_b32 %1, a183\n\tv_mul_f32 %1, %3, %1\n\tv_accvgpr_write_b32 a183, %1\n\t"
                   "v_accvgpr_read_b32 %0, a184\n\tv_mul_f32 %0, %3, %0\n\tv_accvgpr_write_b32 a184, %0\n\t"
                   "v_accvgpr_read_b32 %1, a185\n\tv_mul_f32 %1, %3, %1\n\tv_accvgpr_write_b32 a185, %1\n\t"
                   "v_accvgpr_read_b32 %0, a186\n\tv_mul_f32 %0, %3, %0\n\tv_accvgpr_write_b32 a186, %0\n\t"
                   "v_accvgpr_read_b32 %1, a187\n\tv_mul_f32 %1, %3, %1\n\tv_accvgpr_write_b32 a187, %1\n\t"
                   "v_accvgpr_read_b32 %0, a188\n\tv_mul_f32 %0, %3, %0\n\tv_accvgpr_write_b32 a188, %0\n\t"
                   "v_accvgpr_read_b32 %1, a189\n\tv_mul_f32 %1, %3, %1\n\tv_accvgpr_write_b32 a189, %1\n\t"
                   "v_accvgpr_read_b32 %0, a190\n\tv_mul_f32 %0, %3, %0\n\tv_accvgpr_write_b32 a190, %0\n\t"
                   "v_accvgpr_read_b32 %1, a191\n\tv_mul_f32 %1, %3, %1\n\tv_accvgpr_write_b32 a191, %1\n\t"
                   "v_accvgpr_read_b32 %0, a192\n\tv_mul_f32 %0, %3, %0\n\tv_accvgpr_write_b32 a192, %0\n\t"
                   "v_accvgpr_read_b32 %1, a193\n\tv_mul_f32 %1, %3, %1\n\tv_accvgpr_write_b32 a193, %1\n\t"
                   "v_accvgpr_read_b32 %0, a194\n\tv_mul_f32 %0, %3, %0\n\tv_accvgpr_write_b32 a194, %0\n\t"
                   "v_accvgpr_read_b32 %1, a195\n\tv_mul_f32 %1, %3, %1\n\tv_accvgpr_write_b32 a195, %1\n\t"
                   "v_accvgpr_read_b32 %0, a196\n\tv_mul_f32 %0, %3, %0\n\tv_accvgpr_write_b32 a196, %0\n\t"
                   "v_accvgpr_read_b32 %1, a197\n\tv_mul_f32 %1, %3, %1\n\tv_accvgpr_write_b32 a197, %1\n\t"
                   "v_accvgpr_read_b32 %0, a198\n\tv_mul_f32 %0, %3, %0\n\tv_accvgpr_write_b32 a198, %0\n\t"
                   "v_accvgpr_read_b32 %1, a199\n\tv_mul_f32 %1, %3, %1\n\tv_accvgpr_write_b32 a199, %1\n\t"
                   "v_accvgpr_read_b32 %0, a200\n\tv_mul_f32 %0, %3, %0\n\tv_accvgpr_write_b32 a200, %0\n\t"
                   "v_accvgpr_read_b32 %1, a201\n\tv_mul_f32 %1, %3, %1\n\tv_accvgpr_write_b32 a201, %1\n\t"
                   "v_accvgpr_read_b32 %0, a202\n\tv_mul_f32 %0, %3, %0\n\tv_accvgpr_write_b32 a202, %0\n\t"
                   "v_accvgpr_read_b32 %1, a203\n\tv_mul_f32 %1, %3, %1\n\tv_accvgpr_write_b32 a203, %1\n\t"
                   "v_accvgpr_read_b32 %0, a204\n\tv_mul_f32 %0, %3, %0\n\tv_accvgpr_write_b32 a204, %0\n\t"
                   "v_accvgpr_read_b32 %1, a205\n\tv_mul_f32 %1, %3, %1\n\tv_accvgpr_write_b32 a205, %1\n\t"
                   "v_accvgpr_read_b32 %0, a206\n\tv_mul_f32 %0, %3, %0\n\tv_accvgpr_write_b32 a206, %0\n\t"
                   "v_accvgpr_read_b32 %1, a207\n\tv_mul_f32 %1, %3, %1\n\tv_accvgpr_write_b32 a207, %1\n\t"
                   "v_accvgpr_read_b32 %0, a208\n\tv_mul_f32 %0, %3, %0\n\tv_accvgpr_write_b32 a208, %0\n\t"
                   "v_accvgpr_read_b32 %1, a209\n\tv_mul_f32 %1, %3, %1\n\tv_accvgpr_write_b32 a209, %1\n\t"
                   "v_accvgpr_read_b32 %0, a210\n\tv_mul_f32 %0, %3, %0\n\tv_accvgpr_write_b32 a210, %0\n\t"
                   "v_accvgpr_read_b32 %1, a211\n\tv_mul_f32 %1, %3, %1\n\tv_accvgpr_write_b32 a211, %1\n\t"
                   "v_accvgpr_read_b32 %0, a212\n\tv_mul_f32 %0, %3, %0\n\tv_accvgpr_write_b32 a212, %0\n\t"
                   "v_accvgpr_read_b32 %1, a213\n\tv_mul_f32 %1, %3, %1\n\tv_accvgpr_write_b32 a213, %1\n\t"
                   "v_accvgpr_read_b32 %0, a214\n\tv_mul_f32 %0, %3, %0\n\tv_accvgpr_write_b32 a214, %0\n\t"
                   "v_accvgpr_read_b32 %1, a215\n\tv_mul_f32 %1, %3, %1\n\tv_accvgpr_write_b32 a215, %1\n\t"
                   "v_accvgpr_read_b32 %0, a216\n\tv_mul_f32 %0, %3, %0\n\tv_accvgpr_write_b32 a216, %0\n\t"
                   "v_accvgpr_read_b32 %1, a217\n\tv_mul_f32 %1, %3, %1\n\tv_accvgpr_write_b32 a217, %1\n\t"
                   "v_accvgpr_read_b32 %0, a218\n\tv_mul_f32 %0, %3, %0\n\tv_accvgpr_write_b32 a218, %0\n\t"
                   "v_accvgpr_read_b32 %1, a219\n\tv_mul_f32 %1, %3, %1\n\tv_accvgpr_write_b32 a219, %1\n\t"
                   "v_accvgpr_read_b32 %0, a220\n\tv_mul_f32 %0, %3, %0\n\tv_accvgpr_write_b32 a220, %0\n\t"
                   "v_accvgpr_read_b32 %1, a221\n\tv_mul_f32 %1, %3, %1\n\tv_accvgpr_write_b32 a221, %1\n\t"
                   "v_accvgpr_read_b32 %0, a222\n\tv_mul_f32 %0, %3, %0\n\tv_accvgpr_write_b32 a222, %0\n\t"
                   "v_accvgpr_read_b32 %1, a223\n\tv_mul_f32 %1, %3, %1\n\tv_accvgpr_write_b32 a223, %1\n\t"
                   "v_accvgpr_read_b32 %0, a224\n\tv_mul_f32 %0, %3, %0\n\tv_accvgpr_write_b32 a224, %0\n\t"
                   "v_accvgpr_read_b32 %1, a225\n\tv_mul_f32 %1, %3, %1\n\tv_accvgpr_write_b32 a225, %1\n\t"
                   "v_accvgpr_read_b32 %0, a226\n\tv_mul_f32 %0, %3, %0\n\tv_accvgpr_write_b32 a226, %0\n\t"
                   "v_accvgpr_read_b32 %1, a227\n\tv_mul_f32 %1, %3, %1\n\tv_accvgpr_write_b32 a227, %1\n\t"
                   "v_accvgpr_read_b32 %0, a228\n\tv_mul_f32 %0, %3, %0\n\tv_accvgpr_write_b32 a228, %0\n\t"
                   "v_accvgpr_read_b32 %1, a229\n\tv_mul_f32 %1, %3, %1\n\tv_accvgpr_write_b32 a229, %1\n\t"
                   "v_accvgpr_read_b32 %0, a230\n\tv_mul_f32 %0, %3, %0\n\tv_accvgpr_write_b32 a230, %0\n\t"
                   "v_accvgpr_read_b32 %1, a231\n\tv_mul_f32 %1, %3, %1\n\tv_accvgpr_write_b32 a231, %1\n\t"
                   "v_accvgpr_read_b32 %0, a232\n\tv_mul_f32 %0, %3, %0\n\tv_accvgpr_write_b32 a232, %0\n\t"
                   "v_accvgpr_read_b32 %1, a233\n\tv_mul_f32 %1, %3, %1\n\tv_accvgpr_write_b32 a233, %1\n\t"
                   "v_accvgpr_read_b32 %0, a234\n\tv_mul_f32 %0, %3, %0\n\tv_accvgpr_write_b32 a234, %0\n\t"
                   "v_accvgpr_read_b32 %1, a235\n\tv_mul_f32 %1, %3, %1\n\tv_accvgpr_write_b32 a235, %1\n\t"
                   "v_accvgpr_read_b32 %0, a236\n\tv_mul_f32 %0, %3, %0\n\tv_accvgpr_write_b32 a236, %0\n\t"
                   "v_accvgpr_read_b32 %1, a237\n\tv_mul_f32 %1, %3, %1\n\tv_accvgpr_write_b32 a237, %1\n\t"
                   "v_accvgpr_read_b32 %0, a238\n\tv_mul_f32 %0, %3, %0\n\tv_accvgpr_write_b32 a238, %0\n\t"
                   "v_accvgpr_read_b32 %1, a239\n\tv_mul_f32 %1, %3, %1\n\tv_accvgpr_write_b32 a239, %1\n\t"
                   "v_accvgpr_read_b32 %0, a240\n\tv_mul_f32 %0, %3, %0\n\tv_accvgpr_write_b32 a240, %0\n\t"
                   "v_accvgpr_read_b32 %1, a241\n\tv_mul_f32 %1, %3, %1\n\tv_accvgpr_write_b32 a241, %1\n\t"
                   "v_accvgpr_read_b32 %0, a242\n\tv_mul_f32 %0, %3, %0\n\tv_accvgpr_write_b32 a242, %0\n\t"
                   "v_accvgpr_read_b32 %1, a243\n\tv_mul_f32 %1, %3, %1\n\tv_accvgpr_write_b32 a243, %1\n\t"
                   "v_accvgpr_read_b32 %0, a244\n\tv_mul_f32 %0, %3, %0\n\tv_accvgpr_write_b32 a244, %0\n\t"
                   "v_accvgpr_read_b32 %1, a245\n\tv_mul_f32 %1, %3, %1\n\tv_accvgpr_write_b32 a245, %1\n\t"
                   "v_accvgpr_read_b32 %0, a246\n\tv_mul_f32 %0, %3, %0\n\tv_accvgpr_write_b32 a246, %0\n\t"
                   "v_accvgpr_read_b32 %1, a247\n\tv_mul_f32 %1, %3, %1\n\tv_accvgpr_write_b32 a247, %1\n\t"
                   "v_accvgpr_read_b32 %0, a248\n\tv_mul_f32 %0, %3, %0\n\tv_accvgpr_write_b32 a248, %0\n\t"
                   "v_accvgpr_read_b32 %1, a249\n\tv_mul_f32 %1, %3, %1\n\tv_accvgpr_write_b32 a249, %1\n\t"
                   "v_accvgpr_read_b32 %0, a250\n\tv_mul_f32 %0, %3, %0\n\tv_accvgpr_write_b32 a250, %0\n\t"
                   "v_accvgpr_read_b32 %1, a251\n\tv_mul_f32 %1, %3, %1\n\tv_accvgpr_write_b32 a251, %1\n\t"
                   "v_accvgpr_read_b32 %0, a252\n\tv_mul_f32 %0, %3, %0\n\tv_accvgpr_write_b32 a252, %0\n\t"
                   "v_accvgpr_read_b32 %1, a253\n\tv_mul_f32 %1, %3, %1\n\tv_accvgpr_write_b32 a253, %1\n\t"
                   "v_accvgpr_read_b32 %0, a254\n\tv_mul_f32 %0, %3, %0\n\tv_accvgpr_write_b32 a254, %0\n\t"
                   "v_accvgpr_read_b32 %1, a255\n\tv_mul_f32 %1, %3, %1\n\tv_accvgpr_write_b32 a255, %1\n\t"
                   "s_nop 1"
                   : "=&v"(t0), "=&v"(t1) : "v"(c.fct[0]), "v"(c.fct[1]) : ACLOB);
      c.pend = false;
    }
  }
  asm volatile("s_nop 1" ::: "memory");
  GAP();
}
template <int NS, int T, bool HASPV, bool HASSM> DI void phaseB(Ctx4& c) {
  phaseB_<NS, T, HASPV, HASSM>(c, std::make_integer_sequence<int, 32>{});
}
template <int NS, int... Us> DI void first_units(const Ctx4& c, std::integer_sequence<int, Us...>) { (dma_unit<NS, Us>(c), ...); }
template <int NS> DI void main_loop(Ctx4& c, const u16* __restrict__ wo, int bid, int w, int lane) {
  first_units<NS>(c, std::make_integer_sequence<int, NS>{});
  phaseA<NS, 0>(c); phaseB<NS, 0, false, true>(c);
  phaseA<NS, 1>(c); phaseB<NS, 1, true, true>(c);
  phaseA<NS, 2>(c); phaseB<NS, 2, true, true>(c);
  phaseA<NS, 3>(c); phaseB<NS, 3, true, true>(c);
  phaseA<NS, 4>(c); phaseB<NS, 4, true, true>(c);
  phaseA<NS, 5>(c); phaseB<NS, 5, true, true>(c);
  phaseA<NS, 6>(c); phaseB<NS, 6, true, true>(c);
  {
    int li = (bid * 4 + w) * 28 + (lane < 28 ? lane : 27);
    asm volatile("global_load_dword %0, %1, off" : "+v"(c.pfd) : "v"(wo + (size_t)li * 64) : "memory");
  }
  phaseA<NS, 7>(c); phaseB<NS, 7, true, true>(c);
  phaseB<NS, 8, true, false>(c);
}

template <int A, int OFF> DI void acc_dswrite4(unsigned addr) {
  asm volatile("ds_write_b128 %0, a[%c1:%c2] offset:%c3" ::"v"(addr), "i"(A), "i"(A + 3), "i"(OFF) : "memory", ACLOB);
}
template <int A> DI f32x4 acc_read4() {
  f32x4 r;
  asm volatile("v_accvgpr_read_b32 %0, a[%c4]\n\tv_accvgpr_read_b32 %1, a[%c5]\n\tv_accvgpr_read_b32 %2, a[%c6]\n\t"
               "v_accvgpr_read_b32 %3, a[%c7]"
               : "=v"(r[0]), "=v"(r[1]), "=v"(r[2]), "=v"(r[3]) : "i"(A), "i"(A + 1), "i"(A + 2), "i"(A + 3) : ACLOB);
  return r;
}

__global__ __launch_bounds__(256, 1) void attn4_kernel(const u16* __restrict__ x, const u16* __restrict__ wq,
                                                       const u16* __restrict__ kc, const u16* __restrict__ vc,
                                                       const u16* __restrict__ wo, u16* __restrict__ o,
                                                       unsigned* __restrict__ cnt DBGARG) {
  extern __shared__ __attribute__((aligned(16))) char lds[];
  STAMP(0);
  if (blockIdx.x == 0 && threadIdx.x < 128) cnt[threadIdx.x] = 0u;
  const int tid = threadIdx.x, lane = tid & 63;
  const int w = __builtin_amdgcn_readfirstlane(tid >> 6);
  const int r = lane & 31, h = lane >> 5;
  const int bid = blockIdx.x;
  const int xcd = bid & 7, j = bid >> 3;
  const int grp = (j >> 2) * 8 + xcd, sub = j & 3;
  const int b = grp >> 3, kvh = grp & 7;
  const int head = kvh * 2 + (sub >> 1), rhalf = sub & 1;
  const int row0 = b * SQ + rhalf * 56;

  const u16* Kw = kc + ((size_t)(b * HKV + kvh) * SK + 256 * w) * DH;
  const u16* Vw = vc + ((size_t)(b * HKV + kvh) * SK + 256 * w) * DH;

  Ctx4 c;
  c.pfd = 0; c.sub = sub;
  c.Kp = Kw; c.Vp = Vw; c.pfoff = lane * 128;
  c.ring = lds + w * 40960;
  c.rk = __builtin_amdgcn_make_buffer_rsrc((void*)Kw, 0, 256 * DH * 2, 0x00020000);
  c.rv = __builtin_amdgcn_make_buffer_rsrc((void*)Vw, 0, 256 * DH * 2, 0x00020000);
#pragma unroll
  for (int pi = 0; pi < 4; ++pi) {
    const int krow = 4 * pi + (lane >> 4);
    c.koff[pi] = krow * 512 + (((lane & 15) ^ (krow & 15)) * 16);
  }
#pragma unroll
  for (int pi = 0; pi < 2; ++pi) {
    const int vrow = 2 * pi + (lane >> 5);
    c.voff[pi] = vrow * 512 + (((lane & 31) ^ ((vrow & 3) << 2)) * 16);
  }
  {
    __amdgpu_buffer_rsrc_t rx =
        __builtin_amdgcn_make_buffer_rsrc((void*)(x + (size_t)row0 * DM), 0, 56 * DM * 2, 0x00020000);
#pragma unroll
    for (int p = 0; p < 14; ++p) {
      const int ci = 64 * p + lane;
      const int lr = ci / 56, cp = ci - lr * 56;
      int gr = 16 * w + lr; gr = gr > 55 ? 55 : gr;
      const int cs = (cp & ~7) | ((cp ^ (lr >> 1)) & 7);
      __builtin_amdgcn_raw_ptr_buffer_load_lds(rx, (lds_vp)(c.ring + 3 * 8192 + p * 1024), 16, gr * (DM * 2) + cs * 16,
                                               0, 0, 0);
    }
  }
  {
    const int colw = head * DH + 64 * w;
    __amdgpu_buffer_rsrc_t rw =
        __builtin_amdgcn_make_buffer_rsrc((void*)(wq + colw), 0, (DM * NQ - colw) * 2, 0x00020000);
    const int drow = lane >> 3;
    const int wvoff = drow * (NQ * 2) + (((lane & 7) ^ (((drow >> 1) & 1) << 2)) * 16);
    auto wissue = [&](int ch) {
#pragma unroll
      for (int p = 0; p < 8; ++p)
        __builtin_amdgcn_raw_ptr_buffer_load_lds(rw, (lds_vp)(c.ring + ((ch + 2) % 3) * 8192 + p * 1024), 16, wvoff,
                                                 (64 * ch + 8 * p) * (NQ * 2), 0, 0);
    };
    wissue(0); wissue(1); wissue(2);
    const int g = lane >> 4, qq = (lane >> 2) & 3, pp = lane & 3;
    const unsigned trb = (unsigned)(size_t)c.ring + (8 * (g >> 1) + qq) * 128 + ((32 * (g & 1) + 8 * pp) ^ ((qq >> 1) << 6));
    const char* xa[2][4];
#pragma unroll
    for (int qb = 0; qb < 2; ++qb) {
      const int R = 32 * qb + r, lr = R & 15;
#pragma unroll
      for (int v = 0; v < 4; ++v)
        xa[qb][v] = lds + (R >> 4) * 40960 + 3 * 8192 + lr * 896 + ((((2 * v + h) ^ (lr >> 1)) & 7) * 16);
    }
    f32x16 qa[2][2];
#pragma unroll
    for (int a2 = 0; a2 < 2; ++a2)
#pragma unroll
      for (int b2 = 0; b2 < 2; ++b2)
#pragma unroll
        for (int i = 0; i < 16; ++i) qa[a2][b2][i] = 0.f;
    s16x4 fa[2][4];
    bf16x8 fx[2][2];
    auto rdstep = [&](int i, int bsel) {
      const int ch = i >> 2, s4 = i & 3;
      const unsigned tb = trb + ((ch + 2) % 3) * 8192 + s4 * 2048;
      fa[bsel][0] = trread(tb);
      fa[bsel][1] = trread(tb + 512);
      fa[bsel][2] = trread(tb ^ 64);
      fa[bsel][3] = trread((tb + 512) ^ 64);
      asm volatile("ds_read_b128 %0, %1" : "=v"(fx[bsel][0]) : "v"((unsigned)(size_t)(xa[0][s4] + ch * 128)) : "memory");
      asm volatile("ds_read_b128 %0, %1" : "=v"(fx[bsel][1]) : "v"((unsigned)(size_t)(xa[1][s4] + ch * 128)) : "memory");
    };
    auto refill_piece = [&](int k, int p) {
      if (k <= 3) {
        __builtin_amdgcn_raw_ptr_buffer_load_lds(rw, (lds_vp)(c.ring + ((k + 5) % 3) * 8192 + p * 1024), 16, wvoff,
                                                 (64 * (k + 3) + 8 * p) * (NQ * 2), 0, 0);
      }
    };
    WAITV(16);
    __builtin_amdgcn_s_barrier();
    rdstep(0, 0);
#pragma unroll
    for (int i = 0; i < 28; ++i) {
      const int bs = i & 1, ch = i >> 2, s4 = i & 3;
      if (i + 1 < 28) {
        if (s4 == 3) { if (ch == 0) WAITV(8); else if (ch <= 4) WAITV(6); else WAITV(0); }
        rdstep(i + 1, bs ^ 1);
        asm volatile("s_waitcnt lgkmcnt(6)" : "+v"(fa[bs][0]), "+v"(fa[bs][1]), "+v"(fa[bs][2]), "+v"(fa[bs][3]), "+v"(fx[bs][0]), "+v"(fx[bs][1])::"memory");
      } else {
        asm volatile("s_waitcnt lgkmcnt(0)" : "+v"(fa[bs][0]), "+v"(fa[bs][1]), "+v"(fa[bs][2]), "+v"(fa[bs][3]), "+v"(fx[bs][0]), "+v"(fx[bs][1])::"memory");
      }
      bf16x8 A0 = cat8(fa[bs][0], fa[bs][1]), A1 = cat8(fa[bs][2], fa[bs][3]);
      qa[0][0] = __builtin_amdgcn_mfma_f32_32x32x16_bf16(A0, fx[bs][0], qa[0][0], 0, 0, 0);
      GAP();
      if (ch >= 1) {
        if (ch - 1 <= 3) refill_piece(ch - 1, 2 * s4);
      }
      GAP();
      qa[0][1] = __builtin_amdgcn_mfma_f32_32x32x16_bf16(A1, fx[bs][0], qa[0][1], 0, 0, 0);
      qa[1][0] = __builtin_amdgcn_mfma_f32_32x32x16_bf16(A0, fx[bs][1], qa[1][0], 0, 0, 0);
      GAP();
      if (ch >= 1) {
        if (ch - 1 <= 3) refill_piece(ch - 1, 2 * s4 + 1);
      }
      GAP();
      qa[1][1] = __builtin_amdgcn_mfma_f32_32x32x16_bf16(A1, fx[bs][1], qa[1][1], 0, 0, 0);
    }
    STAMP(12);
    __builtin_amdgcn_s_barrier();
#pragma unroll
    for (int qb = 0; qb < 2; ++qb) {
      const int R = 32 * qb + r;
      char* base = lds + (R >> 4) * 40960 + 4 * 8192 + (R & 15) * 512 + 8 * h;
#pragma unroll
      for (int cb = 0; cb < 2; ++cb)
#pragma unroll
        for (int gg = 0; gg < 4; ++gg) {
          u32x2 v = {cvtpk(qa[qb][cb][4 * gg], qa[qb][cb][4 * gg + 1]), cvtpk(qa[qb][cb][4 * gg + 2], qa[qb][cb][4 * gg + 3])};
          *(u32x2*)(base + (((8 * w + 4 * cb + gg) ^ (R & 15)) * 16)) = v;
        }
    }
    WAITL0();
    __builtin_amdgcn_s_barrier();
  }
  {
#pragma unroll
    for (int qb = 0; qb < 2; ++qb) {
      const int R = 32 * qb + r;
      const char* base = lds + (R >> 4) * 40960 + 4 * 8192 + (R & 15) * 512;
#pragma unroll
      for (int s = 0; s < 16; ++s) c.qf[qb][s] = *(const bf16x8*)(base + (((2 * s + h) ^ (R & 15)) * 16));
    }
#pragma unroll
    for (int qb = 0; qb < 2; ++qb)
#pragma unroll
      for (int s = 0; s < 16; ++s) asm volatile("" : "+v"(c.qf[qb][s]));
  }
  WAITL0();
  __builtin_amdgcn_s_barrier();
  {
    int off = 0;
#pragma unroll
    for (int v = 0; v < 3; ++v) if (v < w) off += (v == sub) ? 65536 : 32768;
    c.ring = lds + off;
  }
  STAMP(1);

  {
    const unsigned rb = (unsigned)(size_t)c.ring;
#pragma unroll
    for (int ss = 0; ss < 8; ++ss) c.kaddr[ss] = rb + r * 256 + ((((2 * ss + h) ^ (r & 15))) * 16);
    const int g = lane >> 4, qq = (lane >> 2) & 3, pp = lane & 3;
#pragma unroll
    for (int d = 0; d < 4; ++d)
      c.vaddr[d] = rb + (4 * (g >> 1) + qq) * 512 + ((64 * d + 32 * (g & 1) + 8 * pp) ^ (qq << 6));
  }
  c.pend = false;
  c.fct[0] = 1.f; c.fct[1] = 1.f;

  if (w == sub) main_loop<8>(c, wo, bid, w, lane); else main_loop<4>(c, wo, bid, w, lane);
  STAMP(9);
  asm volatile("s_nop 7\n\ts_nop 7" ::: "memory");
  STAMP(13);

  __syncthreads();
  float* ml = (float*)lds;
#pragma unroll
  for (int qb = 0; qb < 2; ++qb) {
    auto rr = __builtin_amdgcn_permlane32_swap(__float_as_uint(c.l[qb]), __float_as_uint(c.l[qb]), false, false);
    const float lt = __uint_as_float(rr[0]) + __uint_as_float(rr[1]);
    if (h == 0) {
      ml[((w * 2 + qb) * 32 + r) * 2 + 0] = c.m[qb];
      ml[((w * 2 + qb) * 32 + r) * 2 + 1] = lt;
    }
  }
  __syncthreads();
  float fw[2][4];
#pragma unroll
  for (int qb = 0; qb < 2; ++qb) {
    float mm[4], ll[4];
#pragma unroll
    for (int ww = 0; ww < 4; ++ww) {
      mm[ww] = ml[((ww * 2 + qb) * 32 + r) * 2 + 0];
      ll[ww] = ml[((ww * 2 + qb) * 32 + r) * 2 + 1];
    }
    const float M = fmaxf(fmaxf(mm[0], mm[1]), fmaxf(mm[2], mm[3]));
    float e[4], L = 0.f;
#pragma unroll
    for (int ww = 0; ww < 4; ++ww) { e[ww] = __builtin_amdgcn_exp2f(mm[ww] - M); L += e[ww] * ll[ww]; }
    const float inv = 1.f / L;
#pragma unroll
    for (int ww = 0; ww < 4; ++ww) fw[qb][ww] = e[ww] * inv;
  }
  STAMP(14);
  const unsigned xw = (unsigned)(size_t)lds + 4096 + lane * 16;
  const f32x4* xr = (const f32x4*)(lds + 4096) + lane;
  char* otile = lds + 102400;
  {
    switch (w) {
      case 0: {
        acc_dswrite4<32, 0>(xw + 0);
        acc_dswrite4<36, 1024>(xw + 0);
        acc_dswrite4<40, 2048>(xw + 0);
        acc_dswrite4<44, 3072>(xw + 0);
        acc_dswrite4<160, 4096>(xw + 0);
        acc_dswrite4<164, 5120>(xw + 0);
        acc_dswrite4<168, 6144>(xw + 0);
        acc_dswrite4<172, 7168>(xw + 0);
        acc_dswrite4<64, 8192>(xw + 0);
        acc_dswrite4<68, 9216>(xw + 0);
        acc_dswrite4<72, 10240>(xw + 0);
        acc_dswrite4<76, 11264>(xw + 0);
        acc_dswrite4<192, 12288>(xw + 0);
        acc_dswrite4<196, 13312>(xw + 0);
        acc_dswrite4<200, 14336>(xw + 0);
        acc_dswrite4<204, 15360>(xw + 0);
        acc_dswrite4<96, 16384>(xw + 0);
        acc_dswrite4<100, 17408>(xw + 0);
        acc_dswrite4<104, 18432>(xw + 0);
        acc_dswrite4<108, 19456>(xw + 0);
        acc_dswrite4<224, 20480>(xw + 0);
        acc_dswrite4<228, 21504>(xw + 0);
        acc_dswrite4<232, 22528>(xw + 0);
        acc_dswrite4<236, 23552>(xw + 0);
      } break;
      case 1: {
        acc_dswrite4<0, 24576>(xw + 0);
        acc_dswrite4<4, 25600>(xw + 0);
        acc_dswrite4<8, 26624>(xw + 0);
        acc_dswrite4<12, 27648>(xw + 0);
        acc_dswrite4<128, 28672>(xw + 0);
        acc_dswrite4<132, 29696>(xw + 0);
        acc_dswrite4<136, 30720>(xw + 0);
        acc_dswrite4<140, 31744>(xw + 0);
        acc_dswrite4<64, 32768>(xw + 0);
        acc_dswrite4<68, 33792>(xw + 0);
        acc_dswrite4<72, 34816>(xw + 0);
        acc_dswrite4<76, 35840>(xw + 0);
        acc_dswrite4<192, 36864>(xw + 0);
        acc_dswrite4<196, 37888>(xw + 0);
        acc_dswrite4<200, 38912>(xw + 0);
        acc_dswrite4<204, 39936>(xw + 0);
        acc_dswrite4<96, 40960>(xw + 0);
        acc_dswrite4<100, 41984>(xw + 0);
        acc_dswrite4<104, 43008>(xw + 0);
        acc_dswrite4<108, 44032>(xw + 0);
        acc_dswrite4<224, 45056>(xw + 0);
        acc_dswrite4<228, 46080>(xw + 0);
        acc_dswrite4<232, 47104>(xw + 0);
        acc_dswrite4<236, 48128>(xw + 0);
      } break;
      case 2: {
        acc_dswrite4<0, 49152>(xw + 0);
        acc_dswrite4<4, 50176>(xw + 0);
        acc_dswrite4<8, 51200>(xw + 0);
        acc_dswrite4<12, 52224>(xw + 0);
        acc_dswrite4<128, 53248>(xw + 0);
        acc_dswrite4<132, 54272>(xw + 0);
        acc_dswrite4<136, 55296>(xw + 0);
        acc_dswrite4<140, 56320>(xw + 0);
        acc_dswrite4<32, 57344>(xw + 0);
        acc_dswrite4<36, 58368>(xw + 0);
        acc_dswrite4<40, 59392>(xw + 0);
        acc_dswrite4<44, 60416>(xw + 0);
        acc_dswrite4<160, 61440>(xw + 0);
        acc_dswrite4<164, 62464>(xw + 0);
        acc_dswrite4<168, 63488>(xw + 0);
        acc_dswrite4<172, 64512>(xw + 0);
        acc_dswrite4<96, 0>(xw + 65536);
        acc_dswrite4<100, 1024>(xw + 65536);
        acc_dswrite4<104, 2048>(xw + 65536);
        acc_dswrite4<108, 3072>(xw + 65536);
        acc_dswrite4<224, 4096>(xw + 65536);
        acc_dswrite4<228, 5120>(xw + 65536);
        acc_dswrite4<232, 6144>(xw + 65536);
        acc_dswrite4<236, 7168>(xw + 65536);
      } break;
      case 3: {
        acc_dswrite4<0, 8192>(xw + 65536);
        acc_dswrite4<4, 9216>(xw + 65536);
        acc_dswrite4<8, 10240>(xw + 65536);
        acc_dswrite4<12, 11264>(xw + 65536);
        acc_dswrite4<128, 12288>(xw + 65536);
        acc_dswrite4<132, 13312>(xw + 65536);
        acc_dswrite4<136, 14336>(xw + 65536);
        acc_dswrite4<140, 15360>(xw + 65536);
        acc_dswrite4<32, 16384>(xw + 65536);
        acc_dswrite4<36, 17408>(xw + 65536);
        acc_dswrite4<40, 18432>(xw + 65536);
        acc_dswrite4<44, 19456>(xw + 65536);
        acc_dswrite4<160, 20480>(xw + 65536);
        acc_dswrite4<164, 21504>(xw + 65536);
        acc_dswrite4<168, 22528>(xw + 65536);
        acc_dswrite4<172, 23552>(xw + 65536);
        acc_dswrite4<64, 24576>(xw + 65536);
        acc_dswrite4<68, 25600>(xw + 65536);
        acc_dswrite4<72, 26624>(xw + 65536);
        acc_dswrite4<76, 27648>(xw + 65536);
        acc_dswrite4<192, 28672>(xw + 65536);
        acc_dswrite4<196, 29696>(xw + 65536);
        acc_dswrite4<200, 30720>(xw + 65536);
        acc_dswrite4<204, 31744>(xw + 65536);
      } break;
    }
    WAITL0();
    __builtin_amdgcn_s_barrier();
    switch (w) {
      case 0: {
        { f32x4 sum = acc_read4<0>(); sum *= fw[0][0];
          sum += xr[1536] * fw[0][1];
          sum += xr[3072] * fw[0][2];
          sum += xr[4608] * fw[0][3];
          u32x2 v = {cvtpk(sum[0], sum[1]), cvtpk(sum[2], sum[3])};
          *(u32x2*)(otile + (0 + r) * 512 + (((0) ^ ((0 + r) & 31)) * 16) + 8 * h) = v; }
        { f32x4 sum = acc_read4<4>(); sum *= fw[0][0];
          sum += xr[1600] * fw[0][1];
          sum += xr[3136] * fw[0][2];
          sum += xr[4672] * fw[0][3];
          u32x2 v = {cvtpk(sum[0], sum[1]), cvtpk(sum[2], sum[3])};
          *(u32x2*)(otile + (0 + r) * 512 + (((1) ^ ((0 + r) & 31)) * 16) + 8 * h) = v; }
        { f32x4 sum = acc_read4<8>(); sum *= fw[0][0];
          sum += xr[1664] * fw[0][1];
          sum += xr[3200] * fw[0][2];
          sum += xr[4736] * fw[0][3];
          u32x2 v = {cvtpk(sum[0], sum[1]), cvtpk(sum[2], sum[3])};
          *(u32x2*)(otile + (0 + r) * 512 + (((2) ^ ((0 + r) & 31)) * 16) + 8 * h) = v; }
        { f32x4 sum = acc_read4<12>(); sum *= fw[0][0];
          sum += xr[1728] * fw[0][1];
          sum += xr[3264] * fw[0][2];
          sum += xr[4800] * fw[0][3];
          u32x2 v = {cvtpk(sum[0], sum[1]), cvtpk(sum[2], sum[3])};
          *(u32x2*)(otile + (0 + r) * 512 + (((3) ^ ((0 + r) & 31)) * 16) + 8 * h) = v; }
        { f32x4 sum = acc_read4<128>(); sum *= fw[1][0];
          sum += xr[1792] * fw[1][1];
          sum += xr[3328] * fw[1][2];
          sum += xr[4864] * fw[1][3];
          u32x2 v = {cvtpk(sum[0], sum[1]), cvtpk(sum[2], sum[3])};
          *(u32x2*)(otile + (32 + r) * 512 + (((0) ^ ((32 + r) & 31)) * 16) + 8 * h) = v; }
        { f32x4 sum = acc_read4<132>(); sum *= fw[1][0];
          sum += xr[1856] * fw[1][1];
          sum += xr[3392] * fw[1][2];
          sum += xr[4928] * fw[1][3];
          u32x2 v = {cvtpk(sum[0], sum[1]), cvtpk(sum[2], sum[3])};
          *(u32x2*)(otile + (32 + r) * 512 + (((1) ^ ((32 + r) & 31)) * 16) + 8 * h) = v; }
        { f32x4 sum = acc_read4<136>(); sum *= fw[1][0];
          sum += xr[1920] * fw[1][1];
          sum += xr[3456] * fw[1][2];
          sum += xr[4992] * fw[1][3];
          u32x2 v = {cvtpk(sum[0], sum[1]), cvtpk(sum[2], sum[3])};
          *(u32x2*)(otile + (32 + r) * 512 + (((2) ^ ((32 + r) & 31)) * 16) + 8 * h) = v; }
        { f32x4 sum = acc_read4<140>(); sum *= fw[1][0];
          sum += xr[1984] * fw[1][1];
          sum += xr[3520] * fw[1][2];
          sum += xr[5056] * fw[1][3];
          u32x2 v = {cvtpk(sum[0], sum[1]), cvtpk(sum[2], sum[3])};
          *(u32x2*)(otile + (32 + r) * 512 + (((3) ^ ((32 + r) & 31)) * 16) + 8 * h) = v; }
      } break;
      case 1: {
        { f32x4 sum = acc_read4<32>(); sum *= fw[0][1];
          sum += xr[0] * fw[0][0];
          sum += xr[3584] * fw[0][2];
          sum += xr[5120] * fw[0][3];
          u32x2 v = {cvtpk(sum[0], sum[1]), cvtpk(sum[2], sum[3])};
          *(u32x2*)(otile + (0 + r) * 512 + (((8) ^ ((0 + r) & 31)) * 16) + 8 * h) = v; }
        { f32x4 sum = acc_read4<36>(); sum *= fw[0][1];
          sum += xr[64] * fw[0][0];
          sum += xr[3648] * fw[0][2];
          sum += xr[5184] * fw[0][3];
          u32x2 v = {cvtpk(sum[0], sum[1]), cvtpk(sum[2], sum[3])};
          *(u32x2*)(otile + (0 + r) * 512 + (((9) ^ ((0 + r) & 31)) * 16) + 8 * h) = v; }
        { f32x4 sum = acc_read4<40>(); sum *= fw[0][1];
          sum += xr[128] * fw[0][0];
          sum += xr[3712] * fw[0][2];
          sum += xr[5248] * fw[0][3];
          u32x2 v = {cvtpk(sum[0], sum[1]), cvtpk(sum[2], sum[3])};
          *(u32x2*)(otile + (0 + r) * 512 + (((10) ^ ((0 + r) & 31)) * 16) + 8 * h) = v; }
        { f32x4 sum = acc_read4<44>(); sum *= fw[0][1];
          sum += xr[192] * fw[0][0];
          sum += xr[3776] * fw[0][2];
          sum += xr[5312] * fw[0][3];
          u32x2 v = {cvtpk(sum[0], sum[1]), cvtpk(sum[2], sum[3])};
          *(u32x2*)(otile + (0 + r) * 512 + (((11) ^ ((0 + r) & 31)) * 16) + 8 * h) = v; }
        { f32x4 sum = acc_read4<160>(); sum *= fw[1][1];
          sum += xr[256] * fw[1][0];
          sum += xr[3840] * fw[1][2];
          sum += xr[5376] * fw[1][3];
          u32x2 v = {cvtpk(sum[0], sum[1]), cvtpk(sum[2], sum[3])};
          *(u32x2*)(otile + (32 + r) * 512 + (((8) ^ ((32 + r) & 31)) * 16) + 8 * h) = v; }
        { f32x4 sum = acc_read4<164>(); sum *= fw[1][1];
          sum += xr[320] * fw[1][0];
          sum += xr[3904] * fw[1][2];
          sum += xr[5440] * fw[1][3];
          u32x2 v = {cvtpk(sum[0], sum[1]), cvtpk(sum[2], sum[3])};
          *(u32x2*)(otile + (32 + r) * 512 + (((9) ^ ((32 + r) & 31)) * 16) + 8 * h) = v; }
        { f32x4 sum = acc_read4<168>(); sum *= fw[1][1];
          sum += xr[384] * fw[1][0];
          sum += xr[3968] * fw[1][2];
          sum += xr[5504] * fw[1][3];
          u32x2 v = {cvtpk(sum[0], sum[1]), cvtpk(sum[2], sum[3])};
          *(u32x2*)(otile + (32 + r) * 512 + (((10) ^ ((32 + r) & 31)) * 16) + 8 * h) = v; }
        { f32x4 sum = acc_read4<172>(); sum *= fw[1][1];
          sum += xr[448] * fw[1][0];
          sum += xr[4032] * fw[1][2];
          sum += xr[5568] * fw[1][3];
          u32x2 v = {cvtpk(sum[0], sum[1]), cvtpk(sum[2], sum[3])};
          *(u32x2*)(otile + (32 + r) * 512 + (((11) ^ ((32 + r) & 31)) * 16) + 8 * h) = v; }
      } break;
      case 2: {
        { f32x4 sum = acc_read4<64>(); sum *= fw[0][2];
          sum += xr[512] * fw[0][0];
          sum += xr[2048] * fw[0][1];
          sum += xr[5632] * fw[0][3];
          u32x2 v = {cvtpk(sum[0], sum[1]), cvtpk(sum[2], sum[3])};
          *(u32x2*)(otile + (0 + r) * 512 + (((16) ^ ((0 + r) & 31)) * 16) + 8 * h) = v; }
        { f32x4 sum = acc_read4<68>(); sum *= fw[0][2];
          sum += xr[576] * fw[0][0];
          sum += xr[2112] * fw[0][1];
          sum += xr[5696] * fw[0][3];
          u32x2 v = {cvtpk(sum[0], sum[1]), cvtpk(sum[2], sum[3])};
          *(u32x2*)(otile + (0 + r) * 512 + (((17) ^ ((0 + r) & 31)) * 16) + 8 * h) = v; }
        { f32x4 sum = acc_read4<72>(); sum *= fw[0][2];
          sum += xr[640] * fw[0][0];
          sum += xr[2176] * fw[0][1];
          sum += xr[5760] * fw[0][3];
          u32x2 v = {cvtpk(sum[0], sum[1]), cvtpk(sum[2], sum[3])};
          *(u32x2*)(otile + (0 + r) * 512 + (((18) ^ ((0 + r) & 31)) * 16) + 8 * h) = v; }
        { f32x4 sum = acc_read4<76>(); sum *= fw[0][2];
          sum += xr[704] * fw[0][0];
          sum += xr[2240] * fw[0][1];
          sum += xr[5824] * fw[0][3];
          u32x2 v = {cvtpk(sum[0], sum[1]), cvtpk(sum[2], sum[3])};
          *(u32x2*)(otile + (0 + r) * 512 + (((19) ^ ((0 + r) & 31)) * 16) + 8 * h) = v; }
        { f32x4 sum = acc_read4<192>(); sum *= fw[1][2];
          sum += xr[768] * fw[1][0];
          sum += xr[2304] * fw[1][1];
          sum += xr[5888] * fw[1][3];
          u32x2 v = {cvtpk(sum[0], sum[1]), cvtpk(sum[2], sum[3])};
          *(u32x2*)(otile + (32 + r) * 512 + (((16) ^ ((32 + r) & 31)) * 16) + 8 * h) = v; }
        { f32x4 sum = acc_read4<196>(); sum *= fw[1][2];
          sum += xr[832] * fw[1][0];
          sum += xr[2368] * fw[1][1];
          sum += xr[5952] * fw[1][3];
          u32x2 v = {cvtpk(sum[0], sum[1]), cvtpk(sum[2], sum[3])};
          *(u32x2*)(otile + (32 + r) * 512 + (((17) ^ ((32 + r) & 31)) * 16) + 8 * h) = v; }
        { f32x4 sum = acc_read4<200>(); sum *= fw[1][2];
          sum += xr[896] * fw[1][0];
          sum += xr[2432] * fw[1][1];
          sum += xr[6016] * fw[1][3];
          u32x2 v = {cvtpk(sum[0], sum[1]), cvtpk(sum[2], sum[3])};
          *(u32x2*)(otile + (32 + r) * 512 + (((18) ^ ((32 + r) & 31)) * 16) + 8 * h) = v; }
        { f32x4 sum = acc_read4<204>(); sum *= fw[1][2];
          sum += xr[960] * fw[1][0];
          sum += xr[2496] * fw[1][1];
          sum += xr[6080] * fw[1][3];
          u32x2 v = {cvtpk(sum[0], sum[1]), cvtpk(sum[2], sum[3])};
          *(u32x2*)(otile + (32 + r) * 512 + (((19) ^ ((32 + r) & 31)) * 16) + 8 * h) = v; }
      } break;
      case 3: {
        { f32x4 sum = acc_read4<96>(); sum *= fw[0][3];
          sum += xr[1024] * fw[0][0];
          sum += xr[2560] * fw[0][1];
          sum += xr[4096] * fw[0][2];
          u32x2 v = {cvtpk(sum[0], sum[1]), cvtpk(sum[2], sum[3])};
          *(u32x2*)(otile + (0 + r) * 512 + (((24) ^ ((0 + r) & 31)) * 16) + 8 * h) = v; }
        { f32x4 sum = acc_read4<100>(); sum *= fw[0][3];
          sum += xr[1088] * fw[0][0];
          sum += xr[2624] * fw[0][1];
          sum += xr[4160] * fw[0][2];
          u32x2 v = {cvtpk(sum[0], sum[1]), cvtpk(sum[2], sum[3])};
          *(u32x2*)(otile + (0 + r) * 512 + (((25) ^ ((0 + r) & 31)) * 16) + 8 * h) = v; }
        { f32x4 sum = acc_read4<104>(); sum *= fw[0][3];
          sum += xr[1152] * fw[0][0];
          sum += xr[2688] * fw[0][1];
          sum += xr[4224] * fw[0][2];
          u32x2 v = {cvtpk(sum[0], sum[1]), cvtpk(sum[2], sum[3])};
          *(u32x2*)(otile + (0 + r) * 512 + (((26) ^ ((0 + r) & 31)) * 16) + 8 * h) = v; }
        { f32x4 sum = acc_read4<108>(); sum *= fw[0][3];
          sum += xr[1216] * fw[0][0];
          sum += xr[2752] * fw[0][1];
          sum += xr[4288] * fw[0][2];
          u32x2 v = {cvtpk(sum[0], sum[1]), cvtpk(sum[2], sum[3])};
          *(u32x2*)(otile + (0 + r) * 512 + (((27) ^ ((0 + r) & 31)) * 16) + 8 * h) = v; }
        { f32x4 sum = acc_read4<224>(); sum *= fw[1][3];
          sum += xr[1280] * fw[1][0];
          sum += xr[2816] * fw[1][1];
          sum += xr[4352] * fw[1][2];
          u32x2 v = {cvtpk(sum[0], sum[1]), cvtpk(sum[2], sum[3])};
          *(u32x2*)(otile + (32 + r) * 512 + (((24) ^ ((32 + r) & 31)) * 16) + 8 * h) = v; }
        { f32x4 sum = acc_read4<228>(); sum *= fw[1][3];
          sum += xr[1344] * fw[1][0];
          sum += xr[2880] * fw[1][1];
          sum += xr[4416] * fw[1][2];
          u32x2 v = {cvtpk(sum[0], sum[1]), cvtpk(sum[2], sum[3])};
          *(u32x2*)(otile + (32 + r) * 512 + (((25) ^ ((32 + r) & 31)) * 16) + 8 * h) = v; }
        { f32x4 sum = acc_read4<232>(); sum *= fw[1][3];
          sum += xr[1408] * fw[1][0];
          sum += xr[2944] * fw[1][1];
          sum += xr[4480] * fw[1][2];
          u32x2 v = {cvtpk(sum[0], sum[1]), cvtpk(sum[2], sum[3])};
          *(u32x2*)(otile + (32 + r) * 512 + (((26) ^ ((32 + r) & 31)) * 16) + 8 * h) = v; }
        { f32x4 sum = acc_read4<236>(); sum *= fw[1][3];
          sum += xr[1472] * fw[1][0];
          sum += xr[3008] * fw[1][1];
          sum += xr[4544] * fw[1][2];
          u32x2 v = {cvtpk(sum[0], sum[1]), cvtpk(sum[2], sum[3])};
          *(u32x2*)(otile + (32 + r) * 512 + (((27) ^ ((32 + r) & 31)) * 16) + 8 * h) = v; }
      } break;
    }
    WAITL0();
    __builtin_amdgcn_s_barrier();
  }
  {
    switch (w) {
      case 0: {
        acc_dswrite4<48, 0>(xw + 0);
        acc_dswrite4<52, 1024>(xw + 0);
        acc_dswrite4<56, 2048>(xw + 0);
        acc_dswrite4<60, 3072>(xw + 0);
        acc_dswrite4<176, 4096>(xw + 0);
        acc_dswrite4<180, 5120>(xw + 0);
        acc_dswrite4<184, 6144>(xw + 0);
        acc_dswrite4<188, 7168>(xw + 0);
        acc_dswrite4<80, 8192>(xw + 0);
        acc_dswrite4<84, 9216>(xw + 0);
        acc_dswrite4<88, 10240>(xw + 0);
        acc_dswrite4<92, 11264>(xw + 0);
        acc_dswrite4<208, 12288>(xw + 0);
        acc_dswrite4<212, 13312>(xw + 0);
        acc_dswrite4<216, 14336>(xw + 0);
        acc_dswrite4<220, 15360>(xw + 0);
        acc_dswrite4<112, 16384>(xw + 0);
        acc_dswrite4<116, 17408>(xw + 0);
        acc_dswrite4<120, 18432>(xw + 0);
        acc_dswrite4<124, 19456>(xw + 0);
        acc_dswrite4<240, 20480>(xw + 0);
        acc_dswrite4<244, 21504>(xw + 0);
        acc_dswrite4<248, 22528>(xw + 0);
        acc_dswrite4<252, 23552>(xw + 0);
      } break;
      case 1: {
        acc_dswrite4<16, 24576>(xw + 0);
        acc_dswrite4<20, 25600>(xw + 0);
        acc_dswrite4<24, 26624>(xw + 0);
        acc_dswrite4<28, 27648>(xw + 0);
        acc_dswrite4<144, 28672>(xw + 0);
        acc_dswrite4<148, 29696>(xw + 0);
        acc_dswrite4<152, 30720>(xw + 0);
        acc_dswrite4<156, 31744>(xw + 0);
        acc_dswrite4<80, 32768>(xw + 0);
        acc_dswrite4<84, 33792>(xw + 0);
        acc_dswrite4<88, 34816>(xw + 0);
        acc_dswrite4<92, 35840>(xw + 0);
        acc_dswrite4<208, 36864>(xw + 0);
        acc_dswrite4<212, 37888>(xw + 0);
        acc_dswrite4<216, 38912>(xw + 0);
        acc_dswrite4<220, 39936>(xw + 0);
        acc_dswrite4<112, 40960>(xw + 0);
        acc_dswrite4<116, 41984>(xw + 0);
        acc_dswrite4<120, 43008>(xw + 0);
        acc_dswrite4<124, 44032>(xw + 0);
        acc_dswrite4<240, 45056>(xw + 0);
        acc_dswrite4<244, 46080>(xw + 0);
        acc_dswrite4<248, 47104>(xw + 0);
        acc_dswrite4<252, 48128>(xw + 0);
      } break;
      case 2: {
        acc_dswrite4<16, 49152>(xw + 0);
        acc_dswrite4<20, 50176>(xw + 0);
        acc_dswrite4<24, 51200>(xw + 0);
        acc_dswrite4<28, 52224>(xw + 0);
        acc_dswrite4<144, 53248>(xw + 0);
        acc_dswrite4<148, 54272>(xw + 0);
        acc_dswrite4<152, 55296>(xw + 0);
        acc_dswrite4<156, 56320>(xw + 0);
        acc_dswrite4<48, 57344>(xw + 0);
        acc_dswrite4<52, 58368>(xw + 0);
        acc_dswrite4<56, 59392>(xw + 0);
        acc_dswrite4<60, 60416>(xw + 0);
        acc_dswrite4<176, 61440>(xw + 0);
        acc_dswrite4<180, 62464>(xw + 0);
        acc_dswrite4<184, 63488>(xw + 0);
        acc_dswrite4<188, 64512>(xw + 0);
        acc_dswrite4<112, 0>(xw + 65536);
        acc_dswrite4<116, 1024>(xw + 65536);
        acc_dswrite4<120, 2048>(xw + 65536);
        acc_dswrite4<124, 3072>(xw + 65536);
        acc_dswrite4<240, 4096>(xw + 65536);
        acc_dswrite4<244, 5120>(xw + 65536);
        acc_dswrite4<248, 6144>(xw + 65536);
        acc_dswrite4<252, 7168>(xw + 65536);
      } break;
      case 3: {
        acc_dswrite4<16, 8192>(xw + 65536);
        acc_dswrite4<20, 9216>(xw + 65536);
        acc_dswrite4<24, 10240>(xw + 65536);
        acc_dswrite4<28, 11264>(xw + 65536);
        acc_dswrite4<144, 12288>(xw + 65536);
        acc_dswrite4<148, 13312>(xw + 65536);
        acc_dswrite4<152, 14336>(xw + 65536);
        acc_dswrite4<156, 15360>(xw + 65536);
        acc_dswrite4<48, 16384>(xw + 65536);
        acc_dswrite4<52, 17408>(xw + 65536);
        acc_dswrite4<56, 18432>(xw + 65536);
        acc_dswrite4<60, 19456>(xw + 65536);
        acc_dswrite4<176, 20480>(xw + 65536);
        acc_dswrite4<180, 21504>(xw + 65536);
        acc_dswrite4<184, 22528>(xw + 65536);
        acc_dswrite4<188, 23552>(xw + 65536);
        acc_dswrite4<80, 24576>(xw + 65536);
        acc_dswrite4<84, 25600>(xw + 65536);
        acc_dswrite4<88, 26624>(xw + 65536);
        acc_dswrite4<92, 27648>(xw + 65536);
        acc_dswrite4<208, 28672>(xw + 65536);
        acc_dswrite4<212, 29696>(xw + 65536);
        acc_dswrite4<216, 30720>(xw + 65536);
        acc_dswrite4<220, 31744>(xw + 65536);
      } break;
    }
    WAITL0();
    __builtin_amdgcn_s_barrier();
    switch (w) {
      case 0: {
        { f32x4 sum = acc_read4<16>(); sum *= fw[0][0];
          sum += xr[1536] * fw[0][1];
          sum += xr[3072] * fw[0][2];
          sum += xr[4608] * fw[0][3];
          u32x2 v = {cvtpk(sum[0], sum[1]), cvtpk(sum[2], sum[3])};
          *(u32x2*)(otile + (0 + r) * 512 + (((4) ^ ((0 + r) & 31)) * 16) + 8 * h) = v; }
        { f32x4 sum = acc_read4<20>(); sum *= fw[0][0];
          sum += xr[1600] * fw[0][1];
          sum += xr[3136] * fw[0][2];
          sum += xr[4672] * fw[0][3];
          u32x2 v = {cvtpk(sum[0], sum[1]), cvtpk(sum[2], sum[3])};
          *(u32x2*)(otile + (0 + r) * 512 + (((5) ^ ((0 + r) & 31)) * 16) + 8 * h) = v; }
        { f32x4 sum = acc_read4<24>(); sum *= fw[0][0];
          sum += xr[1664] * fw[0][1];
          sum += xr[3200] * fw[0][2];
          sum += xr[4736] * fw[0][3];
          u32x2 v = {cvtpk(sum[0], sum[1]), cvtpk(sum[2], sum[3])};
          *(u32x2*)(otile + (0 + r) * 512 + (((6) ^ ((0 + r) & 31)) * 16) + 8 * h) = v; }
        { f32x4 sum = acc_read4<28>(); sum *= fw[0][0];
          sum += xr[1728] * fw[0][1];
          sum += xr[3264] * fw[0][2];
          sum += xr[4800] * fw[0][3];
          u32x2 v = {cvtpk(sum[0], sum[1]), cvtpk(sum[2], sum[3])};
          *(u32x2*)(otile + (0 + r) * 512 + (((7) ^ ((0 + r) & 31)) * 16) + 8 * h) = v; }
        { f32x4 sum = acc_read4<144>(); sum *= fw[1][0];
          sum += xr[1792] * fw[1][1];
          sum += xr[3328] * fw[1][2];
          sum += xr[4864] * fw[1][3];
          u32x2 v = {cvtpk(sum[0], sum[1]), cvtpk(sum[2], sum[3])};
          *(u32x2*)(otile + (32 + r) * 512 + (((4) ^ ((32 + r) & 31)) * 16) + 8 * h) = v; }
        { f32x4 sum = acc_read4<148>(); sum *= fw[1][0];
          sum += xr[1856] * fw[1][1];
          sum += xr[3392] * fw[1][2];
          sum += xr[4928] * fw[1][3];
          u32x2 v = {cvtpk(sum[0], sum[1]), cvtpk(sum[2], sum[3])};
          *(u32x2*)(otile + (32 + r) * 512 + (((5) ^ ((32 + r) & 31)) * 16) + 8 * h) = v; }
        { f32x4 sum = acc_read4<152>(); sum *= fw[1][0];
          sum += xr[1920] * fw[1][1];
          sum += xr[3456] * fw[1][2];
          sum += xr[4992] * fw[1][3];
          u32x2 v = {cvtpk(sum[0], sum[1]), cvtpk(sum[2], sum[3])};
          *(u32x2*)(otile + (32 + r) * 512 + (((6) ^ ((32 + r) & 31)) * 16) + 8 * h) = v; }
        { f32x4 sum = acc_read4<156>(); sum *= fw[1][0];
          sum += xr[1984] * fw[1][1];
          sum += xr[3520] * fw[1][2];
          sum += xr[5056] * fw[1][3];
          u32x2 v = {cvtpk(sum[0], sum[1]), cvtpk(sum[2], sum[3])};
          *(u32x2*)(otile + (32 + r) * 512 + (((7) ^ ((32 + r) & 31)) * 16) + 8 * h) = v; }
      } break;
      case 1: {
        { f32x4 sum = acc_read4<48>(); sum *= fw[0][1];
          sum += xr[0] * fw[0][0];
          sum += xr[3584] * fw[0][2];
          sum += xr[5120] * fw[0][3];
          u32x2 v = {cvtpk(sum[0], sum[1]), cvtpk(sum[2], sum[3])};
          *(u32x2*)(otile + (0 + r) * 512 + (((12) ^ ((0 + r) & 31)) * 16) + 8 * h) = v; }
        { f32x4 sum = acc_read4<52>(); sum *= fw[0][1];
          sum += xr[64] * fw[0][0];
          sum += xr[3648] * fw[0][2];
          sum += xr[5184] * fw[0][3];
          u32x2 v = {cvtpk(sum[0], sum[1]), cvtpk(sum[2], sum[3])};
          *(u32x2*)(otile + (0 + r) * 512 + (((13) ^ ((0 + r) & 31)) * 16) + 8 * h) = v; }
        { f32x4 sum = acc_read4<56>(); sum *= fw[0][1];
          sum += xr[128] * fw[0][0];
          sum += xr[3712] * fw[0][2];
          sum += xr[5248] * fw[0][3];
          u32x2 v = {cvtpk(sum[0], sum[1]), cvtpk(sum[2], sum[3])};
          *(u32x2*)(otile + (0 + r) * 512 + (((14) ^ ((0 + r) & 31)) * 16) + 8 * h) = v; }
        { f32x4 sum = acc_read4<60>(); sum *= fw[0][1];
          sum += xr[192] * fw[0][0];
          sum += xr[3776] * fw[0][2];
          sum += xr[5312] * fw[0][3];
          u32x2 v = {cvtpk(sum[0], sum[1]), cvtpk(sum[2], sum[3])};
          *(u32x2*)(otile + (0 + r) * 512 + (((15) ^ ((0 + r) & 31)) * 16) + 8 * h) = v; }
        { f32x4 sum = acc_read4<176>(); sum *= fw[1][1];
          sum += xr[256] * fw[1][0];
          sum += xr[3840] * fw[1][2];
          sum += xr[5376] * fw[1][3];
          u32x2 v = {cvtpk(sum[0], sum[1]), cvtpk(sum[2], sum[3])};
          *(u32x2*)(otile + (32 + r) * 512 + (((12) ^ ((32 + r) & 31)) * 16) + 8 * h) = v; }
        { f32x4 sum = acc_read4<180>(); sum *= fw[1][1];
          sum += xr[320] * fw[1][0];
          sum += xr[3904] * fw[1][2];
          sum += xr[5440] * fw[1][3];
          u32x2 v = {cvtpk(sum[0], sum[1]), cvtpk(sum[2], sum[3])};
          *(u32x2*)(otile + (32 + r) * 512 + (((13) ^ ((32 + r) & 31)) * 16) + 8 * h) = v; }
        { f32x4 sum = acc_read4<184>(); sum *= fw[1][1];
          sum += xr[384] * fw[1][0];
          sum += xr[3968] * fw[1][2];
          sum += xr[5504] * fw[1][3];
          u32x2 v = {cvtpk(sum[0], sum[1]), cvtpk(sum[2], sum[3])};
          *(u32x2*)(otile + (32 + r) * 512 + (((14) ^ ((32 + r) & 31)) * 16) + 8 * h) = v; }
        { f32x4 sum = acc_read4<188>(); sum *= fw[1][1];
          sum += xr[448] * fw[1][0];
          sum += xr[4032] * fw[1][2];
          sum += xr[5568] * fw[1][3];
          u32x2 v = {cvtpk(sum[0], sum[1]), cvtpk(sum[2], sum[3])};
          *(u32x2*)(otile + (32 + r) * 512 + (((15) ^ ((32 + r) & 31)) * 16) + 8 * h) = v; }
      } break;
      case 2: {
        { f32x4 sum = acc_read4<80>(); sum *= fw[0][2];
          sum += xr[512] * fw[0][0];
          sum += xr[2048] * fw[0][1];
          sum += xr[5632] * fw[0][3];
          u32x2 v = {cvtpk(sum[0], sum[1]), cvtpk(sum[2], sum[3])};
          *(u32x2*)(otile + (0 + r) * 512 + (((20) ^ ((0 + r) & 31)) * 16) + 8 * h) = v; }
        { f32x4 sum = acc_read4<84>(); sum *= fw[0][2];
          sum += xr[576] * fw[0][0];
          sum += xr[2112] * fw[0][1];
          sum += xr[5696] * fw[0][3];
          u32x2 v = {cvtpk(sum[0], sum[1]), cvtpk(sum[2], sum[3])};
          *(u32x2*)(otile + (0 + r) * 512 + (((21) ^ ((0 + r) & 31)) * 16) + 8 * h) = v; }
        { f32x4 sum = acc_read4<88>(); sum *= fw[0][2];
          sum += xr[640] * fw[0][0];
          sum += xr[2176] * fw[0][1];
          sum += xr[5760] * fw[0][3];
          u32x2 v = {cvtpk(sum[0], sum[1]), cvtpk(sum[2], sum[3])};
          *(u32x2*)(otile + (0 + r) * 512 + (((22) ^ ((0 + r) & 31)) * 16) + 8 * h) = v; }
        { f32x4 sum = acc_read4<92>(); sum *= fw[0][2];
          sum += xr[704] * fw[0][0];
          sum += xr[2240] * fw[0][1];
          sum += xr[5824] * fw[0][3];
          u32x2 v = {cvtpk(sum[0], sum[1]), cvtpk(sum[2], sum[3])};
          *(u32x2*)(otile + (0 + r) * 512 + (((23) ^ ((0 + r) & 31)) * 16) + 8 * h) = v; }
        { f32x4 sum = acc_read4<208>(); sum *= fw[1][2];
          sum += xr[768] * fw[1][0];
          sum += xr[2304] * fw[1][1];
          sum += xr[5888] * fw[1][3];
          u32x2 v = {cvtpk(sum[0], sum[1]), cvtpk(sum[2], sum[3])};
          *(u32x2*)(otile + (32 + r) * 512 + (((20) ^ ((32 + r) & 31)) * 16) + 8 * h) = v; }
        { f32x4 sum = acc_read4<212>(); sum *= fw[1][2];
          sum += xr[832] * fw[1][0];
          sum += xr[2368] * fw[1][1];
          sum += xr[5952] * fw[1][3];
          u32x2 v = {cvtpk(sum[0], sum[1]), cvtpk(sum[2], sum[3])};
          *(u32x2*)(otile + (32 + r) * 512 + (((21) ^ ((32 + r) & 31)) * 16) + 8 * h) = v; }
        { f32x4 sum = acc_read4<216>(); sum *= fw[1][2];
          sum += xr[896] * fw[1][0];
          sum += xr[2432] * fw[1][1];
          sum += xr[6016] * fw[1][3];
          u32x2 v = {cvtpk(sum[0], sum[1]), cvtpk(sum[2], sum[3])};
          *(u32x2*)(otile + (32 + r) * 512 + (((22) ^ ((32 + r) & 31)) * 16) + 8 * h) = v; }
        { f32x4 sum = acc_read4<220>(); sum *= fw[1][2];
          sum += xr[960] * fw[1][0];
          sum += xr[2496] * fw[1][1];
          sum += xr[6080] * fw[1][3];
          u32x2 v = {cvtpk(sum[0], sum[1]), cvtpk(sum[2], sum[3])};
          *(u32x2*)(otile + (32 + r) * 512 + (((23) ^ ((32 + r) & 31)) * 16) + 8 * h) = v; }
      } break;
      case 3: {
        { f32x4 sum = acc_read4<112>(); sum *= fw[0][3];
          sum += xr[1024] * fw[0][0];
          sum += xr[2560] * fw[0][1];
          sum += xr[4096] * fw[0][2];
          u32x2 v = {cvtpk(sum[0], sum[1]), cvtpk(sum[2], sum[3])};
          *(u32x2*)(otile + (0 + r) * 512 + (((28) ^ ((0 + r) & 31)) * 16) + 8 * h) = v; }
        { f32x4 sum = acc_read4<116>(); sum *= fw[0][3];
          sum += xr[1088] * fw[0][0];
          sum += xr[2624] * fw[0][1];
          sum += xr[4160] * fw[0][2];
          u32x2 v = {cvtpk(sum[0], sum[1]), cvtpk(sum[2], sum[3])};
          *(u32x2*)(otile + (0 + r) * 512 + (((29) ^ ((0 + r) & 31)) * 16) + 8 * h) = v; }
        { f32x4 sum = acc_read4<120>(); sum *= fw[0][3];
          sum += xr[1152] * fw[0][0];
          sum += xr[2688] * fw[0][1];
          sum += xr[4224] * fw[0][2];
          u32x2 v = {cvtpk(sum[0], sum[1]), cvtpk(sum[2], sum[3])};
          *(u32x2*)(otile + (0 + r) * 512 + (((30) ^ ((0 + r) & 31)) * 16) + 8 * h) = v; }
        { f32x4 sum = acc_read4<124>(); sum *= fw[0][3];
          sum += xr[1216] * fw[0][0];
          sum += xr[2752] * fw[0][1];
          sum += xr[4288] * fw[0][2];
          u32x2 v = {cvtpk(sum[0], sum[1]), cvtpk(sum[2], sum[3])};
          *(u32x2*)(otile + (0 + r) * 512 + (((31) ^ ((0 + r) & 31)) * 16) + 8 * h) = v; }
        { f32x4 sum = acc_read4<240>(); sum *= fw[1][3];
          sum += xr[1280] * fw[1][0];
          sum += xr[2816] * fw[1][1];
          sum += xr[4352] * fw[1][2];
          u32x2 v = {cvtpk(sum[0], sum[1]), cvtpk(sum[2], sum[3])};
          *(u32x2*)(otile + (32 + r) * 512 + (((28) ^ ((32 + r) & 31)) * 16) + 8 * h) = v; }
        { f32x4 sum = acc_read4<244>(); sum *= fw[1][3];
          sum += xr[1344] * fw[1][0];
          sum += xr[2880] * fw[1][1];
          sum += xr[4416] * fw[1][2];
          u32x2 v = {cvtpk(sum[0], sum[1]), cvtpk(sum[2], sum[3])};
          *(u32x2*)(otile + (32 + r) * 512 + (((29) ^ ((32 + r) & 31)) * 16) + 8 * h) = v; }
        { f32x4 sum = acc_read4<248>(); sum *= fw[1][3];
          sum += xr[1408] * fw[1][0];
          sum += xr[2944] * fw[1][1];
          sum += xr[4480] * fw[1][2];
          u32x2 v = {cvtpk(sum[0], sum[1]), cvtpk(sum[2], sum[3])};
          *(u32x2*)(otile + (32 + r) * 512 + (((30) ^ ((32 + r) & 31)) * 16) + 8 * h) = v; }
        { f32x4 sum = acc_read4<252>(); sum *= fw[1][3];
          sum += xr[1472] * fw[1][0];
          sum += xr[3008] * fw[1][1];
          sum += xr[4544] * fw[1][2];
          u32x2 v = {cvtpk(sum[0], sum[1]), cvtpk(sum[2], sum[3])};
          *(u32x2*)(otile + (32 + r) * 512 + (((31) ^ ((32 + r) & 31)) * 16) + 8 * h) = v; }
      } break;
    }
  }
  __syncthreads();
  asm volatile("s_waitcnt vmcnt(0)" :: "v"(c.pfd) : "memory");
  STAMP(15);
#pragma unroll
  for (int k = 0; k < 7; ++k) {
    const int R = 14 * w + 2 * k + h;
    u32x4 v = *(const u32x4*)(otile + R * 512 + ((r ^ (R & 31)) * 16));
    *(u32x4*)(o + ((size_t)(head * 8 + (r >> 2)) * NROW + row0 + R) * 32 + (r & 3) * 8) = v;
  }
  STAMP(10);
}
__global__ __launch_bounds__(256, 1) void qproj5_kernel(const u16* __restrict__ x, const u16* __restrict__ wq,
                                                         u16* __restrict__ qfrag, unsigned* __restrict__ cnt) {
  extern __shared__ __attribute__((aligned(16))) char lds[];
  const int tid = threadIdx.x, lane = tid & 63;
  const int w = __builtin_amdgcn_readfirstlane(tid >> 6);
  const int r = lane & 31, h = lane >> 5;
  const int bid = blockIdx.x;
  if (bid == 0 && tid < 64) cnt[tid] = 0u;
  const int xcd = bid & 7, j = bid >> 3;
  const int grp = (j >> 2) * 8 + xcd, sub = j & 3;
  const int b = grp >> 3, kvh = grp & 7;
  const int hl = sub >> 1, head = kvh * 2 + hl, rhalf = sub & 1;
  const int row0 = b * SQ + rhalf * 56;
  char* ring = lds + w * 40960;
  {
    __amdgpu_buffer_rsrc_t rx =
        __builtin_amdgcn_make_buffer_rsrc((void*)(x + (size_t)row0 * DM), 0, 56 * DM * 2, 0x00020000);
#pragma unroll
    for (int p = 0; p < 14; ++p) {
      const int ci = 64 * p + lane;
      const int lr = ci / 56, cp = ci - lr * 56;
      int gr = 16 * w + lr; gr = gr > 55 ? 55 : gr;
      const int cs = (cp & ~7) | ((cp ^ (lr >> 1)) & 7);
      __builtin_amdgcn_raw_ptr_buffer_load_lds(rx, (lds_vp)(ring + 3 * 8192 + p * 1024), 16, gr * (DM * 2) + cs * 16, 0, 0, 0);
    }
  }
  const int colw = head * DH + 64 * w;
  __amdgpu_buffer_rsrc_t rw = __builtin_amdgcn_make_buffer_rsrc((void*)(wq + colw), 0, (DM * NQ - colw) * 2, 0x00020000);
  const int drow = lane >> 3;
  const int wvoff = drow * (NQ * 2) + (((lane & 7) ^ (((drow >> 1) & 1) << 2)) * 16);
  auto wpiece = [&](int ch, int p) {
    __builtin_amdgcn_raw_ptr_buffer_load_lds(rw, (lds_vp)(ring + (ch % 3) * 8192 + p * 1024), 16, wvoff,
                                             (64 * ch + 8 * p) * (NQ * 2), 0, 0);
  };
#pragma unroll
  for (int ch = 0; ch < 3; ++ch)
#pragma unroll
    for (int p = 0; p < 8; ++p) wpiece(ch, p);
  const int g = lane >> 4, qq = (lane >> 2) & 3, pp = lane & 3;
  const unsigned trb = (unsigned)(size_t)ring + (8 * (g >> 1) + qq) * 128 + ((32 * (g & 1) + 8 * pp) ^ ((qq >> 1) << 6));
  const char* xa[2][4];
#pragma unroll
  for (int qb = 0; qb < 2; ++qb) {
    const int R = 32 * qb + r, lr = R & 15;
#pragma unroll
    for (int v = 0; v < 4; ++v)
      xa[qb][v] = lds + (R >> 4) * 40960 + 3 * 8192 + lr * 896 + ((((2 * v + h) ^ (lr >> 1)) & 7) * 16);
  }
  f32x16 qa[2][2];
#pragma unroll
  for (int a2 = 0; a2 < 2; ++a2)
#pragma unroll
    for (int b2 = 0; b2 < 2; ++b2)
#pragma unroll
      for (int i = 0; i < 16; ++i) qa[a2][b2][i] = 0.f;
  s16x4 fa[2][4];
  bf16x8 fx[2][2];
  auto rdstep = [&](int i, int bsel) {
    const int ch = i >> 2, s4 = i & 3;
    const unsigned tb = trb + (ch % 3) * 8192 + s4 * 2048;
    fa[bsel][0] = trread(tb);
    fa[bsel][1] = trread(tb + 512);
    fa[bsel][2] = trread(tb ^ 64);
    fa[bsel][3] = trread((tb + 512) ^ 64);
    asm volatile("ds_read_b128 %0, %1" : "=v"(fx[bsel][0]) : "v"((unsigned)(size_t)(xa[0][s4] + ch * 128)) : "memory");
    asm volatile("ds_read_b128 %0, %1" : "=v"(fx[bsel][1]) : "v"((unsigned)(size_t)(xa[1][s4] + ch * 128)) : "memory");
  };
  WAITV(16);
  __builtin_amdgcn_s_barrier();
  rdstep(0, 0);
#pragma unroll
  for (int i = 0; i < 28; ++i) {
    const int bs = i & 1, ch = i >> 2, s4 = i & 3;
    if (i + 1 < 28) {
      if (s4 == 3) { if (ch == 0) WAITV(8); else if (ch <= 4) WAITV(6); else WAITV(0); }
      rdstep(i + 1, bs ^ 1);
      asm volatile("s_waitcnt lgkmcnt(6)" : "+v"(fa[bs][0]), "+v"(fa[bs][1]), "+v"(fa[bs][2]), "+v"(fa[bs][3]), "+v"(fx[bs][0]), "+v"(fx[bs][1])::"memory");
    } else {
      asm volatile("s_waitcnt lgkmcnt(0)" : "+v"(fa[bs][0]), "+v"(fa[bs][1]), "+v"(fa[bs][2]), "+v"(fa[bs][3]), "+v"(fx[bs][0]), "+v"(fx[bs][1])::"memory");
    }
    bf16x8 A0 = cat8(fa[bs][0], fa[bs][1]), A1 = cat8(fa[bs][2], fa[bs][3]);
    qa[0][0] = __builtin_amdgcn_mfma_f32_32x32x16_bf16(A0, fx[bs][0], qa[0][0], 0, 0, 0);
    GAP();
    if (ch >= 1 && ch <= 4) wpiece(ch + 2, 2 * s4);
    GAP();
    qa[0][1] = __builtin_amdgcn_mfma_f32_32x32x16_bf16(A1, fx[bs][0], qa[0][1], 0, 0, 0);
    qa[1][0] = __builtin_amdgcn_mfma_f32_32x32x16_bf16(A0, fx[bs][1], qa[1][0], 0, 0, 0);
    GAP();
    if (ch >= 1 && ch <= 4) wpiece(ch + 2, 2 * s4 + 1);
    GAP();
    qa[1][1] = __builtin_amdgcn_mfma_f32_32x32x16_bf16(A1, fx[bs][1], qa[1][1], 0, 0, 0);
  }
#pragma unroll
  for (int qb = 0; qb < 2; ++qb) {
    const int lrow = 32 * qb + r;
    if (lrow < 56) {
      const int R = hl * 112 + rhalf * 56 + lrow;
      u16* base = qfrag + ((size_t)(grp * 8 + (R >> 5)) * 16 * 64 + (R & 31)) * 8 + 4 * h;
#pragma unroll
      for (int cb = 0; cb < 2; ++cb)
#pragma unroll
        for (int gg = 0; gg < 4; ++gg) {
          const int s = 4 * w + 2 * cb + (gg >> 1), hp = gg & 1;
          u32x2 v = {cvtpk(qa[qb][cb][4 * gg], qa[qb][cb][4 * gg + 1]), cvtpk(qa[qb][cb][4 * gg + 2], qa[qb][cb][4 * gg + 3])};
          *(u32x2*)(base + (s * 64 + 32 * hp) * 8) = v;
        }
    }
  }
}

template <int N> DI void dma5_unit(const Ctx4& c) {
  if constexpr (N < 32) {
    constexpr int slot = N % 16, t = unit_tile(N), hf = unit_half(N);
    if constexpr (!unit_is_v(N)) {
      __builtin_amdgcn_raw_ptr_buffer_load_lds(c.rk, (lds_vp)(c.ring + slot * 8192), 16, c.koff[0], 32 * t * 512 + hf * 256, 0, 0);
      __builtin_amdgcn_raw_ptr_buffer_load_lds(c.rk, (lds_vp)(c.ring + slot * 8192 + 1024), 16, c.koff[1], 32 * t * 512 + hf * 256, 0, 0);
    } else {
      __builtin_amdgcn_raw_ptr_buffer_load_lds(c.rv, (lds_vp)(c.ring + slot * 8192), 16, c.voff[0], (32 * t + 16 * hf) * 512, 0, 0);
      __builtin_amdgcn_raw_ptr_buffer_load_lds(c.rv, (lds_vp)(c.ring + slot * 8192 + 1024), 16, c.voff[1], (32 * t + 16 * hf) * 512, 0, 0);
    }
  }
}
template <int T, int J> DI bf16x8 kread5(const Ctx4& c) {
  constexpr int n = unit_k(T, J >> 3), slot = n % 16;
  bf16x8 r;
  asm volatile("ds_read_b128 %0, %1 offset:%2" : "=v"(r) : "v"(c.kaddr[J & 7] + (slot >> 3) * 65536), "i"((slot & 7) * 8192) : "memory");
  return r;
}
template <int T, int J> DI void phaseA5_step(Ctx4& c, bf16x8 (&kf)[4]) {
  if constexpr (J + 3 < 16) asm volatile("s_waitcnt lgkmcnt(3)" : "+v"(kf[J & 3])::"memory");
  else if constexpr (J + 3 == 16) asm volatile("s_waitcnt lgkmcnt(2)" : "+v"(kf[J & 3])::"memory");
  else if constexpr (J + 3 == 17) asm volatile("s_waitcnt lgkmcnt(1)" : "+v"(kf[J & 3])::"memory");
  else asm volatile("s_waitcnt lgkmcnt(0)" : "+v"(kf[J & 3])::"memory");
  qk_mfma<J == 0>(c.s[0], kf[J & 3], c.qf[0][J]);
  if constexpr (T >= 1 && (J & 3) == 1 && J < 16) dma5_unit<4 * T + 10 + (J >> 2)>(c);
  GAP();
  qk_mfma<J == 0>(c.s[1], kf[J & 3], c.qf[1][J]);
  if constexpr (J + 4 < 16) kf[J & 3] = kread5<T, J + 4>(c);
  GAP();
}
template <int T, int... Js> DI void phaseA5_(Ctx4& c, std::integer_sequence<int, Js...>) {
  bf16x8 kf[4];
  {
    constexpr int need = 4 * T + 1 > 31 ? 31 : 4 * T + 1;
    constexpr int lastiss = (T == 0 ? 13 : 4 * T + 9) > 31 ? 31 : (T == 0 ? 13 : 4 * T + 9);
    constexpr int after = 2 * (lastiss - need);
    asm volatile("s_waitcnt vmcnt(%0)" ::"i"(after) : "memory");
    __builtin_amdgcn_s_barrier();
  }
  kf[0] = kread5<T, 0>(c); kf[1] = kread5<T, 1>(c); kf[2] = kread5<T, 2>(c); kf[3] = kread5<T, 3>(c);
  GAP();
  (phaseA5_step<T, Js>(c, kf), ...);
}
template <int T> DI void phaseA5(Ctx4& c) { phaseA5_<T>(c, std::make_integer_sequence<int, 16>{}); }

template <int TV, int S> DI void vread5_set(const Ctx4& c, s16x4 (&v)[4]) {
  constexpr int ks = S >> 2, dbp = S & 3;
  constexpr int n = unit_v(TV, ks), slot = n % 16;
  constexpr int db0 = 2 * dbp, db1 = 2 * dbp + 1;
  constexpr int o0 = (slot & 7) * 8192 + (db0 >> 2) * 256, o1 = (slot & 7) * 8192 + (db1 >> 2) * 256;
  constexpr unsigned hi = (slot >> 3) * 65536;
  v[0] = trread_o<o0>(c.vaddr[db0 & 3] + hi);
  v[1] = trread_o<o0 + 4096>(c.vaddr[db0 & 3] + hi);
  v[2] = trread_o<o1>(c.vaddr[db1 & 3] + hi);
  v[3] = trread_o<o1 + 4096>(c.vaddr[db1 & 3] + hi);
}
template <int T, bool HASPV, bool HASSM, int M> DI void phaseB5_gap(Ctx4& c, s16x4 (&va)[4], s16x4 (&vb)[4]) {
  constexpr int TV = T - 1;
  constexpr int S = M >> 2, ks = S >> 2, dbp = S & 3, dd = (M >> 1) & 1, qb = M & 1;
  if constexpr (HASPV) {
    if constexpr ((M & 3) == 0) {
      if constexpr (S + 1 < 8) {
        if constexpr (S & 1) asm volatile("s_waitcnt lgkmcnt(4)" : "+v"(vb[0]), "+v"(vb[1]), "+v"(vb[2]), "+v"(vb[3])::"memory");
        else asm volatile("s_waitcnt lgkmcnt(4)" : "+v"(va[0]), "+v"(va[1]), "+v"(va[2]), "+v"(va[3])::"memory");
      } else {
        asm volatile("s_waitcnt lgkmcnt(0)" : "+v"(vb[0]), "+v"(vb[1]), "+v"(vb[2]), "+v"(vb[3])::"memory");
      }
    }
    {
      constexpr int db = 2 * dbp + dd;
      const bf16x8 vf = (S & 1) ? cat8(vb[2 * dd], vb[2 * dd + 1]) : cat8(va[2 * dd], va[2 * dd + 1]);
      pv_mfma<(qb * 8 + db) * 16, (TV == 0 && ks == 0)>(vf, c.pf[qb][ks]);
    }
    if constexpr ((M & 3) == 3 && S + 2 < 8) {
      if constexpr (S & 1) vread5_set<TV, S + 2>(c, vb); else vread5_set<TV, S + 2>(c, va);
    }
  }
  if constexpr (HASSM) sm_slice<T, M>(c);
  GAP();
}
template <int T, bool HASPV, bool HASSM, int... Ms> DI void phaseB5_(Ctx4& c, std::integer_sequence<int, Ms...>) {
  constexpr int TV = T - 1;
  s16x4 va[4], vb[4];
  if constexpr (!HASSM) {
    asm volatile("s_waitcnt vmcnt(0)" ::: "memory");
    __builtin_amdgcn_s_barrier();
  }
  if constexpr (HASPV) {
    vread5_set<TV, 0>(c, va);
    vread5_set<TV, 1>(c, vb);
  }
  if constexpr (HASSM) asm volatile("s_nop 7\n\ts_nop 7" : "+v"(c.s[0]), "+v"(c.s[1]));
  GAP();
  (phaseB5_gap<T, HASPV, HASSM, Ms>(c, va, vb), ...);
  if constexpr (HASSM) {
    c.l[0] += c.rs[0]; c.l[1] += c.rs[1];
#pragma unroll
    for (int qb = 0; qb < 2; ++qb)
#pragma unroll
      for (int ks = 0; ks < 2; ++ks) {
        u32x4 wv = {c.pn[qb][4 * ks], c.pn[qb][4 * ks + 1], c.pn[qb][4 * ks + 2], c.pn[qb][4 * ks + 3]};
        c.pf[qb][ks] = __builtin_bit_cast(bf16x8, wv);
      }
  }
  if constexpr (HASPV && HASSM) {
    if (__builtin_expect(c.pend, 0)) {
      float t0, t1;
      asm volatile("s_nop 7\n\ts_nop 7\n\t"
                   "v_accvgpr_read_b32 %0, a0\n\tv_mul_f32 %0, %2, %0\n\tv_accvgpr_write_b32 a0, %0\n\t"
                   "v_accvgpr_read_b32 %1, a1\n\tv_mul_f32 %1, %2, %1\n\tv_accvgpr_write_b32 a1, %1\n\t"
                   "v_accvgpr_read_b32 %0, a2\n\tv_mul_f32 %0, %2, %0\n\tv_accvgpr_write_b32 a2, %0\n\t"
                   "v_accvgpr_read_b32 %1, a3\n\tv_mul_f32 %1, %2, %1\n\tv_accvgpr_write_b32 a3, %1\n\t"
                   "v_accvgpr_read_b32 %0, a4\n\tv_mul_f32 %0, %2, %0\n\tv_accvgpr_write_b32 a4, %0\n\t"
                   "v_accvgpr_read_b32 %1, a5\n\tv_mul_f32 %1, %2, %1\n\tv_accvgpr_write_b32 a5, %1\n\t"
                   "v_accvgpr_read_b32 %0, a6\n\tv_mul_f32 %0, %2, %0\n\tv_accvgpr_write_b32 a6, %0\n\t"
                   "v_accvgpr_read_b32 %1, a7\n\tv_mul_f32 %1, %2, %1\n\tv_accvgpr_write_b32 a7, %1\n\t"
                   "v_accvgpr_read_b32 %0, a8\n\tv_mul_f32 %0, %2, %0\n\tv_accvgpr_write_b32 a8, %0\n\t"
                   "v_accvgpr_read_b32 %1, a9\n\tv_mul_f32 %1, %2, %1\n\tv_accvgpr_write_b32 a9, %1\n\t"
                   "v_accvgpr_read_b32 %0, a10\n\tv_mul_f32 %0, %2, %0\n\tv_accvgpr_write_b32 a10, %0\n\t"
                   "v_accvgpr_read_b32 %1, a11\n\tv_mul_f32 %1, %2, %1\n\tv_accvgpr_write_b32 a11, %1\n\t"
                   "v_accvgpr_read_b32 %0, a12\n\tv_mul_f32 %0, %2, %0\n\tv_accvgpr_write_b32 a12, %0\n\t"
                   "v_accvgpr_read_b32 %1, a13\n\tv_mul_f32 %1, %2, %1\n\tv_accvgpr_write_b32 a13, %1\n\t"
                   "v_accvgpr_read_b32 %0, a14\n\tv_mul_f32 %0, %2, %0\n\tv_accvgpr_write_b32 a14, %0\n\t"
                   "v_accvgpr_read_b32 %1, a15\n\tv_mul_f32 %1, %2, %1\n\tv_accvgpr_write_b32 a15, %1\n\t"
                   "v_accvgpr_read_b32 %0, a16\n\tv_mul_f32 %0, %2, %0\n\tv_accvgpr_write_b32 a16, %0\n\t"
                   "v_accvgpr_read_b32 %1, a17\n\tv_mul_f32 %1, %2, %1\n\tv_accvgpr_write_b32 a17, %1\n\t"
                   "v_accvgpr_read_b32 %0, a18\n\tv_mul_f32 %0, %2, %0\n\tv_accvgpr_write_b32 a18, %0\n\t"
                   "v_accvgpr_read_b32 %1, a19\n\tv_mul_f32 %1, %2, %1\n\tv_accvgpr_write_b32 a19, %1\n\t"
                   "v_accvgpr_read_b32 %0, a20\n\tv_mul_f32 %0, %2, %0\n\tv_accvgpr_write_b32 a20, %0\n\t"
                   "v_accvgpr_read_b32 %1, a21\n\tv_mul_f32 %1, %2, %1\n\tv_accvgpr_write_b32 a21, %1\n\t"
                   "v_accvgpr_read_b32 %0, a22\n\tv_mul_f32 %0, %2, %0\n\tv_accvgpr_write_b32 a22, %0\n\t"
                   "v_accvgpr_read_b32 %1, a23\n\tv_mul_f32 %1, %2, %1\n\tv_accvgpr_write_b32 a23, %1\n\t"
                   "v_accvgpr_read_b32 %0, a24\n\tv_mul_f32 %0, %2, %0\n\tv_accvgpr_write_b32 a24, %0\n\t"
                   "v_accvgpr_read_b32 %1, a25\n\tv_mul_f32 %1, %2, %1\n\tv_accvgpr_write_b32 a25, %1\n\t"
                   "v_accvgpr_read_b32 %0, a26\n\tv_mul_f32 %0, %2, %0\n\tv_accvgpr_write_b32 a26, %0\n\t"
                   "v_accvgpr_read_b32 %1, a27\n\tv_mul_f32 %1, %2, %1\n\tv_accvgpr_write_b32 a27, %1\n\t"
                   "v_accvgpr_read_b32 %0, a28\n\tv_mul_f32 %0, %2, %0\n\tv_accvgpr_write_b32 a28, %0\n\t"
                   "v_accvgpr_read_b32 %1, a29\n\tv_mul_f32 %1, %2, %1\n\tv_accvgpr_write_b32 a29, %1\n\t"
                   "v_accvgpr_read_b32 %0, a30\n\tv_mul_f32 %0, %2, %0\n\tv_accvgpr_write_b32 a30, %0\n\t"
                   "v_accvgpr_read_b32 %1, a31\n\tv_mul_f32 %1, %2, %1\n\tv_accvgpr_write_b32 a31, %1\n\t"
                   "v_accvgpr_read_b32 %0, a32\n\tv_mul_f32 %0, %2, %0\n\tv_accvgpr_write_b32 a32, %0\n\t"
                   "v_accvgpr_read_b32 %1, a33\n\tv_mul_f32 %1, %2, %1\n\tv_accvgpr_write_b32 a33, %1\n\t"
                   "v_accvgpr_read_b32 %0, a34\n\tv_mul_f32 %0, %2, %0\n\tv_accvgpr_write_b32 a34, %0\n\t"
                   "v_accvgpr_read_b32 %1, a35\n\tv_mul_f32 %1, %2, %1\n\tv_accvgpr_write_b32 a35, %1\n\t"
                   "v_accvgpr_read_b32 %0, a36\n\tv_mul_f32 %0, %2, %0\n\tv_accvgpr_write_b32 a36, %0\n\t"
                   "v_accvgpr_read_b32 %1, a37\n\tv_mul_f32 %1, %2, %1\n\tv_accvgpr_write_b32 a37, %1\n\t"
                   "v_accvgpr_read_b32 %0, a38\n\tv_mul_f32 %0, %2, %0\n\tv_accvgpr_write_b32 a38, %0\n\t"
                   "v_accvgpr_read_b32 %1, a39\n\tv_mul_f32 %1, %2, %1\n\tv_accvgpr_write_b32 a39, %1\n\t"
                   "v_accvgpr_read_b32 %0, a40\n\tv_mul_f32 %0, %2, %0\n\tv_accvgpr_write_b32 a40, %0\n\t"
                   "v_accvgpr_read_b32 %1, a41\n\tv_mul_f32 %1, %2, %1\n\tv_accvgpr_write_b32 a41, %1\n\t"
                   "v_accvgpr_read_b32 %0, a42\n\tv_mul_f32 %0, %2, %0\n\tv_accvgpr_write_b32 a42, %0\n\t"
                   "v_accvgpr_read_b32 %1, a43\n\tv_mul_f32 %1, %2, %1\n\tv_accvgpr_write_b32 a43, %1\n\t"
                   "v_accvgpr_read_b32 %0, a44\n\tv_mul_f32 %0, %2, %0\n\tv_accvgpr_write_b32 a44, %0\n\t"
                   "v_accvgpr_read_b32 %1, a45\n\tv_mul_f32 %1, %2, %1\n\tv_accvgpr_write_b32 a45, %1\n\t"
                   "v_accvgpr_read_b32 %0, a46\n\tv_mul_f32 %0, %2, %0\n\tv_accvgpr_write_b32 a46, %0\n\t"
                   "v_accvgpr_read_b32 %1, a47\n\tv_mul_f32 %1, %2, %1\n\tv_accvgpr_write_b32 a47, %1\n\t"
                   "v_accvgpr_read_b32 %0, a48\n\tv_mul_f32 %0, %2, %0\n\tv_accvgpr_write_b32 a48, %0\n\t"
                   "v_accvgpr_read_b32 %1, a49\n\tv_mul_f32 %1, %2, %1\n\tv_accvgpr_write_b32 a49, %1\n\t"
                   "v_accvgpr_read_b32 %0, a50\n\tv_mul_f32 %0, %2, %0\n\tv_accvgpr_write_b32 a50, %0\n\t"
                   "v_accvgpr_read_b32 %1, a51\n\tv_mul_f32 %1, %2, %1\n\tv_accvgpr_write_b32 a51, %1\n\t"
                   "v_accvgpr_read_b32 %0, a52\n\tv_mul_f32 %0, %2, %0\n\tv_accvgpr_write_b32 a52, %0\n\t"
                   "v_accvgpr_read_b32 %1, a53\n\tv_mul_f32 %1, %2, %1\n\tv_accvgpr_write_b32 a53, %1\n\t"
                   "v_accvgpr_read_b32 %0, a54\n\tv_mul_f32 %0, %2, %0\n\tv_accvgpr_write_b32 a54, %0\n\t"
                   "v_accvgpr_read_b32 %1, a55\n\tv_mul_f32 %1, %2, %1\n\tv_accvgpr_write_b32 a55, %1\n\t"
                   "v_accvgpr_read_b32 %0, a56\n\tv_mul_f32 %0, %2, %0\n\tv_accvgpr_write_b32 a56, %0\n\t"
                   "v_accvgpr_read_b32 %1, a57\n\tv_mul_f32 %1, %2, %1\n\tv_accvgpr_write_b32 a57, %1\n\t"
                   "v_accvgpr_read_b32 %0, a58\n\tv_mul_f32 %0, %2, %0\n\tv_accvgpr_write_b32 a58, %0\n\t"
                   "v_accvgpr_read_b32 %1, a59\n\tv_mul_f32 %1, %2, %1\n\tv_accvgpr_write_b32 a59, %1\n\t"
                   "v_accvgpr_read_b32 %0, a60\n\tv_mul_f32 %0, %2, %0\n\tv_accvgpr_write_b32 a60, %0\n\t"
                   "v_accvgpr_read_b32 %1, a61\n\tv_mul_f32 %1, %2, %1\n\tv_accvgpr_write_b32 a61, %1\n\t"
                   "v_accvgpr_read_b32 %0, a62\n\tv_mul_f32 %0, %2, %0\n\tv_accvgpr_write_b32 a62, %0\n\t"
                   "v_accvgpr_read_b32 %1, a63\n\tv_mul_f32 %1, %2, %1\n\tv_accvgpr_write_b32 a63, %1\n\t"
                   "v_accvgpr_read_b32 %0, a64\n\tv_mul_f32 %0, %2, %0\n\tv_accvgpr_write_b32 a64, %0\n\t"
                   "v_accvgpr_read_b32 %1, a65\n\tv_mul_f32 %1, %2, %1\n\tv_accvgpr_write_b32 a65, %1\n\t"
                   "v_accvgpr_read_b32 %0, a66\n\tv_mul_f32 %0, %2, %0\n\tv_accvgpr_write_b32 a66, %0\n\t"
                   "v_accvgpr_read_b32 %1, a67\n\tv_mul_f32 %1, %2, %1\n\tv_accvgpr_write_b32 a67, %1\n\t"
                   "v_accvgpr_read_b32 %0, a68\n\tv_mul_f32 %0, %2, %0\n\tv_accvgpr_write_b32 a68, %0\n\t"
                   "v_accvgpr_read_b32 %1, a69\n\tv_mul_f32 %1, %2, %1\n\tv_accvgpr_write_b32 a69, %1\n\t"
                   "v_accvgpr_read_b32 %0, a70\n\tv_mul_f32 %0, %2, %0\n\tv_accvgpr_write_b32 a70, %0\n\t"
                   "v_accvgpr_read_b32 %1, a71\n\tv_mul_f32 %1, %2, %1\n\tv_accvgpr_write_b32 a71, %1\n\t"
                   "v_accvgpr_read_b32 %0, a72\n\tv_mul_f32 %0, %2, %0\n\tv_accvgpr_write_b32 a72, %0\n\t"
                   "v_accvgpr_read_b32 %1, a73\n\tv_mul_f32 %1, %2, %1\n\tv_accvgpr_write_b32 a73, %1\n\t"
                   "v_accvgpr_read_b32 %0, a74\n\tv_mul_f32 %0, %2, %0\n\tv_accvgpr_write_b32 a74, %0\n\t"
                   "v_accvgpr_read_b32 %1, a75\n\tv_mul_f32 %1, %2, %1\n\tv_accvgpr_write_b32 a75, %1\n\t"
                   "v_accvgpr_read_b32 %0, a76\n\tv_mul_f32 %0, %2, %0\n\tv_accvgpr_write_b32 a76, %0\n\t"
                   "v_accvgpr_read_b32 %1, a77\n\tv_mul_f32 %1, %2, %1\n\tv_accvgpr_write_b32 a77, %1\n\t"
                   "v_accvgpr_read_b32 %0, a78\n\tv_mul_f32 %0, %2, %0\n\tv_accvgpr_write_b32 a78, %0\n\t"
                   "v_accvgpr_read_b32 %1, a79\n\tv_mul_f32 %1, %2, %1\n\tv_accvgpr_write_b32 a79, %1\n\t"
                   "v_accvgpr_read_b32 %0, a80\n\tv_mul_f32 %0, %2, %0\n\tv_accvgpr_write_b32 a80, %0\n\t"
                   "v_accvgpr_read_b32 %1, a81\n\tv_mul_f32 %1, %2, %1\n\tv_accvgpr_write_b32 a81, %1\n\t"
                   "v_accvgpr_read_b32 %0, a82\n\tv_mul_f32 %0, %2, %0\n\tv_accvgpr_write_b32 a82, %0\n\t"
                   "v_accvgpr_read_b32 %1, a83\n\tv_mul_f32 %1, %2, %1\n\tv_accvgpr_write_b32 a83, %1\n\t"
                   "v_accvgpr_read_b32 %0, a84\n\tv_mul_f32 %0, %2, %0\n\tv_accvgpr_write_b32 a84, %0\n\t"
                   "v_accvgpr_read_b32 %1, a85\n\tv_mul_f32 %1, %2, %1\n\tv_accvgpr_write_b32 a85, %1\n\t"
                   "v_accvgpr_read_b32 %0, a86\n\tv_mul_f32 %0, %2, %0\n\tv_accvgpr_write_b32 a86, %0\n\t"
                   "v_accvgpr_read_b32 %1, a87\n\tv_mul_f32 %1, %2, %1\n\tv_accvgpr_write_b32 a87, %1\n\t"
                   "v_accvgpr_read_b32 %0, a88\n\tv_mul_f32 %0, %2, %0\n\tv_accvgpr_write_b32 a88, %0\n\t"
                   "v_accvgpr_read_b32 %1, a89\n\tv_mul_f32 %1, %2, %1\n\tv_accvgpr_write_b32 a89, %1\n\t"
                   "v_accvgpr_read_b32 %0, a90\n\tv_mul_f32 %0, %2, %0\n\tv_accvgpr_write_b32 a90, %0\n\t"
                   "v_accvgpr_read_b32 %1, a91\n\tv_mul_f32 %1, %2, %1\n\tv_accvgpr_write_b32 a91, %1\n\t"
                   "v_accvgpr_read_b32 %0, a92\n\tv_mul_f32 %0, %2, %0\n\tv_accvgpr_write_b32 a92, %0\n\t"
                   "v_accvgpr_read_b32 %1, a93\n\tv_mul_f32 %1, %2, %1\n\tv_accvgpr_write_b32 a93, %1\n\t"
                   "v_accvgpr_read_b32 %0, a94\n\tv_mul_f32 %0, %2, %0\n\tv_accvgpr_write_b32 a94, %0\n\t"
                   "v_accvgpr_read_b32 %1, a95\n\tv_mul_f32 %1, %2, %1\n\tv_accvgpr_write_b32 a95, %1\n\t"
                   "v_accvgpr_read_b32 %0, a96\n\tv_mul_f32 %0, %2, %0\n\tv_accvgpr_write_b32 a96, %0\n\t"
                   "v_accvgpr_read_b32 %1, a97\n\tv_mul_f32 %1, %2, %1\n\tv_accvgpr_write_b32 a97, %1\n\t"
                   "v_accvgpr_read_b32 %0, a98\n\tv_mul_f32 %0, %2, %0\n\tv_accvgpr_write_b32 a98, %0\n\t"
                   "v_accvgpr_read_b32 %1, a99\n\tv_mul_f32 %1, %2, %1\n\tv_accvgpr_write_b32 a99, %1\n\t"
                   "v_accvgpr_read_b32 %0, a100\n\tv_mul_f32 %0, %2, %0\n\tv_accvgpr_write_b32 a100, %0\n\t"
                   "v_accvgpr_read_b32 %1, a101\n\tv_mul_f32 %1, %2, %1\n\tv_accvgpr_write_b32 a101, %1\n\t"
                   "v_accvgpr_read_b32 %0, a102\n\tv_mul_f32 %0, %2, %0\n\tv_accvgpr_write_b32 a102, %0\n\t"
                   "v_accvgpr_read_b32 %1, a103\n\tv_mul_f32 %1, %2, %1\n\tv_accvgpr_write_b32 a103, %1\n\t"
                   "v_accvgpr_read_b32 %0, a104\n\tv_mul_f32 %0, %2, %0\n\tv_accvgpr_write_b32 a104, %0\n\t"
                   "v_accvgpr_read_b32 %1, a105\n\tv_mul_f32 %1, %2, %1\n\tv_accvgpr_write_b32 a105, %1\n\t"
                   "v_accvgpr_read_b32 %0, a106\n\tv_mul_f32 %0, %2, %0\n\tv_accvgpr_write_b32 a106, %0\n\t"
                   "v_accvgpr_read_b32 %1, a107\n\tv_mul_f32 %1, %2, %1\n\tv_accvgpr_write_b32 a107, %1\n\t"
                   "v_accvgpr_read_b32 %0, a108\n\tv_mul_f32 %0, %2, %0\n\tv_accvgpr_write_b32 a108, %0\n\t"
                   "v_accvgpr_read_b32 %1, a109\n\tv_mul_f32 %1, %2, %1\n\tv_accvgpr_write_b32 a109, %1\n\t"
                   "v_accvgpr_read_b32 %0, a110\n\tv_mul_f32 %0, %2, %0\n\tv_accvgpr_write_b32 a110, %0\n\t"
                   "v_accvgpr_read_b32 %1, a111\n\tv_mul_f32 %1, %2, %1\n\tv_accvgpr_write_b32 a111, %1\n\t"
                   "v_accvgpr_read_b32 %0, a112\n\tv_mul_f32 %0, %2, %0\n\tv_accvgpr_write_b32 a112, %0\n\t"
                   "v_accvgpr_read_b32 %1, a113\n\tv_mul_f32 %1, %2, %1\n\tv_accvgpr_write_b32 a113, %1\n\t"
                   "v_accvgpr_read_b32 %0, a114\n\tv_mul_f32 %0, %2, %0\n\tv_accvgpr_write_b32 a114, %0\n\t"
                   "v_accvgpr_read_b32 %1, a115\n\tv_mul_f32 %1, %2, %1\n\tv_accvgpr_write_b32 a115, %1\n\t"
                   "v_accvgpr_read_b32 %0, a116\n\tv_mul_f32 %0, %2, %0\n\tv_accvgpr_write_b32 a116, %0\n\t"
                   "v_accvgpr_read_b32 %1, a117\n\tv_mul_f32 %1, %2, %1\n\tv_accvgpr_write_b32 a117, %1\n\t"
                   "v_accvgpr_read_b32 %0, a118\n\tv_mul_f32 %0, %2, %0\n\tv_accvgpr_write_b32 a118, %0\n\t"
                   "v_accvgpr_read_b32 %1, a119\n\tv_mul_f32 %1, %2, %1\n\tv_accvgpr_write_b32 a119, %1\n\t"
                   "v_accvgpr_read_b32 %0, a120\n\tv_mul_f32 %0, %2, %0\n\tv_accvgpr_write_b32 a120, %0\n\t"
                   "v_accvgpr_read_b32 %1, a121\n\tv_mul_f32 %1, %2, %1\n\tv_accvgpr_write_b32 a121, %1\n\t"
                   "v_accvgpr_read_b32 %0, a122\n\tv_mul_f32 %0, %2, %0\n\tv_accvgpr_write_b32 a122, %0\n\t"
                   "v_accvgpr_read_b32 %1, a123\n\tv_mul_f32 %1, %2, %1\n\tv_accvgpr_write_b32 a123, %1\n\t"
                   "v_accvgpr_read_b32 %0, a124\n\tv_mul_f32 %0, %2, %0\n\tv_accvgpr_write_b32 a124, %0\n\t"
                   "v_accvgpr_read_b32 %1, a125\n\tv_mul_f32 %1, %2, %1\n\tv_accvgpr_write_b32 a125, %1\n\t"
                   "v_accvgpr_read_b32 %0, a126\n\tv_mul_f32 %0, %2, %0\n\tv_accvgpr_write_b32 a126, %0\n\t"
                   "v_accvgpr_read_b32 %1, a127\n\tv_mul_f32 %1, %2, %1\n\tv_accvgpr_write_b32 a127, %1\n\t"
                   "v_accvgpr_read_b32 %0, a128\n\tv_mul_f32 %0, %3, %0\n\tv_accvgpr_write_b32 a128, %0\n\t"
                   "v_accvgpr_read_b32 %1, a129\n\tv_mul_f32 %1, %3, %1\n\tv_accvgpr_write_b32 a129, %1\n\t"
                   "v_accvgpr_read_b32 %0, a130\n\tv_mul_f32 %0, %3, %0\n\tv_accvgpr_write_b32 a130, %0\n\t"
                   "v_accvgpr_read_b32 %1, a131\n\tv_mul_f32 %1, %3, %1\n\tv_accvgpr_write_b32 a131, %1\n\t"
                   "v_accvgpr_read_b32 %0, a132\n\tv_mul_f32 %0, %3, %0\n\tv_accvgpr_write_b32 a132, %0\n\t"
                   "v_accvgpr_read_b32 %1, a133\n\tv_mul_f32 %1, %3, %1\n\tv_accvgpr_write_b32 a133, %1\n\t"
                   "v_accvgpr_read_b32 %0, a134\n\tv_mul_f32 %0, %3, %0\n\tv_accvgpr_write_b32 a134, %0\n\t"
                   "v_accvgpr_read_b32 %1, a135\n\tv_mul_f32 %1, %3, %1\n\tv_accvgpr_write_b32 a135, %1\n\t"
                   "v_accvgpr_read_b32 %0, a136\n\tv_mul_f32 %0, %3, %0\n\tv_accvgpr_write_b32 a136, %0\n\t"
                   "v_accvgpr_read_b32 %1, a137\n\tv_mul_f32 %1, %3, %1\n\tv_accvgpr_write_b32 a137, %1\n\t"
                   "v_accvgpr_read_b32 %0, a138\n\tv_mul_f32 %0, %3, %0\n\tv_accvgpr_write_b32 a138, %0\n\t"
                   "v_accvgpr_read_b32 %1, a139\n\tv_mul_f32 %1, %3, %1\n\tv_accvgpr_write_b32 a139, %1\n\t"
                   "v_accvgpr_read_b32 %0, a140\n\tv_mul_f32 %0, %3, %0\n\tv_accvgpr_write_b32 a140, %0\n\t"
                   "v_accvgpr_read_b32 %1, a141\n\tv_mul_f32 %1, %3, %1\n\tv_accvgpr_write_b32 a141, %1\n\t"
                   "v_accvgpr_read_b32 %0, a142\n\tv_mul_f32 %0, %3, %0\n\tv_accvgpr_write_b32 a142, %0\n\t"
                   "v_accvgpr_read_b32 %1, a143\n\tv_mul_f32 %1, %3, %1\n\tv_accvgpr_write_b32 a143, %1\n\t"
                   "v_accvgpr_read_b32 %0, a144\n\tv_mul_f32 %0, %3, %0\n\tv_accvgpr_write_b32 a144, %0\n\t"
                   "v_accvgpr_read_b32 %1, a145\n\tv_mul_f32 %1, %3, %1\n\tv_accvgpr_write_b32 a145, %1\n\t"
                   "v_accvgpr_read_b32 %0, a146\n\tv_mul_f32 %0, %3, %0\n\tv_accvgpr_write_b32 a146, %0\n\t"
                   "v_accvgpr_read_b32 %1, a147\n\tv_mul_f32 %1, %3, %1\n\tv_accvgpr_write_b32 a147, %1\n\t"
                   "v_accvgpr_read_b32 %0, a148\n\tv_mul_f32 %0, %3, %0\n\tv_accvgpr_write_b32 a148, %0\n\t"
                   "v_accvgpr_read_b32 %1, a149\n\tv_mul_f32 %1, %3, %1\n\tv_accvgpr_write_b32 a149, %1\n\t"
                   "v_accvgpr_read_b32 %0, a150\n\tv_mul_f32 %0, %3, %0\n\tv_accvgpr_write_b32 a150, %0\n\t"
                   "v_accvgpr_read_b32 %1, a151\n\tv_mul_f32 %1, %3, %1\n\tv_accvgpr_write_b32 a151, %1\n\t"
                   "v_accvgpr_read_b32 %0, a152\n\tv_mul_f32 %0, %3, %0\n\tv_accvgpr_write_b32 a152, %0\n\t"
                   "v_accvgpr_read_b32 %1, a153\n\tv_mul_f32 %1, %3, %1\n\tv_accvgpr_write_b32 a153, %1\n\t"
                   "v_accvgpr_read_b32 %0, a154\n\tv_mul_f32 %0, %3, %0\n\tv_accvgpr_write_b32 a154, %0\n\t"
                   "v_accvgpr_read_b32 %1, a155\n\tv_mul_f32 %1, %3, %1\n\tv_accvgpr_write_b32 a155, %1\n\t"
                   "v_accvgpr_read_b32 %0, a156\n\tv_mul_f32 %0, %3, %0\n\tv_accvgpr_write_b32 a156, %0\n\t"
                   "v_accvgpr_read_b32 %1, a157\n\tv_mul_f32 %1, %3, %1\n\tv_accvgpr_write_b32 a157, %1\n\t"
                   "v_accvgpr_read_b32 %0, a158\n\tv_mul_f32 %0, %3, %0\n\tv_accvgpr_write_b32 a158, %0\n\t"
                   "v_accvgpr_read_b32 %1, a159\n\tv_mul_f32 %1, %3, %1\n\tv_accvgpr_write_b32 a159, %1\n\t"
                   "v_accvgpr_read_b32 %0, a160\n\tv_mul_f32 %0, %3, %0\n\tv_accvgpr_write_b32 a160, %0\n\t"
                   "v_accvgpr_read_b32 %1, a161\n\tv_mul_f32 %1, %3, %1\n\tv_accvgpr_write_b32 a161, %1\n\t"
                   "v_accvgpr_read_b32 %0, a162\n\tv_mul_f32 %0, %3, %0\n\tv_accvgpr_write_b32 a162, %0\n\t"
                   "v_accvgpr_read_b32 %1, a163\n\tv_mul_f32 %1, %3, %1\n\tv_accvgpr_write_b32 a163, %1\n\t"
                   "v_accvgpr_read_b32 %0, a164\n\tv_mul_f32 %0, %3, %0\n\tv_accvgpr_write_b32 a164, %0\n\t"
                   "v_accvgpr_read_b32 %1, a165\n\tv_mul_f32 %1, %3, %1\n\tv_accvgpr_write_b32 a165, %1\n\t"
                   "v_accvgpr_read_b32 %0, a166\n\tv_mul_f32 %0, %3, %0\n\tv_accvgpr_write_b32 a166, %0\n\t"
                   "v_accvgpr_read_b32 %1, a167\n\tv_mul_f32 %1, %3, %1\n\tv_accvgpr_write_b32 a167, %1\n\t"
                   "v_accvgpr_read_b32 %0, a168\n\tv_mul_f32 %0, %3, %0\n\tv_accvgpr_write_b32 a168, %0\n\t"
                   "v_accvgpr_read_b32 %1, a169\n\tv_mul_f32 %1, %3, %1\n\tv_accvgpr_write_b32 a169, %1\n\t"
                   "v_accvgpr_read_b32 %0, a170\n\tv_mul_f32 %0, %3, %0\n\tv_accvgpr_write_b32 a170, %0\n\t"
                   "v_accvgpr_read_b32 %1, a171\n\tv_mul_f32 %1, %3, %1\n\tv_accvgpr_write_b32 a171, %1\n\t"
                   "v_accvgpr_read_b32 %0, a172\n\tv_mul_f32 %0, %3, %0\n\tv_accvgpr_write_b32 a172, %0\n\t"
                   "v_accvgpr_read_b32 %1, a173\n\tv_mul_f32 %1, %3, %1\n\tv_accvgpr_write_b32 a173, %1\n\t"
                   "v_accvgpr_read_b32 %0, a174\n\tv_mul_f32 %0, %3, %0\n\tv_accvgpr_write_b32 a174, %0\n\t"
                   "v_accvgpr_read_b32 %1, a175\n\tv_mul_f32 %1, %3, %1\n\tv_accvgpr_write_b32 a175, %1\n\t"
                   "v_accvgpr_read_b32 %0, a176\n\tv_mul_f32 %0, %3, %0\n\tv_accvgpr_write_b32 a176, %0\n\t"
                   "v_accvgpr_read_b32 %1, a177\n\tv_mul_f32 %1, %3, %1\n\tv_accvgpr_write_b32 a177, %1\n\t"
                   "v_accvgpr_read_b32 %0, a178\n\tv_mul_f32 %0, %3, %0\n\tv_accvgpr_write_b32 a178, %0\n\t"
                   "v_accvgpr_read_b32 %1, a179\n\tv_mul_f32 %1, %3, %1\n\tv_accvgpr_write_b32 a179, %1\n\t"
                   "v_accvgpr_read_b32 %0, a180\n\tv_mul_f32 %0, %3, %0\n\tv_accvgpr_write_b32 a180, %0\n\t"
                   "v_accvgpr_read_b32 %1, a181\n\tv_mul_f32 %1, %3, %1\n\tv_accvgpr_write_b32 a181, %1\n\t"
                   "v_accvgpr_read_b32 %0, a182\n\tv_mul_f32 %0, %3, %0\n\tv_accvgpr_write_b32 a182, %0\n\t"
                   "v_accvgpr_read_b32 %1, a183\n\tv_mul_f32 %1, %3, %1\n\tv_accvgpr_write_b32 a183, %1\n\t"
                   "v_accvgpr_read_b32 %0, a184\n\tv_mul_f32 %0, %3, %0\n\tv_accvgpr_write_b32 a184, %0\n\t"
                   "v_accvgpr_read_b32 %1, a185\n\tv_mul_f32 %1, %3, %1\n\tv_accvgpr_write_b32 a185, %1\n\t"
                   "v_accvgpr_read_b32 %0, a186\n\tv_mul_f32 %0, %3, %0\n\tv_accvgpr_write_b32 a186, %0\n\t"
                   "v_accvgpr_read_b32 %1, a187\n\tv_mul_f32 %1, %3, %1\n\tv_accvgpr_write_b32 a187, %1\n\t"
                   "v_accvgpr_read_b32 %0, a188\n\tv_mul_f32 %0, %3, %0\n\tv_accvgpr_write_b32 a188, %0\n\t"
                   "v_accvgpr_read_b32 %1, a189\n\tv_mul_f32 %1, %3, %1\n\tv_accvgpr_write_b32 a189, %1\n\t"
                   "v_accvgpr_read_b32 %0, a190\n\tv_mul_f32 %0, %3, %0\n\tv_accvgpr_write_b32 a190, %0\n\t"
                   "v_accvgpr_read_b32 %1, a191\n\tv_mul_f32 %1, %3, %1\n\tv_accvgpr_write_b32 a191, %1\n\t"
                   "v_accvgpr_read_b32 %0, a192\n\tv_mul_f32 %0, %3, %0\n\tv_accvgpr_write_b32 a192, %0\n\t"
                   "v_accvgpr_read_b32 %1, a193\n\tv_mul_f32 %1, %3, %1\n\tv_accvgpr_write_b32 a193, %1\n\t"
                   "v_accvgpr_read_b32 %0, a194\n\tv_mul_f32 %0, %3, %0\n\tv_accvgpr_write_b32 a194, %0\n\t"
                   "v_accvgpr_read_b32 %1, a195\n\tv_mul_f32 %1, %3, %1\n\tv_accvgpr_write_b32 a195, %1\n\t"
                   "v_accvgpr_read_b32 %0, a196\n\tv_mul_f32 %0, %3, %0\n\tv_accvgpr_write_b32 a196, %0\n\t"
                   "v_accvgpr_read_b32 %1, a197\n\tv_mul_f32 %1, %3, %1\n\tv_accvgpr_write_b32 a197, %1\n\t"
                   "v_accvgpr_read_b32 %0, a198\n\tv_mul_f32 %0, %3, %0\n\tv_accvgpr_write_b32 a198, %0\n\t"
                   "v_accvgpr_read_b32 %1, a199\n\tv_mul_f32 %1, %3, %1\n\tv_accvgpr_write_b32 a199, %1\n\t"
                   "v_accvgpr_read_b32 %0, a200\n\tv_mul_f32 %0, %3, %0\n\tv_accvgpr_write_b32 a200, %0\n\t"
                   "v_accvgpr_read_b32 %1, a201\n\tv_mul_f32 %1, %3, %1\n\tv_accvgpr_write_b32 a201, %1\n\t"
                   "v_accvgpr_read_b32 %0, a202\n\tv_mul_f32 %0, %3, %0\n\tv_accvgpr_write_b32 a202, %0\n\t"
                   "v_accvgpr_read_b32 %1, a203\n\tv_mul_f32 %1, %3, %1\n\tv_accvgpr_write_b32 a203, %1\n\t"
                   "v_accvgpr_read_b32 %0, a204\n\tv_mul_f32 %0, %3, %0\n\tv_accvgpr_write_b32 a204, %0\n\t"
                   "v_accvgpr_read_b32 %1, a205\n\tv_mul_f32 %1, %3, %1\n\tv_accvgpr_write_b32 a205, %1\n\t"
                   "v_accvgpr_read_b32 %0, a206\n\tv_mul_f32 %0, %3, %0\n\tv_accvgpr_write_b32 a206, %0\n\t"
                   "v_accvgpr_read_b32 %1, a207\n\tv_mul_f32 %1, %3, %1\n\tv_accvgpr_write_b32 a207, %1\n\t"
                   "v_accvgpr_read_b32 %0, a208\n\tv_mul_f32 %0, %3, %0\n\tv_accvgpr_write_b32 a208, %0\n\t"
                   "v_accvgpr_read_b32 %1, a209\n\tv_mul_f32 %1, %3, %1\n\tv_accvgpr_write_b32 a209, %1\n\t"
                   "v_accvgpr_read_b32 %0, a210\n\tv_mul_f32 %0, %3, %0\n\tv_accvgpr_write_b32 a210, %0\n\t"
                   "v_accvgpr_read_b32 %1, a211\n\tv_mul_f32 %1, %3, %1\n\tv_accvgpr_write_b32 a211, %1\n\t"
                   "v_accvgpr_read_b32 %0, a212\n\tv_mul_f32 %0, %3, %0\n\tv_accvgpr_write_b32 a212, %0\n\t"
                   "v_accvgpr_read_b32 %1, a213\n\tv_mul_f32 %1, %3, %1\n\tv_accvgpr_write_b32 a213, %1\n\t"
                   "v_accvgpr_read_b32 %0, a214\n\tv_mul_f32 %0, %3, %0\n\tv_accvgpr_write_b32 a214, %0\n\t"
                   "v_accvgpr_read_b32 %1, a215\n\tv_mul_f32 %1, %3, %1\n\tv_accvgpr_write_b32 a215, %1\n\t"
                   "v_accvgpr_read_b32 %0, a216\n\tv_mul_f32 %0, %3, %0\n\tv_accvgpr_write_b32 a216, %0\n\t"
                   "v_accvgpr_read_b32 %1, a217\n\tv_mul_f32 %1, %3, %1\n\tv_accvgpr_write_b32 a217, %1\n\t"
                   "v_accvgpr_read_b32 %0, a218\n\tv_mul_f32 %0, %3, %0\n\tv_accvgpr_write_b32 a218, %0\n\t"
                   "v_accvgpr_read_b32 %1, a219\n\tv_mul_f32 %1, %3, %1\n\tv_accvgpr_write_b32 a219, %1\n\t"
                   "v_accvgpr_read_b32 %0, a220\n\tv_mul_f32 %0, %3, %0\n\tv_accvgpr_write_b32 a220, %0\n\t"
                   "v_accvgpr_read_b32 %1, a221\n\tv_mul_f32 %1, %3, %1\n\tv_accvgpr_write_b32 a221, %1\n\t"
                   "v_accvgpr_read_b32 %0, a222\n\tv_mul_f32 %0, %3, %0\n\tv_accvgpr_write_b32 a222, %0\n\t"
                   "v_accvgpr_read_b32 %1, a223\n\tv_mul_f32 %1, %3, %1\n\tv_accvgpr_write_b32 a223, %1\n\t"
                   "v_accvgpr_read_b32 %0, a224\n\tv_mul_f32 %0, %3, %0\n\tv_accvgpr_write_b32 a224, %0\n\t"
                   "v_accvgpr_read_b32 %1, a225\n\tv_mul_f32 %1, %3, %1\n\tv_accvgpr_write_b32 a225, %1\n\t"
                   "v_accvgpr_read_b32 %0, a226\n\tv_mul_f32 %0, %3, %0\n\tv_accvgpr_write_b32 a226, %0\n\t"
                   "v_accvgpr_read_b32 %1, a227\n\tv_mul_f32 %1, %3, %1\n\tv_accvgpr_write_b32 a227, %1\n\t"
                   "v_accvgpr_read_b32 %0, a228\n\tv_mul_f32 %0, %3, %0\n\tv_accvgpr_write_b32 a228, %0\n\t"
                   "v_accvgpr_read_b32 %1, a229\n\tv_mul_f32 %1, %3, %1\n\tv_accvgpr_write_b32 a229, %1\n\t"
                   "v_accvgpr_read_b32 %0, a230\n\tv_mul_f32 %0, %3, %0\n\tv_accvgpr_write_b32 a230, %0\n\t"
                   "v_accvgpr_read_b32 %1, a231\n\tv_mul_f32 %1, %3, %1\n\tv_accvgpr_write_b32 a231, %1\n\t"
                   "v_accvgpr_read_b32 %0, a232\n\tv_mul_f32 %0, %3, %0\n\tv_accvgpr_write_b32 a232, %0\n\t"
                   "v_accvgpr_read_b32 %1, a233\n\tv_mul_f32 %1, %3, %1\n\tv_accvgpr_write_b32 a233, %1\n\t"
                   "v_accvgpr_read_b32 %0, a234\n\tv_mul_f32 %0, %3, %0\n\tv_accvgpr_write_b32 a234, %0\n\t"
                   "v_accvgpr_read_b32 %1, a235\n\tv_mul_f32 %1, %3, %1\n\tv_accvgpr_write_b32 a235, %1\n\t"
                   "v_accvgpr_read_b32 %0, a236\n\tv_mul_f32 %0, %3, %0\n\tv_accvgpr_write_b32 a236, %0\n\t"
                   "v_accvgpr_read_b32 %1, a237\n\tv_mul_f32 %1, %3, %1\n\tv_accvgpr_write_b32 a237, %1\n\t"
                   "v_accvgpr_read_b32 %0, a238\n\tv_mul_f32 %0, %3, %0\n\tv_accvgpr_write_b32 a238, %0\n\t"
                   "v_accvgpr_read_b32 %1, a239\n\tv_mul_f32 %1, %3, %1\n\tv_accvgpr_write_b32 a239, %1\n\t"
                   "v_accvgpr_read_b32 %0, a240\n\tv_mul_f32 %0, %3, %0\n\tv_accvgpr_write_b32 a240, %0\n\t"
                   "v_accvgpr_read_b32 %1, a241\n\tv_mul_f32 %1, %3, %1\n\tv_accvgpr_write_b32 a241, %1\n\t"
                   "v_accvgpr_read_b32 %0, a242\n\tv_mul_f32 %0, %3, %0\n\tv_accvgpr_write_b32 a242, %0\n\t"
                   "v_accvgpr_read_b32 %1, a243\n\tv_mul_f32 %1, %3, %1\n\tv_accvgpr_write_b32 a243, %1\n\t"
                   "v_accvgpr_read_b32 %0, a244\n\tv_mul_f32 %0, %3, %0\n\tv_accvgpr_write_b32 a244, %0\n\t"
                   "v_accvgpr_read_b32 %1, a245\n\tv_mul_f32 %1, %3, %1\n\tv_accvgpr_write_b32 a245, %1\n\t"
                   "v_accvgpr_read_b32 %0, a246\n\tv_mul_f32 %0, %3, %0\n\tv_accvgpr_write_b32 a246, %0\n\t"
                   "v_accvgpr_read_b32 %1, a247\n\tv_mul_f32 %1, %3, %1\n\tv_accvgpr_write_b32 a247, %1\n\t"
                   "v_accvgpr_read_b32 %0, a248\n\tv_mul_f32 %0, %3, %0\n\tv_accvgpr_write_b32 a248, %0\n\t"
                   "v_accvgpr_read_b32 %1, a249\n\tv_mul_f32 %1, %3, %1\n\tv_accvgpr_write_b32 a249, %1\n\t"
                   "v_accvgpr_read_b32 %0, a250\n\tv_mul_f32 %0, %3, %0\n\tv_accvgpr_write_b32 a250, %0\n\t"
                   "v_accvgpr_read_b32 %1, a251\n\tv_mul_f32 %1, %3, %1\n\tv_accvgpr_write_b32 a251, %1\n\t"
                   "v_accvgpr_read_b32 %0, a252\n\tv_mul_f32 %0, %3, %0\n\tv_accvgpr_write_b32 a252, %0\n\t"
                   "v_accvgpr_read_b32 %1, a253\n\tv_mul_f32 %1, %3, %1\n\tv_accvgpr_write_b32 a253, %1\n\t"
                   "v_accvgpr_read_b32 %0, a254\n\tv_mul_f32 %0, %3, %0\n\tv_accvgpr_write_b32 a254, %0\n\t"
                   "v_accvgpr_read_b32 %1, a255\n\tv_mul_f32 %1, %3, %1\n\tv_accvgpr_write_b32 a255, %1\n\t"
                   "s_nop 1"
                   : "=&v"(t0), "=&v"(t1) : "v"(c.fct[0]), "v"(c.fct[1]) : ACLOB);
      c.pend = false;
    }
  }
  asm volatile("s_nop 1" ::: "memory");
  GAP();
}
template <int T, bool HASPV, bool HASSM> DI void phaseB5(Ctx4& c) {
  phaseB5_<T, HASPV, HASSM>(c, std::make_integer_sequence<int, 32>{});
}

__global__ __launch_bounds__(256, 1) void attn5_kernel(const u16* __restrict__ qfrag, const u16* __restrict__ kc,
                                                       const u16* __restrict__ vc, const u16* __restrict__ wo,
                                                       u16* __restrict__ o, u16* __restrict__ part,
                                                       float* __restrict__ mlg, unsigned* __restrict__ cnt DBGARG) {
  extern __shared__ __attribute__((aligned(16))) char lds[];
  STAMP(0);
  const int tid = threadIdx.x, lane = tid & 63;
  const int w = __builtin_amdgcn_readfirstlane(tid >> 6);
  const int r = lane & 31, h = lane >> 5;
  const int bid = blockIdx.x;
  const int xcd = bid & 7, j = bid >> 3;
  const int grp = (j >> 2) * 8 + xcd, sp = j & 3;
  const int b = grp >> 3, kvh = grp & 7;

  const u16* Kw = kc + ((size_t)(b * HKV + kvh) * SK + 256 * sp) * DH;
  const u16* Vw = vc + ((size_t)(b * HKV + kvh) * SK + 256 * sp) * DH;

  Ctx4 c;
  c.pfd = 0;
  c.ring = lds + w * 2048;
  c.rk = __builtin_amdgcn_make_buffer_rsrc((void*)Kw, 0, 256 * DH * 2, 0x00020000);
  c.rv = __builtin_amdgcn_make_buffer_rsrc((void*)Vw, 0, 256 * DH * 2, 0x00020000);
#pragma unroll
  for (int pi = 0; pi < 2; ++pi) {
    const int krow = 4 * (2 * w + pi) + (lane >> 4);
    c.koff[pi] = krow * 512 + (((lane & 15) ^ (krow & 15)) * 16);
    const int vrow = 2 * (2 * w + pi) + (lane >> 5);
    c.voff[pi] = vrow * 512 + (((lane & 31) ^ ((vrow & 3) << 2)) * 16);
  }
  {
    const u16* qp = qfrag + ((size_t)(grp * 8 + 2 * w) * 16 * 64 + lane) * 8;
#pragma unroll
    for (int qb = 0; qb < 2; ++qb)
#pragma unroll
      for (int s = 0; s < 16; ++s) c.qf[qb][s] = *(const bf16x8*)(qp + (size_t)(qb * 16 + s) * 512);
  }
  GAP();
  dma5_unit<0>(c); dma5_unit<1>(c); dma5_unit<2>(c); dma5_unit<3>(c); dma5_unit<4>(c); dma5_unit<5>(c); dma5_unit<6>(c);
  dma5_unit<7>(c); dma5_unit<8>(c); dma5_unit<9>(c); dma5_unit<10>(c); dma5_unit<11>(c); dma5_unit<12>(c); dma5_unit<13>(c);
#pragma unroll
  for (int qb = 0; qb < 2; ++qb)
#pragma unroll
    for (int s = 0; s < 16; ++s) asm volatile("" : "+v"(c.qf[qb][s]));
  if (w == 3) {
#pragma unroll
    for (int s = 0; s < 16; ++s) c.qf[1][s] = bf16x8{0, 0, 0, 0, 0, 0, 0, 0};
  }
  STAMP(1);
  {
    const unsigned rb = (unsigned)(size_t)lds;
#pragma unroll
    for (int ss = 0; ss < 8; ++ss) c.kaddr[ss] = rb + r * 256 + ((((2 * ss + h) ^ (r & 15))) * 16);
    const int g = lane >> 4, qq = (lane >> 2) & 3, pp = lane & 3;
#pragma unroll
    for (int d = 0; d < 4; ++d)
      c.vaddr[d] = rb + (4 * (g >> 1) + qq) * 512 + ((64 * d + 32 * (g & 1) + 8 * pp) ^ (qq << 6));
  }
  c.pend = false;
  c.fct[0] = 1.f; c.fct[1] = 1.f;

  phaseA5<0>(c); phaseB5<0, false, true>(c); STAMP(2);
  phaseA5<1>(c); phaseB5<1, true, true>(c); STAMP(3);
  phaseA5<2>(c); phaseB5<2, true, true>(c); STAMP(4);
  phaseA5<3>(c); phaseB5<3, true, true>(c); STAMP(5);
  phaseA5<4>(c); phaseB5<4, true, true>(c); STAMP(6);
  phaseA5<5>(c); phaseB5<5, true, true>(c); STAMP(7);
  phaseA5<6>(c); phaseB5<6, true, true>(c); STAMP(8);
  {
    int li = (bid * 4 + w) * 28 + (lane < 28 ? lane : 27);
    asm volatile("global_load_dword %0, %1, off" : "+v"(c.pfd) : "v"(wo + (size_t)li * 64) : "memory");
  }
  phaseA5<7>(c); phaseB5<7, true, true>(c); STAMP(9);
  phaseB5<8, true, false>(c);
  asm volatile("s_nop 7\n\ts_nop 7" :: "v"(c.pfd) : "memory");
  STAMP(13);

  float lt[2], inv[2];
#pragma unroll
  for (int qb = 0; qb < 2; ++qb) {
    auto rr = __builtin_amdgcn_permlane32_swap(__float_as_uint(c.l[qb]), __float_as_uint(c.l[qb]), false, false);
    lt[qb] = __uint_as_float(rr[0]) + __uint_as_float(rr[1]);
    inv[qb] = 1.f / lt[qb];
  }
  __amdgpu_buffer_rsrc_t rml = __builtin_amdgcn_make_buffer_rsrc((void*)(mlg + (size_t)grp * 4 * 512), 0, 4 * 512 * 4, 0x00020000);
  __amdgpu_buffer_rsrc_t rpt = __builtin_amdgcn_make_buffer_rsrc((void*)(part + (size_t)grp * 16 * 16384), 0, 16 * 32768, 0x00020000);
  if (h == 0) {
#pragma unroll
    for (int qb = 0; qb < 2; ++qb) {
      u32x2 v = {__float_as_uint(c.m[qb]), __float_as_uint(lt[qb])};
      __builtin_amdgcn_raw_buffer_store_b64(v, rml, (sp * 256 + 64 * w + 32 * qb + r) * 8, 0, 16);
    }
  }
  switch (sp) {
    case 0: {
      { f32x4 a = acc_read4<32>(), b2 = acc_read4<36>(); a *= inv[0]; b2 *= inv[0];
        u32x4 v = {cvtpk(a[0], a[1]), cvtpk(a[2], a[3]), cvtpk(b2[0], b2[1]), cvtpk(b2[2], b2[3])};
        __builtin_amdgcn_raw_buffer_store_b128(v, rpt, 32768 + w * 8192 + 0 + lane * 16, 0, 16); }
      { f32x4 a = acc_read4<40>(), b2 = acc_read4<44>(); a *= inv[0]; b2 *= inv[0];
        u32x4 v = {cvtpk(a[0], a[1]), cvtpk(a[2], a[3]), cvtpk(b2[0], b2[1]), cvtpk(b2[2], b2[3])};
        __builtin_amdgcn_raw_buffer_store_b128(v, rpt, 32768 + w * 8192 + 1024 + lane * 16, 0, 16); }
      { f32x4 a = acc_read4<48>(), b2 = acc_read4<52>(); a *= inv[0]; b2 *= inv[0];
        u32x4 v = {cvtpk(a[0], a[1]), cvtpk(a[2], a[3]), cvtpk(b2[0], b2[1]), cvtpk(b2[2], b2[3])};
        __builtin_amdgcn_raw_buffer_store_b128(v, rpt, 32768 + w * 8192 + 2048 + lane * 16, 0, 16); }
      { f32x4 a = acc_read4<56>(), b2 = acc_read4<60>(); a *= inv[0]; b2 *= inv[0];
        u32x4 v = {cvtpk(a[0], a[1]), cvtpk(a[2], a[3]), cvtpk(b2[0], b2[1]), cvtpk(b2[2], b2[3])};
        __builtin_amdgcn_raw_buffer_store_b128(v, rpt, 32768 + w * 8192 + 3072 + lane * 16, 0, 16); }
      { f32x4 a = acc_read4<160>(), b2 = acc_read4<164>(); a *= inv[1]; b2 *= inv[1];
        u32x4 v = {cvtpk(a[0], a[1]), cvtpk(a[2], a[3]), cvtpk(b2[0], b2[1]), cvtpk(b2[2], b2[3])};
        __builtin_amdgcn_raw_buffer_store_b128(v, rpt, 32768 + w * 8192 + 4096 + lane * 16, 0, 16); }
      { f32x4 a = acc_read4<168>(), b2 = acc_read4<172>(); a *= inv[1]; b2 *= inv[1];
        u32x4 v = {cvtpk(a[0], a[1]), cvtpk(a[2], a[3]), cvtpk(b2[0], b2[1]), cvtpk(b2[2], b2[3])};
        __builtin_amdgcn_raw_buffer_store_b128(v, rpt, 32768 + w * 8192 + 5120 + lane * 16, 0, 16); }
      { f32x4 a = acc_read4<176>(), b2 = acc_read4<180>(); a *= inv[1]; b2 *= inv[1];
        u32x4 v = {cvtpk(a[0], a[1]), cvtpk(a[2], a[3]), cvtpk(b2[0], b2[1]), cvtpk(b2[2], b2[3])};
        __builtin_amdgcn_raw_buffer_store_b128(v, rpt, 32768 + w * 8192 + 6144 + lane * 16, 0, 16); }
      { f32x4 a = acc_read4<184>(), b2 = acc_read4<188>(); a *= inv[1]; b2 *= inv[1];
        u32x4 v = {cvtpk(a[0], a[1]), cvtpk(a[2], a[3]), cvtpk(b2[0], b2[1]), cvtpk(b2[2], b2[3])};
        __builtin_amdgcn_raw_buffer_store_b128(v, rpt, 32768 + w * 8192 + 7168 + lane * 16, 0, 16); }
      { f32x4 a = acc_read4<64>(), b2 = acc_read4<68>(); a *= inv[0]; b2 *= inv[0];
        u32x4 v = {cvtpk(a[0], a[1]), cvtpk(a[2], a[3]), cvtpk(b2[0], b2[1]), cvtpk(b2[2], b2[3])};
        __builtin_amdgcn_raw_buffer_store_b128(v, rpt, 65536 + w * 8192 + 0 + lane * 16, 0, 16); }
      { f32x4 a = acc_read4<72>(), b2 = acc_read4<76>(); a *= inv[0]; b2 *= inv[0];
        u32x4 v = {cvtpk(a[0], a[1]), cvtpk(a[2], a[3]), cvtpk(b2[0], b2[1]), cvtpk(b2[2], b2[3])};
        __builtin_amdgcn_raw_buffer_store_b128(v, rpt, 65536 + w * 8192 + 1024 + lane * 16, 0, 16); }
      { f32x4 a = acc_read4<80>(), b2 = acc_read4<84>(); a *= inv[0]; b2 *= inv[0];
        u32x4 v = {cvtpk(a[0], a[1]), cvtpk(a[2], a[3]), cvtpk(b2[0], b2[1]), cvtpk(b2[2], b2[3])};
        __builtin_amdgcn_raw_buffer_store_b128(v, rpt, 65536 + w * 8192 + 2048 + lane * 16, 0, 16); }
      { f32x4 a = acc_read4<88>(), b2 = acc_read4<92>(); a *= inv[0]; b2 *= inv[0];
        u32x4 v = {cvtpk(a[0], a[1]), cvtpk(a[2], a[3]), cvtpk(b2[0], b2[1]), cvtpk(b2[2], b2[3])};
        __builtin_amdgcn_raw_buffer_store_b128(v, rpt, 65536 + w * 8192 + 3072 + lane * 16, 0, 16); }
      { f32x4 a = acc_read4<192>(), b2 = acc_read4<196>(); a *= inv[1]; b2 *= inv[1];
        u32x4 v = {cvtpk(a[0], a[1]), cvtpk(a[2], a[3]), cvtpk(b2[0], b2[1]), cvtpk(b2[2], b2[3])};
        __builtin_amdgcn_raw_buffer_store_b128(v, rpt, 65536 + w * 8192 + 4096 + lane * 16, 0, 16); }
      { f32x4 a = acc_read4<200>(), b2 = acc_read4<204>(); a *= inv[1]; b2 *= inv[1];
        u32x4 v = {cvtpk(a[0], a[1]), cvtpk(a[2], a[3]), cvtpk(b2[0], b2[1]), cvtpk(b2[2], b2[3])};
        __builtin_amdgcn_raw_buffer_store_b128(v, rpt, 65536 + w * 8192 + 5120 + lane * 16, 0, 16); }
      { f32x4 a = acc_read4<208>(), b2 = acc_read4<212>(); a *= inv[1]; b2 *= inv[1];
        u32x4 v = {cvtpk(a[0], a[1]), cvtpk(a[2], a[3]), cvtpk(b2[0], b2[1]), cvtpk(b2[2], b2[3])};
        __builtin_amdgcn_raw_buffer_store_b128(v, rpt, 65536 + w * 8192 + 6144 + lane * 16, 0, 16); }
      { f32x4 a = acc_read4<216>(), b2 = acc_read4<220>(); a *= inv[1]; b2 *= inv[1];
        u32x4 v = {cvtpk(a[0], a[1]), cvtpk(a[2], a[3]), cvtpk(b2[0], b2[1]), cvtpk(b2[2], b2[3])};
        __builtin_amdgcn_raw_buffer_store_b128(v, rpt, 65536 + w * 8192 + 7168 + lane * 16, 0, 16); }
      { f32x4 a = acc_read4<96>(), b2 = acc_read4<100>(); a *= inv[0]; b2 *= inv[0];
        u32x4 v = {cvtpk(a[0], a[1]), cvtpk(a[2], a[3]), cvtpk(b2[0], b2[1]), cvtpk(b2[2], b2[3])};
        __builtin_amdgcn_raw_buffer_store_b128(v, rpt, 98304 + w * 8192 + 0 + lane * 16, 0, 16); }
      { f32x4 a = acc_read4<104>(), b2 = acc_read4<108>(); a *= inv[0]; b2 *= inv[0];
        u32x4 v = {cvtpk(a[0], a[1]), cvtpk(a[2], a[3]), cvtpk(b2[0], b2[1]), cvtpk(b2[2], b2[3])};
        __builtin_amdgcn_raw_buffer_store_b128(v, rpt, 98304 + w * 8192 + 1024 + lane * 16, 0, 16); }
      { f32x4 a = acc_read4<112>(), b2 = acc_read4<116>(); a *= inv[0]; b2 *= inv[0];
        u32x4 v = {cvtpk(a[0], a[1]), cvtpk(a[2], a[3]), cvtpk(b2[0], b2[1]), cvtpk(b2[2], b2[3])};
        __builtin_amdgcn_raw_buffer_store_b128(v, rpt, 98304 + w * 8192 + 2048 + lane * 16, 0, 16); }
      { f32x4 a = acc_read4<120>(), b2 = acc_read4<124>(); a *= inv[0]; b2 *= inv[0];
        u32x4 v = {cvtpk(a[0], a[1]), cvtpk(a[2], a[3]), cvtpk(b2[0], b2[1]), cvtpk(b2[2], b2[3])};
        __builtin_amdgcn_raw_buffer_store_b128(v, rpt, 98304 + w * 8192 + 3072 + lane * 16, 0, 16); }
      { f32x4 a = acc_read4<224>(), b2 = acc_read4<228>(); a *= inv[1]; b2 *= inv[1];
        u32x4 v = {cvtpk(a[0], a[1]), cvtpk(a[2], a[3]), cvtpk(b2[0], b2[1]), cvtpk(b2[2], b2[3])};
        __builtin_amdgcn_raw_buffer_store_b128(v, rpt, 98304 + w * 8192 + 4096 + lane * 16, 0, 16); }
      { f32x4 a = acc_read4<232>(), b2 = acc_read4<236>(); a *= inv[1]; b2 *= inv[1];
        u32x4 v = {cvtpk(a[0], a[1]), cvtpk(a[2], a[3]), cvtpk(b2[0], b2[1]), cvtpk(b2[2], b2[3])};
        __builtin_amdgcn_raw_buffer_store_b128(v, rpt, 98304 + w * 8192 + 5120 + lane * 16, 0, 16); }
      { f32x4 a = acc_read4<240>(), b2 = acc_read4<244>(); a *= inv[1]; b2 *= inv[1];
        u32x4 v = {cvtpk(a[0], a[1]), cvtpk(a[2], a[3]), cvtpk(b2[0], b2[1]), cvtpk(b2[2], b2[3])};
        __builtin_amdgcn_raw_buffer_store_b128(v, rpt, 98304 + w * 8192 + 6144 + lane * 16, 0, 16); }
      { f32x4 a = acc_read4<248>(), b2 = acc_read4<252>(); a *= inv[1]; b2 *= inv[1];
        u32x4 v = {cvtpk(a[0], a[1]), cvtpk(a[2], a[3]), cvtpk(b2[0], b2[1]), cvtpk(b2[2], b2[3])};
        __builtin_amdgcn_raw_buffer_store_b128(v, rpt, 98304 + w * 8192 + 7168 + lane * 16, 0, 16); }
    } break;
    case 1: {
      { f32x4 a = acc_read4<0>(), b2 = acc_read4<4>(); a *= inv[0]; b2 *= inv[0];
        u32x4 v = {cvtpk(a[0], a[1]), cvtpk(a[2], a[3]), cvtpk(b2[0], b2[1]), cvtpk(b2[2], b2[3])};
        __builtin_amdgcn_raw_buffer_store_b128(v, rpt, 131072 + w * 8192 + 0 + lane * 16, 0, 16); }
      { f32x4 a = acc_read4<8>(), b2 = acc_read4<12>(); a *= inv[0]; b2 *= inv[0];
        u32x4 v = {cvtpk(a[0], a[1]), cvtpk(a[2], a[3]), cvtpk(b2[0], b2[1]), cvtpk(b2[2], b2[3])};
        __builtin_amdgcn_raw_buffer_store_b128(v, rpt, 131072 + w * 8192 + 1024 + lane * 16, 0, 16); }
      { f32x4 a = acc_read4<16>(), b2 = acc_read4<20>(); a *= inv[0]; b2 *= inv[0];
        u32x4 v = {cvtpk(a[0], a[1]), cvtpk(a[2], a[3]), cvtpk(b2[0], b2[1]), cvtpk(b2[2], b2[3])};
        __builtin_amdgcn_raw_buffer_store_b128(v, rpt, 131072 + w * 8192 + 2048 + lane * 16, 0, 16); }
      { f32x4 a = acc_read4<24>(), b2 = acc_read4<28>(); a *= inv[0]; b2 *= inv[0];
        u32x4 v = {cvtpk(a[0], a[1]), cvtpk(a[2], a[3]), cvtpk(b2[0], b2[1]), cvtpk(b2[2], b2[3])};
        __builtin_amdgcn_raw_buffer_store_b128(v, rpt, 131072 + w * 8192 + 3072 + lane * 16, 0, 16); }
      { f32x4 a = acc_read4<128>(), b2 = acc_read4<132>(); a *= inv[1]; b2 *= inv[1];
        u32x4 v = {cvtpk(a[0], a[1]), cvtpk(a[2], a[3]), cvtpk(b2[0], b2[1]), cvtpk(b2[2], b2[3])};
        __builtin_amdgcn_raw_buffer_store_b128(v, rpt, 131072 + w * 8192 + 4096 + lane * 16, 0, 16); }
      { f32x4 a = acc_read4<136>(), b2 = acc_read4<140>(); a *= inv[1]; b2 *= inv[1];
        u32x4 v = {cvtpk(a[0], a[1]), cvtpk(a[2], a[3]), cvtpk(b2[0], b2[1]), cvtpk(b2[2], b2[3])};
        __builtin_amdgcn_raw_buffer_store_b128(v, rpt, 131072 + w * 8192 + 5120 + lane * 16, 0, 16); }
      { f32x4 a = acc_read4<144>(), b2 = acc_read4<148>(); a *= inv[1]; b2 *= inv[1];
        u32x4 v = {cvtpk(a[0], a[1]), cvtpk(a[2], a[3]), cvtpk(b2[0], b2[1]), cvtpk(b2[2], b2[3])};
        __builtin_amdgcn_raw_buffer_store_b128(v, rpt, 131072 + w * 8192 + 6144 + lane * 16, 0, 16); }
      { f32x4 a = acc_read4<152>(), b2 = acc_read4<156>(); a *= inv[1]; b2 *= inv[1];
        u32x4 v = {cvtpk(a[0], a[1]), cvtpk(a[2], a[3]), cvtpk(b2[0], b2[1]), cvtpk(b2[2], b2[3])};
        __builtin_amdgcn_raw_buffer_store_b128(v, rpt, 131072 + w * 8192 + 7168 + lane * 16, 0, 16); }
      { f32x4 a = acc_read4<64>(), b2 = acc_read4<68>(); a *= inv[0]; b2 *= inv[0];
        u32x4 v = {cvtpk(a[0], a[1]), cvtpk(a[2], a[3]), cvtpk(b2[0], b2[1]), cvtpk(b2[2], b2[3])};
        __builtin_amdgcn_raw_buffer_store_b128(v, rpt, 196608 + w * 8192 + 0 + lane * 16, 0, 16); }
      { f32x4 a = acc_read4<72>(), b2 = acc_read4<76>(); a *= inv[0]; b2 *= inv[0];
        u32x4 v = {cvtpk(a[0], a[1]), cvtpk(a[2], a[3]), cvtpk(b2[0], b2[1]), cvtpk(b2[2], b2[3])};
        __builtin_amdgcn_raw_buffer_store_b128(v, rpt, 196608 + w * 8192 + 1024 + lane * 16, 0, 16); }
      { f32x4 a = acc_read4<80>(), b2 = acc_read4<84>(); a *= inv[0]; b2 *= inv[0];
        u32x4 v = {cvtpk(a[0], a[1]), cvtpk(a[2], a[3]), cvtpk(b2[0], b2[1]), cvtpk(b2[2], b2[3])};
        __builtin_amdgcn_raw_buffer_store_b128(v, rpt, 196608 + w * 8192 + 2048 + lane * 16, 0, 16); }
      { f32x4 a = acc_read4<88>(), b2 = acc_read4<92>(); a *= inv[0]; b2 *= inv[0];
        u32x4 v = {cvtpk(a[0], a[1]), cvtpk(a[2], a[3]), cvtpk(b2[0], b2[1]), cvtpk(b2[2], b2[3])};
        __builtin_amdgcn_raw_buffer_store_b128(v, rpt, 196608 + w * 8192 + 3072 + lane * 16, 0, 16); }
      { f32x4 a = acc_read4<192>(), b2 = acc_read4<196>(); a *= inv[1]; b2 *= inv[1];
        u32x4 v = {cvtpk(a[0], a[1]), cvtpk(a[2], a[3]), cvtpk(b2[0], b2[1]), cvtpk(b2[2], b2[3])};
        __builtin_amdgcn_raw_buffer_store_b128(v, rpt, 196608 + w * 8192 + 4096 + lane * 16, 0, 16); }
      { f32x4 a = acc_read4<200>(), b2 = acc_read4<204>(); a *= inv[1]; b2 *= inv[1];
        u32x4 v = {cvtpk(a[0], a[1]), cvtpk(a[2], a[3]), cvtpk(b2[0], b2[1]), cvtpk(b2[2], b2[3])};
        __builtin_amdgcn_raw_buffer_store_b128(v, rpt, 196608 + w * 8192 + 5120 + lane * 16, 0, 16); }
      { f32x4 a = acc_read4<208>(), b2 = acc_read4<212>(); a *= inv[1]; b2 *= inv[1];
        u32x4 v = {cvtpk(a[0], a[1]), cvtpk(a[2], a[3]), cvtpk(b2[0], b2[1]), cvtpk(b2[2], b2[3])};
        __builtin_amdgcn_raw_buffer_store_b128(v, rpt, 196608 + w * 8192 + 6144 + lane * 16, 0, 16); }
      { f32x4 a = acc_read4<216>(), b2 = acc_read4<220>(); a *= inv[1]; b2 *= inv[1];
        u32x4 v = {cvtpk(a[0], a[1]), cvtpk(a[2], a[3]), cvtpk(b2[0], b2[1]), cvtpk(b2[2], b2[3])};
        __builtin_amdgcn_raw_buffer_store_b128(v, rpt, 196608 + w * 8192 + 7168 + lane * 16, 0, 16); }
      { f32x4 a = acc_read4<96>(), b2 = acc_read4<100>(); a *= inv[0]; b2 *= inv[0];
        u32x4 v = {cvtpk(a[0], a[1]), cvtpk(a[2], a[3]), cvtpk(b2[0], b2[1]), cvtpk(b2[2], b2[3])};
        __builtin_amdgcn_raw_buffer_store_b128(v, rpt, 229376 + w * 8192 + 0 + lane * 16, 0, 16); }
      { f32x4 a = acc_read4<104>(), b2 = acc_read4<108>(); a *= inv[0]; b2 *= inv[0];
        u32x4 v = {cvtpk(a[0], a[1]), cvtpk(a[2], a[3]), cvtpk(b2[0], b2[1]), cvtpk(b2[2], b2[3])};
        __builtin_amdgcn_raw_buffer_store_b128(v, rpt, 229376 + w * 8192 + 1024 + lane * 16, 0, 16); }
      { f32x4 a = acc_read4<112>(), b2 = acc_read4<116>(); a *= inv[0]; b2 *= inv[0];
        u32x4 v = {cvtpk(a[0], a[1]), cvtpk(a[2], a[3]), cvtpk(b2[0], b2[1]), cvtpk(b2[2], b2[3])};
        __builtin_amdgcn_raw_buffer_store_b128(v, rpt, 229376 + w * 8192 + 2048 + lane * 16, 0, 16); }
      { f32x4 a = acc_read4<120>(), b2 = acc_read4<124>(); a *= inv[0]; b2 *= inv[0];
        u32x4 v = {cvtpk(a[0], a[1]), cvtpk(a[2], a[3]), cvtpk(b2[0], b2[1]), cvtpk(b2[2], b2[3])};
        __builtin_amdgcn_raw_buffer_store_b128(v, rpt, 229376 + w * 8192 + 3072 + lane * 16, 0, 16); }
      { f32x4 a = acc_read4<224>(), b2 = acc_read4<228>(); a *= inv[1]; b2 *= inv[1];
        u32x4 v = {cvtpk(a[0], a[1]), cvtpk(a[2], a[3]), cvtpk(b2[0], b2[1]), cvtpk(b2[2], b2[3])};
        __builtin_amdgcn_raw_buffer_store_b128(v, rpt, 229376 + w * 8192 + 4096 + lane * 16, 0, 16); }
      { f32x4 a = acc_read4<232>(), b2 = acc_read4<236>(); a *= inv[1]; b2 *= inv[1];
        u32x4 v = {cvtpk(a[0], a[1]), cvtpk(a[2], a[3]), cvtpk(b2[0], b2[1]), cvtpk(b2[2], b2[3])};
        __builtin_amdgcn_raw_buffer_store_b128(v, rpt, 229376 + w * 8192 + 5120 + lane * 16, 0, 16); }
      { f32x4 a = acc_read4<240>(), b2 = acc_read4<244>(); a *= inv[1]; b2 *= inv[1];
        u32x4 v = {cvtpk(a[0], a[1]), cvtpk(a[2], a[3]), cvtpk(b2[0], b2[1]), cvtpk(b2[2], b2[3])};
        __builtin_amdgcn_raw_buffer_store_b128(v, rpt, 229376 + w * 8192 + 6144 + lane * 16, 0, 16); }
      { f32x4 a = acc_read4<248>(), b2 = acc_read4<252>(); a *= inv[1]; b2 *= inv[1];
        u32x4 v = {cvtpk(a[0], a[1]), cvtpk(a[2], a[3]), cvtpk(b2[0], b2[1]), cvtpk(b2[2], b2[3])};
        __builtin_amdgcn_raw_buffer_store_b128(v, rpt, 229376 + w * 8192 + 7168 + lane * 16, 0, 16); }
    } break;
    case 2: {
      { f32x4 a = acc_read4<0>(), b2 = acc_read4<4>(); a *= inv[0]; b2 *= inv[0];
        u32x4 v = {cvtpk(a[0], a[1]), cvtpk(a[2], a[3]), cvtpk(b2[0], b2[1]), cvtpk(b2[2], b2[3])};
        __builtin_amdgcn_raw_buffer_store_b128(v, rpt, 262144 + w * 8192 + 0 + lane * 16, 0, 16); }
      { f32x4 a = acc_read4<8>(), b2 = acc_read4<12>(); a *= inv[0]; b2 *= inv[0];
        u32x4 v = {cvtpk(a[0], a[1]), cvtpk(a[2], a[3]), cvtpk(b2[0], b2[1]), cvtpk(b2[2], b2[3])};
        __builtin_amdgcn_raw_buffer_store_b128(v, rpt, 262144 + w * 8192 + 1024 + lane * 16, 0, 16); }
      { f32x4 a = acc_read4<16>(), b2 = acc_read4<20>(); a *= inv[0]; b2 *= inv[0];
        u32x4 v = {cvtpk(a[0], a[1]), cvtpk(a[2], a[3]), cvtpk(b2[0], b2[1]), cvtpk(b2[2], b2[3])};
        __builtin_amdgcn_raw_buffer_store_b128(v, rpt, 262144 + w * 8192 + 2048 + lane * 16, 0, 16); }
      { f32x4 a = acc_read4<24>(), b2 = acc_read4<28>(); a *= inv[0]; b2 *= inv[0];
        u32x4 v = {cvtpk(a[0], a[1]), cvtpk(a[2], a[3]), cvtpk(b2[0], b2[1]), cvtpk(b2[2], b2[3])};
        __builtin_amdgcn_raw_buffer_store_b128(v, rpt, 262144 + w * 8192 + 3072 + lane * 16, 0, 16); }
      { f32x4 a = acc_read4<128>(), b2 = acc_read4<132>(); a *= inv[1]; b2 *= inv[1];
        u32x4 v = {cvtpk(a[0], a[1]), cvtpk(a[2], a[3]), cvtpk(b2[0], b2[1]), cvtpk(b2[2], b2[3])};
        __builtin_amdgcn_raw_buffer_store_b128(v, rpt, 262144 + w * 8192 + 4096 + lane * 16, 0, 16); }
      { f32x4 a = acc_read4<136>(), b2 = acc_read4<140>(); a *= inv[1]; b2 *= inv[1];
        u32x4 v = {cvtpk(a[0], a[1]), cvtpk(a[2], a[3]), cvtpk(b2[0], b2[1]), cvtpk(b2[2], b2[3])};
        __builtin_amdgcn_raw_buffer_store_b128(v, rpt, 262144 + w * 8192 + 5120 + lane * 16, 0, 16); }
      { f32x4 a = acc_read4<144>(), b2 = acc_read4<148>(); a *= inv[1]; b2 *= inv[1];
        u32x4 v = {cvtpk(a[0], a[1]), cvtpk(a[2], a[3]), cvtpk(b2[0], b2[1]), cvtpk(b2[2], b2[3])};
        __builtin_amdgcn_raw_buffer_store_b128(v, rpt, 262144 + w * 8192 + 6144 + lane * 16, 0, 16); }
      { f32x4 a = acc_read4<152>(), b2 = acc_read4<156>(); a *= inv[1]; b2 *= inv[1];
        u32x4 v = {cvtpk(a[0], a[1]), cvtpk(a[2], a[3]), cvtpk(b2[0], b2[1]), cvtpk(b2[2], b2[3])};
        __builtin_amdgcn_raw_buffer_store_b128(v, rpt, 262144 + w * 8192 + 7168 + lane * 16, 0, 16); }
      { f32x4 a = acc_read4<32>(), b2 = acc_read4<36>(); a *= inv[0]; b2 *= inv[0];
        u32x4 v = {cvtpk(a[0], a[1]), cvtpk(a[2], a[3]), cvtpk(b2[0], b2[1]), cvtpk(b2[2], b2[3])};
        __builtin_amdgcn_raw_buffer_store_b128(v, rpt, 294912 + w * 8192 + 0 + lane * 16, 0, 16); }
      { f32x4 a = acc_read4<40>(), b2 = acc_read4<44>(); a *= inv[0]; b2 *= inv[0];
        u32x4 v = {cvtpk(a[0], a[1]), cvtpk(a[2], a[3]), cvtpk(b2[0], b2[1]), cvtpk(b2[2], b2[3])};
        __builtin_amdgcn_raw_buffer_store_b128(v, rpt, 294912 + w * 8192 + 1024 + lane * 16, 0, 16); }
      { f32x4 a = acc_read4<48>(), b2 = acc_read4<52>(); a *= inv[0]; b2 *= inv[0];
        u32x4 v = {cvtpk(a[0], a[1]), cvtpk(a[2], a[3]), cvtpk(b2[0], b2[1]), cvtpk(b2[2], b2[3])};
        __builtin_amdgcn_raw_buffer_store_b128(v, rpt, 294912 + w * 8192 + 2048 + lane * 16, 0, 16); }
      { f32x4 a = acc_read4<56>(), b2 = acc_read4<60>(); a *= inv[0]; b2 *= inv[0];
        u32x4 v = {cvtpk(a[0], a[1]), cvtpk(a[2], a[3]), cvtpk(b2[0], b2[1]), cvtpk(b2[2], b2[3])};
        __builtin_amdgcn_raw_buffer_store_b128(v, rpt, 294912 + w * 8192 + 3072 + lane * 16, 0, 16); }
      { f32x4 a = acc_read4<160>(), b2 = acc_read4<164>(); a *= inv[1]; b2 *= inv[1];
        u32x4 v = {cvtpk(a[0], a[1]), cvtpk(a[2], a[3]), cvtpk(b2[0], b2[1]), cvtpk(b2[2], b2[3])};
        __builtin_amdgcn_raw_buffer_store_b128(v, rpt, 294912 + w * 8192 + 4096 + lane * 16, 0, 16); }
      { f32x4 a = acc_read4<168>(), b2 = acc_read4<172>(); a *= inv[1]; b2 *= inv[1];
        u32x4 v = {cvtpk(a[0], a[1]), cvtpk(a[2], a[3]), cvtpk(b2[0], b2[1]), cvtpk(b2[2], b2[3])};
        __builtin_amdgcn_raw_buffer_store_b128(v, rpt, 294912 + w * 8192 + 5120 + lane * 16, 0, 16); }
      { f32x4 a = acc_read4<176>(), b2 = acc_read4<180>(); a *= inv[1]; b2 *= inv[1];
        u32x4 v = {cvtpk(a[0], a[1]), cvtpk(a[2], a[3]), cvtpk(b2[0], b2[1]), cvtpk(b2[2], b2[3])};
        __builtin_amdgcn_raw_buffer_store_b128(v, rpt, 294912 + w * 8192 + 6144 + lane * 16, 0, 16); }
      { f32x4 a = acc_read4<184>(), b2 = acc_read4<188>(); a *= inv[1]; b2 *= inv[1];
        u32x4 v = {cvtpk(a[0], a[1]), cvtpk(a[2], a[3]), cvtpk(b2[0], b2[1]), cvtpk(b2[2], b2[3])};
        __builtin_amdgcn_raw_buffer_store_b128(v, rpt, 294912 + w * 8192 + 7168 + lane * 16, 0, 16); }
      { f32x4 a = acc_read4<96>(), b2 = acc_read4<100>(); a *= inv[0]; b2 *= inv[0];
        u32x4 v = {cvtpk(a[0], a[1]), cvtpk(a[2], a[3]), cvtpk(b2[0], b2[1]), cvtpk(b2[2], b2[3])};
        __builtin_amdgcn_raw_buffer_store_b128(v, rpt, 360448 + w * 8192 + 0 + lane * 16, 0, 16); }
      { f32x4 a = acc_read4<104>(), b2 = acc_read4<108>(); a *= inv[0]; b2 *= inv[0];
        u32x4 v = {cvtpk(a[0], a[1]), cvtpk(a[2], a[3]), cvtpk(b2[0], b2[1]), cvtpk(b2[2], b2[3])};
        __builtin_amdgcn_raw_buffer_store_b128(v, rpt, 360448 + w * 8192 + 1024 + lane * 16, 0, 16); }
      { f32x4 a = acc_read4<112>(), b2 = acc_read4<116>(); a *= inv[0]; b2 *= inv[0];
        u32x4 v = {cvtpk(a[0], a[1]), cvtpk(a[2], a[3]), cvtpk(b2[0], b2[1]), cvtpk(b2[2], b2[3])};
        __builtin_amdgcn_raw_buffer_store_b128(v, rpt, 360448 + w * 8192 + 2048 + lane * 16, 0, 16); }
      { f32x4 a = acc_read4<120>(), b2 = acc_read4<124>(); a *= inv[0]; b2 *= inv[0];
        u32x4 v = {cvtpk(a[0], a[1]), cvtpk(a[2], a[3]), cvtpk(b2[0], b2[1]), cvtpk(b2[2], b2[3])};
        __builtin_amdgcn_raw_buffer_store_b128(v, rpt, 360448 + w * 8192 + 3072 + lane * 16, 0, 16); }
      { f32x4 a = acc_read4<224>(), b2 = acc_read4<228>(); a *= inv[1]; b2 *= inv[1];
        u32x4 v = {cvtpk(a[0], a[1]), cvtpk(a[2], a[3]), cvtpk(b2[0], b2[1]), cvtpk(b2[2], b2[3])};
        __builtin_amdgcn_raw_buffer_store_b128(v, rpt, 360448 + w * 8192 + 4096 + lane * 16, 0, 16); }
      { f32x4 a = acc_read4<232>(), b2 = acc_read4<236>(); a *= inv[1]; b2 *= inv[1];
        u32x4 v = {cvtpk(a[0], a[1]), cvtpk(a[2], a[3]), cvtpk(b2[0], b2[1]), cvtpk(b2[2], b2[3])};
        __builtin_amdgcn_raw_buffer_store_b128(v, rpt, 360448 + w * 8192 + 5120 + lane * 16, 0, 16); }
      { f32x4 a = acc_read4<240>(), b2 = acc_read4<244>(); a *= inv[1]; b2 *= inv[1];
        u32x4 v = {cvtpk(a[0], a[1]), cvtpk(a[2], a[3]), cvtpk(b2[0], b2[1]), cvtpk(b2[2], b2[3])};
        __builtin_amdgcn_raw_buffer_store_b128(v, rpt, 360448 + w * 8192 + 6144 + lane * 16, 0, 16); }
      { f32x4 a = acc_read4<248>(), b2 = acc_read4<252>(); a *= inv[1]; b2 *= inv[1];
        u32x4 v = {cvtpk(a[0], a[1]), cvtpk(a[2], a[3]), cvtpk(b2[0], b2[1]), cvtpk(b2[2], b2[3])};
        __builtin_amdgcn_raw_buffer_store_b128(v, rpt, 360448 + w * 8192 + 7168 + lane * 16, 0, 16); }
    } break;
    case 3: {
      { f32x4 a = acc_read4<0>(), b2 = acc_read4<4>(); a *= inv[0]; b2 *= inv[0];
        u32x4 v = {cvtpk(a[0], a[1]), cvtpk(a[2], a[3]), cvtpk(b2[0], b2[1]), cvtpk(b2[2], b2[3])};
        __builtin_amdgcn_raw_buffer_store_b128(v, rpt, 393216 + w * 8192 + 0 + lane * 16, 0, 16); }
      { f32x4 a = acc_read4<8>(), b2 = acc_read4<12>(); a *= inv[0]; b2 *= inv[0];
        u32x4 v = {cvtpk(a[0], a[1]), cvtpk(a[2], a[3]), cvtpk(b2[0], b2[1]), cvtpk(b2[2], b2[3])};
        __builtin_amdgcn_raw_buffer_store_b128(v, rpt, 393216 + w * 8192 + 1024 + lane * 16, 0, 16); }
      { f32x4 a = acc_read4<16>(), b2 = acc_read4<20>(); a *= inv[0]; b2 *= inv[0];
        u32x4 v = {cvtpk(a[0], a[1]), cvtpk(a[2], a[3]), cvtpk(b2[0], b2[1]), cvtpk(b2[2], b2[3])};
        __builtin_amdgcn_raw_buffer_store_b128(v, rpt, 393216 + w * 8192 + 2048 + lane * 16, 0, 16); }
      { f32x4 a = acc_read4<24>(), b2 = acc_read4<28>(); a *= inv[0]; b2 *= inv[0];
        u32x4 v = {cvtpk(a[0], a[1]), cvtpk(a[2], a[3]), cvtpk(b2[0], b2[1]), cvtpk(b2[2], b2[3])};
        __builtin_amdgcn_raw_buffer_store_b128(v, rpt, 393216 + w * 8192 + 3072 + lane * 16, 0, 16); }
      { f32x4 a = acc_read4<128>(), b2 = acc_read4<132>(); a *= inv[1]; b2 *= inv[1];
        u32x4 v = {cvtpk(a[0], a[1]), cvtpk(a[2], a[3]), cvtpk(b2[0], b2[1]), cvtpk(b2[2], b2[3])};
        __builtin_amdgcn_raw_buffer_store_b128(v, rpt, 393216 + w * 8192 + 4096 + lane * 16, 0, 16); }
      { f32x4 a = acc_read4<136>(), b2 = acc_read4<140>(); a *= inv[1]; b2 *= inv[1];
        u32x4 v = {cvtpk(a[0], a[1]), cvtpk(a[2], a[3]), cvtpk(b2[0], b2[1]), cvtpk(b2[2], b2[3])};
        __builtin_amdgcn_raw_buffer_store_b128(v, rpt, 393216 + w * 8192 + 5120 + lane * 16, 0, 16); }
      { f32x4 a = acc_read4<144>(), b2 = acc_read4<148>(); a *= inv[1]; b2 *= inv[1];
        u32x4 v = {cvtpk(a[0], a[1]), cvtpk(a[2], a[3]), cvtpk(b2[0], b2[1]), cvtpk(b2[2], b2[3])};
        __builtin_amdgcn_raw_buffer_store_b128(v, rpt, 393216 + w * 8192 + 6144 + lane * 16, 0, 16); }
      { f32x4 a = acc_read4<152>(), b2 = acc_read4<156>(); a *= inv[1]; b2 *= inv[1];
        u32x4 v = {cvtpk(a[0], a[1]), cvtpk(a[2], a[3]), cvtpk(b2[0], b2[1]), cvtpk(b2[2], b2[3])};
        __builtin_amdgcn_raw_buffer_store_b128(v, rpt, 393216 + w * 8192 + 7168 + lane * 16, 0, 16); }
      { f32x4 a = acc_read4<32>(), b2 = acc_read4<36>(); a *= inv[0]; b2 *= inv[0];
        u32x4 v = {cvtpk(a[0], a[1]), cvtpk(a[2], a[3]), cvtpk(b2[0], b2[1]), cvtpk(b2[2], b2[3])};
        __builtin_amdgcn_raw_buffer_store_b128(v, rpt, 425984 + w * 8192 + 0 + lane * 16, 0, 16); }
      { f32x4 a = acc_read4<40>(), b2 = acc_read4<44>(); a *= inv[0]; b2 *= inv[0];
        u32x4 v = {cvtpk(a[0], a[1]), cvtpk(a[2], a[3]), cvtpk(b2[0], b2[1]), cvtpk(b2[2], b2[3])};
        __builtin_amdgcn_raw_buffer_store_b128(v, rpt, 425984 + w * 8192 + 1024 + lane * 16, 0, 16); }
      { f32x4 a = acc_read4<48>(), b2 = acc_read4<52>(); a *= inv[0]; b2 *= inv[0];
        u32x4 v = {cvtpk(a[0], a[1]), cvtpk(a[2], a[3]), cvtpk(b2[0], b2[1]), cvtpk(b2[2], b2[3])};
        __builtin_amdgcn_raw_buffer_store_b128(v, rpt, 425984 + w * 8192 + 2048 + lane * 16, 0, 16); }
      { f32x4 a = acc_read4<56>(), b2 = acc_read4<60>(); a *= inv[0]; b2 *= inv[0];
        u32x4 v = {cvtpk(a[0], a[1]), cvtpk(a[2], a[3]), cvtpk(b2[0], b2[1]), cvtpk(b2[2], b2[3])};
        __builtin_amdgcn_raw_buffer_store_b128(v, rpt, 425984 + w * 8192 + 3072 + lane * 16, 0, 16); }
      { f32x4 a = acc_read4<160>(), b2 = acc_read4<164>(); a *= inv[1]; b2 *= inv[1];
        u32x4 v = {cvtpk(a[0], a[1]), cvtpk(a[2], a[3]), cvtpk(b2[0], b2[1]), cvtpk(b2[2], b2[3])};
        __builtin_amdgcn_raw_buffer_store_b128(v, rpt, 425984 + w * 8192 + 4096 + lane * 16, 0, 16); }
      { f32x4 a = acc_read4<168>(), b2 = acc_read4<172>(); a *= inv[1]; b2 *= inv[1];
        u32x4 v = {cvtpk(a[0], a[1]), cvtpk(a[2], a[3]), cvtpk(b2[0], b2[1]), cvtpk(b2[2], b2[3])};
        __builtin_amdgcn_raw_buffer_store_b128(v, rpt, 425984 + w * 8192 + 5120 + lane * 16, 0, 16); }
      { f32x4 a = acc_read4<176>(), b2 = acc_read4<180>(); a *= inv[1]; b2 *= inv[1];
        u32x4 v = {cvtpk(a[0], a[1]), cvtpk(a[2], a[3]), cvtpk(b2[0], b2[1]), cvtpk(b2[2], b2[3])};
        __builtin_amdgcn_raw_buffer_store_b128(v, rpt, 425984 + w * 8192 + 6144 + lane * 16, 0, 16); }
      { f32x4 a = acc_read4<184>(), b2 = acc_read4<188>(); a *= inv[1]; b2 *= inv[1];
        u32x4 v = {cvtpk(a[0], a[1]), cvtpk(a[2], a[3]), cvtpk(b2[0], b2[1]), cvtpk(b2[2], b2[3])};
        __builtin_amdgcn_raw_buffer_store_b128(v, rpt, 425984 + w * 8192 + 7168 + lane * 16, 0, 16); }
      { f32x4 a = acc_read4<64>(), b2 = acc_read4<68>(); a *= inv[0]; b2 *= inv[0];
        u32x4 v = {cvtpk(a[0], a[1]), cvtpk(a[2], a[3]), cvtpk(b2[0], b2[1]), cvtpk(b2[2], b2[3])};
        __builtin_amdgcn_raw_buffer_store_b128(v, rpt, 458752 + w * 8192 + 0 + lane * 16, 0, 16); }
      { f32x4 a = acc_read4<72>(), b2 = acc_read4<76>(); a *= inv[0]; b2 *= inv[0];
        u32x4 v = {cvtpk(a[0], a[1]), cvtpk(a[2], a[3]), cvtpk(b2[0], b2[1]), cvtpk(b2[2], b2[3])};
        __builtin_amdgcn_raw_buffer_store_b128(v, rpt, 458752 + w * 8192 + 1024 + lane * 16, 0, 16); }
      { f32x4 a = acc_read4<80>(), b2 = acc_read4<84>(); a *= inv[0]; b2 *= inv[0];
        u32x4 v = {cvtpk(a[0], a[1]), cvtpk(a[2], a[3]), cvtpk(b2[0], b2[1]), cvtpk(b2[2], b2[3])};
        __builtin_amdgcn_raw_buffer_store_b128(v, rpt, 458752 + w * 8192 + 2048 + lane * 16, 0, 16); }
      { f32x4 a = acc_read4<88>(), b2 = acc_read4<92>(); a *= inv[0]; b2 *= inv[0];
        u32x4 v = {cvtpk(a[0], a[1]), cvtpk(a[2], a[3]), cvtpk(b2[0], b2[1]), cvtpk(b2[2], b2[3])};
        __builtin_amdgcn_raw_buffer_store_b128(v, rpt, 458752 + w * 8192 + 3072 + lane * 16, 0, 16); }
      { f32x4 a = acc_read4<192>(), b2 = acc_read4<196>(); a *= inv[1]; b2 *= inv[1];
        u32x4 v = {cvtpk(a[0], a[1]), cvtpk(a[2], a[3]), cvtpk(b2[0], b2[1]), cvtpk(b2[2], b2[3])};
        __builtin_amdgcn_raw_buffer_store_b128(v, rpt, 458752 + w * 8192 + 4096 + lane * 16, 0, 16); }
      { f32x4 a = acc_read4<200>(), b2 = acc_read4<204>(); a *= inv[1]; b2 *= inv[1];
        u32x4 v = {cvtpk(a[0], a[1]), cvtpk(a[2], a[3]), cvtpk(b2[0], b2[1]), cvtpk(b2[2], b2[3])};
        __builtin_amdgcn_raw_buffer_store_b128(v, rpt, 458752 + w * 8192 + 5120 + lane * 16, 0, 16); }
      { f32x4 a = acc_read4<208>(), b2 = acc_read4<212>(); a *= inv[1]; b2 *= inv[1];
        u32x4 v = {cvtpk(a[0], a[1]), cvtpk(a[2], a[3]), cvtpk(b2[0], b2[1]), cvtpk(b2[2], b2[3])};
        __builtin_amdgcn_raw_buffer_store_b128(v, rpt, 458752 + w * 8192 + 6144 + lane * 16, 0, 16); }
      { f32x4 a = acc_read4<216>(), b2 = acc_read4<220>(); a *= inv[1]; b2 *= inv[1];
        u32x4 v = {cvtpk(a[0], a[1]), cvtpk(a[2], a[3]), cvtpk(b2[0], b2[1]), cvtpk(b2[2], b2[3])};
        __builtin_amdgcn_raw_buffer_store_b128(v, rpt, 458752 + w * 8192 + 7168 + lane * 16, 0, 16); }
    } break;
  }
  asm volatile("s_waitcnt vmcnt(0)" ::: "memory");
  __syncthreads();
  STAMP(14);
  if (tid == 0) {
    __hip_atomic_fetch_add(cnt + grp, 1u, __ATOMIC_RELAXED, __HIP_MEMORY_SCOPE_AGENT);
    for (int it = 0; it < (1 << 24); ++it) {
      if (__hip_atomic_load(cnt + grp, __ATOMIC_RELAXED, __HIP_MEMORY_SCOPE_AGENT) >= 4u) break;
      __builtin_amdgcn_s_sleep(1);
    }
  }
  __syncthreads();
  STAMP(15);
  float fw[2][4];
#pragma unroll
  for (int qb = 0; qb < 2; ++qb) {
    float mm[4], ll[4];
#pragma unroll
    for (int s4 = 0; s4 < 4; ++s4) {
      u32x2 v = __builtin_amdgcn_raw_buffer_load_b64(rml, (s4 * 256 + 64 * w + 32 * qb + r) * 8, 0, 16);
      mm[s4] = __uint_as_float(v[0]); ll[s4] = __uint_as_float(v[1]);
    }
    const float M = fmaxf(fmaxf(mm[0], mm[1]), fmaxf(mm[2], mm[3]));
    float e[4], L = 0.f;
#pragma unroll
    for (int s4 = 0; s4 < 4; ++s4) { e[s4] = __builtin_amdgcn_exp2f(mm[s4] - M) * ll[s4]; L += e[s4]; }
    const float iL = 1.f / L;
#pragma unroll
    for (int s4 = 0; s4 < 4; ++s4) fw[qb][s4] = e[s4] * iL;
  }
  switch (sp) {
    case 0: {
      { const int R = 64 * w + 0 + r; const int hl = R >= 112 ? 1 : 0; const int orow = b * SQ + R - 112 * hl;
        { f32x4 a = acc_read4<0>(), b2 = acc_read4<4>(); const float fo = fw[0][0] * inv[0]; a *= fo; b2 *= fo;
          { u32x4 v = __builtin_amdgcn_raw_buffer_load_b128(rpt, 131072 + w * 8192 + 0 + lane * 16, 0, 16); const float f = fw[0][1];
            a[0] += f * __uint_as_float(v[0] << 16); a[1] += f * __uint_as_float(v[0] & 0xffff0000u); a[2] += f * __uint_as_float(v[1] << 16); a[3] += f * __uint_as_float(v[1] & 0xffff0000u);
            b2[0] += f * __uint_as_float(v[2] << 16); b2[1] += f * __uint_as_float(v[2] & 0xffff0000u); b2[2] += f * __uint_as_float(v[3] << 16); b2[3] += f * __uint_as_float(v[3] & 0xffff0000u); }
          { u32x4 v = __builtin_amdgcn_raw_buffer_load_b128(rpt, 262144 + w * 8192 + 0 + lane * 16, 0, 16); const float f = fw[0][2];
            a[0] += f * __uint_as_float(v[0] << 16); a[1] += f * __uint_as_float(v[0] & 0xffff0000u); a[2] += f * __uint_as_float(v[1] << 16); a[3] += f * __uint_as_float(v[1] & 0xffff0000u);
            b2[0] += f * __uint_as_float(v[2] << 16); b2[1] += f * __uint_as_float(v[2] & 0xffff0000u); b2[2] += f * __uint_as_float(v[3] << 16); b2[3] += f * __uint_as_float(v[3] & 0xffff0000u); }
          { u32x4 v = __builtin_amdgcn_raw_buffer_load_b128(rpt, 393216 + w * 8192 + 0 + lane * 16, 0, 16); const float f = fw[0][3];
            a[0] += f * __uint_as_float(v[0] << 16); a[1] += f * __uint_as_float(v[0] & 0xffff0000u); a[2] += f * __uint_as_float(v[1] << 16); a[3] += f * __uint_as_float(v[1] & 0xffff0000u);
            b2[0] += f * __uint_as_float(v[2] << 16); b2[1] += f * __uint_as_float(v[2] & 0xffff0000u); b2[2] += f * __uint_as_float(v[3] << 16); b2[3] += f * __uint_as_float(v[3] & 0xffff0000u); }
          if (R < 224) { u16* dst = o + ((size_t)((kvh * 2 + hl) * 8 + 0) * NROW + orow) * 32 + 0 + 4 * h;
            u32x2 v0 = {cvtpk(a[0], a[1]), cvtpk(a[2], a[3])}; u32x2 v1 = {cvtpk(b2[0], b2[1]), cvtpk(b2[2], b2[3])};
            *(u32x2*)dst = v0; *(u32x2*)(dst + 8) = v1; } }
        { f32x4 a = acc_read4<8>(), b2 = acc_read4<12>(); const float fo = fw[0][0] * inv[0]; a *= fo; b2 *= fo;
          { u32x4 v = __builtin_amdgcn_raw_buffer_load_b128(rpt, 131072 + w * 8192 + 1024 + lane * 16, 0, 16); const float f = fw[0][1];
            a[0] += f * __uint_as_float(v[0] << 16); a[1] += f * __uint_as_float(v[0] & 0xffff0000u); a[2] += f * __uint_as_float(v[1] << 16); a[3] += f * __uint_as_float(v[1] & 0xffff0000u);
            b2[0] += f * __uint_as_float(v[2] << 16); b2[1] += f * __uint_as_float(v[2] & 0xffff0000u); b2[2] += f * __uint_as_float(v[3] << 16); b2[3] += f * __uint_as_float(v[3] & 0xffff0000u); }
          { u32x4 v = __builtin_amdgcn_raw_buffer_load_b128(rpt, 262144 + w * 8192 + 1024 + lane * 16, 0, 16); const float f = fw[0][2];
            a[0] += f * __uint_as_float(v[0] << 16); a[1] += f * __uint_as_float(v[0] & 0xffff0000u); a[2] += f * __uint_as_float(v[1] << 16); a[3] += f * __uint_as_float(v[1] & 0xffff0000u);
            b2[0] += f * __uint_as_float(v[2] << 16); b2[1] += f * __uint_as_float(v[2] & 0xffff0000u); b2[2] += f * __uint_as_float(v[3] << 16); b2[3] += f * __uint_as_float(v[3] & 0xffff0000u); }
          { u32x4 v = __builtin_amdgcn_raw_buffer_load_b128(rpt, 393216 + w * 8192 + 1024 + lane * 16, 0, 16); const float f = fw[0][3];
            a[0] += f * __uint_as_float(v[0] << 16); a[1] += f * __uint_as_float(v[0] & 0xffff0000u); a[2] += f * __uint_as_float(v[1] << 16); a[3] += f * __uint_as_float(v[1] & 0xffff0000u);
            b2[0] += f * __uint_as_float(v[2] << 16); b2[1] += f * __uint_as_float(v[2] & 0xffff0000u); b2[2] += f * __uint_as_float(v[3] << 16); b2[3] += f * __uint_as_float(v[3] & 0xffff0000u); }
          if (R < 224) { u16* dst = o + ((size_t)((kvh * 2 + hl) * 8 + 0) * NROW + orow) * 32 + 16 + 4 * h;
            u32x2 v0 = {cvtpk(a[0], a[1]), cvtpk(a[2], a[3])}; u32x2 v1 = {cvtpk(b2[0], b2[1]), cvtpk(b2[2], b2[3])};
            *(u32x2*)dst = v0; *(u32x2*)(dst + 8) = v1; } }
        { f32x4 a = acc_read4<16>(), b2 = acc_read4<20>(); const float fo = fw[0][0] * inv[0]; a *= fo; b2 *= fo;
          { u32x4 v = __builtin_amdgcn_raw_buffer_load_b128(rpt, 131072 + w * 8192 + 2048 + lane * 16, 0, 16); const float f = fw[0][1];
            a[0] += f * __uint_as_float(v[0] << 16); a[1] += f * __uint_as_float(v[0] & 0xffff0000u); a[2] += f * __uint_as_float(v[1] << 16); a[3] += f * __uint_as_float(v[1] & 0xffff0000u);
            b2[0] += f * __uint_as_float(v[2] << 16); b2[1] += f * __uint_as_float(v[2] & 0xffff0000u); b2[2] += f * __uint_as_float(v[3] << 16); b2[3] += f * __uint_as_float(v[3] & 0xffff0000u); }
          { u32x4 v = __builtin_amdgcn_raw_buffer_load_b128(rpt, 262144 + w * 8192 + 2048 + lane * 16, 0, 16); const float f = fw[0][2];
            a[0] += f * __uint_as_float(v[0] << 16); a[1] += f * __uint_as_float(v[0] & 0xffff0000u); a[2] += f * __uint_as_float(v[1] << 16); a[3] += f * __uint_as_float(v[1] & 0xffff0000u);
            b2[0] += f * __uint_as_float(v[2] << 16); b2[1] += f * __uint_as_float(v[2] & 0xffff0000u); b2[2] += f * __uint_as_float(v[3] << 16); b2[3] += f * __uint_as_float(v[3] & 0xffff0000u); }
          { u32x4 v = __builtin_amdgcn_raw_buffer_load_b128(rpt, 393216 + w * 8192 + 2048 + lane * 16, 0, 16); const float f = fw[0][3];
            a[0] += f * __uint_as_float(v[0] << 16); a[1] += f * __uint_as_float(v[0] & 0xffff0000u); a[2] += f * __uint_as_float(v[1] << 16); a[3] += f * __uint_as_float(v[1] & 0xffff0000u);
            b2[0] += f * __uint_as_float(v[2] << 16); b2[1] += f * __uint_as_float(v[2] & 0xffff0000u); b2[2] += f * __uint_as_float(v[3] << 16); b2[3] += f * __uint_as_float(v[3] & 0xffff0000u); }
          if (R < 224) { u16* dst = o + ((size_t)((kvh * 2 + hl) * 8 + 1) * NROW + orow) * 32 + 0 + 4 * h;
            u32x2 v0 = {cvtpk(a[0], a[1]), cvtpk(a[2], a[3])}; u32x2 v1 = {cvtpk(b2[0], b2[1]), cvtpk(b2[2], b2[3])};
            *(u32x2*)dst = v0; *(u32x2*)(dst + 8) = v1; } }
        { f32x4 a = acc_read4<24>(), b2 = acc_read4<28>(); const float fo = fw[0][0] * inv[0]; a *= fo; b2 *= fo;
          { u32x4 v = __builtin_amdgcn_raw_buffer_load_b128(rpt, 131072 + w * 8192 + 3072 + lane * 16, 0, 16); const float f = fw[0][1];
            a[0] += f * __uint_as_float(v[0] << 16); a[1] += f * __uint_as_float(v[0] & 0xffff0000u); a[2] += f * __uint_as_float(v[1] << 16); a[3] += f * __uint_as_float(v[1] & 0xffff0000u);
            b2[0] += f * __uint_as_float(v[2] << 16); b2[1] += f * __uint_as_float(v[2] & 0xffff0000u); b2[2] += f * __uint_as_float(v[3] << 16); b2[3] += f * __uint_as_float(v[3] & 0xffff0000u); }
          { u32x4 v = __builtin_amdgcn_raw_buffer_load_b128(rpt, 262144 + w * 8192 + 3072 + lane * 16, 0, 16); const float f = fw[0][2];
            a[0] += f * __uint_as_float(v[0] << 16); a[1] += f * __uint_as_float(v[0] & 0xffff0000u); a[2] += f * __uint_as_float(v[1] << 16); a[3] += f * __uint_as_float(v[1] & 0xffff0000u);
            b2[0] += f * __uint_as_float(v[2] << 16); b2[1] += f * __uint_as_float(v[2] & 0xffff0000u); b2[2] += f * __uint_as_float(v[3] << 16); b2[3] += f * __uint_as_float(v[3] & 0xffff0000u); }
          { u32x4 v = __builtin_amdgcn_raw_buffer_load_b128(rpt, 393216 + w * 8192 + 3072 + lane * 16, 0, 16); const float f = fw[0][3];
            a[0] += f * __uint_as_float(v[0] << 16); a[1] += f * __uint_as_float(v[0] & 0xffff0000u); a[2] += f * __uint_as_float(v[1] << 16); a[3] += f * __uint_as_float(v[1] & 0xffff0000u);
            b2[0] += f * __uint_as_float(v[2] << 16); b2[1] += f * __uint_as_float(v[2] & 0xffff0000u); b2[2] += f * __uint_as_float(v[3] << 16); b2[3] += f * __uint_as_float(v[3] & 0xffff0000u); }
          if (R < 224) { u16* dst = o + ((size_t)((kvh * 2 + hl) * 8 + 1) * NROW + orow) * 32 + 16 + 4 * h;
            u32x2 v0 = {cvtpk(a[0], a[1]), cvtpk(a[2], a[3])}; u32x2 v1 = {cvtpk(b2[0], b2[1]), cvtpk(b2[2], b2[3])};
            *(u32x2*)dst = v0; *(u32x2*)(dst + 8) = v1; } }
      }
      { const int R = 64 * w + 32 + r; const int hl = R >= 112 ? 1 : 0; const int orow = b * SQ + R - 112 * hl;
        { f32x4 a = acc_read4<128>(), b2 = acc_read4<132>(); const float fo = fw[1][0] * inv[1]; a *= fo; b2 *= fo;
          { u32x4 v = __builtin_amdgcn_raw_buffer_load_b128(rpt, 131072 + w * 8192 + 4096 + lane * 16, 0, 16); const float f = fw[1][1];
            a[0] += f * __uint_as_float(v[0] << 16); a[1] += f * __uint_as_float(v[0] & 0xffff0000u); a[2] += f * __uint_as_float(v[1] << 16); a[3] += f * __uint_as_float(v[1] & 0xffff0000u);
            b2[0] += f * __uint_as_float(v[2] << 16); b2[1] += f * __uint_as_float(v[2] & 0xffff0000u); b2[2] += f * __uint_as_float(v[3] << 16); b2[3] += f * __uint_as_float(v[3] & 0xffff0000u); }
          { u32x4 v = __builtin_amdgcn_raw_buffer_load_b128(rpt, 262144 + w * 8192 + 4096 + lane * 16, 0, 16); const float f = fw[1][2];
            a[0] += f * __uint_as_float(v[0] << 16); a[1] += f * __uint_as_float(v[0] & 0xffff0000u); a[2] += f * __uint_as_float(v[1] << 16); a[3] += f * __uint_as_float(v[1] & 0xffff0000u);
            b2[0] += f * __uint_as_float(v[2] << 16); b2[1] += f * __uint_as_float(v[2] & 0xffff0000u); b2[2] += f * __uint_as_float(v[3] << 16); b2[3] += f * __uint_as_float(v[3] & 0xffff0000u); }
          { u32x4 v = __builtin_amdgcn_raw_buffer_load_b128(rpt, 393216 + w * 8192 + 4096 + lane * 16, 0, 16); const float f = fw[1][3];
            a[0] += f * __uint_as_float(v[0] << 16); a[1] += f * __uint_as_float(v[0] & 0xffff0000u); a[2] += f * __uint_as_float(v[1] << 16); a[3] += f * __uint_as_float(v[1] & 0xffff0000u);
            b2[0] += f * __uint_as_float(v[2] << 16); b2[1] += f * __uint_as_float(v[2] & 0xffff0000u); b2[2] += f * __uint_as_float(v[3] << 16); b2[3] += f * __uint_as_float(v[3] & 0xffff0000u); }
          if (R < 224) { u16* dst = o + ((size_t)((kvh * 2 + hl) * 8 + 0) * NROW + orow) * 32 + 0 + 4 * h;
            u32x2 v0 = {cvtpk(a[0], a[1]), cvtpk(a[2], a[3])}; u32x2 v1 = {cvtpk(b2[0], b2[1]), cvtpk(b2[2], b2[3])};
            *(u32x2*)dst = v0; *(u32x2*)(dst + 8) = v1; } }
        { f32x4 a = acc_read4<136>(), b2 = acc_read4<140>(); const float fo = fw[1][0] * inv[1]; a *= fo; b2 *= fo;
          { u32x4 v = __builtin_amdgcn_raw_buffer_load_b128(rpt, 131072 + w * 8192 + 5120 + lane * 16, 0, 16); const float f = fw[1][1];
            a[0] += f * __uint_as_float(v[0] << 16); a[1] += f * __uint_as_float(v[0] & 0xffff0000u); a[2] += f * __uint_as_float(v[1] << 16); a[3] += f * __uint_as_float(v[1] & 0xffff0000u);
            b2[0] += f * __uint_as_float(v[2] << 16); b2[1] += f * __uint_as_float(v[2] & 0xffff0000u); b2[2] += f * __uint_as_float(v[3] << 16); b2[3] += f * __uint_as_float(v[3] & 0xffff0000u); }
          { u32x4 v = __builtin_amdgcn_raw_buffer_load_b128(rpt, 262144 + w * 8192 + 5120 + lane * 16, 0, 16); const float f = fw[1][2];
            a[0] += f * __uint_as_float(v[0] << 16); a[1] += f * __uint_as_float(v[0] & 0xffff0000u); a[2] += f * __uint_as_float(v[1] << 16); a[3] += f * __uint_as_float(v[1] & 0xffff0000u);
            b2[0] += f * __uint_as_float(v[2] << 16); b2[1] += f * __uint_as_float(v[2] & 0xffff0000u); b2[2] += f * __uint_as_float(v[3] << 16); b2[3] += f * __uint_as_float(v[3] & 0xffff0000u); }
          { u32x4 v = __builtin_amdgcn_raw_buffer_load_b128(rpt, 393216 + w * 8192 + 5120 + lane * 16, 0, 16); const float f = fw[1][3];
            a[0] += f * __uint_as_float(v[0] << 16); a[1] += f * __uint_as_float(v[0] & 0xffff0000u); a[2] += f * __uint_as_float(v[1] << 16); a[3] += f * __uint_as_float(v[1] & 0xffff0000u);
            b2[0] += f * __uint_as_float(v[2] << 16); b2[1] += f * __uint_as_float(v[2] & 0xffff0000u); b2[2] += f * __uint_as_float(v[3] << 16); b2[3] += f * __uint_as_float(v[3] & 0xffff0000u); }
          if (R < 224) { u16* dst = o + ((size_t)((kvh * 2 + hl) * 8 + 0) * NROW + orow) * 32 + 16 + 4 * h;
            u32x2 v0 = {cvtpk(a[0], a[1]), cvtpk(a[2], a[3])}; u32x2 v1 = {cvtpk(b2[0], b2[1]), cvtpk(b2[2], b2[3])};
            *(u32x2*)dst = v0; *(u32x2*)(dst + 8) = v1; } }
        { f32x4 a = acc_read4<144>(), b2 = acc_read4<148>(); const float fo = fw[1][0] * inv[1]; a *= fo; b2 *= fo;
          { u32x4 v = __builtin_amdgcn_raw_buffer_load_b128(rpt, 131072 + w * 8192 + 6144 + lane * 16, 0, 16); const float f = fw[1][1];
            a[0] += f * __uint_as_float(v[0] << 16); a[1] += f * __uint_as_float(v[0] & 0xffff0000u); a[2] += f * __uint_as_float(v[1] << 16); a[3] += f * __uint_as_float(v[1] & 0xffff0000u);
            b2[0] += f * __uint_as_float(v[2] << 16); b2[1] += f * __uint_as_float(v[2] & 0xffff0000u); b2[2] += f * __uint_as_float(v[3] << 16); b2[3] += f * __uint_as_float(v[3] & 0xffff0000u); }
          { u32x4 v = __builtin_amdgcn_raw_buffer_load_b128(rpt, 262144 + w * 8192 + 6144 + lane * 16, 0, 16); const float f = fw[1][2];
            a[0] += f * __uint_as_float(v[0] << 16); a[1] += f * __uint_as_float(v[0] & 0xffff0000u); a[2] += f * __uint_as_float(v[1] << 16); a[3] += f * __uint_as_float(v[1] & 0xffff0000u);
            b2[0] += f * __uint_as_float(v[2] << 16); b2[1] += f * __uint_as_float(v[2] & 0xffff0000u); b2[2] += f * __uint_as_float(v[3] << 16); b2[3] += f * __uint_as_float(v[3] & 0xffff0000u); }
          { u32x4 v = __builtin_amdgcn_raw_buffer_load_b128(rpt, 393216 + w * 8192 + 6144 + lane * 16, 0, 16); const float f = fw[1][3];
            a[0] += f * __uint_as_float(v[0] << 16); a[1] += f * __uint_as_float(v[0] & 0xffff0000u); a[2] += f * __uint_as_float(v[1] << 16); a[3] += f * __uint_as_float(v[1] & 0xffff0000u);
            b2[0] += f * __uint_as_float(v[2] << 16); b2[1] += f * __uint_as_float(v[2] & 0xffff0000u); b2[2] += f * __uint_as_float(v[3] << 16); b2[3] += f * __uint_as_float(v[3] & 0xffff0000u); }
          if (R < 224) { u16* dst = o + ((size_t)((kvh * 2 + hl) * 8 + 1) * NROW + orow) * 32 + 0 + 4 * h;
            u32x2 v0 = {cvtpk(a[0], a[1]), cvtpk(a[2], a[3])}; u32x2 v1 = {cvtpk(b2[0], b2[1]), cvtpk(b2[2], b2[3])};
            *(u32x2*)dst = v0; *(u32x2*)(dst + 8) = v1; } }
        { f32x4 a = acc_read4<152>(), b2 = acc_read4<156>(); const float fo = fw[1][0] * inv[1]; a *= fo; b2 *= fo;
          { u32x4 v = __builtin_amdgcn_raw_buffer_load_b128(rpt, 131072 + w * 8192 + 7168 + lane * 16, 0, 16); const float f = fw[1][1];
            a[0] += f * __uint_as_float(v[0] << 16); a[1] += f * __uint_as_float(v[0] & 0xffff0000u); a[2] += f * __uint_as_float(v[1] << 16); a[3] += f * __uint_as_float(v[1] & 0xffff0000u);
            b2[0] += f * __uint_as_float(v[2] << 16); b2[1] += f * __uint_as_float(v[2] & 0xffff0000u); b2[2] += f * __uint_as_float(v[3] << 16); b2[3] += f * __uint_as_float(v[3] & 0xffff0000u); }
          { u32x4 v = __builtin_amdgcn_raw_buffer_load_b128(rpt, 262144 + w * 8192 + 7168 + lane * 16, 0, 16); const float f = fw[1][2];
            a[0] += f * __uint_as_float(v[0] << 16); a[1] += f * __uint_as_float(v[0] & 0xffff0000u); a[2] += f * __uint_as_float(v[1] << 16); a[3] += f * __uint_as_float(v[1] & 0xffff0000u);
            b2[0] += f * __uint_as_float(v[2] << 16); b2[1] += f * __uint_as_float(v[2] & 0xffff0000u); b2[2] += f * __uint_as_float(v[3] << 16); b2[3] += f * __uint_as_float(v[3] & 0xffff0000u); }
          { u32x4 v = __builtin_amdgcn_raw_buffer_load_b128(rpt, 393216 + w * 8192 + 7168 + lane * 16, 0, 16); const float f = fw[1][3];
            a[0] += f * __uint_as_float(v[0] << 16); a[1] += f * __uint_as_float(v[0] & 0xffff0000u); a[2] += f * __uint_as_float(v[1] << 16); a[3] += f * __uint_as_float(v[1] & 0xffff0000u);
            b2[0] += f * __uint_as_float(v[2] << 16); b2[1] += f * __uint_as_float(v[2] & 0xffff0000u); b2[2] += f * __uint_as_float(v[3] << 16); b2[3] += f * __uint_as_float(v[3] & 0xffff0000u); }
          if (R < 224) { u16* dst = o + ((size_t)((kvh * 2 + hl) * 8 + 1) * NROW + orow) * 32 + 16 + 4 * h;
            u32x2 v0 = {cvtpk(a[0], a[1]), cvtpk(a[2], a[3])}; u32x2 v1 = {cvtpk(b2[0], b2[1]), cvtpk(b2[2], b2[3])};
            *(u32x2*)dst = v0; *(u32x2*)(dst + 8) = v1; } }
      }
    } break;
    case 1: {
      { const int R = 64 * w + 0 + r; const int hl = R >= 112 ? 1 : 0; const int orow = b * SQ + R - 112 * hl;
        { f32x4 a = acc_read4<32>(), b2 = acc_read4<36>(); const float fo = fw[0][1] * inv[0]; a *= fo; b2 *= fo;
          { u32x4 v = __builtin_amdgcn_raw_buffer_load_b128(rpt, 32768 + w * 8192 + 0 + lane * 16, 0, 16); const float f = fw[0][0];
            a[0] += f * __uint_as_float(v[0] << 16); a[1] += f * __uint_as_float(v[0] & 0xffff0000u); a[2] += f * __uint_as_float(v[1] << 16); a[3] += f * __uint_as_float(v[1] & 0xffff0000u);
            b2[0] += f * __uint_as_float(v[2] << 16); b2[1] += f * __uint_as_float(v[2] & 0xffff0000u); b2[2] += f * __uint_as_float(v[3] << 16); b2[3] += f * __uint_as_float(v[3] & 0xffff0000u); }
          { u32x4 v = __builtin_amdgcn_raw_buffer_load_b128(rpt, 294912 + w * 8192 + 0 + lane * 16, 0, 16); const float f = fw[0][2];
            a[0] += f * __uint_as_float(v[0] << 16); a[1] += f * __uint_as_float(v[0] & 0xffff0000u); a[2] += f * __uint_as_float(v[1] << 16); a[3] += f * __uint_as_float(v[1] & 0xffff0000u);
            b2[0] += f * __uint_as_float(v[2] << 16); b2[1] += f * __uint_as_float(v[2] & 0xffff0000u); b2[2] += f * __uint_as_float(v[3] << 16); b2[3] += f * __uint_as_float(v[3] & 0xffff0000u); }
          { u32x4 v = __builtin_amdgcn_raw_buffer_load_b128(rpt, 425984 + w * 8192 + 0 + lane * 16, 0, 16); const float f = fw[0][3];
            a[0] += f * __uint_as_float(v[0] << 16); a[1] += f * __uint_as_float(v[0] & 0xffff0000u); a[2] += f * __uint_as_float(v[1] << 16); a[3] += f * __uint_as_float(v[1] & 0xffff0000u);
            b2[0] += f * __uint_as_float(v[2] << 16); b2[1] += f * __uint_as_float(v[2] & 0xffff0000u); b2[2] += f * __uint_as_float(v[3] << 16); b2[3] += f * __uint_as_float(v[3] & 0xffff0000u); }
          if (R < 224) { u16* dst = o + ((size_t)((kvh * 2 + hl) * 8 + 2) * NROW + orow) * 32 + 0 + 4 * h;
            u32x2 v0 = {cvtpk(a[0], a[1]), cvtpk(a[2], a[3])}; u32x2 v1 = {cvtpk(b2[0], b2[1]), cvtpk(b2[2], b2[3])};
            *(u32x2*)dst = v0; *(u32x2*)(dst + 8) = v1; } }
        { f32x4 a = acc_read4<40>(), b2 = acc_read4<44>(); const float fo = fw[0][1] * inv[0]; a *= fo; b2 *= fo;
          { u32x4 v = __builtin_amdgcn_raw_buffer_load_b128(rpt, 32768 + w * 8192 + 1024 + lane * 16, 0, 16); const float f = fw[0][0];
            a[0] += f * __uint_as_float(v[0] << 16); a[1] += f * __uint_as_float(v[0] & 0xffff0000u); a[2] += f * __uint_as_float(v[1] << 16); a[3] += f * __uint_as_float(v[1] & 0xffff0000u);
            b2[0] += f * __uint_as_float(v[2] << 16); b2[1] += f * __uint_as_float(v[2] & 0xffff0000u); b2[2] += f * __uint_as_float(v[3] << 16); b2[3] += f * __uint_as_float(v[3] & 0xffff0000u); }
          { u32x4 v = __builtin_amdgcn_raw_buffer_load_b128(rpt, 294912 + w * 8192 + 1024 + lane * 16, 0, 16); const float f = fw[0][2];
            a[0] += f * __uint_as_float(v[0] << 16); a[1] += f * __uint_as_float(v[0] & 0xffff0000u); a[2] += f * __uint_as_float(v[1] << 16); a[3] += f * __uint_as_float(v[1] & 0xffff0000u);
            b2[0] += f * __uint_as_float(v[2] << 16); b2[1] += f * __uint_as_float(v[2] & 0xffff0000u); b2[2] += f * __uint_as_float(v[3] << 16); b2[3] += f * __uint_as_float(v[3] & 0xffff0000u); }
          { u32x4 v = __builtin_amdgcn_raw_buffer_load_b128(rpt, 425984 + w * 8192 + 1024 + lane * 16, 0, 16); const float f = fw[0][3];
            a[0] += f * __uint_as_float(v[0] << 16); a[1] += f * __uint_as_float(v[0] & 0xffff0000u); a[2] += f * __uint_as_float(v[1] << 16); a[3] += f * __uint_as_float(v[1] & 0xffff0000u);
            b2[0] += f * __uint_as_float(v[2] << 16); b2[1] += f * __uint_as_float(v[2] & 0xffff0000u); b2[2] += f * __uint_as_float(v[3] << 16); b2[3] += f * __uint_as_float(v[3] & 0xffff0000u); }
          if (R < 224) { u16* dst = o + ((size_t)((kvh * 2 + hl) * 8 + 2) * NROW + orow) * 32 + 16 + 4 * h;
            u32x2 v0 = {cvtpk(a[0], a[1]), cvtpk(a[2], a[3])}; u32x2 v1 = {cvtpk(b2[0], b2[1]), cvtpk(b2[2], b2[3])};
            *(u32x2*)dst = v0; *(u32x2*)(dst + 8) = v1; } }
        { f32x4 a = acc_read4<48>(), b2 = acc_read4<52>(); const float fo = fw[0][1] * inv[0]; a *= fo; b2 *= fo;
          { u32x4 v = __builtin_amdgcn_raw_buffer_load_b128(rpt, 32768 + w * 8192 + 2048 + lane * 16, 0, 16); const float f = fw[0][0];
            a[0] += f * __uint_as_float(v[0] << 16); a[1] += f * __uint_as_float(v[0] & 0xffff0000u); a[2] += f * __uint_as_float(v[1] << 16); a[3] += f * __uint_as_float(v[1] & 0xffff0000u);
            b2[0] += f * __uint_as_float(v[2] << 16); b2[1] += f * __uint_as_float(v[2] & 0xffff0000u); b2[2] += f * __uint_as_float(v[3] << 16); b2[3] += f * __uint_as_float(v[3] & 0xffff0000u); }
          { u32x4 v = __builtin_amdgcn_raw_buffer_load_b128(rpt, 294912 + w * 8192 + 2048 + lane * 16, 0, 16); const float f = fw[0][2];
            a[0] += f * __uint_as_float(v[0] << 16); a[1] += f * __uint_as_float(v[0] & 0xffff0000u); a[2] += f * __uint_as_float(v[1] << 16); a[3] += f * __uint_as_float(v[1] & 0xffff0000u);
            b2[0] += f * __uint_as_float(v[2] << 16); b2[1] += f * __uint_as_float(v[2] & 0xffff0000u); b2[2] += f * __uint_as_float(v[3] << 16); b2[3] += f * __uint_as_float(v[3] & 0xffff0000u); }
          { u32x4 v = __builtin_amdgcn_raw_buffer_load_b128(rpt, 425984 + w * 8192 + 2048 + lane * 16, 0, 16); const float f = fw[0][3];
            a[0] += f * __uint_as_float(v[0] << 16); a[1] += f * __uint_as_float(v[0] & 0xffff0000u); a[2] += f * __uint_as_float(v[1] << 16); a[3] += f * __uint_as_float(v[1] & 0xffff0000u);
            b2[0] += f * __uint_as_float(v[2] << 16); b2[1] += f * __uint_as_float(v[2] & 0xffff0000u); b2[2] += f * __uint_as_float(v[3] << 16); b2[3] += f * __uint_as_float(v[3] & 0xffff0000u); }
          if (R < 224) { u16* dst = o + ((size_t)((kvh * 2 + hl) * 8 + 3) * NROW + orow) * 32 + 0 + 4 * h;
            u32x2 v0 = {cvtpk(a[0], a[1]), cvtpk(a[2], a[3])}; u32x2 v1 = {cvtpk(b2[0], b2[1]), cvtpk(b2[2], b2[3])};
            *(u32x2*)dst = v0; *(u32x2*)(dst + 8) = v1; } }
        { f32x4 a = acc_read4<56>(), b2 = acc_read4<60>(); const float fo = fw[0][1] * inv[0]; a *= fo; b2 *= fo;
          { u32x4 v = __builtin_amdgcn_raw_buffer_load_b128(rpt, 32768 + w * 8192 + 3072 + lane * 16, 0, 16); const float f = fw[0][0];
            a[0] += f * __uint_as_float(v[0] << 16); a[1] += f * __uint_as_float(v[0] & 0xffff0000u); a[2] += f * __uint_as_float(v[1] << 16); a[3] += f * __uint_as_float(v[1] & 0xffff0000u);
            b2[0] += f * __uint_as_float(v[2] << 16); b2[1] += f * __uint_as_float(v[2] & 0xffff0000u); b2[2] += f * __uint_as_float(v[3] << 16); b2[3] += f * __uint_as_float(v[3] & 0xffff0000u); }
          { u32x4 v = __builtin_amdgcn_raw_buffer_load_b128(rpt, 294912 + w * 8192 + 3072 + lane * 16, 0, 16); const float f = fw[0][2];
            a[0] += f * __uint_as_float(v[0] << 16); a[1] += f * __uint_as_float(v[0] & 0xffff0000u); a[2] += f * __uint_as_float(v[1] << 16); a[3] += f * __uint_as_float(v[1] & 0xffff0000u);
            b2[0] += f * __uint_as_float(v[2] << 16); b2[1] += f * __uint_as_float(v[2] & 0xffff0000u); b2[2] += f * __uint_as_float(v[3] << 16); b2[3] += f * __uint_as_float(v[3] & 0xffff0000u); }
          { u32x4 v = __builtin_amdgcn_raw_buffer_load_b128(rpt, 425984 + w * 8192 + 3072 + lane * 16, 0, 16); const float f = fw[0][3];
            a[0] += f * __uint_as_float(v[0] << 16); a[1] += f * __uint_as_float(v[0] & 0xffff0000u); a[2] += f * __uint_as_float(v[1] << 16); a[3] += f * __uint_as_float(v[1] & 0xffff0000u);
            b2[0] += f * __uint_as_float(v[2] << 16); b2[1] += f * __uint_as_float(v[2] & 0xffff0000u); b2[2] += f * __uint_as_float(v[3] << 16); b2[3] += f * __uint_as_float(v[3] & 0xffff0000u); }
          if (R < 224) { u16* dst = o + ((size_t)((kvh * 2 + hl) * 8 + 3) * NROW + orow) * 32 + 16 + 4 * h;
            u32x2 v0 = {cvtpk(a[0], a[1]), cvtpk(a[2], a[3])}; u32x2 v1 = {cvtpk(b2[0], b2[1]), cvtpk(b2[2], b2[3])};
            *(u32x2*)dst = v0; *(u32x2*)(dst + 8) = v1; } }
      }
      { const int R = 64 * w + 32 + r; const int hl = R >= 112 ? 1 : 0; const int orow = b * SQ + R - 112 * hl;
        { f32x4 a = acc_read4<160>(), b2 = acc_read4<164>(); const float fo = fw[1][1] * inv[1]; a *= fo; b2 *= fo;
          { u32x4 v = __builtin_amdgcn_raw_buffer_load_b128(rpt, 32768 + w * 8192 + 4096 + lane * 16, 0, 16); const float f = fw[1][0];
            a[0] += f * __uint_as_float(v[0] << 16); a[1] += f * __uint_as_float(v[0] & 0xffff0000u); a[2] += f * __uint_as_float(v[1] << 16); a[3] += f * __uint_as_float(v[1] & 0xffff0000u);
            b2[0] += f * __uint_as_float(v[2] << 16); b2[1] += f * __uint_as_float(v[2] & 0xffff0000u); b2[2] += f * __uint_as_float(v[3] << 16); b2[3] += f * __uint_as_float(v[3] & 0xffff0000u); }
          { u32x4 v = __builtin_amdgcn_raw_buffer_load_b128(rpt, 294912 + w * 8192 + 4096 + lane * 16, 0, 16); const float f = fw[1][2];
            a[0] += f * __uint_as_float(v[0] << 16); a[1] += f * __uint_as_float(v[0] & 0xffff0000u); a[2] += f * __uint_as_float(v[1] << 16); a[3] += f * __uint_as_float(v[1] & 0xffff0000u);
            b2[0] += f * __uint_as_float(v[2] << 16); b2[1] += f * __uint_as_float(v[2] & 0xffff0000u); b2[2] += f * __uint_as_float(v[3] << 16); b2[3] += f * __uint_as_float(v[3] & 0xffff0000u); }
          { u32x4 v = __builtin_amdgcn_raw_buffer_load_b128(rpt, 425984 + w * 8192 + 4096 + lane * 16, 0, 16); const float f = fw[1][3];
            a[0] += f * __uint_as_float(v[0] << 16); a[1] += f * __uint_as_float(v[0] & 0xffff0000u); a[2] += f * __uint_as_float(v[1] << 16); a[3] += f * __uint_as_float(v[1] & 0xffff0000u);
            b2[0] += f * __uint_as_float(v[2] << 16); b2[1] += f * __uint_as_float(v[2] & 0xffff0000u); b2[2] += f * __uint_as_float(v[3] << 16); b2[3] += f * __uint_as_float(v[3] & 0xffff0000u); }
          if (R < 224) { u16* dst = o + ((size_t)((kvh * 2 + hl) * 8 + 2) * NROW + orow) * 32 + 0 + 4 * h;
            u32x2 v0 = {cvtpk(a[0], a[1]), cvtpk(a[2], a[3])}; u32x2 v1 = {cvtpk(b2[0], b2[1]), cvtpk(b2[2], b2[3])};
            *(u32x2*)dst = v0; *(u32x2*)(dst + 8) = v1; } }
        { f32x4 a = acc_read4<168>(), b2 = acc_read4<172>(); const float fo = fw[1][1] * inv[1]; a *= fo; b2 *= fo;
          { u32x4 v = __builtin_amdgcn_raw_buffer_load_b128(rpt, 32768 + w * 8192 + 5120 + lane * 16, 0, 16); const float f = fw[1][0];
            a[0] += f * __uint_as_float(v[0] << 16); a[1] += f * __uint_as_float(v[0] & 0xffff0000u); a[2] += f * __uint_as_float(v[1] << 16); a[3] += f * __uint_as_float(v[1] & 0xffff0000u);
            b2[0] += f * __uint_as_float(v[2] << 16); b2[1] += f * __uint_as_float(v[2] & 0xffff0000u); b2[2] += f * __uint_as_float(v[3] << 16); b2[3] += f * __uint_as_float(v[3] & 0xffff0000u); }
          { u32x4 v = __builtin_amdgcn_raw_buffer_load_b128(rpt, 294912 + w * 8192 + 5120 + lane * 16, 0, 16); const float f = fw[1][2];
            a[0] += f * __uint_as_float(v[0] << 16); a[1] += f * __uint_as_float(v[0] & 0xffff0000u); a[2] += f * __uint_as_float(v[1] << 16); a[3] += f * __uint_as_float(v[1] & 0xffff0000u);
            b2[0] += f * __uint_as_float(v[2] << 16); b2[1] += f * __uint_as_float(v[2] & 0xffff0000u); b2[2] += f * __uint_as_float(v[3] << 16); b2[3] += f * __uint_as_float(v[3] & 0xffff0000u); }
          { u32x4 v = __builtin_amdgcn_raw_buffer_load_b128(rpt, 425984 + w * 8192 + 5120 + lane * 16, 0, 16); const float f = fw[1][3];
            a[0] += f * __uint_as_float(v[0] << 16); a[1] += f * __uint_as_float(v[0] & 0xffff0000u); a[2] += f * __uint_as_float(v[1] << 16); a[3] += f * __uint_as_float(v[1] & 0xffff0000u);
            b2[0] += f * __uint_as_float(v[2] << 16); b2[1] += f * __uint_as_float(v[2] & 0xffff0000u); b2[2] += f * __uint_as_float(v[3] << 16); b2[3] += f * __uint_as_float(v[3] & 0xffff0000u); }
          if (R < 224) { u16* dst = o + ((size_t)((kvh * 2 + hl) * 8 + 2) * NROW + orow) * 32 + 16 + 4 * h;
            u32x2 v0 = {cvtpk(a[0], a[1]), cvtpk(a[2], a[3])}; u32x2 v1 = {cvtpk(b2[0], b2[1]), cvtpk(b2[2], b2[3])};
            *(u32x2*)dst = v0; *(u32x2*)(dst + 8) = v1; } }
        { f32x4 a = acc_read4<176>(), b2 = acc_read4<180>(); const float fo = fw[1][1] * inv[1]; a *= fo; b2 *= fo;
          { u32x4 v = __builtin_amdgcn_raw_buffer_load_b128(rpt, 32768 + w * 8192 + 6144 + lane * 16, 0, 16); const float f = fw[1][0];
            a[0] += f * __uint_as_float(v[0] << 16); a[1] += f * __uint_as_float(v[0] & 0xffff0000u); a[2] += f * __uint_as_float(v[1] << 16); a[3] += f * __uint_as_float(v[1] & 0xffff0000u);
            b2[0] += f * __uint_as_float(v[2] << 16); b2[1] += f * __uint_as_float(v[2] & 0xffff0000u); b2[2] += f * __uint_as_float(v[3] << 16); b2[3] += f * __uint_as_float(v[3] & 0xffff0000u); }
          { u32x4 v = __builtin_amdgcn_raw_buffer_load_b128(rpt, 294912 + w * 8192 + 6144 + lane * 16, 0, 16); const float f = fw[1][2];
            a[0] += f * __uint_as_float(v[0] << 16); a[1] += f * __uint_as_float(v[0] & 0xffff0000u); a[2] += f * __uint_as_float(v[1] << 16); a[3] += f * __uint_as_float(v[1] & 0xffff0000u);
            b2[0] += f * __uint_as_float(v[2] << 16); b2[1] += f * __uint_as_float(v[2] & 0xffff0000u); b2[2] += f * __uint_as_float(v[3] << 16); b2[3] += f * __uint_as_float(v[3] & 0xffff0000u); }
          { u32x4 v = __builtin_amdgcn_raw_buffer_load_b128(rpt, 425984 + w * 8192 + 6144 + lane * 16, 0, 16); const float f = fw[1][3];
            a[0] += f * __uint_as_float(v[0] << 16); a[1] += f * __uint_as_float(v[0] & 0xffff0000u); a[2] += f * __uint_as_float(v[1] << 16); a[3] += f * __uint_as_float(v[1] & 0xffff0000u);
            b2[0] += f * __uint_as_float(v[2] << 16); b2[1] += f * __uint_as_float(v[2] & 0xffff0000u); b2[2] += f * __uint_as_float(v[3] << 16); b2[3] += f * __uint_as_float(v[3] & 0xffff0000u); }
          if (R < 224) { u16* dst = o + ((size_t)((kvh * 2 + hl) * 8 + 3) * NROW + orow) * 32 + 0 + 4 * h;
            u32x2 v0 = {cvtpk(a[0], a[1]), cvtpk(a[2], a[3])}; u32x2 v1 = {cvtpk(b2[0], b2[1]), cvtpk(b2[2], b2[3])};
            *(u32x2*)dst = v0; *(u32x2*)(dst + 8) = v1; } }
        { f32x4 a = acc_read4<184>(), b2 = acc_read4<188>(); const float fo = fw[1][1] * inv[1]; a *= fo; b2 *= fo;
          { u32x4 v = __builtin_amdgcn_raw_buffer_load_b128(rpt, 32768 + w * 8192 + 7168 + lane * 16, 0, 16); const float f = fw[1][0];
            a[0] += f * __uint_as_float(v[0] << 16); a[1] += f * __uint_as_float(v[0] & 0xffff0000u); a[2] += f * __uint_as_float(v[1] << 16); a[3] += f * __uint_as_float(v[1] & 0xffff0000u);
            b2[0] += f * __uint_as_float(v[2] << 16); b2[1] += f * __uint_as_float(v[2] & 0xffff0000u); b2[2] += f * __uint_as_float(v[3] << 16); b2[3] += f * __uint_as_float(v[3] & 0xffff0000u); }
          { u32x4 v = __builtin_amdgcn_raw_buffer_load_b128(rpt, 294912 + w * 8192 + 7168 + lane * 16, 0, 16); const float f = fw[1][2];
            a[0] += f * __uint_as_float(v[0] << 16); a[1] += f * __uint_as_float(v[0] & 0xffff0000u); a[2] += f * __uint_as_float(v[1] << 16); a[3] += f * __uint_as_float(v[1] & 0xffff0000u);
            b2[0] += f * __uint_as_float(v[2] << 16); b2[1] += f * __uint_as_float(v[2] & 0xffff0000u); b2[2] += f * __uint_as_float(v[3] << 16); b2[3] += f * __uint_as_float(v[3] & 0xffff0000u); }
          { u32x4 v = __builtin_amdgcn_raw_buffer_load_b128(rpt, 425984 + w * 8192 + 7168 + lane * 16, 0, 16); const float f = fw[1][3];
            a[0] += f * __uint_as_float(v[0] << 16); a[1] += f * __uint_as_float(v[0] & 0xffff0000u); a[2] += f * __uint_as_float(v[1] << 16); a[3] += f * __uint_as_float(v[1] & 0xffff0000u);
            b2[0] += f * __uint_as_float(v[2] << 16); b2[1] += f * __uint_as_float(v[2] & 0xffff0000u); b2[2] += f * __uint_as_float(v[3] << 16); b2[3] += f * __uint_as_float(v[3] & 0xffff0000u); }
          if (R < 224) { u16* dst = o + ((size_t)((kvh * 2 + hl) * 8 + 3) * NROW + orow) * 32 + 16 + 4 * h;
            u32x2 v0 = {cvtpk(a[0], a[1]), cvtpk(a[2], a[3])}; u32x2 v1 = {cvtpk(b2[0], b2[1]), cvtpk(b2[2], b2[3])};
            *(u32x2*)dst = v0; *(u32x2*)(dst + 8) = v1; } }
      }
    } break;
    case 2: {
      { const int R = 64 * w + 0 + r; const int hl = R >= 112 ? 1 : 0; const int orow = b * SQ + R - 112 * hl;
        { f32x4 a = acc_read4<64>(), b2 = acc_read4<68>(); const float fo = fw[0][2] * inv[0]; a *= fo; b2 *= fo;
          { u32x4 v = __builtin_amdgcn_raw_buffer_load_b128(rpt, 65536 + w * 8192 + 0 + lane * 16, 0, 16); const float f = fw[0][0];
            a[0] += f * __uint_as_float(v[0] << 16); a[1] += f * __uint_as_float(v[0] & 0xffff0000u); a[2] += f * __uint_as_float(v[1] << 16); a[3] += f * __uint_as_float(v[1] & 0xffff0000u);
            b2[0] += f * __uint_as_float(v[2] << 16); b2[1] += f * __uint_as_float(v[2] & 0xffff0000u); b2[2] += f * __uint_as_float(v[3] << 16); b2[3] += f * __uint_as_float(v[3] & 0xffff0000u); }
          { u32x4 v = __builtin_amdgcn_raw_buffer_load_b128(rpt, 196608 + w * 8192 + 0 + lane * 16, 0, 16); const float f = fw[0][1];
            a[0] += f * __uint_as_float(v[0] << 16); a[1] += f * __uint_as_float(v[0] & 0xffff0000u); a[2] += f * __uint_as_float(v[1] << 16); a[3] += f * __uint_as_float(v[1] & 0xffff0000u);
            b2[0] += f * __uint_as_float(v[2] << 16); b2[1] += f * __uint_as_float(v[2] & 0xffff0000u); b2[2] += f * __uint_as_float(v[3] << 16); b2[3] += f * __uint_as_float(v[3] & 0xffff0000u); }
          { u32x4 v = __builtin_amdgcn_raw_buffer_load_b128(rpt, 458752 + w * 8192 + 0 + lane * 16, 0, 16); const float f = fw[0][3];
            a[0] += f * __uint_as_float(v[0] << 16); a[1] += f * __uint_as_float(v[0] & 0xffff0000u); a[2] += f * __uint_as_float(v[1] << 16); a[3] += f * __uint_as_float(v[1] & 0xffff0000u);
            b2[0] += f * __uint_as_float(v[2] << 16); b2[1] += f * __uint_as_float(v[2] & 0xffff0000u); b2[2] += f * __uint_as_float(v[3] << 16); b2[3] += f * __uint_as_float(v[3] & 0xffff0000u); }
          if (R < 224) { u16* dst = o + ((size_t)((kvh * 2 + hl) * 8 + 4) * NROW + orow) * 32 + 0 + 4 * h;
            u32x2 v0 = {cvtpk(a[0], a[1]), cvtpk(a[2], a[3])}; u32x2 v1 = {cvtpk(b2[0], b2[1]), cvtpk(b2[2], b2[3])};
            *(u32x2*)dst = v0; *(u32x2*)(dst + 8) = v1; } }
        { f32x4 a = acc_read4<72>(), b2 = acc_read4<76>(); const float fo = fw[0][2] * inv[0]; a *= fo; b2 *= fo;
          { u32x4 v = __builtin_amdgcn_raw_buffer_load_b128(rpt, 65536 + w * 8192 + 1024 + lane * 16, 0, 16); const float f = fw[0][0];
            a[0] += f * __uint_as_float(v[0] << 16); a[1] += f * __uint_as_float(v[0] & 0xffff0000u); a[2] += f * __uint_as_float(v[1] << 16); a[3] += f * __uint_as_float(v[1] & 0xffff0000u);
            b2[0] += f * __uint_as_float(v[2] << 16); b2[1] += f * __uint_as_float(v[2] & 0xffff0000u); b2[2] += f * __uint_as_float(v[3] << 16); b2[3] += f * __uint_as_float(v[3] & 0xffff0000u); }
          { u32x4 v = __builtin_amdgcn_raw_buffer_load_b128(rpt, 196608 + w * 8192 + 1024 + lane * 16, 0, 16); const float f = fw[0][1];
            a[0] += f * __uint_as_float(v[0] << 16); a[1] += f * __uint_as_float(v[0] & 0xffff0000u); a[2] += f * __uint_as_float(v[1] << 16); a[3] += f * __uint_as_float(v[1] & 0xffff0000u);
            b2[0] += f * __uint_as_float(v[2] << 16); b2[1] += f * __uint_as_float(v[2] & 0xffff0000u); b2[2] += f * __uint_as_float(v[3] << 16); b2[3] += f * __uint_as_float(v[3] & 0xffff0000u); }
          { u32x4 v = __builtin_amdgcn_raw_buffer_load_b128(rpt, 458752 + w * 8192 + 1024 + lane * 16, 0, 16); const float f = fw[0][3];
            a[0] += f * __uint_as_float(v[0] << 16); a[1] += f * __uint_as_float(v[0] & 0xffff0000u); a[2] += f * __uint_as_float(v[1] << 16); a[3] += f * __uint_as_float(v[1] & 0xffff0000u);
            b2[0] += f * __uint_as_float(v[2] << 16); b2[1] += f * __uint_as_float(v[2] & 0xffff0000u); b2[2] += f * __uint_as_float(v[3] << 16); b2[3] += f * __uint_as_float(v[3] & 0xffff0000u); }
          if (R < 224) { u16* dst = o + ((size_t)((kvh * 2 + hl) * 8 + 4) * NROW + orow) * 32 + 16 + 4 * h;
            u32x2 v0 = {cvtpk(a[0], a[1]), cvtpk(a[2], a[3])}; u32x2 v1 = {cvtpk(b2[0], b2[1]), cvtpk(b2[2], b2[3])};
            *(u32x2*)dst = v0; *(u32x2*)(dst + 8) = v1; } }
        { f32x4 a = acc_read4<80>(), b2 = acc_read4<84>(); const float fo = fw[0][2] * inv[0]; a *= fo; b2 *= fo;
          { u32x4 v = __builtin_amdgcn_raw_buffer_load_b128(rpt, 65536 + w * 8192 + 2048 + lane * 16, 0, 16); const float f = fw[0][0];
            a[0] += f * __uint_as_float(v[0] << 16); a[1] += f * __uint_as_float(v[0] & 0xffff0000u); a[2] += f * __uint_as_float(v[1] << 16); a[3] += f * __uint_as_float(v[1] & 0xffff0000u);
            b2[0] += f * __uint_as_float(v[2] << 16); b2[1] += f * __uint_as_float(v[2] & 0xffff0000u); b2[2] += f * __uint_as_float(v[3] << 16); b2[3] += f * __uint_as_float(v[3] & 0xffff0000u); }
          { u32x4 v = __builtin_amdgcn_raw_buffer_load_b128(rpt, 196608 + w * 8192 + 2048 + lane * 16, 0, 16); const float f = fw[0][1];
            a[0] += f * __uint_as_float(v[0] << 16); a[1] += f * __uint_as_float(v[0] & 0xffff0000u); a[2] += f * __uint_as_float(v[1] << 16); a[3] += f * __uint_as_float(v[1] & 0xffff0000u);
            b2[0] += f * __uint_as_float(v[2] << 16); b2[1] += f * __uint_as_float(v[2] & 0xffff0000u); b2[2] += f * __uint_as_float(v[3] << 16); b2[3] += f * __uint_as_float(v[3] & 0xffff0000u); }
          { u32x4 v = __builtin_amdgcn_raw_buffer_load_b128(rpt, 458752 + w * 8192 + 2048 + lane * 16, 0, 16); const float f = fw[0][3];
            a[0] += f * __uint_as_float(v[0] << 16); a[1] += f * __uint_as_float(v[0] & 0xffff0000u); a[2] += f * __uint_as_float(v[1] << 16); a[3] += f * __uint_as_float(v[1] & 0xffff0000u);
            b2[0] += f * __uint_as_float(v[2] << 16); b2[1] += f * __uint_as_float(v[2] & 0xffff0000u); b2[2] += f * __uint_as_float(v[3] << 16); b2[3] += f * __uint_as_float(v[3] & 0xffff0000u); }
          if (R < 224) { u16* dst = o + ((size_t)((kvh * 2 + hl) * 8 + 5) * NROW + orow) * 32 + 0 + 4 * h;
            u32x2 v0 = {cvtpk(a[0], a[1]), cvtpk(a[2], a[3])}; u32x2 v1 = {cvtpk(b2[0], b2[1]), cvtpk(b2[2], b2[3])};
            *(u32x2*)dst = v0; *(u32x2*)(dst + 8) = v1; } }
        { f32x4 a = acc_read4<88>(), b2 = acc_read4<92>(); const float fo = fw[0][2] * inv[0]; a *= fo; b2 *= fo;
          { u32x4 v = __builtin_amdgcn_raw_buffer_load_b128(rpt, 65536 + w * 8192 + 3072 + lane * 16, 0, 16); const float f = fw[0][0];
            a[0] += f * __uint_as_float(v[0] << 16); a[1] += f * __uint_as_float(v[0] & 0xffff0000u); a[2] += f * __uint_as_float(v[1] << 16); a[3] += f * __uint_as_float(v[1] & 0xffff0000u);
            b2[0] += f * __uint_as_float(v[2] << 16); b2[1] += f * __uint_as_float(v[2] & 0xffff0000u); b2[2] += f * __uint_as_float(v[3] << 16); b2[3] += f * __uint_as_float(v[3] & 0xffff0000u); }
          { u32x4 v = __builtin_amdgcn_raw_buffer_load_b128(rpt, 196608 + w * 8192 + 3072 + lane * 16, 0, 16); const float f = fw[0][1];
            a[0] += f * __uint_as_float(v[0] << 16); a[1] += f * __uint_as_float(v[0] & 0xffff0000u); a[2] += f * __uint_as_float(v[1] << 16); a[3] += f * __uint_as_float(v[1] & 0xffff0000u);
            b2[0] += f * __uint_as_float(v[2] << 16); b2[1] += f * __uint_as_float(v[2] & 0xffff0000u); b2[2] += f * __uint_as_float(v[3] << 16); b2[3] += f * __uint_as_float(v[3] & 0xffff0000u); }
          { u32x4 v = __builtin_amdgcn_raw_buffer_load_b128(rpt, 458752 + w * 8192 + 3072 + lane * 16, 0, 16); const float f = fw[0][3];
            a[0] += f * __uint_as_float(v[0] << 16); a[1] += f * __uint_as_float(v[0] & 0xffff0000u); a[2] += f * __uint_as_float(v[1] << 16); a[3] += f * __uint_as_float(v[1] & 0xffff0000u);
            b2[0] += f * __uint_as_float(v[2] << 16); b2[1] += f * __uint_as_float(v[2] & 0xffff0000u); b2[2] += f * __uint_as_float(v[3] << 16); b2[3] += f * __uint_as_float(v[3] & 0xffff0000u); }
          if (R < 224) { u16* dst = o + ((size_t)((kvh * 2 + hl) * 8 + 5) * NROW + orow) * 32 + 16 + 4 * h;
            u32x2 v0 = {cvtpk(a[0], a[1]), cvtpk(a[2], a[3])}; u32x2 v1 = {cvtpk(b2[0], b2[1]), cvtpk(b2[2], b2[3])};
            *(u32x2*)dst = v0; *(u32x2*)(dst + 8) = v1; } }
      }
      { const int R = 64 * w + 32 + r; const int hl = R >= 112 ? 1 : 0; const int orow = b * SQ + R - 112 * hl;
        { f32x4 a = acc_read4<192>(), b2 = acc_read4<196>(); const float fo = fw[1][2] * inv[1]; a *= fo; b2 *= fo;
          { u32x4 v = __builtin_amdgcn_raw_buffer_load_b128(rpt, 65536 + w * 8192 + 4096 + lane * 16, 0, 16); const float f = fw[1][0];
            a[0] += f * __uint_as_float(v[0] << 16); a[1] += f * __uint_as_float(v[0] & 0xffff0000u); a[2] += f * __uint_as_float(v[1] << 16); a[3] += f * __uint_as_float(v[1] & 0xffff0000u);
            b2[0] += f * __uint_as_float(v[2] << 16); b2[1] += f * __uint_as_float(v[2] & 0xffff0000u); b2[2] += f * __uint_as_float(v[3] << 16); b2[3] += f * __uint_as_float(v[3] & 0xffff0000u); }
          { u32x4 v = __builtin_amdgcn_raw_buffer_load_b128(rpt, 196608 + w * 8192 + 4096 + lane * 16, 0, 16); const float f = fw[1][1];
            a[0] += f * __uint_as_float(v[0] << 16); a[1] += f * __uint_as_float(v[0] & 0xffff0000u); a[2] += f * __uint_as_float(v[1] << 16); a[3] += f * __uint_as_float(v[1] & 0xffff0000u);
            b2[0] += f * __uint_as_float(v[2] << 16); b2[1] += f * __uint_as_float(v[2] & 0xffff0000u); b2[2] += f * __uint_as_float(v[3] << 16); b2[3] += f * __uint_as_float(v[3] & 0xffff0000u); }
          { u32x4 v = __builtin_amdgcn_raw_buffer_load_b128(rpt, 458752 + w * 8192 + 4096 + lane * 16, 0, 16); const float f = fw[1][3];
            a[0] += f * __uint_as_float(v[0] << 16); a[1] += f * __uint_as_float(v[0] & 0xffff0000u); a[2] += f * __uint_as_float(v[1] << 16); a[3] += f * __uint_as_float(v[1] & 0xffff0000u);
            b2[0] += f * __uint_as_float(v[2] << 16); b2[1] += f * __uint_as_float(v[2] & 0xffff0000u); b2[2] += f * __uint_as_float(v[3] << 16); b2[3] += f * __uint_as_float(v[3] & 0xffff0000u); }
          if (R < 224) { u16* dst = o + ((size_t)((kvh * 2 + hl) * 8 + 4) * NROW + orow) * 32 + 0 + 4 * h;
            u32x2 v0 = {cvtpk(a[0], a[1]), cvtpk(a[2], a[3])}; u32x2 v1 = {cvtpk(b2[0], b2[1]), cvtpk(b2[2], b2[3])};
            *(u32x2*)dst = v0; *(u32x2*)(dst + 8) = v1; } }
        { f32x4 a = acc_read4<200>(), b2 = acc_read4<204>(); const float fo = fw[1][2] * inv[1]; a *= fo; b2 *= fo;
          { u32x4 v = __builtin_amdgcn_raw_buffer_load_b128(rpt, 65536 + w * 8192 + 5120 + lane * 16, 0, 16); const float f = fw[1][0];
            a[0] += f * __uint_as_float(v[0] << 16); a[1] += f * __uint_as_float(v[0] & 0xffff0000u); a[2] += f * __uint_as_float(v[1] << 16); a[3] += f * __uint_as_float(v[1] & 0xffff0000u);
            b2[0] += f * __uint_as_float(v[2] << 16); b2[1] += f * __uint_as_float(v[2] & 0xffff0000u); b2[2] += f * __uint_as_float(v[3] << 16); b2[3] += f * __uint_as_float(v[3] & 0xffff0000u); }
          { u32x4 v = __builtin_amdgcn_raw_buffer_load_b128(rpt, 196608 + w * 8192 + 5120 + lane * 16, 0, 16); const float f = fw[1][1];
            a[0] += f * __uint_as_float(v[0] << 16); a[1] += f * __uint_as_float(v[0] & 0xffff0000u); a[2] += f * __uint_as_float(v[1] << 16); a[3] += f * __uint_as_float(v[1] & 0xffff0000u);
            b2[0] += f * __uint_as_float(v[2] << 16); b2[1] += f * __uint_as_float(v[2] & 0xffff0000u); b2[2] += f * __uint_as_float(v[3] << 16); b2[3] += f * __uint_as_float(v[3] & 0xffff0000u); }
          { u32x4 v = __builtin_amdgcn_raw_buffer_load_b128(rpt, 458752 + w * 8192 + 5120 + lane * 16, 0, 16); const float f = fw[1][3];
            a[0] += f * __uint_as_float(v[0] << 16); a[1] += f * __uint_as_float(v[0] & 0xffff0000u); a[2] += f * __uint_as_float(v[1] << 16); a[3] += f * __uint_as_float(v[1] & 0xffff0000u);
            b2[0] += f * __uint_as_float(v[2] << 16); b2[1] += f * __uint_as_float(v[2] & 0xffff0000u); b2[2] += f * __uint_as_float(v[3] << 16); b2[3] += f * __uint_as_float(v[3] & 0xffff0000u); }
          if (R < 224) { u16* dst = o + ((size_t)((kvh * 2 + hl) * 8 + 4) * NROW + orow) * 32 + 16 + 4 * h;
            u32x2 v0 = {cvtpk(a[0], a[1]), cvtpk(a[2], a[3])}; u32x2 v1 = {cvtpk(b2[0], b2[1]), cvtpk(b2[2], b2[3])};
            *(u32x2*)dst = v0; *(u32x2*)(dst + 8) = v1; } }
        { f32x4 a = acc_read4<208>(), b2 = acc_read4<212>(); const float fo = fw[1][2] * inv[1]; a *= fo; b2 *= fo;
          { u32x4 v = __builtin_amdgcn_raw_buffer_load_b128(rpt, 65536 + w * 8192 + 6144 + lane * 16, 0, 16); const float f = fw[1][0];
            a[0] += f * __uint_as_float(v[0] << 16); a[1] += f * __uint_as_float(v[0] & 0xffff0000u); a[2] += f * __uint_as_float(v[1] << 16); a[3] += f * __uint_as_float(v[1] & 0xffff0000u);
            b2[0] += f * __uint_as_float(v[2] << 16); b2[1] += f * __uint_as_float(v[2] & 0xffff0000u); b2[2] += f * __uint_as_float(v[3] << 16); b2[3] += f * __uint_as_float(v[3] & 0xffff0000u); }
          { u32x4 v = __builtin_amdgcn_raw_buffer_load_b128(rpt, 196608 + w * 8192 + 6144 + lane * 16, 0, 16); const float f = fw[1][1];
            a[0] += f * __uint_as_float(v[0] << 16); a[1] += f * __uint_as_float(v[0] & 0xffff0000u); a[2] += f * __uint_as_float(v[1] << 16); a[3] += f * __uint_as_float(v[1] & 0xffff0000u);
            b2[0] += f * __uint_as_float(v[2] << 16); b2[1] += f * __uint_as_float(v[2] & 0xffff0000u); b2[2] += f * __uint_as_float(v[3] << 16); b2[3] += f * __uint_as_float(v[3] & 0xffff0000u); }
          { u32x4 v = __builtin_amdgcn_raw_buffer_load_b128(rpt, 458752 + w * 8192 + 6144 + lane * 16, 0, 16); const float f = fw[1][3];
            a[0] += f * __uint_as_float(v[0] << 16); a[1] += f * __uint_as_float(v[0] & 0xffff0000u); a[2] += f * __uint_as_float(v[1] << 16); a[3] += f * __uint_as_float(v[1] & 0xffff0000u);
            b2[0] += f * __uint_as_float(v[2] << 16); b2[1] += f * __uint_as_float(v[2] & 0xffff0000u); b2[2] += f * __uint_as_float(v[3] << 16); b2[3] += f * __uint_as_float(v[3] & 0xffff0000u); }
          if (R < 224) { u16* dst = o + ((size_t)((kvh * 2 + hl) * 8 + 5) * NROW + orow) * 32 + 0 + 4 * h;
            u32x2 v0 = {cvtpk(a[0], a[1]), cvtpk(a[2], a[3])}; u32x2 v1 = {cvtpk(b2[0], b2[1]), cvtpk(b2[2], b2[3])};
            *(u32x2*)dst = v0; *(u32x2*)(dst + 8) = v1; } }
        { f32x4 a = acc_read4<216>(), b2 = acc_read4<220>(); const float fo = fw[1][2] * inv[1]; a *= fo; b2 *= fo;
          { u32x4 v = __builtin_amdgcn_raw_buffer_load_b128(rpt, 65536 + w * 8192 + 7168 + lane * 16, 0, 16); const float f = fw[1][0];
            a[0] += f * __uint_as_float(v[0] << 16); a[1] += f * __uint_as_float(v[0] & 0xffff0000u); a[2] += f * __uint_as_float(v[1] << 16); a[3] += f * __uint_as_float(v[1] & 0xffff0000u);
            b2[0] += f * __uint_as_float(v[2] << 16); b2[1] += f * __uint_as_float(v[2] & 0xffff0000u); b2[2] += f * __uint_as_float(v[3] << 16); b2[3] += f * __uint_as_float(v[3] & 0xffff0000u); }
          { u32x4 v = __builtin_amdgcn_raw_buffer_load_b128(rpt, 196608 + w * 8192 + 7168 + lane * 16, 0, 16); const float f = fw[1][1];
            a[0] += f * __uint_as_float(v[0] << 16); a[1] += f * __uint_as_float(v[0] & 0xffff0000u); a[2] += f * __uint_as_float(v[1] << 16); a[3] += f * __uint_as_float(v[1] & 0xffff0000u);
            b2[0] += f * __uint_as_float(v[2] << 16); b2[1] += f * __uint_as_float(v[2] & 0xffff0000u); b2[2] += f * __uint_as_float(v[3] << 16); b2[3] += f * __uint_as_float(v[3] & 0xffff0000u); }
          { u32x4 v = __builtin_amdgcn_raw_buffer_load_b128(rpt, 458752 + w * 8192 + 7168 + lane * 16, 0, 16); const float f = fw[1][3];
            a[0] += f * __uint_as_float(v[0] << 16); a[1] += f * __uint_as_float(v[0] & 0xffff0000u); a[2] += f * __uint_as_float(v[1] << 16); a[3] += f * __uint_as_float(v[1] & 0xffff0000u);
            b2[0] += f * __uint_as_float(v[2] << 16); b2[1] += f * __uint_as_float(v[2] & 0xffff0000u); b2[2] += f * __uint_as_float(v[3] << 16); b2[3] += f * __uint_as_float(v[3] & 0xffff0000u); }
          if (R < 224) { u16* dst = o + ((size_t)((kvh * 2 + hl) * 8 + 5) * NROW + orow) * 32 + 16 + 4 * h;
            u32x2 v0 = {cvtpk(a[0], a[1]), cvtpk(a[2], a[3])}; u32x2 v1 = {cvtpk(b2[0], b2[1]), cvtpk(b2[2], b2[3])};
            *(u32x2*)dst = v0; *(u32x2*)(dst + 8) = v1; } }
      }
    } break;
    case 3: {
      { const int R = 64 * w + 0 + r; const int hl = R >= 112 ? 1 : 0; const int orow = b * SQ + R - 112 * hl;
        { f32x4 a = acc_read4<96>(), b2 = acc_read4<100>(); const float fo = fw[0][3] * inv[0]; a *= fo; b2 *= fo;
          { u32x4 v = __builtin_amdgcn_raw_buffer_load_b128(rpt, 98304 + w * 8192 + 0 + lane * 16, 0, 16); const float f = fw[0][0];
            a[0] += f * __uint_as_float(v[0] << 16); a[1] += f * __uint_as_float(v[0] & 0xffff0000u); a[2] += f * __uint_as_float(v[1] << 16); a[3] += f * __uint_as_float(v[1] & 0xffff0000u);
            b2[0] += f * __uint_as_float(v[2] << 16); b2[1] += f * __uint_as_float(v[2] & 0xffff0000u); b2[2] += f * __uint_as_float(v[3] << 16); b2[3] += f * __uint_as_float(v[3] & 0xffff0000u); }
          { u32x4 v = __builtin_amdgcn_raw_buffer_load_b128(rpt, 229376 + w * 8192 + 0 + lane * 16, 0, 16); const float f = fw[0][1];
            a[0] += f * __uint_as_float(v[0] << 16); a[1] += f * __uint_as_float(v[0] & 0xffff0000u); a[2] += f * __uint_as_float(v[1] << 16); a[3] += f * __uint_as_float(v[1] & 0xffff0000u);
            b2[0] += f * __uint_as_float(v[2] << 16); b2[1] += f * __uint_as_float(v[2] & 0xffff0000u); b2[2] += f * __uint_as_float(v[3] << 16); b2[3] += f * __uint_as_float(v[3] & 0xffff0000u); }
          { u32x4 v = __builtin_amdgcn_raw_buffer_load_b128(rpt, 360448 + w * 8192 + 0 + lane * 16, 0, 16); const float f = fw[0][2];
            a[0] += f * __uint_as_float(v[0] << 16); a[1] += f * __uint_as_float(v[0] & 0xffff0000u); a[2] += f * __uint_as_float(v[1] << 16); a[3] += f * __uint_as_float(v[1] & 0xffff0000u);
            b2[0] += f * __uint_as_float(v[2] << 16); b2[1] += f * __uint_as_float(v[2] & 0xffff0000u); b2[2] += f * __uint_as_float(v[3] << 16); b2[3] += f * __uint_as_float(v[3] & 0xffff0000u); }
          if (R < 224) { u16* dst = o + ((size_t)((kvh * 2 + hl) * 8 + 6) * NROW + orow) * 32 + 0 + 4 * h;
            u32x2 v0 = {cvtpk(a[0], a[1]), cvtpk(a[2], a[3])}; u32x2 v1 = {cvtpk(b2[0], b2[1]), cvtpk(b2[2], b2[3])};
            *(u32x2*)dst = v0; *(u32x2*)(dst + 8) = v1; } }
        { f32x4 a = acc_read4<104>(), b2 = acc_read4<108>(); const float fo = fw[0][3] * inv[0]; a *= fo; b2 *= fo;
          { u32x4 v = __builtin_amdgcn_raw_buffer_load_b128(rpt, 98304 + w * 8192 + 1024 + lane * 16, 0, 16); const float f = fw[0][0];
            a[0] += f * __uint_as_float(v[0] << 16); a[1] += f * __uint_as_float(v[0] & 0xffff0000u); a[2] += f * __uint_as_float(v[1] << 16); a[3] += f * __uint_as_float(v[1] & 0xffff0000u);
            b2[0] += f * __uint_as_float(v[2] << 16); b2[1] += f * __uint_as_float(v[2] & 0xffff0000u); b2[2] += f * __uint_as_float(v[3] << 16); b2[3] += f * __uint_as_float(v[3] & 0xffff0000u); }
          { u32x4 v = __builtin_amdgcn_raw_buffer_load_b128(rpt, 229376 + w * 8192 + 1024 + lane * 16, 0, 16); const float f = fw[0][1];
            a[0] += f * __uint_as_float(v[0] << 16); a[1] += f * __uint_as_float(v[0] & 0xffff0000u); a[2] += f * __uint_as_float(v[1] << 16); a[3] += f * __uint_as_float(v[1] & 0xffff0000u);
            b2[0] += f * __uint_as_float(v[2] << 16); b2[1] += f * __uint_as_float(v[2] & 0xffff0000u); b2[2] += f * __uint_as_float(v[3] << 16); b2[3] += f * __uint_as_float(v[3] & 0xffff0000u); }
          { u32x4 v = __builtin_amdgcn_raw_buffer_load_b128(rpt, 360448 + w * 8192 + 1024 + lane * 16, 0, 16); const float f = fw[0][2];
            a[0] += f * __uint_as_float(v[0] << 16); a[1] += f * __uint_as_float(v[0] & 0xffff0000u); a[2] += f * __uint_as_float(v[1] << 16); a[3] += f * __uint_as_float(v[1] & 0xffff0000u);
            b2[0] += f * __uint_as_float(v[2] << 16); b2[1] += f * __uint_as_float(v[2] & 0xffff0000u); b2[2] += f * __uint_as_float(v[3] << 16); b2[3] += f * __uint_as_float(v[3] & 0xffff0000u); }
          if (R < 224) { u16* dst = o + ((size_t)((kvh * 2 + hl) * 8 + 6) * NROW + orow) * 32 + 16 + 4 * h;
            u32x2 v0 = {cvtpk(a[0], a[1]), cvtpk(a[2], a[3])}; u32x2 v1 = {cvtpk(b2[0], b2[1]), cvtpk(b2[2], b2[3])};
            *(u32x2*)dst = v0; *(u32x2*)(dst + 8) = v1; } }
        { f32x4 a = acc_read4<112>(), b2 = acc_read4<116>(); const float fo = fw[0][3] * inv[0]; a *= fo; b2 *= fo;
          { u32x4 v = __builtin_amdgcn_raw_buffer_load_b128(rpt, 98304 + w * 8192 + 2048 + lane * 16, 0, 16); const float f = fw[0][0];
            a[0] += f * __uint_as_float(v[0] << 16); a[1] += f * __uint_as_float(v[0] & 0xffff0000u); a[2] += f * __uint_as_float(v[1] << 16); a[3] += f * __uint_as_float(v[1] & 0xffff0000u);
            b2[0] += f * __uint_as_float(v[2] << 16); b2[1] += f * __uint_as_float(v[2] & 0xffff0000u); b2[2] += f * __uint_as_float(v[3] << 16); b2[3] += f * __uint_as_float(v[3] & 0xffff0000u); }
          { u32x4 v = __builtin_amdgcn_raw_buffer_load_b128(rpt, 229376 + w * 8192 + 2048 + lane * 16, 0, 16); const float f = fw[0][1];
            a[0] += f * __uint_as_float(v[0] << 16); a[1] += f * __uint_as_float(v[0] & 0xffff0000u); a[2] += f * __uint_as_float(v[1] << 16); a[3] += f * __uint_as_float(v[1] & 0xffff0000u);
            b2[0] += f * __uint_as_float(v[2] << 16); b2[1] += f * __uint_as_float(v[2] & 0xffff0000u); b2[2] += f * __uint_as_float(v[3] << 16); b2[3] += f * __uint_as_float(v[3] & 0xffff0000u); }
          { u32x4 v = __builtin_amdgcn_raw_buffer_load_b128(rpt, 360448 + w * 8192 + 2048 + lane * 16, 0, 16); const float f = fw[0][2];
            a[0] += f * __uint_as_float(v[0] << 16); a[1] += f * __uint_as_float(v[0] & 0xffff0000u); a[2] += f * __uint_as_float(v[1] << 16); a[3] += f * __uint_as_float(v[1] & 0xffff0000u);
            b2[0] += f * __uint_as_float(v[2] << 16); b2[1] += f * __uint_as_float(v[2] & 0xffff0000u); b2[2] += f * __uint_as_float(v[3] << 16); b2[3] += f * __uint_as_float(v[3] & 0xffff0000u); }
          if (R < 224) { u16* dst = o + ((size_t)((kvh * 2 + hl) * 8 + 7) * NROW + orow) * 32 + 0 + 4 * h;
            u32x2 v0 = {cvtpk(a[0], a[1]), cvtpk(a[2], a[3])}; u32x2 v1 = {cvtpk(b2[0], b2[1]), cvtpk(b2[2], b2[3])};
            *(u32x2*)dst = v0; *(u32x2*)(dst + 8) = v1; } }
        { f32x4 a = acc_read4<120>(), b2 = acc_read4<124>(); const float fo = fw[0][3] * inv[0]; a *= fo; b2 *= fo;
          { u32x4 v = __builtin_amdgcn_raw_buffer_load_b128(rpt, 98304 + w * 8192 + 3072 + lane * 16, 0, 16); const float f = fw[0][0];
            a[0] += f * __uint_as_float(v[0] << 16); a[1] += f * __uint_as_float(v[0] & 0xffff0000u); a[2] += f * __uint_as_float(v[1] << 16); a[3] += f * __uint_as_float(v[1] & 0xffff0000u);
            b2[0] += f * __uint_as_float(v[2] << 16); b2[1] += f * __uint_as_float(v[2] & 0xffff0000u); b2[2] += f * __uint_as_float(v[3] << 16); b2[3] += f * __uint_as_float(v[3] & 0xffff0000u); }
          { u32x4 v = __builtin_amdgcn_raw_buffer_load_b128(rpt, 229376 + w * 8192 + 3072 + lane * 16, 0, 16); const float f = fw[0][1];
            a[0] += f * __uint_as_float(v[0] << 16); a[1] += f * __uint_as_float(v[0] & 0xffff0000u); a[2] += f * __uint_as_float(v[1] << 16); a[3] += f * __uint_as_float(v[1] & 0xffff0000u);
            b2[0] += f * __uint_as_float(v[2] << 16); b2[1] += f * __uint_as_float(v[2] & 0xffff0000u); b2[2] += f * __uint_as_float(v[3] << 16); b2[3] += f * __uint_as_float(v[3] & 0xffff0000u); }
          { u32x4 v = __builtin_amdgcn_raw_buffer_load_b128(rpt, 360448 + w * 8192 + 3072 + lane * 16, 0, 16); const float f = fw[0][2];
            a[0] += f * __uint_as_float(v[0] << 16); a[1] += f * __uint_as_float(v[0] & 0xffff0000u); a[2] += f * __uint_as_float(v[1] << 16); a[3] += f * __uint_as_float(v[1] & 0xffff0000u);
            b2[0] += f * __uint_as_float(v[2] << 16); b2[1] += f * __uint_as_float(v[2] & 0xffff0000u); b2[2] += f * __uint_as_float(v[3] << 16); b2[3] += f * __uint_as_float(v[3] & 0xffff0000u); }
          if (R < 224) { u16* dst = o + ((size_t)((kvh * 2 + hl) * 8 + 7) * NROW + orow) * 32 + 16 + 4 * h;
            u32x2 v0 = {cvtpk(a[0], a[1]), cvtpk(a[2], a[3])}; u32x2 v1 = {cvtpk(b2[0], b2[1]), cvtpk(b2[2], b2[3])};
            *(u32x2*)dst = v0; *(u32x2*)(dst + 8) = v1; } }
      }
      { const int R = 64 * w + 32 + r; const int hl = R >= 112 ? 1 : 0; const int orow = b * SQ + R - 112 * hl;
        { f32x4 a = acc_read4<224>(), b2 = acc_read4<228>(); const float fo = fw[1][3] * inv[1]; a *= fo; b2 *= fo;
          { u32x4 v = __builtin_amdgcn_raw_buffer_load_b128(rpt, 98304 + w * 8192 + 4096 + lane * 16, 0, 16); const float f = fw[1][0];
            a[0] += f * __uint_as_float(v[0] << 16); a[1] += f * __uint_as_float(v[0] & 0xffff0000u); a[2] += f * __uint_as_float(v[1] << 16); a[3] += f * __uint_as_float(v[1] & 0xffff0000u);
            b2[0] += f * __uint_as_float(v[2] << 16); b2[1] += f * __uint_as_float(v[2] & 0xffff0000u); b2[2] += f * __uint_as_float(v[3] << 16); b2[3] += f * __uint_as_float(v[3] & 0xffff0000u); }
          { u32x4 v = __builtin_amdgcn_raw_buffer_load_b128(rpt, 229376 + w * 8192 + 4096 + lane * 16, 0, 16); const float f = fw[1][1];
            a[0] += f * __uint_as_float(v[0] << 16); a[1] += f * __uint_as_float(v[0] & 0xffff0000u); a[2] += f * __uint_as_float(v[1] << 16); a[3] += f * __uint_as_float(v[1] & 0xffff0000u);
            b2[0] += f * __uint_as_float(v[2] << 16); b2[1] += f * __uint_as_float(v[2] & 0xffff0000u); b2[2] += f * __uint_as_float(v[3] << 16); b2[3] += f * __uint_as_float(v[3] & 0xffff0000u); }
          { u32x4 v = __builtin_amdgcn_raw_buffer_load_b128(rpt, 360448 + w * 8192 + 4096 + lane * 16, 0, 16); const float f = fw[1][2];
            a[0] += f * __uint_as_float(v[0] << 16); a[1] += f * __uint_as_float(v[0] & 0xffff0000u); a[2] += f * __uint_as_float(v[1] << 16); a[3] += f * __uint_as_float(v[1] & 0xffff0000u);
            b2[0] += f * __uint_as_float(v[2] << 16); b2[1] += f * __uint_as_float(v[2] & 0xffff0000u); b2[2] += f * __uint_as_float(v[3] << 16); b2[3] += f * __uint_as_float(v[3] & 0xffff0000u); }
          if (R < 224) { u16* dst = o + ((size_t)((kvh * 2 + hl) * 8 + 6) * NROW + orow) * 32 + 0 + 4 * h;
            u32x2 v0 = {cvtpk(a[0], a[1]), cvtpk(a[2], a[3])}; u32x2 v1 = {cvtpk(b2[0], b2[1]), cvtpk(b2[2], b2[3])};
            *(u32x2*)dst = v0; *(u32x2*)(dst + 8) = v1; } }
        { f32x4 a = acc_read4<232>(), b2 = acc_read4<236>(); const float fo = fw[1][3] * inv[1]; a *= fo; b2 *= fo;
          { u32x4 v = __builtin_amdgcn_raw_buffer_load_b128(rpt, 98304 + w * 8192 + 5120 + lane * 16, 0, 16); const float f = fw[1][0];
            a[0] += f * __uint_as_float(v[0] << 16); a[1] += f * __uint_as_float(v[0] & 0xffff0000u); a[2] += f * __uint_as_float(v[1] << 16); a[3] += f * __uint_as_float(v[1] & 0xffff0000u);
            b2[0] += f * __uint_as_float(v[2] << 16); b2[1] += f * __uint_as_float(v[2] & 0xffff0000u); b2[2] += f * __uint_as_float(v[3] << 16); b2[3] += f * __uint_as_float(v[3] & 0xffff0000u); }
          { u32x4 v = __builtin_amdgcn_raw_buffer_load_b128(rpt, 229376 + w * 8192 + 5120 + lane * 16, 0, 16); const float f = fw[1][1];
            a[0] += f * __uint_as_float(v[0] << 16); a[1] += f * __uint_as_float(v[0] & 0xffff0000u); a[2] += f * __uint_as_float(v[1] << 16); a[3] += f * __uint_as_float(v[1] & 0xffff0000u);
            b2[0] += f * __uint_as_float(v[2] << 16); b2[1] += f * __uint_as_float(v[2] & 0xffff0000u); b2[2] += f * __uint_as_float(v[3] << 16); b2[3] += f * __uint_as_float(v[3] & 0xffff0000u); }
          { u32x4 v = __builtin_amdgcn_raw_buffer_load_b128(rpt, 360448 + w * 8192 + 5120 + lane * 16, 0, 16); const float f = fw[1][2];
            a[0] += f * __uint_as_float(v[0] << 16); a[1] += f * __uint_as_float(v[0] & 0xffff0000u); a[2] += f * __uint_as_float(v[1] << 16); a[3] += f * __uint_as_float(v[1] & 0xffff0000u);
            b2[0] += f * __uint_as_float(v[2] << 16); b2[1] += f * __uint_as_float(v[2] & 0xffff0000u); b2[2] += f * __uint_as_float(v[3] << 16); b2[3] += f * __uint_as_float(v[3] & 0xffff0000u); }
          if (R < 224) { u16* dst = o + ((size_t)((kvh * 2 + hl) * 8 + 6) * NROW + orow) * 32 + 16 + 4 * h;
            u32x2 v0 = {cvtpk(a[0], a[1]), cvtpk(a[2], a[3])}; u32x2 v1 = {cvtpk(b2[0], b2[1]), cvtpk(b2[2], b2[3])};
            *(u32x2*)dst = v0; *(u32x2*)(dst + 8) = v1; } }
        { f32x4 a = acc_read4<240>(), b2 = acc_read4<244>(); const float fo = fw[1][3] * inv[1]; a *= fo; b2 *= fo;
          { u32x4 v = __builtin_amdgcn_raw_buffer_load_b128(rpt, 98304 + w * 8192 + 6144 + lane * 16, 0, 16); const float f = fw[1][0];
            a[0] += f * __uint_as_float(v[0] << 16); a[1] += f * __uint_as_float(v[0] & 0xffff0000u); a[2] += f * __uint_as_float(v[1] << 16); a[3] += f * __uint_as_float(v[1] & 0xffff0000u);
            b2[0] += f * __uint_as_float(v[2] << 16); b2[1] += f * __uint_as_float(v[2] & 0xffff0000u); b2[2] += f * __uint_as_float(v[3] << 16); b2[3] += f * __uint_as_float(v[3] & 0xffff0000u); }
          { u32x4 v = __builtin_amdgcn_raw_buffer_load_b128(rpt, 229376 + w * 8192 + 6144 + lane * 16, 0, 16); const float f = fw[1][1];
            a[0] += f * __uint_as_float(v[0] << 16); a[1] += f * __uint_as_float(v[0] & 0xffff0000u); a[2] += f * __uint_as_float(v[1] << 16); a[3] += f * __uint_as_float(v[1] & 0xffff0000u);
            b2[0] += f * __uint_as_float(v[2] << 16); b2[1] += f * __uint_as_float(v[2] & 0xffff0000u); b2[2] += f * __uint_as_float(v[3] << 16); b2[3] += f * __uint_as_float(v[3] & 0xffff0000u); }
          { u32x4 v = __builtin_amdgcn_raw_buffer_load_b128(rpt, 360448 + w * 8192 + 6144 + lane * 16, 0, 16); const float f = fw[1][2];
            a[0] += f * __uint_as_float(v[0] << 16); a[1] += f * __uint_as_float(v[0] & 0xffff0000u); a[2] += f * __uint_as_float(v[1] << 16); a[3] += f * __uint_as_float(v[1] & 0xffff0000u);
            b2[0] += f * __uint_as_float(v[2] << 16); b2[1] += f * __uint_as_float(v[2] & 0xffff0000u); b2[2] += f * __uint_as_float(v[3] << 16); b2[3] += f * __uint_as_float(v[3] & 0xffff0000u); }
          if (R < 224) { u16* dst = o + ((size_t)((kvh * 2 + hl) * 8 + 7) * NROW + orow) * 32 + 0 + 4 * h;
            u32x2 v0 = {cvtpk(a[0], a[1]), cvtpk(a[2], a[3])}; u32x2 v1 = {cvtpk(b2[0], b2[1]), cvtpk(b2[2], b2[3])};
            *(u32x2*)dst = v0; *(u32x2*)(dst + 8) = v1; } }
        { f32x4 a = acc_read4<248>(), b2 = acc_read4<252>(); const float fo = fw[1][3] * inv[1]; a *= fo; b2 *= fo;
          { u32x4 v = __builtin_amdgcn_raw_buffer_load_b128(rpt, 98304 + w * 8192 + 7168 + lane * 16, 0, 16); const float f = fw[1][0];
            a[0] += f * __uint_as_float(v[0] << 16); a[1] += f * __uint_as_float(v[0] & 0xffff0000u); a[2] += f * __uint_as_float(v[1] << 16); a[3] += f * __uint_as_float(v[1] & 0xffff0000u);
            b2[0] += f * __uint_as_float(v[2] << 16); b2[1] += f * __uint_as_float(v[2] & 0xffff0000u); b2[2] += f * __uint_as_float(v[3] << 16); b2[3] += f * __uint_as_float(v[3] & 0xffff0000u); }
          { u32x4 v = __builtin_amdgcn_raw_buffer_load_b128(rpt, 229376 + w * 8192 + 7168 + lane * 16, 0, 16); const float f = fw[1][1];
            a[0] += f * __uint_as_float(v[0] << 16); a[1] += f * __uint_as_float(v[0] & 0xffff0000u); a[2] += f * __uint_as_float(v[1] << 16); a[3] += f * __uint_as_float(v[1] & 0xffff0000u);
            b2[0] += f * __uint_as_float(v[2] << 16); b2[1] += f * __uint_as_float(v[2] & 0xffff0000u); b2[2] += f * __uint_as_float(v[3] << 16); b2[3] += f * __uint_as_float(v[3] & 0xffff0000u); }
          { u32x4 v = __builtin_amdgcn_raw_buffer_load_b128(rpt, 360448 + w * 8192 + 7168 + lane * 16, 0, 16); const float f = fw[1][2];
            a[0] += f * __uint_as_float(v[0] << 16); a[1] += f * __uint_as_float(v[0] & 0xffff0000u); a[2] += f * __uint_as_float(v[1] << 16); a[3] += f * __uint_as_float(v[1] & 0xffff0000u);
            b2[0] += f * __uint_as_float(v[2] << 16); b2[1] += f * __uint_as_float(v[2] & 0xffff0000u); b2[2] += f * __uint_as_float(v[3] << 16); b2[3] += f * __uint_as_float(v[3] & 0xffff0000u); }
          if (R < 224) { u16* dst = o + ((size_t)((kvh * 2 + hl) * 8 + 7) * NROW + orow) * 32 + 16 + 4 * h;
            u32x2 v0 = {cvtpk(a[0], a[1]), cvtpk(a[2], a[3])}; u32x2 v1 = {cvtpk(b2[0], b2[1]), cvtpk(b2[2], b2[3])};
            *(u32x2*)dst = v0; *(u32x2*)(dst + 8) = v1; } }
      }
    } break;
  }
  STAMP(10);
}
__global__ __launch_bounds__(512, 2) void oproj2_kernel(const u16* __restrict__ o, const u16* __restrict__ wo,
                                                        u16* __restrict__ out) {
  extern __shared__ __attribute__((aligned(16))) char lds[];
  const int tid = threadIdx.x, lane = tid & 63;
  const int w = __builtin_amdgcn_readfirstlane(tid >> 6);
  const int r = lane & 31, h = lane >> 5;
  const int xcd = blockIdx.x & 7, idx = blockIdx.x >> 3;
  const int rt = xcd + 8 * (idx / 7), ct = idx % 7;
  if (rt >= 28) return;
  const int col0 = ct * 64;
  char* ring = lds + w * 18432;
  __amdgpu_buffer_rsrc_t rw = __builtin_amdgcn_make_buffer_rsrc((void*)(wo + (size_t)(512 * w) * DM + col0), 0,
                                                                 (512 * DM - col0) * 2, 0x00020000);
  __amdgpu_buffer_rsrc_t ra = __builtin_amdgcn_make_buffer_rsrc((void*)(o + ((size_t)(16 * w) * NROW + rt * 32) * 32), 0,
                                                                 (16 * NROW - rt * 32) * 64, 0x00020000);
  const int drow = lane >> 3;
  const int wvoff = drow * (DM * 2) + (((lane & 7) ^ (((drow >> 1) & 1) << 2)) * 16);
  const int arow = lane >> 2;
  const int avoff = arow * 64 + (((lane & 3) ^ ((arow >> 2) & 3)) * 16);
  auto issue = [&](int u) {
    char* dst = ring + (u % 3) * 6144;
#pragma unroll
    for (int p = 0; p < 4; ++p)
      __builtin_amdgcn_raw_ptr_buffer_load_lds(rw, (lds_vp)(dst + p * 1024), 16, wvoff, (32 * u + 8 * p) * (DM * 2), 0, 0);
#pragma unroll
    for (int p = 0; p < 2; ++p)
      __builtin_amdgcn_raw_ptr_buffer_load_lds(ra, (lds_vp)(dst + 4096 + p * 1024), 16, avoff, 1024 * p + u * (NROW * 64), 0, 0);
  };
  issue(0); issue(1); issue(2);
  const int g = lane >> 4, qq = (lane >> 2) & 3, pp = lane & 3;
  const unsigned rb = (unsigned)(size_t)ring;
  const unsigned trb = rb + (8 * (g >> 1) + qq) * 128 + ((32 * (g & 1) + 8 * pp) ^ ((qq >> 1) << 6));
  const unsigned ab0 = rb + 4096 + r * 64 + (((0 + h) ^ ((r >> 2) & 3)) * 16);
  const unsigned ab1 = rb + 4096 + r * 64 + (((2 + h) ^ ((r >> 2) & 3)) * 16);
  f32x16 acc[2];
#pragma unroll
  for (int b2 = 0; b2 < 2; ++b2)
#pragma unroll
    for (int i = 0; i < 16; ++i) acc[b2][i] = 0.f;
#pragma unroll
  for (int u = 0; u < 16; ++u) {
    if (u <= 13) WAITV(12); else if (u == 14) WAITV(6); else WAITV(0);
    const unsigned so = (u % 3) * 6144;
    s16x4 a[2][4];
    bf16x8 xb[2];
#pragma unroll
    for (int s = 0; s < 2; ++s) {
      a[s][0] = trread(trb + so + s * 2048);
      a[s][1] = trread(trb + so + s * 2048 + 512);
      a[s][2] = trread((trb + so + s * 2048) ^ 64);
      a[s][3] = trread((trb + so + s * 2048 + 512) ^ 64);
    }
    asm volatile("ds_read_b128 %0, %1" : "=v"(xb[0]) : "v"(ab0 + so) : "memory");
    asm volatile("ds_read_b128 %0, %1" : "=v"(xb[1]) : "v"(ab1 + so) : "memory");
    asm volatile("s_waitcnt lgkmcnt(0)"
                 : "+v"(a[0][0]), "+v"(a[0][1]), "+v"(a[0][2]), "+v"(a[0][3]), "+v"(a[1][0]), "+v"(a[1][1]),
                   "+v"(a[1][2]), "+v"(a[1][3]), "+v"(xb[0]), "+v"(xb[1])::"memory");
    if (u + 3 < 16) issue(u + 3);
#pragma unroll
    for (int s = 0; s < 2; ++s) {
      acc[0] = __builtin_amdgcn_mfma_f32_32x32x16_bf16(cat8(a[s][0], a[s][1]), xb[s], acc[0], 0, 0, 0);
      acc[1] = __builtin_amdgcn_mfma_f32_32x32x16_bf16(cat8(a[s][2], a[s][3]), xb[s], acc[1], 0, 0, 0);
    }
  }
  __syncthreads();
  float* red = (float*)lds;
#pragma unroll
  for (int cb = 0; cb < 2; ++cb)
#pragma unroll
    for (int gg = 0; gg < 4; ++gg) {
      f32x4 v = {acc[cb][4 * gg], acc[cb][4 * gg + 1], acc[cb][4 * gg + 2], acc[cb][4 * gg + 3]};
      *(f32x4*)(red + ((w * 8 + cb * 4 + gg) * 64 + lane) * 4) = v;
    }
  __syncthreads();
  {
    const int cg = w, cb = cg >> 2, gg = cg & 3;
    f32x4 s = {0.f, 0.f, 0.f, 0.f};
#pragma unroll
    for (int ww = 0; ww < 8; ++ww) s += *(const f32x4*)(red + ((ww * 8 + cg) * 64 + lane) * 4);
    u32x2 v = {cvtpk(s[0], s[1]), cvtpk(s[2], s[3])};
    *(u32x2*)(out + (size_t)(rt * 32 + r) * DM + col0 + 32 * cb + 8 * gg + 4 * h) = v;
  }
}
__global__ __launch_bounds__(512, 2) void oproj3_kernel(const u16* __restrict__ o, const u16* __restrict__ wo,
                                                        u16* __restrict__ out, float* __restrict__ slab,
                                                        unsigned* __restrict__ cnt DBGARG) {
  extern __shared__ __attribute__((aligned(16))) char lds[];
  STAMP(0);
  const int tid = threadIdx.x, lane = tid & 63;
  const int w = __builtin_amdgcn_readfirstlane(tid >> 6);
  const int r = lane & 31, h = lane >> 5;
  const int xcd = blockIdx.x & 7, idx = blockIdx.x >> 3;
  const int rt = xcd + 8 * (idx / 14), ct = (idx % 14) >> 1, kh = idx & 1;
  if (rt >= 14) return;
  const int tile = rt * 7 + ct;
  const int col0 = ct * 64, k0 = kh * 2048 + 256 * w;
  char* ring = lds + w * 16384;
  __amdgpu_buffer_rsrc_t rw = __builtin_amdgcn_make_buffer_rsrc((void*)(wo + (size_t)k0 * DM + col0), 0,
                                                                 (256 * DM - col0) * 2, 0x00020000);
  __amdgpu_buffer_rsrc_t ra = __builtin_amdgcn_make_buffer_rsrc((void*)(o + ((size_t)(k0 >> 5) * NROW + rt * 64) * 32), 0,
                                                                 (8 * NROW - rt * 64) * 64, 0x00020000);
  const int drow = lane >> 3;
  const int wvoff = drow * (DM * 2) + (((lane & 7) ^ (((drow >> 1) & 1) << 2)) * 16);
  const int arow = lane >> 2;
  const int avoff = arow * 64 + (((lane & 3) ^ ((arow >> 2) & 3)) * 16);
  auto issue = [&](int u) {
    char* dst = ring + (u & 1) * 8192;
#pragma unroll
    for (int p = 0; p < 4; ++p)
      __builtin_amdgcn_raw_ptr_buffer_load_lds(rw, (lds_vp)(dst + p * 1024), 16, wvoff, (32 * u + 8 * p) * (DM * 2), 0, 0);
#pragma unroll
    for (int p = 0; p < 4; ++p)
      __builtin_amdgcn_raw_ptr_buffer_load_lds(ra, (lds_vp)(dst + 4096 + p * 1024), 16, avoff, 1024 * p + u * (NROW * 64), 0, 0);
  };
  issue(0); issue(1);
  const int g = lane >> 4, qq = (lane >> 2) & 3, pp = lane & 3;
  const unsigned rb = (unsigned)(size_t)ring;
  const unsigned trb = rb + (8 * (g >> 1) + qq) * 128 + ((32 * (g & 1) + 8 * pp) ^ ((qq >> 1) << 6));
  const unsigned ab0 = rb + 4096 + r * 64 + (((0 + h) ^ ((r >> 2) & 3)) * 16);
  const unsigned ab1 = rb + 4096 + r * 64 + (((2 + h) ^ ((r >> 2) & 3)) * 16);
  f32x16 acc[2][2];
#pragma unroll
  for (int a2 = 0; a2 < 2; ++a2)
#pragma unroll
    for (int b2 = 0; b2 < 2; ++b2)
#pragma unroll
      for (int i = 0; i < 16; ++i) acc[a2][b2][i] = 0.f;
#pragma unroll
  for (int u = 0; u < 8; ++u) {
    if (u <= 6) WAITV(8); else WAITV(0);
    const unsigned so = (u & 1) * 8192;
    s16x4 a[2][4];
    bf16x8 xb[2][2];
#pragma unroll
    for (int s = 0; s < 2; ++s) {
      a[s][0] = trread(trb + so + s * 2048);
      a[s][1] = trread(trb + so + s * 2048 + 512);
      a[s][2] = trread((trb + so + s * 2048) ^ 64);
      a[s][3] = trread((trb + so + s * 2048 + 512) ^ 64);
    }
    asm volatile("ds_read_b128 %0, %1" : "=v"(xb[0][0]) : "v"(ab0 + so) : "memory");
    asm volatile("ds_read_b128 %0, %1" : "=v"(xb[1][0]) : "v"(ab1 + so) : "memory");
    asm volatile("ds_read_b128 %0, %1 offset:2048" : "=v"(xb[0][1]) : "v"(ab0 + so) : "memory");
    asm volatile("ds_read_b128 %0, %1 offset:2048" : "=v"(xb[1][1]) : "v"(ab1 + so) : "memory");
    asm volatile("s_waitcnt lgkmcnt(0)"
                 : "+v"(a[0][0]), "+v"(a[0][1]), "+v"(a[0][2]), "+v"(a[0][3]), "+v"(a[1][0]), "+v"(a[1][1]),
                   "+v"(a[1][2]), "+v"(a[1][3]), "+v"(xb[0][0]), "+v"(xb[1][0]), "+v"(xb[0][1]), "+v"(xb[1][1])::"memory");
    if (u + 2 < 8) issue(u + 2);
#pragma unroll
    for (int s = 0; s < 2; ++s)
#pragma unroll
      for (int rb2 = 0; rb2 < 2; ++rb2) {
        acc[rb2][0] = __builtin_amdgcn_mfma_f32_32x32x16_bf16(cat8(a[s][0], a[s][1]), xb[s][rb2], acc[rb2][0], 0, 0, 0);
        acc[rb2][1] = __builtin_amdgcn_mfma_f32_32x32x16_bf16(cat8(a[s][2], a[s][3]), xb[s][rb2], acc[rb2][1], 0, 0, 0);
      }
  }
  STAMP(1);
  __syncthreads();
  float* red = (float*)lds;
#pragma unroll
  for (int rb2 = 0; rb2 < 2; ++rb2)
#pragma unroll
    for (int cb = 0; cb < 2; ++cb)
#pragma unroll
      for (int gg = 0; gg < 4; ++gg) {
        f32x4 v = {acc[rb2][cb][4 * gg], acc[rb2][cb][4 * gg + 1], acc[rb2][cb][4 * gg + 2], acc[rb2][cb][4 * gg + 3]};
        *(f32x4*)(red + ((w * 16 + rb2 * 8 + cb * 4 + gg) * 64 + lane) * 4) = v;
      }
  __syncthreads();
  f32x4 sum[2];
#pragma unroll
  for (int jj = 0; jj < 2; ++jj) {
    const int grp = 2 * w + jj;
    f32x4 s = {0.f, 0.f, 0.f, 0.f};
#pragma unroll
    for (int ww = 0; ww < 8; ++ww) s += *(const f32x4*)(red + ((ww * 16 + grp) * 64 + lane) * 4);
    sum[jj] = s;
  }
  STAMP(2);
  __amdgpu_buffer_rsrc_t rs = __builtin_amdgcn_make_buffer_rsrc((void*)(slab + (size_t)tile * 2 * 4096), 0, 2 * 4096 * 4, 0x00020000);
#pragma unroll
  for (int jj = 0; jj < 2; ++jj)
    __builtin_amdgcn_raw_buffer_store_b128(__builtin_bit_cast(u32x4, sum[jj]), rs, (kh * 16 + 2 * w + jj) * 1024 + lane * 16, 0, 16);
  asm volatile("s_waitcnt vmcnt(0)" ::: "memory");
  __syncthreads();
  unsigned* flag = (unsigned*)(lds + 131072);
  if (tid == 0) *flag = __hip_atomic_fetch_add(cnt + tile, 1u, __ATOMIC_RELAXED, __HIP_MEMORY_SCOPE_AGENT);
  __syncthreads();
  STAMP(3);
  if (*flag == 1u) {
#pragma unroll
    for (int jj = 0; jj < 2; ++jj) {
      u32x4 pv = __builtin_amdgcn_raw_buffer_load_b128(rs, ((kh ^ 1) * 16 + 2 * w + jj) * 1024 + lane * 16, 0, 16);
      f32x4 t = sum[jj] + __builtin_bit_cast(f32x4, pv);
      const int grp = 2 * w + jj, rb2 = grp >> 3, cb = (grp >> 2) & 1, gg = grp & 3;
      u32x2 v = {cvtpk(t[0], t[1]), cvtpk(t[2], t[3])};
      *(u32x2*)(out + (size_t)(rt * 64 + 32 * rb2 + r) * DM + col0 + 32 * cb + 8 * gg + 4 * h) = v;
    }
  }
  STAMP(4);
}
extern "C" void kernel_launch(void* const* d_in, const int* in_sizes, int n_in, void* d_out, int out_size,
                              void* d_ws, size_t ws_size, hipStream_t stream) {
  const u16* x = (const u16*)d_in[0];
  const u16* kc = (const u16*)d_in[1];
  const u16* vc = (const u16*)d_in[2];
  const u16* wq = (const u16*)d_in[3];
  const u16* wo = (const u16*)d_in[4];
  u16* out = (u16*)d_out;
  u16* qws = (u16*)d_ws;
  u16* ows = (u16*)((char*)d_ws + (8  __H0__ ));
  float* slab = (float*)((char*)d_ws + (16 << 20));
  unsigned* cnt = (unsigned*)((char*)d_ws + (24 << 20));
  (void)hipFuncSetAttribute((const void*)attn4_kernel, hipFuncAttributeMaxDynamicSharedMemorySize, 163840);
  hipLaunchKernelGGL(attn4_kernel, dim3(256), dim3(256), 163840, stream, x, wq, kc, vc, wo, ows, cnt
  );
  (void)hipFuncSetAttribute((const void*)oproj2_kernel, hipFuncAttributeMaxDynamicSharedMemorySize, 163840);
  hipLaunchKernelGGL(oproj2_kernel, dim3(8 * 28), dim3(512), 163840, stream, (const u16*)ows, wo, out);
  return;
  (void)hipFuncSetAttribute((const void*)oproj3_kernel, hipFuncAttributeMaxDynamicSharedMemorySize, 131328);
  hipLaunchKernelGGL(oproj3_kernel, dim3(8 * 28), dim3(512), 131328, stream, (const u16*)ows, wo, out, slab, cnt
  );
}
```

```cpp
#define FUSE_OPROJ
#include <hip/hip_runtime.h>
#include <hip/hip_bf16.h>
#include <utility>

typedef unsigned short u16;
typedef __attribute__((ext_vector_type(8))) short bf16x8;
typedef __attribute__((ext_vector_type(4))) short s16x4;
typedef __attribute__((ext_vector_type(16))) float f32x16;
typedef __attribute__((ext_vector_type(4))) float f32x4;
typedef __attribute__((ext_vector_type(2))) float f32x2;
typedef __attribute__((ext_vector_type(2))) __bf16 bf16x2v;
typedef __attribute__((ext_vector_type(2))) unsigned u32x2;
typedef __attribute__((ext_vector_type(4))) unsigned u32x4;
#define DI __device__ __forceinline__
#define LDSP(p) ((__attribute__((address_space(3))) unsigned*)(p))

constexpr int NB = 8, SQ = 112, DM = 448, HQ = 16, HKV = 8, SK = 1024, DH = 256;
constexpr int NROW = NB * SQ;
constexpr int NQ = HQ * DH;

DI unsigned cvtpk(float lo, float hi) {
  f32x2 v = {lo, hi};
  bf16x2v b = __builtin_convertvector(v, bf16x2v);
  return __builtin_bit_cast(unsigned, b);
}
DI void glds16(const void* g, void* l) {
  __builtin_amdgcn_global_load_lds((const unsigned*)g, LDSP(l), 16, 0, 0);
}
#define WAITV(n) asm volatile("s_waitcnt vmcnt(" #n ")" ::: "memory")
#define WAITL0() asm volatile("s_waitcnt lgkmcnt(0)" ::: "memory")

DI s16x4 trread(unsigned addr) {
  s16x4 r;
  asm volatile("ds_read_b64_tr_b16 %0, %1" : "=v"(r) : "v"(addr) : "memory");
  return r;
}
template <int OFF> DI s16x4 trread_o(unsigned addr) {
  s16x4 r;
  asm volatile("ds_read_b64_tr_b16 %0, %1 offset:%2" : "=v"(r) : "v"(addr), "i"(OFF) : "memory");
  return r;
}
DI bf16x8 cat8(s16x4 a, s16x4 b) { return __builtin_shufflevector(a, b, 0, 1, 2, 3, 4, 5, 6, 7); }

template <int RB, int NCH>
DI void wave_gemm(const u16* __restrict__ act, int lda, const int (&arow)[RB], const u16* __restrict__ W, int ldw,
                  char* wl, f32x16 (&acc)[RB][2], int lane) {
  const int r = lane & 31, h = lane >> 5;
  const int g = lane >> 4, q = (lane >> 2) & 3, pp = lane & 3;
  const unsigned ldsb = (unsigned)(size_t)wl;
  const unsigned trb = ldsb + (8 * (g >> 1) + q) * 128 + ((32 * (g & 1) + 8 * pp) ^ ((q >> 1) << 6));
  const int drow = lane >> 3;
  const int dchk = (lane & 7) ^ (((drow >> 1) & 1) << 2);
  const u16* wsrc = W + (size_t)drow * ldw + dchk * 8;
  auto issue = [&](int c, int buf) {
#pragma unroll
    for (int p = 0; p < 8; ++p) glds16(wsrc + (size_t)(64 * c + 8 * p) * ldw, wl + buf * 8192 + p * 1024);
  };
  issue(0, 0);
#pragma unroll
  for (int c = 0; c < NCH; ++c) {
    const int buf = c & 1;
    bf16x8 bfr[4][RB];
#pragma unroll
    for (int s = 0; s < 4; ++s)
#pragma unroll
      for (int rb = 0; rb < RB; ++rb)
        bfr[s][rb] = *(const bf16x8*)(act + (size_t)arow[rb] * lda + 64 * c + 16 * s + 8 * h);
    WAITV(0);
    if (c + 1 < NCH) issue(c + 1, buf ^ 1);
#pragma unroll
    for (int s = 0; s < 4; ++s) {
      s16x4 a00 = trread(trb + buf * 8192 + s * 2048 + 0);
      s16x4 a01 = trread(trb + buf * 8192 + s * 2048 + 512);
      s16x4 a10 = trread((trb + buf * 8192 + s * 2048 + 0) ^ 64);
      s16x4 a11 = trread((trb + buf * 8192 + s * 2048 + 512) ^ 64);
      asm volatile("s_waitcnt lgkmcnt(0)" : "+v"(a00), "+v"(a01), "+v"(a10), "+v"(a11)::"memory");
      bf16x8 A0 = cat8(a00, a01), A1 = cat8(a10, a11);
#pragma unroll
      for (int rb = 0; rb < RB; ++rb) {
        acc[rb][0] = __builtin_amdgcn_mfma_f32_32x32x16_bf16(A0, bfr[s][rb], acc[rb][0], 0, 0, 0);
        acc[rb][1] = __builtin_amdgcn_mfma_f32_32x32x16_bf16(A1, bfr[s][rb], acc[rb][1], 0, 0, 0);
      }
    }
  }
  (void)r;
}

__global__ __launch_bounds__(256) void qproj_kernel(const u16* __restrict__ x, const u16* __restrict__ wq,
                                                    u16* __restrict__ qout) {
  extern __shared__ __attribute__((aligned(16))) char lds[];
  const int tid = threadIdx.x, lane = tid & 63;
  const int w = __builtin_amdgcn_readfirstlane(tid >> 6);
  const int r = lane & 31, h = lane >> 5;
  const int rt = blockIdx.x % 14, head = blockIdx.x / 14;
  const int col0 = head * 256 + 64 * w;
  f32x16 acc[2][2];
#pragma unroll
  for (int a = 0; a < 2; ++a)
#pragma unroll
    for (int b = 0; b < 2; ++b)
#pragma unroll
      for (int i = 0; i < 16; ++i) acc[a][b][i] = 0.f;
  const int arow[2] = {rt * 64 + r, rt * 64 + 32 + r};
  wave_gemm<2, 7>(x, DM, arow, wq + col0, NQ, lds + w * 16384, acc, lane);
#pragma unroll
  for (int rb = 0; rb < 2; ++rb)
#pragma unroll
    for (int cb = 0; cb < 2; ++cb)
#pragma unroll
      for (int g = 0; g < 4; ++g) {
        u32x2 v = {cvtpk(acc[rb][cb][4 * g + 0], acc[rb][cb][4 * g + 1]),
                   cvtpk(acc[rb][cb][4 * g + 2], acc[rb][cb][4 * g + 3])};
        *(u32x2*)(qout + (size_t)arow[rb] * NQ + col0 + 32 * cb + 8 * g + 4 * h) = v;
      }
}

__global__ __launch_bounds__(256) void oproj_kernel(const u16* __restrict__ o, const u16* __restrict__ wo,
                                                    u16* __restrict__ out) {
  extern __shared__ __attribute__((aligned(16))) char lds[];
  const int tid = threadIdx.x, lane = tid & 63;
  const int w = __builtin_amdgcn_readfirstlane(tid >> 6);
  const int r = lane & 31, h = lane >> 5;
  const int rt = blockIdx.x % 28, ct = blockIdx.x / 28;
  const int col0 = ct * 64;
  f32x16 acc[1][2];
#pragma unroll
  for (int b = 0; b < 2; ++b)
#pragma unroll
    for (int i = 0; i < 16; ++i) acc[0][b][i] = 0.f;
  const int arow[1] = {rt * 32 + r};
  wave_gemm<1, 16>(o + 1024 * w, NQ, arow, wo + (size_t)(1024 * w) * DM + col0, DM, lds + w * 16384, acc, lane);
  __syncthreads();
  float* red = (float*)lds;
#pragma unroll
  for (int cb = 0; cb < 2; ++cb)
#pragma unroll
    for (int g = 0; g < 4; ++g) {
      f32x4 v = {acc[0][cb][4 * g], acc[0][cb][4 * g + 1], acc[0][cb][4 * g + 2], acc[0][cb][4 * g + 3]};
      *(f32x4*)(red + ((w * 8 + cb * 4 + g) * 64 + lane) * 4) = v;
    }
  __syncthreads();
#pragma unroll
  for (int j = 0; j < 2; ++j) {
    const int cg = 2 * w + j, cb = cg >> 2, g = cg & 3;
    f32x4 s = {0.f, 0.f, 0.f, 0.f};
#pragma unroll
    for (int ww = 0; ww < 4; ++ww) {
      f32x4 v = *(const f32x4*)(red + ((ww * 8 + cg) * 64 + lane) * 4);
      s += v;
    }
    u32x2 v = {cvtpk(s[0], s[1]), cvtpk(s[2], s[3])};
    *(u32x2*)(out + (size_t)arow[0] * DM + col0 + 32 * cb + 8 * g + 4 * h) = v;
  }
}

constexpr float SCL = 0.0625f * 1.4426950408889634f;
constexpr float THR = 8.0f;

typedef __attribute__((address_space(3))) void* lds_vp;
struct DmaCtx {
  __amdgpu_buffer_rsrc_t rk, rv;
  int koff[4], voff[4];
  char* dst;
};
template <int U>
DI void issue_unit(const DmaCtx& c) {
  constexpr int t = U >> 2, kind = U & 3, slot = U % 5;
  if constexpr (kind < 2) {
#pragma unroll
    for (int pi = 0; pi < 4; ++pi)
      __builtin_amdgcn_raw_ptr_buffer_load_lds(c.rk, (lds_vp)(c.dst + slot * 8192 + pi * 1024), 16, c.koff[pi],
                                               32 * t * 512 + kind * 256, 0, 0);
  } else {
    constexpr int ks = kind - 2;
#pragma unroll
    for (int pi = 0; pi < 4; ++pi)
      __builtin_amdgcn_raw_ptr_buffer_load_lds(c.rv, (lds_vp)(c.dst + slot * 8192 + pi * 1024), 16, c.voff[pi],
                                               (32 * t + 16 * ks) * 512, 0, 0);
  }
}
template <int U> DI void wait_unit_bar() {
  constexpr int after = (U + 3 > 31 ? 31 : U + 3) - U;
  if constexpr (after == 3) WAITV(12);
  else if constexpr (after == 2) WAITV(8);
  else if constexpr (after == 1) WAITV(4);
  else WAITV(0);
  WAITL0();
  __builtin_amdgcn_s_barrier();
}

struct AttnState {
  bf16x8 qf[16];
  f32x16 oacc[8];
  float m, l;
};

template <int T, int DHF>
DI void qk_half(AttnState& st, f32x16& s, const DmaCtx& c, char* ring, unsigned kaddr) {
  constexpr int U = 4 * T + DHF, slot = U % 5;
  wait_unit_bar<U>();
  if constexpr (U + 4 < 32) issue_unit<U + 4>(c);
#pragma unroll
  for (int ss = 0; ss < 8; ++ss) {
    bf16x8 kf = *(const bf16x8*)(ring + slot * 8192 + (kaddr ^ (ss * 32)));
    s = __builtin_amdgcn_mfma_f32_32x32x16_bf16(kf, st.qf[DHF * 8 + ss], s, 0, 0, 0);
  }
}
template <int T, int KS, int HF>
DI void pv_quarter(AttnState& st, const bf16x8& pf, const unsigned (&vaddr)[4]) {
  constexpr int U = 4 * T + 2 + KS, slot = U % 5;
  constexpr int OFF = slot * 8192 + HF * 256;
  s16x4 v0[4], v1[4];
  v0[0] = trread_o<OFF>(vaddr[0]); v1[0] = trread_o<OFF + 4096>(vaddr[0]);
  v0[1] = trread_o<OFF>(vaddr[1]); v1[1] = trread_o<OFF + 4096>(vaddr[1]);
  v0[2] = trread_o<OFF>(vaddr[2]); v1[2] = trread_o<OFF + 4096>(vaddr[2]);
  v0[3] = trread_o<OFF>(vaddr[3]); v1[3] = trread_o<OFF + 4096>(vaddr[3]);
  asm volatile("s_waitcnt lgkmcnt(0)"
               : "+v"(v0[0]), "+v"(v0[1]), "+v"(v0[2]), "+v"(v0[3]), "+v"(v1[0]), "+v"(v1[1]), "+v"(v1[2]),
                 "+v"(v1[3])::"memory");
#pragma unroll
  for (int d = 0; d < 4; ++d) {
    bf16x8 vf = cat8(v0[d], v1[d]);
    st.oacc[HF * 4 + d] = __builtin_amdgcn_mfma_f32_32x32x16_bf16(vf, pf, st.oacc[HF * 4 + d], 0, 0, 0);
  }
}
template <int T, int KS>
DI void pv_half(AttnState& st, const bf16x8& pf, const DmaCtx& c, const unsigned (&vaddr)[4]) {
  constexpr int U = 4 * T + 2 + KS;
  wait_unit_bar<U>();
  if constexpr (U + 4 < 32) issue_unit<U + 4>(c);
  pv_quarter<T, KS, 0>(st, pf, vaddr);
  pv_quarter<T, KS, 1>(st, pf, vaddr);
}

template <int T>
DI void attn_tile(AttnState& st, const DmaCtx& c, char* ring, unsigned kaddr, const unsigned (&vaddr)[4]) {
  f32x16 s;
#pragma unroll
  for (int i = 0; i < 16; ++i) s[i] = 0.f;
  qk_half<T, 0>(st, s, c, ring, kaddr);
  qk_half<T, 1>(st, s, c, ring, kaddr);
  bf16x8 pf[2];
  {
    float a = s[0];
#pragma unroll
    for (int i = 1; i < 16; ++i) a = fmaxf(a, s[i]);
    auto rr = __builtin_amdgcn_permlane32_swap(__float_as_uint(a), __float_as_uint(a), false, false);
    const float mx = fmaxf(__uint_as_float(rr[0]), __uint_as_float(rr[1])) * SCL;
    const bool grew = (mx - st.m > THR);
    if (T == 0 || __any((int)grew)) {
      const float mn = fmaxf(st.m, mx);
      const float alpha = __builtin_amdgcn_exp2f(st.m - mn);
      st.m = mn;
      st.l *= alpha;
      if (T != 0) {
#pragma unroll
        for (int db = 0; db < 8; ++db)
#pragma unroll
          for (int i = 0; i < 16; ++i) st.oacc[db][i] *= alpha;
      }
    }
    const float nm = -st.m;
    float ps = 0.f;
#pragma unroll
    for (int i = 0; i < 16; ++i) {
      s[i] = __builtin_amdgcn_exp2f(fmaf(s[i], SCL, nm));
      ps += s[i];
    }
    st.l += ps;
#pragma unroll
    for (int ks = 0; ks < 2; ++ks) {
      u32x4 wv = {cvtpk(s[8 * ks + 0], s[8 * ks + 1]), cvtpk(s[8 * ks + 2], s[8 * ks + 3]),
                  cvtpk(s[8 * ks + 4], s[8 * ks + 5]), cvtpk(s[8 * ks + 6], s[8 * ks + 7])};
      pf[ks] = __builtin_bit_cast(bf16x8, wv);
    }
  }
  pv_half<T, 0>(st, pf[0], c, vaddr);
  pv_half<T, 1>(st, pf[1], c, vaddr);
}

#define STAMP(i)
#define DBGARG
__global__ __launch_bounds__(512, 2) void attn_kernel(const u16* __restrict__ q, const u16* __restrict__ kc,
                                                      const u16* __restrict__ vc, u16* __restrict__ o DBGARG) {
  extern __shared__ __attribute__((aligned(16))) char lds[];
  STAMP(0);
  const int tid = threadIdx.x, lane = tid & 63;
  const int w = __builtin_amdgcn_readfirstlane(tid >> 6);
  const int rb = w >> 2, kq = w & 3;
  const int r = lane & 31, h = lane >> 5;
  const int bid = blockIdx.x;
  const int xcd = bid & 7, j = bid >> 3;
  const int grp = (j >> 2) * 8 + xcd, sub = j & 3;
  const int b = grp >> 3, kvh = grp & 7;
  const int head = kvh * 2 + (sub >> 1), rhalf = sub & 1;
  const int row0 = b * SQ + rhalf * 56;

  const u16* Kw = kc + ((size_t)(b * HKV + kvh) * SK + 256 * kq) * DH;
  const u16* Vw = vc + ((size_t)(b * HKV + kvh) * SK + 256 * kq) * DH;
  char* ring = lds + kq * 40960;

  AttnState st;
  {
    int rr = 32 * rb + r;
    rr = rr > 55 ? 55 : rr;
    const u16* qrow = q + (size_t)(row0 + rr) * NQ + head * DH + 8 * h;
#pragma unroll
    for (int s = 0; s < 16; ++s) st.qf[s] = *(const bf16x8*)(qrow + 16 * s);
    st.m = -__builtin_inff();
    st.l = 0.f;
#pragma unroll
    for (int db = 0; db < 8; ++db)
#pragma unroll
      for (int i = 0; i < 16; ++i) st.oacc[db][i] = 0.f;
  }
#pragma unroll
  for (int s = 0; s < 16; ++s) asm volatile("" : "+v"(st.qf[s]));
  WAITV(0);

  STAMP(1);
  DmaCtx c;
  c.rk = __builtin_amdgcn_make_buffer_rsrc((void*)Kw, 0, 256 * DH * 2, 0x00020000);
  c.rv = __builtin_amdgcn_make_buffer_rsrc((void*)Vw, 0, 256 * DH * 2, 0x00020000);
  c.dst = ring + rb * 4096;
#pragma unroll
  for (int pi = 0; pi < 4; ++pi) {
    const int krow = 16 * rb + 4 * pi + (lane >> 4);
    c.koff[pi] = krow * 512 + (((lane & 15) ^ (krow & 15)) * 16);
    const int vrow = 8 * rb + 2 * pi + (lane >> 5);
    c.voff[pi] = vrow * 512 + (((lane & 31) ^ ((vrow & 3) << 2)) * 16);
  }
  issue_unit<0>(c);
  issue_unit<1>(c);
  issue_unit<2>(c);
  issue_unit<3>(c);

  const unsigned kaddr = r * 256 + ((h ^ (r & 15)) * 16);
  const int g = lane >> 4, qq = (lane >> 2) & 3, pp = lane & 3;
  unsigned vaddr[4];
#pragma unroll
  for (int d = 0; d < 4; ++d)
    vaddr[d] = (unsigned)(size_t)ring + (4 * (g >> 1) + qq) * 512 + ((64 * d + 32 * (g & 1) + 8 * pp) ^ (qq << 6));

  attn_tile<0>(st, c, ring, kaddr, vaddr);
  STAMP(2);
  attn_tile<1>(st, c, ring, kaddr, vaddr);
  STAMP(3);
  attn_tile<2>(st, c, ring, kaddr, vaddr);
  STAMP(4);
  attn_tile<3>(st, c, ring, kaddr, vaddr);
  STAMP(5);
  attn_tile<4>(st, c, ring, kaddr, vaddr);
  STAMP(6);
  attn_tile<5>(st, c, ring, kaddr, vaddr);
  STAMP(7);
  attn_tile<6>(st, c, ring, kaddr, vaddr);
  STAMP(8);
  attn_tile<7>(st, c, ring, kaddr, vaddr);
  STAMP(9);

  __syncthreads();
  float* ml = (float*)lds;
  {
    auto rr = __builtin_amdgcn_permlane32_swap(__float_as_uint(st.l), __float_as_uint(st.l), false, false);
    const float lt = __uint_as_float(rr[0]) + __uint_as_float(rr[1]);
    if (h == 0) {
      ml[(w * 32 + r) * 2 + 0] = st.m;
      ml[(w * 32 + r) * 2 + 1] = lt;
    }
  }
  __syncthreads();
  float f;
  {
    float mm[4], ll[4];
#pragma unroll
    for (int k = 0; k < 4; ++k) {
      mm[k] = ml[((rb * 4 + k) * 32 + r) * 2 + 0];
      ll[k] = ml[((rb * 4 + k) * 32 + r) * 2 + 1];
    }
    const float M = fmaxf(fmaxf(mm[0], mm[1]), fmaxf(mm[2], mm[3]));
    float L = 0.f;
#pragma unroll
    for (int k = 0; k < 4; ++k) L += __builtin_amdgcn_exp2f(mm[k] - M) * ll[k];
    f = __builtin_amdgcn_exp2f(st.m - M) / L;
  }
  float* xch = (float*)(lds + 4096);
#pragma unroll
  for (int rnd = 0; rnd < 2; ++rnd) {
#pragma unroll
    for (int db = 0; db < 8; ++db) {
      if ((db & 1) != rnd) continue;
      const int owner = db >> 1;
      if (owner == kq) continue;
      const int dslot = owner < kq ? owner : owner - 1;
#pragma unroll
      for (int gg = 0; gg < 4; ++gg) {
        f32x4 v = {st.oacc[db][4 * gg] * f, st.oacc[db][4 * gg + 1] * f, st.oacc[db][4 * gg + 2] * f,
                   st.oacc[db][4 * gg + 3] * f};
        *(f32x4*)(xch + (((w * 3 + dslot) * 4 + gg) * 64 + lane) * 4) = v;
      }
    }
    __syncthreads();
#pragma unroll
    for (int db = 0; db < 8; ++db) {
      if ((db & 1) != rnd) continue;
      if ((db >> 1) != kq) continue;
#pragma unroll
      for (int gg = 0; gg < 4; ++gg) {
        f32x4 sum = {st.oacc[db][4 * gg] * f, st.oacc[db][4 * gg + 1] * f, st.oacc[db][4 * gg + 2] * f,
                     st.oacc[db][4 * gg + 3] * f};
#pragma unroll
        for (int sk = 0; sk < 4; ++sk) {
          if (sk == kq) continue;
          const int dslot = kq < sk ? kq : kq - 1;
          f32x4 v = *(const f32x4*)(xch + ((((rb * 4 + sk) * 3 + dslot) * 4 + gg) * 64 + lane) * 4);
          sum += v;
        }
        const int rr = 32 * rb + r;
        if (rr < 56) {
          u32x2 v = {cvtpk(sum[0], sum[1]), cvtpk(sum[2], sum[3])};
          *(u32x2*)(o + (size_t)(row0 + rr) * NQ + head * DH + 32 * db + 8 * gg + 4 * h) = v;
        }
      }
    }
    __syncthreads();
  }
  STAMP(10);
}

#define GAP() __builtin_amdgcn_sched_barrier(0)
#define ACLOB "a0","a1","a2","a3","a4","a5","a6","a7","a8","a9","a10","a11","a12","a13","a14","a15","a16","a17","a18","a19","a20","a21","a22","a23","a24","a25","a26","a27","a28","a29","a30","a31","a32","a33","a34","a35","a36","a37","a38","a39","a40","a41","a42","a43","a44","a45","a46","a47","a48","a49","a50","a51","a52","a53","a54","a55","a56","a57","a58","a59","a60","a61","a62","a63","a64","a65","a66","a67","a68","a69","a70","a71","a72","a73","a74","a75","a76","a77","a78","a79","a80","a81","a82","a83","a84","a85","a86","a87","a88","a89","a90","a91","a92","a93","a94","a95","a96","a97","a98","a99","a100","a101","a102","a103","a104","a105","a106","a107","a108","a109","a110","a111","a112","a113","a114","a115","a116","a117","a118","a119","a120","a121","a122","a123","a124","a125","a126","a127","a128","a129","a130","a131","a132","a133","a134","a135","a136","a137","a138","a139","a140","a141","a142","a143","a144","a145","a146","a147","a148","a149","a150","a151","a152","a153","a154","a155","a156","a157","a158","a159","a160","a161","a162","a163","a164","a165","a166","a167","a168","a169","a170","a171","a172","a173","a174","a175","a176","a177","a178","a179","a180","a181","a182","a183","a184","a185","a186","a187","a188","a189","a190","a191","a192","a193","a194","a195","a196","a197","a198","a199","a200","a201","a202","a203","a204","a205","a206","a207","a208","a209","a210","a211","a212","a213","a214","a215","a216","a217","a218","a219","a220","a221","a222","a223","a224","a225","a226","a227","a228","a229","a230","a231","a232","a233","a234","a235","a236","a237","a238","a239","a240","a241","a242","a243","a244","a245","a246","a247","a248","a249","a250","a251","a252","a253","a254","a255"

template <int AO, bool ZERO> DI void pv_mfma(const bf16x8& vf, const bf16x8& pf) {
  if constexpr (ZERO)
    asm volatile("v_mfma_f32_32x32x16_bf16 a[%c2:%c3], %0, %1, 0" ::"v"(vf), "v"(pf), "i"(AO), "i"(AO + 15) : ACLOB);
  else
    asm volatile("v_mfma_f32_32x32x16_bf16 a[%c2:%c3], %0, %1, a[%c2:%c3]" ::"v"(vf), "v"(pf), "i"(AO), "i"(AO + 15)
                 : ACLOB);
}
template <bool FIRST> DI void qk_mfma(f32x16& s, const bf16x8& kf, const bf16x8& qf) {
  if constexpr (FIRST)
    asm volatile("v_mfma_f32_32x32x16_bf16 %0, %1, %2, 0" : "=&v"(s) : "v"(kf), "v"(qf) : ACLOB);
  else
    asm volatile("v_mfma_f32_32x32x16_bf16 %0, %1, %2, %0" : "+v"(s) : "v"(kf), "v"(qf) : ACLOB);
}
DI float max3f(float a, float b, float c) { float r; asm("v_max3_f32 %0, %1, %2, %3" : "=v"(r) : "v"(a), "v"(b), "v"(c)); return r; }
DI float max2f(float a, float b) { float r; asm("v_max_f32_e32 %0, %1, %2" : "=v"(r) : "v"(a), "v"(b)); return r; }
DI float fadd_s(float a, float b) { float r; asm("v_add_f32_e32 %0, %1, %2" : "=v"(r) : "v"(a), "v"(b)); return r; }
DI float ffma_s(float a, float b, float c) { float r; asm("v_fma_f32 %0, %1, %2, %3" : "=v"(r) : "v"(a), "v"(b), "v"(c)); return r; }

constexpr int unit_is_v(int n) { return n < 2 ? 0 : (n >= 30 ? 1 : (((n - 2) & 3) >= 2)); }
constexpr int unit_tile(int n) { return n < 2 ? 0 : (n >= 30 ? 7 : (((n - 2) & 3) >= 2 ? (n - 2) / 4 : (n - 2) / 4 + 1)); }
constexpr int unit_half(int n) { return n & 1; }
constexpr int unit_k(int t, int hf) { return t == 0 ? hf : 4 * (t - 1) + 2 + hf; }
constexpr int unit_v(int t, int hf) { return t == 7 ? 30 + hf : 4 * t + 4 + hf; }

struct Ctx4 {
  __amdgpu_buffer_rsrc_t rk, rv;
  int koff[4], voff[2];
  char* ring;
  unsigned kaddr[8];
  unsigned vaddr[4];
  bf16x8 qf[2][16];
  f32x16 s[2];
  bf16x8 pf[2][2];
  unsigned pn[2][8];
  float m[2], l[2], nm[2], mx[2], fct[2];
  float rs[2];
  bool pend;
  const u16* Kp; const u16* Vp;
  unsigned pfoff;
  int sub;
  unsigned pfd;
};
DI void prefetch_unit(Ctx4& c, int n) {
}

template <int N, int P> DI void dma_piece(const Ctx4& c) {
  if constexpr (N < 32) {
    constexpr int slot = N % 5, t = unit_tile(N), hf = unit_half(N);
    if constexpr (!unit_is_v(N))
      __builtin_amdgcn_raw_ptr_buffer_load_lds(c.rk, (lds_vp)(c.ring + slot * 8192 + P * 1024), 16, c.koff[P & 3],
                                               (32 * t + 16 * (P >> 2)) * 512 + hf * 256, 0, 0);
    else
      __builtin_amdgcn_raw_ptr_buffer_load_lds(c.rv, (lds_vp)(c.ring + slot * 8192 + P * 1024), 16, c.voff[P & 1],
                                               (32 * t + 16 * hf + 4 * (P >> 1)) * 512, 0, 0);
  }
}
template <int N> DI void dma_unit(const Ctx4& c) {
  dma_piece<N, 0>(c); dma_piece<N, 1>(c); dma_piece<N, 2>(c); dma_piece<N, 3>(c);
  dma_piece<N, 4>(c); dma_piece<N, 5>(c); dma_piece<N, 6>(c); dma_piece<N, 7>(c);
}
template <int N, int AHEAD> DI void wait_unit4() {
  constexpr int last = N + AHEAD > 31 ? 31 : N + AHEAD;
  constexpr int after = last - N;
  if constexpr (after == 4) WAITV(32);
  else if constexpr (after == 3) WAITV(24);
  else if constexpr (after == 2) WAITV(16);
  else if constexpr (after == 1) WAITV(8);
  else WAITV(0);
}

template <int T, int J> DI bf16x8 kread(const Ctx4& c) {
  constexpr int n = unit_k(T, J >> 3), slot = n % 5;
  bf16x8 r;
  asm volatile("ds_read_b128 %0, %1 offset:%2" : "=v"(r) : "v"(c.kaddr[J & 7]), "i"(slot * 8192) : "memory");
  return r;
}
template <int T, int J> DI void phaseA_step(Ctx4& c, bf16x8 (&kf)[4]) {
  constexpr int n0 = unit_k(T, 0), n1 = unit_k(T, 1);
  if constexpr (J + 3 < 16) asm volatile("s_waitcnt lgkmcnt(3)" : "+v"(kf[J & 3])::"memory");
  else if constexpr (J + 3 == 16) asm volatile("s_waitcnt lgkmcnt(2)" : "+v"(kf[J & 3])::"memory");
  else if constexpr (J + 3 == 17) asm volatile("s_waitcnt lgkmcnt(1)" : "+v"(kf[J & 3])::"memory");
  else asm volatile("s_waitcnt lgkmcnt(0)" : "+v"(kf[J & 3])::"memory");
  qk_mfma<J == 0>(c.s[0], kf[J & 3], c.qf[0][J]);
  if constexpr (J == 8 && T <= 4) prefetch_unit(c, 14 + 4 * T + c.sub);
  if constexpr (J >= 8) { dma_piece<n0 + 5, J - 8>(c); }
  GAP();
  qk_mfma<J == 0>(c.s[1], kf[J & 3], c.qf[1][J]);
  if constexpr (J + 4 == 8) wait_unit4<n1, 3>();
  if constexpr (J + 4 < 16) kf[J & 3] = kread<T, J + 4>(c);
  GAP();
}
template <int T, int... Js> DI void phaseA_(Ctx4& c, std::integer_sequence<int, Js...>) {
  bf16x8 kf[4];
  wait_unit4<unit_k(T, 0), 4>();
  kf[0] = kread<T, 0>(c); kf[1] = kread<T, 1>(c); kf[2] = kread<T, 2>(c); kf[3] = kread<T, 3>(c);
  GAP();
  (phaseA_step<T, Js>(c, kf), ...);
}
template <int T> DI void phaseA(Ctx4& c) { phaseA_<T>(c, std::make_integer_sequence<int, 16>{}); }

constexpr int egap(int e) { return 6 + (e * 25) / 32; }
template <int T, int G> DI void sm_slice(Ctx4& c) {
  if constexpr (G < 4) {
    constexpr int qb = G >> 1;
    if constexpr ((G & 1) == 0) {
      float a = max3f(c.s[qb][0], c.s[qb][1], c.s[qb][2]);
      a = max3f(a, c.s[qb][3], c.s[qb][4]);
      a = max3f(a, c.s[qb][5], c.s[qb][6]);
      c.mx[qb] = max3f(a, c.s[qb][7], c.s[qb][8]);
    } else {
      float a = max3f(c.mx[qb], c.s[qb][9], c.s[qb][10]);
      a = max3f(a, c.s[qb][11], c.s[qb][12]);
      a = max3f(a, c.s[qb][13], c.s[qb][14]);
      c.mx[qb] = max2f(a, c.s[qb][15]);
    }
  } else if constexpr (G == 4) {
#pragma unroll
    for (int qb = 0; qb < 2; ++qb) {
      auto rr = __builtin_amdgcn_permlane32_swap(__float_as_uint(c.mx[qb]), __float_as_uint(c.mx[qb]), false, false);
      c.mx[qb] = max2f(__uint_as_float(rr[0]), __uint_as_float(rr[1])) * SCL;
    }
  } else if constexpr (G == 5) {
    if constexpr (T == 0) {
      c.m[0] = c.mx[0]; c.m[1] = c.mx[1];
      c.l[0] = 0.f; c.l[1] = 0.f;
    } else {
      const bool grew = (c.mx[0] - c.m[0] > THR) | (c.mx[1] - c.m[1] > THR);
      if (__builtin_expect(__any((int)grew), 0)) {
#pragma unroll
        for (int qb = 0; qb < 2; ++qb) {
          const float mn = fmaxf(c.m[qb], c.mx[qb]);
          const float al = __builtin_amdgcn_exp2f(c.m[qb] - mn);
          c.m[qb] = mn;
          c.l[qb] *= al;
          c.fct[qb] = al;
        }
        c.pend = true;
      }
    }
    c.nm[0] = -c.m[0]; c.nm[1] = -c.m[1];
    c.rs[0] = 0.f; c.rs[1] = 0.f;
  }
  if constexpr (G >= 6) {
#pragma unroll
    for (int e = 0; e < 32; ++e) {
      if (egap(e) == G) {
        const int qb = e >> 4, i = e & 15;
        c.s[qb][i] = __builtin_amdgcn_exp2f(ffma_s(c.s[qb][i], SCL, c.nm[qb]));
      }
    }
#pragma unroll
    for (int e = 0; e < 32; ++e) {
      const int ge = egap(e);
      if (ge == G - 1 || (G == 31 && ge == 31)) {
        const int qb = e >> 4, i = e & 15;
        c.rs[qb] += c.s[qb][i];
      }
    }
#pragma unroll
    for (int d = 0; d < 16; ++d) {
      const int ge = egap(2 * d + 1);
      if (ge == G - 1 || (G == 31 && ge == 31)) {
        const int qb = d >> 3, i = (2 * d) & 15;
        c.pn[qb][d & 7] = cvtpk(c.s[qb][i], c.s[qb][i + 1]);
      }
    }
  }
}

template <int TV, int S> DI void vread_set(const Ctx4& c, s16x4 (&v)[4]) {
  constexpr int ks = S >> 2, dbp = S & 3;
  constexpr int n = unit_v(TV, ks), slot = n % 5;
  constexpr int db0 = 2 * dbp, db1 = 2 * dbp + 1;
  constexpr int o0 = slot * 8192 + (db0 >> 2) * 256, o1 = slot * 8192 + (db1 >> 2) * 256;
  v[0] = trread_o<o0>(c.vaddr[db0 & 3]);
  v[1] = trread_o<o0 + 4096>(c.vaddr[db0 & 3]);
  v[2] = trread_o<o1>(c.vaddr[db1 & 3]);
  v[3] = trread_o<o1 + 4096>(c.vaddr[db1 & 3]);
}
template <int T, bool HASPV, bool HASSM, int M> DI void phaseB_gap(Ctx4& c, s16x4 (&va)[4], s16x4 (&vb)[4]) {
  constexpr int TV = T - 1;
  constexpr int S = M >> 2, ks = S >> 2, dbp = S & 3, dd = (M >> 1) & 1, qb = M & 1;
  if constexpr (HASPV) {
    if constexpr ((M & 3) == 0) {
      if constexpr (S + 1 < 8) {
        if constexpr (S & 1) asm volatile("s_waitcnt lgkmcnt(4)" : "+v"(vb[0]), "+v"(vb[1]), "+v"(vb[2]), "+v"(vb[3])::"memory");
        else asm volatile("s_waitcnt lgkmcnt(4)" : "+v"(va[0]), "+v"(va[1]), "+v"(va[2]), "+v"(va[3])::"memory");
      } else {
        asm volatile("s_waitcnt lgkmcnt(0)" : "+v"(vb[0]), "+v"(vb[1]), "+v"(vb[2]), "+v"(vb[3])::"memory");
      }
    }
    {
      constexpr int db = 2 * dbp + dd;
      const bf16x8 vf = (S & 1) ? cat8(vb[2 * dd], vb[2 * dd + 1]) : cat8(va[2 * dd], va[2 * dd + 1]);
      pv_mfma<(qb * 8 + db) * 16, (TV == 0 && ks == 0)>(vf, c.pf[qb][ks]);
    }
    if constexpr ((M & 3) == 3 && S + 2 < 8) {
      if constexpr (S + 2 == 4) wait_unit4<unit_v(TV, 1), 3>();
      if constexpr (S & 1) vread_set<TV, S + 2>(c, vb); else vread_set<TV, S + 2>(c, va);
    }
    if constexpr (M >= 16 && M < 24) dma_piece<unit_v(TV, 0) + 5, M - 16>(c);
  }
  if constexpr (HASSM) sm_slice<T, M>(c);
  GAP();
}
template <int T, bool HASPV, bool HASSM, int... Ms> DI void phaseB_(Ctx4& c, std::integer_sequence<int, Ms...>) {
  constexpr int TV = T - 1;
  s16x4 va[4], vb[4];
  if constexpr (HASPV) {
    wait_unit4<unit_v(TV, 0), 3>();
    vread_set<TV, 0>(c, va);
    vread_set<TV, 1>(c, vb);
  }
  if constexpr (HASSM) dma_unit<unit_k(T, 1) + 5>(c);
  if constexpr (HASSM) {
    asm volatile("s_nop 7\n\ts_nop 7" : "+v"(c.s[0]), "+v"(c.s[1]));
  }
  GAP();
  (phaseB_gap<T, HASPV, HASSM, Ms>(c, va, vb), ...);
  if constexpr (HASPV) dma_unit<unit_v(TV, 1) + 5>(c);
  if constexpr (HASSM) {
    c.l[0] += c.rs[0]; c.l[1] += c.rs[1];
#pragma unroll
    for (int qb = 0; qb < 2; ++qb)
#pragma unroll
      for (int ks = 0; ks < 2; ++ks) {
        u32x4 wv = {c.pn[qb][4 * ks], c.pn[qb][4 * ks + 1], c.pn[qb][4 * ks + 2], c.pn[qb][4 * ks + 3]};
        c.pf[qb][ks] = __builtin_bit_cast(bf16x8, wv);
      }
  }
  if constexpr (HASPV && HASSM) {
    if (__builtin_expect(c.pend, 0)) {
      float t0, t1;
      asm volatile("s_nop 7\n\ts_nop 7\n\t"
                   "v_accvgpr_read_b32 %0, a0\n\tv_mul_f32 %0, %2, %0\n\tv_accvgpr_write_b32 a0, %0\n\t"
                   "v_accvgpr_read_b32 %1, a1\n\tv_mul_f32 %1, %2, %1\n\tv_accvgpr_write_b32 a1, %1\n\t"
                   "v_accvgpr_read_b32 %0, a2\n\tv_mul_f32 %0, %2, %0\n\tv_accvgpr_write_b32 a2, %0\n\t"
                   "v_accvgpr_read_b32 %1, a3\n\tv_mul_f32 %1, %2, %1\n\tv_accvgpr_write_b32 a3, %1\n\t"
                   "v_accvgpr_read_b32 %0, a4\n\tv_mul_f32 %0, %2, %0\n\tv_accvgpr_write_b32 a4, %0\n\t"
                   "v_accvgpr_read_b32 %1, a5\n\tv_mul_f32 %1, %2, %1\n\tv_accvgpr_write_b32 a5, %1\n\t"
                   "v_accvgpr_read_b32 %0, a6\n\tv_mul_f32 %0, %2, %0\n\tv_accvgpr_write_b32 a6, %0\n\t"
                   "v_accvgpr_read_b32 %1, a7\n\tv_mul_f32 %1, %2, %1\n\tv_accvgpr_write_b32 a7, %1\n\t"
                   "v_accvgpr_read_b32 %0, a8\n\tv_mul_f32 %0, %2, %0\n\tv_accvgpr_write_b32 a8, %0\n\t"
                   "v_accvgpr_read_b32 %1, a9\n\tv_mul_f32 %1, %2, %1\n\tv_accvgpr_write_b32 a9, %1\n\t"
                   "v_accvgpr_read_b32 %0, a10\n\tv_mul_f32 %0, %2, %0\n\tv_accvgpr_write_b32 a10, %0\n\t"
                   "v_accvgpr_read_b32 %1, a11\n\tv_mul_f32 %1, %2, %1\n\tv_accvgpr_write_b32 a11, %1\n\t"
                   "v_accvgpr_read_b32 %0, a12\n\tv_mul_f32 %0, %2, %0\n\tv_accvgpr_write_b32 a12, %0\n\t"
                   "v_accvgpr_read_b32 %1, a13\n\tv_mul_f32 %1, %2, %1\n\tv_accvgpr_write_b32 a13, %1\n\t"
                   "v_accvgpr_read_b32 %0, a14\n\tv_mul_f32 %0, %2, %0\n\tv_accvgpr_write_b32 a14, %0\n\t"
                   "v_accvgpr_read_b32 %1, a15\n\tv_mul_f32 %1, %2, %1\n\tv_accvgpr_write_b32 a15, %1\n\t"
                   "v_accvgpr_read_b32 %0, a16\n\tv_mul_f32 %0, %2, %0\n\tv_accvgpr_write_b32 a16, %0\n\t"
                   "v_accvgpr_read_b32 %1, a17\n\tv_mul_f32 %1, %2, %1\n\tv_accvgpr_write_b32 a17, %1\n\t"
                   "v_accvgpr_read_b32 %0, a18\n\tv_mul_f32 %0, %2, %0\n\tv_accvgpr_write_b32 a18, %0\n\t"
                   "v_accvgpr_read_b32 %1, a19\n\tv_mul_f32 %1, %2, %1\n\tv_accvgpr_write_b32 a19, %1\n\t"
                   "v_accvgpr_read_b32 %0, a20\n\tv_mul_f32 %0, %2, %0\n\tv_accvgpr_write_b32 a20, %0\n\t"
                   "v_accvgpr_read_b32 %1, a21\n\tv_mul_f32 %1, %2, %1\n\tv_accvgpr_write_b32 a21, %1\n\t"
                   "v_accvgpr_read_b32 %0, a22\n\tv_mul_f32 %0, %2, %0\n\tv_accvgpr_write_b32 a22, %0\n\t"
                   "v_accvgpr_read_b32 %1, a23\n\tv_mul_f32 %1, %2, %1\n\tv_accvgpr_write_b32 a23, %1\n\t"
                   "v_accvgpr_read_b32 %0, a24\n\tv_mul_f32 %0, %2, %0\n\tv_accvgpr_write_b32 a24, %0\n\t"
                   "v_accvgpr_read_b32 %1, a25\n\tv_mul_f32 %1, %2, %1\n\tv_accvgpr_write_b32 a25, %1\n\t"
                   "v_accvgpr_read_b32 %0, a26\n\tv_mul_f32 %0, %2, %0\n\tv_accvgpr_write_b32 a26, %0\n\t"
                   "v_accvgpr_read_b32 %1, a27\n\tv_mul_f32 %1, %2, %1\n\tv_accvgpr_write_b32 a27, %1\n\t"
                   "v_accvgpr_read_b32 %0, a28\n\tv_mul_f32 %0, %2, %0\n\tv_accvgpr_write_b32 a28, %0\n\t"
                   "v_accvgpr_read_b32 %1, a29\n\tv_mul_f32 %1, %2, %1\n\tv_accvgpr_write_b32 a29, %1\n\t"
                   "v_accvgpr_read_b32 %0, a30\n\tv_mul_f32 %0, %2, %0\n\tv_accvgpr_write_b32 a30, %0\n\t"
                   "v_accvgpr_read_b32 %1, a31\n\tv_mul_f32 %1, %2, %1\n\tv_accvgpr_write_b32 a31, %1\n\t"
                   "v_accvgpr_read_b32 %0, a32\n\tv_mul_f32 %0, %2, %0\n\tv_accvgpr_write_b32 a32, %0\n\t"
                   "v_accvgpr_read_b32 %1, a33\n\tv_mul_f32 %1, %2, %1\n\tv_accvgpr_write_b32 a33, %1\n\t"
                   "v_accvgpr_read_b32 %0, a34\n\tv_mul_f32 %0, %2, %0\n\tv_accvgpr_write_b32 a34, %0\n\t"
                   "v_accvgpr_read_b32 %1, a35\n\tv_mul_f32 %1, %2, %1\n\tv_accvgpr_write_b32 a35, %1\n\t"
                   "v_accvgpr_read_b32 %0, a36\n\tv_mul_f32 %0, %2, %0\n\tv_accvgpr_write_b32 a36, %0\n\t"
                   "v_accvgpr_read_b32 %1, a37\n\tv_mul_f32 %1, %2, %1\n\tv_accvgpr_write_b32 a37, %1\n\t"
                   "v_accvgpr_read_b32 %0, a38\n\tv_mul_f32 %0, %2, %0\n\tv_accvgpr_write_b32 a38, %0\n\t"
                   "v_accvgpr_read_b32 %1, a39\n\tv_mul_f32 %1, %2, %1\n\tv_accvgpr_write_b32 a39, %1\n\t"
                   "v_accvgpr_read_b32 %0, a40\n\tv_mul_f32 %0, %2, %0\n\tv_accvgpr_write_b32 a40, %0\n\t"
                   "v_accvgpr_read_b32 %1, a41\n\tv_mul_f32 %1, %2, %1\n\tv_accvgpr_write_b32 a41, %1\n\t"
                   "v_accvgpr_read_b32 %0, a42\n\tv_mul_f32 %0, %2, %0\n\tv_accvgpr_write_b32 a42, %0\n\t"
                   "v_accvgpr_read_b32 %1, a43\n\tv_mul_f32 %1, %2, %1\n\tv_accvgpr_write_b32 a43, %1\n\t"
                   "v_accvgpr_read_b32 %0, a44\n\tv_mul_f32 %0, %2, %0\n\tv_accvgpr_write_b32 a44, %0\n\t"
                   "v_accvgpr_read_b32 %1, a45\n\tv_mul_f32 %1, %2, %1\n\tv_accvgpr_write_b32 a45, %1\n\t"
                   "v_accvgpr_read_b32 %0, a46\n\tv_mul_f32 %0, %2, %0\n\tv_accvgpr_write_b32 a46, %0\n\t"
                   "v_accvgpr_read_b32 %1, a47\n\tv_mul_f32 %1, %2, %1\n\tv_accvgpr_write_b32 a47, %1\n\t"
                   "v_accvgpr_read_b32 %0, a48\n\tv_mul_f32 %0, %2, %0\n\tv_accvgpr_write_b32 a48, %0\n\t"
                   "v_accvgpr_read_b32 %1, a49\n\tv_mul_f32 %1, %2, %1\n\tv_accvgpr_write_b32 a49, %1\n\t"
                   "v_accvgpr_read_b32 %0, a50\n\tv_mul_f32 %0, %2, %0\n\tv_accvgpr_write_b32 a50, %0\n\t"
                   "v_accvgpr_read_b32 %1, a51\n\tv_mul_f32 %1, %2, %1\n\tv_accvgpr_write_b32 a51, %1\n\t"
                   "v_accvgpr_read_b32 %0, a52\n\tv_mul_f32 %0, %2, %0\n\tv_accvgpr_write_b32 a52, %0\n\t"
                   "v_accvgpr_read_b32 %1, a53\n\tv_mul_f32 %1, %2, %1\n\tv_accvgpr_write_b32 a53, %1\n\t"
                   "v_accvgpr_read_b32 %0, a54\n\tv_mul_f32 %0, %2, %0\n\tv_accvgpr_write_b32 a54, %0\n\t"
                   "v_accvgpr_read_b32 %1, a55\n\tv_mul_f32 %1, %2, %1\n\tv_accvgpr_write_b32 a55, %1\n\t"
                   "v_accvgpr_read_b32 %0, a56\n\tv_mul_f32 %0, %2, %0\n\tv_accvgpr_write_b32 a56, %0\n\t"
                   "v_accvgpr_read_b32 %1, a57\n\tv_mul_f32 %1, %2, %1\n\tv_accvgpr_write_b32 a57, %1\n\t"
                   "v_accvgpr_read_b32 %0, a58\n\tv_mul_f32 %0, %2, %0\n\tv_accvgpr_write_b32 a58, %0\n\t"
                   "v_accvgpr_read_b32 %1, a59\n\tv_mul_f32 %1, %2, %1\n\tv_accvgpr_write_b32 a59, %1\n\t"
                   "v_accvgpr_read_b32 %0, a60\n\tv_mul_f32 %0, %2, %0\n\tv_accvgpr_write_b32 a60, %0\n\t"
                   "v_accvgpr_read_b32 %1, a61\n\tv_mul_f32 %1, %2, %1\n\tv_accvgpr_write_b32 a61, %1\n\t"
                   "v_accvgpr_read_b32 %0, a62\n\tv_mul_f32 %0, %2, %0\n\tv_accvgpr_write_b32 a62, %0\n\t"
                   "v_accvgpr_read_b32 %1, a63\n\tv_mul_f32 %1, %2, %1\n\tv_accvgpr_write_b32 a63, %1\n\t"
                   "v_accvgpr_read_b32 %0, a64\n\tv_mul_f32 %0, %2, %0\n\tv_accvgpr_write_b32 a64, %0\n\t"
                   "v_accvgpr_read_b32 %1, a65\n\tv_mul_f32 %1, %2, %1\n\tv_accvgpr_write_b32 a65, %1\n\t"
                   "v_accvgpr_read_b32 %0, a66\n\tv_mul_f32 %0, %2, %0\n\tv_accvgpr_write_b32 a66, %0\n\t"
                   "v_accvgpr_read_b32 %1, a67\n\tv_mul_f32 %1, %2, %1\n\tv_accvgpr_write_b32 a67, %1\n\t"
                   "v_accvgpr_read_b32 %0, a68\n\tv_mul_f32 %0, %2, %0\n\tv_accvgpr_write_b32 a68, %0\n\t"
                   "v_accvgpr_read_b32 %1, a69\n\tv_mul_f32 %1, %2, %1\n\tv_accvgpr_write_b32 a69, %1\n\t"
                   "v_accvgpr_read_b32 %0, a70\n\tv_mul_f32 %0, %2, %0\n\tv_accvgpr_write_b32 a70, %0\n\t"
                   "v_accvgpr_read_b32 %1, a71\n\tv_mul_f32 %1, %2, %1\n\tv_accvgpr_write_b32 a71, %1\n\t"
                   "v_accvgpr_read_b32 %0, a72\n\tv_mul_f32 %0, %2, %0\n\tv_accvgpr_write_b32 a72, %0\n\t"
                   "v_accvgpr_read_b32 %1, a73\n\tv_mul_f32 %1, %2, %1\n\tv_accvgpr_write_b32 a73, %1\n\t"
                   "v_accvgpr_read_b32 %0, a74\n\tv_mul_f32 %0, %2, %0\n\tv_accvgpr_write_b32 a74, %0\n\t"
                   "v_accvgpr_read_b32 %1, a75\n\tv_mul_f32 %1, %2, %1\n\tv_accvgpr_write_b32 a75, %1\n\t"
                   "v_accvgpr_read_b32 %0, a76\n\tv_mul_f32 %0, %2, %0\n\tv_accvgpr_write_b32 a76, %0\n\t"
                   "v_accvgpr_read_b32 %1, a77\n\tv_mul_f32 %1, %2, %1\n\tv_accvgpr_write_b32 a77, %1\n\t"
                   "v_accvgpr_read_b32 %0, a78\n\tv_mul_f32 %0, %2, %0\n\tv_accvgpr_write_b32 a78, %0\n\t"
                   "v_accvgpr_read_b32 %1, a79\n\tv_mul_f32 %1, %2, %1\n\tv_accvgpr_write_b32 a79, %1\n\t"
                   "v_accvgpr_read_b32 %0, a80\n\tv_mul_f32 %0, %2, %0\n\tv_accvgpr_write_b32 a80, %0\n\t"
                   "v_accvgpr_read_b32 %1, a81\n\tv_mul_f32 %1, %2, %1\n\tv_accvgpr_write_b32 a81, %1\n\t"
                   "v_accvgpr_read_b32 %0, a82\n\tv_mul_f32 %0, %2, %0\n\tv_accvgpr_write_b32 a82, %0\n\t"
                   "v_accvgpr_read_b32 %1, a83\n\tv_mul_f32 %1, %2, %1\n\tv_accvgpr_write_b32 a83, %1\n\t"
                   "v_accvgpr_read_b32 %0, a84\n\tv_mul_f32 %0, %2, %0\n\tv_accvgpr_write_b32 a84, %0\n\t"
                   "v_accvgpr_read_b32 %1, a85\n\tv_mul_f32 %1, %2, %1\n\tv_accvgpr_write_b32 a85, %1\n\t"
                   "v_accvgpr_read_b32 %0, a86\n\tv_mul_f32 %0, %2, %0\n\tv_accvgpr_write_b32 a86, %0\n\t"
                   "v_accvgpr_read_b32 %1, a87\n\tv_mul_f32 %1, %2, %1\n\tv_accvgpr_write_b32 a87, %1\n\t"
                   "v_accvgpr_read_b32 %0, a88\n\tv_mul_f32 %0, %2, %0\n\tv_accvgpr_write_b32 a88, %0\n\t"
                   "v_accvgpr_read_b32 %1, a89\n\tv_mul_f32 %1, %2, %1\n\tv_accvgpr_write_b32 a89, %1\n\t"
                   "v_accvgpr_read_b32 %0, a90\n\tv_mul_f32 %0, %2, %0\n\tv_accvgpr_write_b32 a90, %0\n\t"
                   "v_accvgpr_read_b32 %1, a91\n\tv_mul_f32 %1, %2, %1\n\tv_accvgpr_write_b32 a91, %1\n\t"
                   "v_accvgpr_read_b32 %0, a92\n\tv_mul_f32 %0, %2, %0\n\tv_accvgpr_write_b32 a92, %0\n\t"
                   "v_accvgpr_read_b32 %1, a93\n\tv_mul_f32 %1, %2, %1\n\tv_accvgpr_write_b32 a93, %1\n\t"
                   "v_accvgpr_read_b32 %0, a94\n\tv_mul_f32 %0, %2, %0\n\tv_accvgpr_write_b32 a94, %0\n\t"
                   "v_accvgpr_read_b32 %1, a95\n\tv_mul_f32 %1, %2, %1\n\tv_accvgpr_write_b32 a95, %1\n\t"
                   "v_accvgpr_read_b32 %0, a96\n\tv_mul_f32 %0, %2, %0\n\tv_accvgpr_write_b32 a96, %0\n\t"
                   "v_accvgpr_read_b32 %1, a97\n\tv_mul_f32 %1, %2, %1\n\tv_accvgpr_write_b32 a97, %1\n\t"
                   "v_accvgpr_read_b32 %0, a98\n\tv_mul_f32 %0, %2, %0\n\tv_accvgpr_write_b32 a98, %0\n\t"
                   "v_accvgpr_read_b32 %1, a99\n\tv_mul_f32 %1, %2, %1\n\tv_accvgpr_write_b32 a99, %1\n\t"
                   "v_accvgpr_read_b32 %0, a100\n\tv_mul_f32 %0, %2, %0\n\tv_accvgpr_write_b32 a100, %0\n\t"
                   "v_accvgpr_read_b32 %1, a101\n\tv_mul_f32 %1, %2, %1\n\tv_accvgpr_write_b32 a101, %1\n\t"
                   "v_accvgpr_read_b32 %0, a102\n\tv_mul_f32 %0, %2, %0\n\tv_accvgpr_write_b32 a102, %0\n\t"
                   "v_accvgpr_read_b32 %1, a103\n\tv_mul_f32 %1, %2, %1\n\tv_accvgpr_write_b32 a103, %1\n\t"
                   "v_accvgpr_read_b32 %0, a104\n\tv_mul_f32 %0, %2, %0\n\tv_accvgpr_write_b32 a104, %0\n\t"
                   "v_accvgpr_read_b32 %1, a105\n\tv_mul_f32 %1, %2, %1\n\tv_accvgpr_write_b32 a105, %1\n\t"
                   "v_accvgpr_read_b32 %0, a106\n\tv_mul_f32 %0, %2, %0\n\tv_accvgpr_write_b32 a106, %0\n\t"
                   "v_accvgpr_read_b32 %1, a107\n\tv_mul_f32 %1, %2, %1\n\tv_accvgpr_write_b32 a107, %1\n\t"
                   "v_accvgpr_read_b32 %0, a108\n\tv_mul_f32 %0, %2, %0\n\tv_accvgpr_write_b32 a108, %0\n\t"
                   "v_accvgpr_read_b32 %1, a109\n\tv_mul_f32 %1, %2, %1\n\tv_accvgpr_write_b32 a109, %1\n\t"
                   "v_accvgpr_read_b32 %0, a110\n\tv_mul_f32 %0, %2, %0\n\tv_accvgpr_write_b32 a110, %0\n\t"
                   "v_accvgpr_read_b32 %1, a111\n\tv_mul_f32 %1, %2, %1\n\tv_accvgpr_write_b32 a111, %1\n\t"
                   "v_accvgpr_read_b32 %0, a112\n\tv_mul_f32 %0, %2, %0\n\tv_accvgpr_write_b32 a112, %0\n\t"
                   "v_accvgpr_read_b32 %1, a113\n\tv_mul_f32 %1, %2, %1\n\tv_accvgpr_write_b32 a113, %1\n\t"
                   "v_accvgpr_read_b32 %0, a114\n\tv_mul_f32 %0, %2, %0\n\tv_accvgpr_write_b32 a114, %0\n\t"
                   "v_accvgpr_read_b32 %1, a115\n\tv_mul_f32 %1, %2, %1\n\tv_accvgpr_write_b32 a115, %1\n\t"
                   "v_accvgpr_read_b32 %0, a116\n\tv_mul_f32 %0, %2, %0\n\tv_accvgpr_write_b32 a116, %0\n\t"
                   "v_accvgpr_read_b32 %1, a117\n\tv_mul_f32 %1, %2, %1\n\tv_accvgpr_write_b32 a117, %1\n\t"
                   "v_accvgpr_read_b32 %0, a118\n\tv_mul_f32 %0, %2, %0\n\tv_accvgpr_write_b32 a118, %0\n\t"
                   "v_accvgpr_read_b32 %1, a119\n\tv_mul_f32 %1, %2, %1\n\tv_accvgpr_write_b32 a119, %1\n\t"
                   "v_accvgpr_read_b32 %0, a120\n\tv_mul_f32 %0, %2, %0\n\tv_accvgpr_write_b32 a120, %0\n\t"
                   "v_accvgpr_read_b32 %1, a121\n\tv_mul_f32 %1, %2, %1\n\tv_accvgpr_write_b32 a121, %1\n\t"
                   "v_accvgpr_read_b32 %0, a122\n\tv_mul_f32 %0, %2, %0\n\tv_accvgpr_write_b32 a122, %0\n\t"
                   "v_accvgpr_read_b32 %1, a123\n\tv_mul_f32 %1, %2, %1\n\tv_accvgpr_write_b32 a123, %1\n\t"
                   "v_accvgpr_read_b32 %0, a124\n\tv_mul_f32 %0, %2, %0\n\tv_accvgpr_write_b32 a124, %0\n\t"
                   "v_accvgpr_read_b32 %1, a125\n\tv_mul_f32 %1, %2, %1\n\tv_accvgpr_write_b32 a125, %1\n\t"
                   "v_accvgpr_read_b32 %0, a126\n\tv_mul_f32 %0, %2, %0\n\tv_accvgpr_write_b32 a126, %0\n\t"
                   "v_accvgpr_read_b32 %1, a127\n\tv_mul_f32 %1, %2, %1\n\tv_accvgpr_write_b32 a127, %1\n\t"
                   "v_accvgpr_read_b32 %0, a128\n\tv_mul_f32 %0, %3, %0\n\tv_accvgpr_write_b32 a128, %0\n\t"
                   "v_accvgpr_read_b32 %1, a129\n\tv_mul_f32 %1, %3, %1\n\tv_accvgpr_write_b32 a129, %1\n\t"
                   "v_accvgpr_read_b32 %0, a130\n\tv_mul_f32 %0, %3, %0\n\tv_accvgpr_write_b32 a130, %0\n\t"
                   "v_accvgpr_read_b32 %1, a131\n\tv_mul_f32 %1, %3, %1\n\tv_accvgpr_write_b32 a131, %1\n\t"
                   "v_accvgpr_read_b32 %0, a132\n\tv_mul_f32 %0, %3, %0\n\tv_accvgpr_write_b32 a132, %0\n\t"
                   "v_accvgpr_read_b32 %1, a133\n\tv_mul_f32 %1, %3, %1\n\tv_accvgpr_write_b32 a133, %1\n\t"
                   "v_accvgpr_read_b32 %0, a134\n\tv_mul_f32 %0, %3, %0\n\tv_accvgpr_write_b32 a134, %0\n\t"
                   "v_accvgpr_read_b32 %1, a135\n\tv_mul_f32 %1, %3, %1\n\tv_accvgpr_write_b32 a135, %1\n\t"
                   "v_accvgpr_read_b32 %0, a136\n\tv_mul_f32 %0, %3, %0\n\tv_accvgpr_write_b32 a136, %0\n\t"
                   "v_accvgpr_read_b32 %1, a137\n\tv_mul_f32 %1, %3, %1\n\tv_accvgpr_write_b32 a137, %1\n\t"
                   "v_accvgpr_read_b32 %0, a138\n\tv_mul_f32 %0, %3, %0\n\tv_accvgpr_write_b32 a138, %0\n\t"
                   "v_accvgpr_read_b32 %1, a139\n\tv_mul_f32 %1, %3, %1\n\tv_accvgpr_write_b32 a139, %1\n\t"
                   "v_accvgpr_read_b32 %0, a140\n\tv_mul_f32 %0, %3, %0\n\tv_accvgpr_write_b32 a140, %0\n\t"
                   "v_accvgpr_read_b32 %1, a141\n\tv_mul_f32 %1, %3, %1\n\tv_accvgpr_write_b32 a141, %1\n\t"
                   "v_accvgpr_read_b32 %0, a142\n\tv_mul_f32 %0, %3, %0\n\tv_accvgpr_write_b32 a142, %0\n\t"
                   "v_accvgpr_read_b32 %1, a143\n\tv_mul_f32 %1, %3, %1\n\tv_accvgpr_write_b32 a143, %1\n\t"
                   "v_accvgpr_read_b32 %0, a144\n\tv_mul_f32 %0, %3, %0\n\tv_accvgpr_write_b32 a144, %0\n\t"
                   "v_accvgpr_read_b32 %1, a145\n\tv_mul_f32 %1, %3, %1\n\tv_accvgpr_write_b32 a145, %1\n\t"
                   "v_accvgpr_read_b32 %0, a146\n\tv_mul_f32 %0, %3, %0\n\tv_accvgpr_write_b32 a146, %0\n\t"
                   "v_accvgpr_read_b32 %1, a147\n\tv_mul_f32 %1, %3, %1\n\tv_accvgpr_write_b32 a147, %1\n\t"
                   "v_accvgpr_read_b32 %0, a148\n\tv_mul_f32 %0, %3, %0\n\tv_accvgpr_write_b32 a148, %0\n\t"
                   "v_accvgpr_read_b32 %1, a149\n\tv_mul_f32 %1, %3, %1\n\tv_accvgpr_write_b32 a149, %1\n\t"
                   "v_accvgpr_read_b32 %0, a150\n\tv_mul_f32 %0, %3, %0\n\tv_accvgpr_write_b32 a150, %0\n\t"
                   "v_accvgpr_read_b32 %1, a151\n\tv_mul_f32 %1, %3, %1\n\tv_accvgpr_write_b32 a151, %1\n\t"
                   "v_accvgpr_read_b32 %0, a152\n\tv_mul_f32 %0, %3, %0\n\tv_accvgpr_write_b32 a152, %0\n\t"
                   "v_accvgpr_read_b32 %1, a153\n\tv_mul_f32 %1, %3, %1\n\tv_accvgpr_write_b32 a153, %1\n\t"
                   "v_accvgpr_read_b32 %0, a154\n\tv_mul_f32 %0, %3, %0\n\tv_accvgpr_write_b32 a154, %0\n\t"
                   "v_accvgpr_read_b32 %1, a155\n\tv_mul_f32 %1, %3, %1\n\tv_accvgpr_write_b32 a155, %1\n\t"
                   "v_accvgpr_read_b32 %0, a156\n\tv_mul_f32 %0, %3, %0\n\tv_accvgpr_write_b32 a156, %0\n\t"
                   "v_accvgpr_read_b32 %1, a157\n\tv_mul_f32 %1, %3, %1\n\tv_accvgpr_write_b32 a157, %1\n\t"
                   "v_accvgpr_read_b32 %0, a158\n\tv_mul_f32 %0, %3, %0\n\tv_accvgpr_write_b32 a158, %0\n\t"
                   "v_accvgpr_read_b32 %1, a159\n\tv_mul_f32 %1, %3, %1\n\tv_accvgpr_write_b32 a159, %1\n\t"
                   "v_accvgpr_read_b32 %0, a160\n\tv_mul_f32 %0, %3, %0\n\tv_accvgpr_write_b32 a160, %0\n\t"
                   "v_accvgpr_read_b32 %1, a161\n\tv_mul_f32 %1, %3, %1\n\tv_accvgpr_write_b32 a161, %1\n\t"
                   "v_accvgpr_read_b32 %0, a162\n\tv_mul_f32 %0, %3, %0\n\tv_accvgpr_write_b32 a162, %0\n\t"
                   "v_accvgpr_read_b32 %1, a163\n\tv_mul_f32 %1, %3, %1\n\tv_accvgpr_write_b32 a163, %1\n\t"
                   "v_accvgpr_read_b32 %0, a164\n\tv_mul_f32 %0, %3, %0\n\tv_accvgpr_write_b32 a164, %0\n\t"
                   "v_accvgpr_read_b32 %1, a165\n\tv_mul_f32 %1, %3, %1\n\tv_accvgpr_write_b32 a165, %1\n\t"
                   "v_accvgpr_read_b32 %0, a166\n\tv_mul_f32 %0, %3, %0\n\tv_accvgpr_write_b32 a166, %0\n\t"
                   "v_accvgpr_read_b32 %1, a167\n\tv_mul_f32 %1, %3, %1\n\tv_accvgpr_write_b32 a167, %1\n\t"
                   "v_accvgpr_read_b32 %0, a168\n\tv_mul_f32 %0, %3, %0\n\tv_accvgpr_write_b32 a168, %0\n\t"
                   "v_accvgpr_read_b32 %1, a169\n\tv_mul_f32 %1, %3, %1\n\tv_accvgpr_write_b32 a169, %1\n\t"
                   "v_accvgpr_read_b32 %0, a170\n\tv_mul_f32 %0, %3, %0\n\tv_accvgpr_write_b32 a170, %0\n\t"
                   "v_accvgpr_read_b32 %1, a171\n\tv_mul_f32 %1, %3, %1\n\tv_accvgpr_write_b32 a171, %1\n\t"
                   "v_accvgpr_read_b32 %0, a172\n\tv_mul_f32 %0, %3, %0\n\tv_accvgpr_write_b32 a172, %0\n\t"
                   "v_accvgpr_read_b32 %1, a173\n\tv_mul_f32 %1, %3, %1\n\tv_accvgpr_write_b32 a173, %1\n\t"
                   "v_accvgpr_read_b32 %0, a174\n\tv_mul_f32 %0, %3, %0\n\tv_accvgpr_write_b32 a174, %0\n\t"
                   "v_accvgpr_read_b32 %1, a175\n\tv_mul_f32 %1, %3, %1\n\tv_accvgpr_write_b32 a175, %1\n\t"
                   "v_accvgpr_read_b32 %0, a176\n\tv_mul_f32 %0, %3, %0\n\tv_accvgpr_write_b32 a176, %0\n\t"
                   "v_accvgpr_read_b32 %1, a177\n\tv_mul_f32 %1, %3, %1\n\tv_accvgpr_write_b32 a177, %1\n\t"
                   "v_accvgpr_read_b32 %0, a178\n\tv_mul_f32 %0, %3, %0\n\tv_accvgpr_write_b32 a178, %0\n\t"
                   "v_accvgpr_read_b32 %1, a179\n\tv_mul_f32 %1, %3, %1\n\tv_accvgpr_write_b32 a179, %1\n\t"
                   "v_accvgpr_read_b32 %0, a180\n\tv_mul_f32 %0, %3, %0\n\tv_accvgpr_write_b32 a180, %0\n\t"
                   "v_accvgpr_read_b32 %1, a181\n\tv_mul_f32 %1, %3, %1\n\tv_accvgpr_write_b32 a181, %1\n\t"
                   "v_accvgpr_read_b32 %0, a182\n\tv_mul_f32 %0, %3, %0\n\tv_accvgpr_write_b32 a182, %0\n\t"
                   "v_accvgpr_read_b32 %1, a183\n\tv_mul_f32 %1, %3, %1\n\tv_accvgpr_write_b32 a183, %1\n\t"
                   "v_accvgpr_read_b32 %0, a184\n\tv_mul_f32 %0, %3, %0\n\tv_accvgpr_write_b32 a184, %0\n\t"
                   "v_accvgpr_read_b32 %1, a185\n\tv_mul_f32 %1, %3, %1\n\tv_accvgpr_write_b32 a185, %1\n\t"
                   "v_accvgpr_read_b32 %0, a186\n\tv_mul_f32 %0, %3, %0\n\tv_accvgpr_write_b32 a186, %0\n\t"
                   "v_accvgpr_read_b32 %1, a187\n\tv_mul_f32 %1, %3, %1\n\tv_accvgpr_write_b32 a187, %1\n\t"
                   "v_accvgpr_read_b32 %0, a188\n\tv_mul_f32 %0, %3, %0\n\tv_accvgpr_write_b32 a188, %0\n\t"
                   "v_accvgpr_read_b32 %1, a189\n\tv_mul_f32 %1, %3, %1\n\tv_accvgpr_write_b32 a189, %1\n\t"
                   "v_accvgpr_read_b32 %0, a190\n\tv_mul_f32 %0, %3, %0\n\tv_accvgpr_write_b32 a190, %0\n\t"
                   "v_accvgpr_read_b32 %1, a191\n\tv_mul_f32 %1, %3, %1\n\tv_accvgpr_write_b32 a191, %1\n\t"
                   "v_accvgpr_read_b32 %0, a192\n\tv_mul_f32 %0, %3, %0\n\tv_accvgpr_write_b32 a192, %0\n\t"
                   "v_accvgpr_read_b32 %1, a193\n\tv_mul_f32 %1, %3, %1\n\tv_accvgpr_write_b32 a193, %1\n\t"
                   "v_accvgpr_read_b32 %0, a194\n\tv_mul_f32 %0, %3, %0\n\tv_accvgpr_write_b32 a194, %0\n\t"
                   "v_accvgpr_read_b32 %1, a195\n\tv_mul_f32 %1, %3, %1\n\tv_accvgpr_write_b32 a195, %1\n\t"
                   "v_accvgpr_read_b32 %0, a196\n\tv_mul_f32 %0, %3, %0\n\tv_accvgpr_write_b32 a196, %0\n\t"
                   "v_accvgpr_read_b32 %1, a197\n\tv_mul_f32 %1, %3, %1\n\tv_accvgpr_write_b32 a197, %1\n\t"
                   "v_accvgpr_read_b32 %0, a198\n\tv_mul_f32 %0, %3, %0\n\tv_accvgpr_write_b32 a198, %0\n\t"
                   "v_accvgpr_read_b32 %1, a199\n\tv_mul_f32 %1, %3, %1\n\tv_accvgpr_write_b32 a199, %1\n\t"
                   "v_accvgpr_read_b32 %0, a200\n\tv_mul_f32 %0, %3, %0\n\tv_accvgpr_write_b32 a200, %0\n\t"
                   "v_accvgpr_read_b32 %1, a201\n\tv_mul_f32 %1, %3, %1\n\tv_accvgpr_write_b32 a201, %1\n\t"
                   "v_accvgpr_read_b32 %0, a202\n\tv_mul_f32 %0, %3, %0\n\tv_accvgpr_write_b32 a202, %0\n\t"
                   "v_accvgpr_read_b32 %1, a203\n\tv_mul_f32 %1, %3, %1\n\tv_accvgpr_write_b32 a203, %1\n\t"
                   "v_accvgpr_read_b32 %0, a204\n\tv_mul_f32 %0, %3, %0\n\tv_accvgpr_write_b32 a204, %0\n\t"
                   "v_accvgpr_read_b32 %1, a205\n\tv_mul_f32 %1, %3, %1\n\tv_accvgpr_write_b32 a205, %1\n\t"
                   "v_accvgpr_read_b32 %0, a206\n\tv_mul_f32 %0, %3, %0\n\tv_accvgpr_write_b32 a206, %0\n\t"
                   "v_accvgpr_read_b32 %1, a207\n\tv_mul_f32 %1, %3, %1\n\tv_accvgpr_write_b32 a207, %1\n\t"
                   "v_accvgpr_read_b32 %0, a208\n\tv_mul_f32 %0, %3, %0\n\tv_accvgpr_write_b32 a208, %0\n\t"
                   "v_accvgpr_read_b32 %1, a209\n\tv_mul_f32 %1, %3, %1\n\tv_accvgpr_write_b32 a209, %1\n\t"
                   "v_accvgpr_read_b32 %0, a210\n\tv_mul_f32 %0, %3, %0\n\tv_accvgpr_write_b32 a210, %0\n\t"
                   "v_accvgpr_read_b32 %1, a211\n\tv_mul_f32 %1, %3, %1\n\tv_accvgpr_write_b32 a211, %1\n\t"
                   "v_accvgpr_read_b32 %0, a212\n\tv_mul_f32 %0, %3, %0\n\tv_accvgpr_write_b32 a212, %0\n\t"
                   "v_accvgpr_read_b32 %1, a213\n\tv_mul_f32 %1, %3, %1\n\tv_accvgpr_write_b32 a213, %1\n\t"
                   "v_accvgpr_read_b32 %0, a214\n\tv_mul_f32 %0, %3, %0\n\tv_accvgpr_write_b32 a214, %0\n\t"
                   "v_accvgpr_read_b32 %1, a215\n\tv_mul_f32 %1, %3, %1\n\tv_accvgpr_write_b32 a215, %1\n\t"
                   "v_accvgpr_read_b32 %0, a216\n\tv_mul_f32 %0, %3, %0\n\tv_accvgpr_write_b32 a216, %0\n\t"
                   "v_accvgpr_read_b32 %1, a217\n\tv_mul_f32 %1, %3, %1\n\tv_accvgpr_write_b32 a217, %1\n\t"
                   "v_accvgpr_read_b32 %0, a218\n\tv_mul_f32 %0, %3, %0\n\tv_accvgpr_write_b32 a218, %0\n\t"
                   "v_accvgpr_read_b32 %1, a219\n\tv_mul_f32 %1, %3, %1\n\tv_accvgpr_write_b32 a219, %1\n\t"
                   "v_accvgpr_read_b32 %0, a220\n\tv_mul_f32 %0, %3, %0\n\tv_accvgpr_write_b32 a220, %0\n\t"
                   "v_accvgpr_read_b32 %1, a221\n\tv_mul_f32 %1, %3, %1\n\tv_accvgpr_write_b32 a221, %1\n\t"
                   "v_accvgpr_read_b32 %0, a222\n\tv_mul_f32 %0, %3, %0\n\tv_accvgpr_write_b32 a222, %0\n\t"
                   "v_accvgpr_read_b32 %1, a223\n\tv_mul_f32 %1, %3, %1\n\tv_accvgpr_write_b32 a223, %1\n\t"
                   "v_accvgpr_read_b32 %0, a224\n\tv_mul_f32 %0, %3, %0\n\tv_accvgpr_write_b32 a224, %0\n\t"
                   "v_accvgpr_read_b32 %1, a225\n\tv_mul_f32 %1, %3, %1\n\tv_accvgpr_write_b32 a225, %1\n\t"
                   "v_accvgpr_read_b32 %0, a226\n\tv_mul_f32 %0, %3, %0\n\tv_accvgpr_write_b32 a226, %0\n\t"
                   "v_accvgpr_read_b32 %1, a227\n\tv_mul_f32 %1, %3, %1\n\tv_accvgpr_write_b32 a227, %1\n\t"
                   "v_accvgpr_read_b32 %0, a228\n\tv_mul_f32 %0, %3, %0\n\tv_accvgpr_write_b32 a228, %0\n\t"
                   "v_accvgpr_read_b32 %1, a229\n\tv_mul_f32 %1, %3, %1\n\tv_accvgpr_write_b32 a229, %1\n\t"
                   "v_accvgpr_read_b32 %0, a230\n\tv_mul_f32 %0, %3, %0\n\tv_accvgpr_write_b32 a230, %0\n\t"
                   "v_accvgpr_read_b32 %1, a231\n\tv_mul_f32 %1, %3, %1\n\tv_accvgpr_write_b32 a231, %1\n\t"
                   "v_accvgpr_read_b32 %0, a232\n\tv_mul_f32 %0, %3, %0\n\tv_accvgpr_write_b32 a232, %0\n\t"
                   "v_accvgpr_read_b32 %1, a233\n\tv_mul_f32 %1, %3, %1\n\tv_accvgpr_write_b32 a233, %1\n\t"
                   "v_accvgpr_read_b32 %0, a234\n\tv_mul_f32 %0, %3, %0\n\tv_accvgpr_write_b32 a234, %0\n\t"
                   "v_accvgpr_read_b32 %1, a235\n\tv_mul_f32 %1, %3, %1\n\tv_accvgpr_write_b32 a235, %1\n\t"
                   "v_accvgpr_read_b32 %0, a236\n\tv_mul_f32 %0, %3, %0\n\tv_accvgpr_write_b32 a236, %0\n\t"
                   "v_accvgpr_read_b32 %1, a237\n\tv_mul_f32 %1, %3, %1\n\tv_accvgpr_write_b32 a237, %1\n\t"
                   "v_accvgpr_read_b32 %0, a238\n\tv_mul_f32 %0, %3, %0\n\tv_accvgpr_write_b32 a238, %0\n\t"
                   "v_accvgpr_read_b32 %1, a239\n\tv_mul_f32 %1, %3, %1\n\tv_accvgpr_write_b32 a239, %1\n\t"
                   "v_accvgpr_read_b32 %0, a240\n\tv_mul_f32 %0, %3, %0\n\tv_accvgpr_write_b32 a240, %0\n\t"
                   "v_accvgpr_read_b32 %1, a241\n\tv_mul_f32 %1, %3, %1\n\tv_accvgpr_write_b32 a241, %1\n\t"
                   "v_accvgpr_read_b32 %0, a242\n\tv_mul_f32 %0, %3, %0\n\tv_accvgpr_write_b32 a242, %0\n\t"
                   "v_accvgpr_read_b32 %1, a243\n\tv_mul_f32 %1, %3, %1\n\tv_accvgpr_write_b32 a243, %1\n\t"
                   "v_accvgpr_read_b32 %0, a244\n\tv_mul_f32 %0, %3, %0\n\tv_accvgpr_write_b32 a244, %0\n\t"
                   "v_accvgpr_read_b32 %1, a245\n\tv_mul_f32 %1, %3, %1\n\tv_accvgpr_write_b32 a245, %1\n\t"
                   "v_accvgpr_read_b32 %0, a246\n\tv_mul_f32 %0, %3, %0\n\tv_accvgpr_write_b32 a246, %0\n\t"
                   "v_accvgpr_read_b32 %1, a247\n\tv_mul_f32 %1, %3, %1\n\tv_accvgpr_write_b32 a247, %1\n\t"
                   "v_accvgpr_read_b32 %0, a248\n\tv_mul_f32 %0, %3, %0\n\tv_accvgpr_write_b32 a248, %0\n\t"
                   "v_accvgpr_read_b32 %1, a249\n\tv_mul_f32 %1, %3, %1\n\tv_accvgpr_write_b32 a249, %1\n\t"
                   "v_accvgpr_read_b32 %0, a250\n\tv_mul_f32 %0, %3, %0\n\tv_accvgpr_write_b32 a250, %0\n\t"
                   "v_accvgpr_read_b32 %1, a251\n\tv_mul_f32 %1, %3, %1\n\tv_accvgpr_write_b32 a251, %1\n\t"
                   "v_accvgpr_read_b32 %0, a252\n\tv_mul_f32 %0, %3, %0\n\tv_accvgpr_write_b32 a252, %0\n\t"
                   "v_accvgpr_read_b32 %1, a253\n\tv_mul_f32 %1, %3, %1\n\tv_accvgpr_write_b32 a253, %1\n\t"
                   "v_accvgpr_read_b32 %0, a254\n\tv_mul_f32 %0, %3, %0\n\tv_accvgpr_write_b32 a254, %0\n\t"
                   "v_accvgpr_read_b32 %1, a255\n\tv_mul_f32 %1, %3, %1\n\tv_accvgpr_write_b32 a255, %1\n\t"
                   "s_nop 1"
                   : "=&v"(t0), "=&v"(t1) : "v"(c.fct[0]), "v"(c.fct[1]) : ACLOB);
      c.pend = false;
    }
  }
  asm volatile("s_nop 1" ::: "memory");
  GAP();
}
template <int T, bool HASPV, bool HASSM> DI void phaseB(Ctx4& c) {
  phaseB_<T, HASPV, HASSM>(c, std::make_integer_sequence<int, 32>{});
}

template <int A, int OFF> DI void acc_dswrite4(unsigned addr) {
  asm volatile("ds_write_b128 %0, a[%c1:%c2] offset:%c3" ::"v"(addr), "i"(A), "i"(A + 3), "i"(OFF) : "memory", ACLOB);
}
template <int A> DI f32x4 acc_read4() {
  f32x4 r;
  asm volatile("v_accvgpr_read_b32 %0, a[%c4]\n\tv_accvgpr_read_b32 %1, a[%c5]\n\tv_accvgpr_read_b32 %2, a[%c6]\n\t"
               "v_accvgpr_read_b32 %3, a[%c7]"
               : "=v"(r[0]), "=v"(r[1]), "=v"(r[2]), "=v"(r[3]) : "i"(A), "i"(A + 1), "i"(A + 2), "i"(A + 3) : ACLOB);
  return r;
}

__global__ __launch_bounds__(256, 1) void attn4_kernel(const u16* __restrict__ x, const u16* __restrict__ wq,
                                                       const u16* __restrict__ kc, const u16* __restrict__ vc,
                                                       const u16* __restrict__ wo, u16* __restrict__ o,
                                                       unsigned* __restrict__ cnt DBGARG) {
  extern __shared__ __attribute__((aligned(16))) char lds[];
  STAMP(0);
  if (blockIdx.x == 0 && threadIdx.x < 128) cnt[threadIdx.x] = 0u;
  const int tid = threadIdx.x, lane = tid & 63;
  const int w = __builtin_amdgcn_readfirstlane(tid >> 6);
  const int r = lane & 31, h = lane >> 5;
  const int bid = blockIdx.x;
  const int xcd = bid & 7, j = bid >> 3;
  const int grp = (j >> 2) * 8 + xcd, sub = j & 3;
  const int b = grp >> 3, kvh = grp & 7;
  const int head = kvh * 2 + (sub >> 1), rhalf = sub & 1;
  const int row0 = b * SQ + rhalf * 56;

  const u16* Kw = kc + ((size_t)(b * HKV + kvh) * SK + 256 * w) * DH;
  const u16* Vw = vc + ((size_t)(b * HKV + kvh) * SK + 256 * w) * DH;

  Ctx4 c;
  c.pfd = 0; c.sub = sub;
  c.Kp = Kw; c.Vp = Vw; c.pfoff = lane * 128;
  c.ring = lds + w * 40960;
  c.rk = __builtin_amdgcn_make_buffer_rsrc((void*)Kw, 0, 256 * DH * 2, 0x00020000);
  c.rv = __builtin_amdgcn_make_buffer_rsrc((void*)Vw, 0, 256 * DH * 2, 0x00020000);
#pragma unroll
  for (int pi = 0; pi < 4; ++pi) {
    const int krow = 4 * pi + (lane >> 4);
    c.koff[pi] = krow * 512 + (((lane & 15) ^ (krow & 15)) * 16);
  }
#pragma unroll
  for (int pi = 0; pi < 2; ++pi) {
    const int vrow = 2 * pi + (lane >> 5);
    c.voff[pi] = vrow * 512 + (((lane & 31) ^ ((vrow & 3) << 2)) * 16);
  }
  {
    __amdgpu_buffer_rsrc_t rx =
        __builtin_amdgcn_make_buffer_rsrc((void*)(x + (size_t)row0 * DM), 0, 56 * DM * 2, 0x00020000);
#pragma unroll
    for (int p = 0; p < 14; ++p) {
      const int ci = 64 * p + lane;
      const int lr = ci / 56, cp = ci - lr * 56;
      int gr = 16 * w + lr; gr = gr > 55 ? 55 : gr;
      const int cs = (cp & ~7) | ((cp ^ (lr >> 1)) & 7);
      __builtin_amdgcn_raw_ptr_buffer_load_lds(rx, (lds_vp)(c.ring + 3 * 8192 + p * 1024), 16, gr * (DM * 2) + cs * 16,
                                               0, 0, 0);
    }
  }
  {
    const int colw = head * DH + 64 * w;
    __amdgpu_buffer_rsrc_t rw =
        __builtin_amdgcn_make_buffer_rsrc((void*)(wq + colw), 0, (DM * NQ - colw) * 2, 0x00020000);
    const int drow = lane >> 3;
    const int wvoff = drow * (NQ * 2) + (((lane & 7) ^ (((drow >> 1) & 1) << 2)) * 16);
    auto wissue = [&](int ch) {
#pragma unroll
      for (int p = 0; p < 8; ++p)
        __builtin_amdgcn_raw_ptr_buffer_load_lds(rw, (lds_vp)(c.ring + ((ch + 2) % 3) * 8192 + p * 1024), 16, wvoff,
                                                 (64 * ch + 8 * p) * (NQ * 2), 0, 0);
    };
    wissue(0); wissue(1); wissue(2);
    const int g = lane >> 4, qq = (lane >> 2) & 3, pp = lane & 3;
    const unsigned trb = (unsigned)(size_t)c.ring + (8 * (g >> 1) + qq) * 128 + ((32 * (g & 1) + 8 * pp) ^ ((qq >> 1) << 6));
    const char* xa[2][4];
#pragma unroll
    for (int qb = 0; qb < 2; ++qb) {
      const int R = 32 * qb + r, lr = R & 15;
#pragma unroll
      for (int v = 0; v < 4; ++v)
        xa[qb][v] = lds + (R >> 4) * 40960 + 3 * 8192 + lr * 896 + ((((2 * v + h) ^ (lr >> 1)) & 7) * 16);
    }
    f32x16 qa[2][2];
#pragma unroll
    for (int a2 = 0; a2 < 2; ++a2)
#pragma unroll
      for (int b2 = 0; b2 < 2; ++b2)
#pragma unroll
        for (int i = 0; i < 16; ++i) qa[a2][b2][i] = 0.f;
    s16x4 fa[2][4];
    bf16x8 fx[2][2];
    auto rdstep = [&](int i, int bsel) {
      const int ch = i >> 2, s4 = i & 3;
      const unsigned tb = trb + ((ch + 2) % 3) * 8192 + s4 * 2048;
      fa[bsel][0] = trread(tb);
      fa[bsel][1] = trread(tb + 512);
      fa[bsel][2] = trread(tb ^ 64);
      fa[bsel][3] = trread((tb + 512) ^ 64);
      asm volatile("ds_read_b128 %0, %1" : "=v"(fx[bsel][0]) : "v"((unsigned)(size_t)(xa[0][s4] + ch * 128)) : "memory");
      asm volatile("ds_read_b128 %0, %1" : "=v"(fx[bsel][1]) : "v"((unsigned)(size_t)(xa[1][s4] + ch * 128)) : "memory");
    };
    auto refill_piece = [&](int k, int p) {
      if (k <= 3) {
        __builtin_amdgcn_raw_ptr_buffer_load_lds(rw, (lds_vp)(c.ring + ((k + 5) % 3) * 8192 + p * 1024), 16, wvoff,
                                                 (64 * (k + 3) + 8 * p) * (NQ * 2), 0, 0);
      }
    };
    WAITV(16);
    __builtin_amdgcn_s_barrier();
    rdstep(0, 0);
#pragma unroll
    for (int i = 0; i < 28; ++i) {
      const int bs = i & 1, ch = i >> 2, s4 = i & 3;
      if (i + 1 < 28) {
        if (s4 == 3) { if (ch == 0) WAITV(8); else WAITV(6); }
        rdstep(i + 1, bs ^ 1);
        asm volatile("s_waitcnt lgkmcnt(6)" : "+v"(fa[bs][0]), "+v"(fa[bs][1]), "+v"(fa[bs][2]), "+v"(fa[bs][3]), "+v"(fx[bs][0]), "+v"(fx[bs][1])::"memory");
      } else {
        asm volatile("s_waitcnt lgkmcnt(0)" : "+v"(fa[bs][0]), "+v"(fa[bs][1]), "+v"(fa[bs][2]), "+v"(fa[bs][3]), "+v"(fx[bs][0]), "+v"(fx[bs][1])::"memory");
      }
      bf16x8 A0 = cat8(fa[bs][0], fa[bs][1]), A1 = cat8(fa[bs][2], fa[bs][3]);
      qa[0][0] = __builtin_amdgcn_mfma_f32_32x32x16_bf16(A0, fx[bs][0], qa[0][0], 0, 0, 0);
      GAP();
      if (ch == 5 && s4 == 0) { prefetch_unit(c, 6 + sub); prefetch_unit(c, 10 + sub); }
      if (ch >= 1) {
        if (ch - 1 <= 3) refill_piece(ch - 1, 2 * s4);
        else if (ch == 5) { if (s4 == 0) dma_piece<0, 0>(c); else if (s4 == 1) dma_piece<0, 2>(c); else if (s4 == 2) dma_piece<0, 4>(c); else dma_piece<0, 6>(c); }
        else { if (s4 == 0) dma_piece<1, 0>(c); else if (s4 == 1) dma_piece<1, 2>(c); else if (s4 == 2) dma_piece<1, 4>(c); else dma_piece<1, 6>(c); }
      }
      GAP();
      qa[0][1] = __builtin_amdgcn_mfma_f32_32x32x16_bf16(A1, fx[bs][0], qa[0][1], 0, 0, 0);
      qa[1][0] = __builtin_amdgcn_mfma_f32_32x32x16_bf16(A0, fx[bs][1], qa[1][0], 0, 0, 0);
      GAP();
      if (ch >= 1) {
        if (ch - 1 <= 3) refill_piece(ch - 1, 2 * s4 + 1);
        else if (ch == 5) { if (s4 == 0) dma_piece<0, 1>(c); else if (s4 == 1) dma_piece<0, 3>(c); else if (s4 == 2) dma_piece<0, 5>(c); else dma_piece<0, 7>(c); }
        else { if (s4 == 0) dma_piece<1, 1>(c); else if (s4 == 1) dma_piece<1, 3>(c); else if (s4 == 2) dma_piece<1, 5>(c); else dma_piece<1, 7>(c); }
      }
      GAP();
      qa[1][1] = __builtin_amdgcn_mfma_f32_32x32x16_bf16(A1, fx[bs][1], qa[1][1], 0, 0, 0);
    }
    dma_unit<2>(c);
    STAMP(12);
    __builtin_amdgcn_s_barrier();
#pragma unroll
    for (int qb = 0; qb < 2; ++qb) {
      const int R = 32 * qb + r;
      char* base = lds + (R >> 4) * 40960 + 4 * 8192 + (R & 15) * 512 + 8 * h;
#pragma unroll
      for (int cb = 0; cb < 2; ++cb)
#pragma unroll
        for (int gg = 0; gg < 4; ++gg) {
          u32x2 v = {cvtpk(qa[qb][cb][4 * gg], qa[qb][cb][4 * gg + 1]), cvtpk(qa[qb][cb][4 * gg + 2], qa[qb][cb][4 * gg + 3])};
          *(u32x2*)(base + (((8 * w + 4 * cb + gg) ^ (R & 15)) * 16)) = v;
        }
    }
    WAITL0();
    __builtin_amdgcn_s_barrier();
  }
  {
#pragma unroll
    for (int qb = 0; qb < 2; ++qb) {
      const int R = 32 * qb + r;
      const char* base = lds + (R >> 4) * 40960 + 4 * 8192 + (R & 15) * 512;
#pragma unroll
      for (int s = 0; s < 16; ++s) c.qf[qb][s] = *(const bf16x8*)(base + (((2 * s + h) ^ (R & 15)) * 16));
    }
#pragma unroll
    for (int qb = 0; qb < 2; ++qb)
#pragma unroll
      for (int s = 0; s < 16; ++s) asm volatile("" : "+v"(c.qf[qb][s]));
  }
  WAITL0();
  __builtin_amdgcn_s_barrier();
  dma_unit<3>(c);
  dma_unit<4>(c);
  STAMP(1);

  {
    const unsigned rb = (unsigned)(size_t)c.ring;
#pragma unroll
    for (int ss = 0; ss < 8; ++ss) c.kaddr[ss] = rb + r * 256 + ((((2 * ss + h) ^ (r & 15))) * 16);
    const int g = lane >> 4, qq = (lane >> 2) & 3, pp = lane & 3;
#pragma unroll
    for (int d = 0; d < 4; ++d)
      c.vaddr[d] = rb + (4 * (g >> 1) + qq) * 512 + ((64 * d + 32 * (g & 1) + 8 * pp) ^ (qq << 6));
  }
  c.pend = false;
  c.fct[0] = 1.f; c.fct[1] = 1.f;

  phaseA<0>(c); phaseB<0, false, true>(c); STAMP(2);
  phaseA<1>(c); phaseB<1, true, true>(c); STAMP(3);
  phaseA<2>(c); phaseB<2, true, true>(c); STAMP(4);
  phaseA<3>(c); STAMP(11); phaseB<3, true, true>(c); STAMP(5);
  phaseA<4>(c); phaseB<4, true, true>(c); STAMP(6);
  phaseA<5>(c); phaseB<5, true, true>(c); STAMP(7);
  phaseA<6>(c); phaseB<6, true, true>(c); STAMP(8);
  {
    int li = (bid * 4 + w) * 28 + (lane < 28 ? lane : 27);
    asm volatile("global_load_dword %0, %1, off" : "+v"(c.pfd) : "v"(wo + (size_t)li * 64) : "memory");
  }
  phaseA<7>(c); phaseB<7, true, true>(c); STAMP(9);
  phaseB<8, true, false>(c);
  asm volatile("s_nop 7\n\ts_nop 7" ::: "memory");
  STAMP(13);

  __syncthreads();
  float* ml = (float*)lds;
#pragma unroll
  for (int qb = 0; qb < 2; ++qb) {
    auto rr = __builtin_amdgcn_permlane32_swap(__float_as_uint(c.l[qb]), __float_as_uint(c.l[qb]), false, false);
    const float lt = __uint_as_float(rr[0]) + __uint_as_float(rr[1]);
    if (h == 0) {
      ml[((w * 2 + qb) * 32 + r) * 2 + 0] = c.m[qb];
      ml[((w * 2 + qb) * 32 + r) * 2 + 1] = lt;
    }
  }
  __syncthreads();
  float fw[2][4];
#pragma unroll
  for (int qb = 0; qb < 2; ++qb) {
    float mm[4], ll[4];
#pragma unroll
    for (int ww = 0; ww < 4; ++ww) {
      mm[ww] = ml[((ww * 2 + qb) * 32 + r) * 2 + 0];
      ll[ww] = ml[((ww * 2 + qb) * 32 + r) * 2 + 1];
    }
    const float M = fmaxf(fmaxf(mm[0], mm[1]), fmaxf(mm[2], mm[3]));
    float e[4], L = 0.f;
#pragma unroll
    for (int ww = 0; ww < 4; ++ww) { e[ww] = __builtin_amdgcn_exp2f(mm[ww] - M); L += e[ww] * ll[ww]; }
    const float inv = 1.f / L;
#pragma unroll
    for (int ww = 0; ww < 4; ++ww) fw[qb][ww] = e[ww] * inv;
  }
  STAMP(14);
  const unsigned xw = (unsigned)(size_t)lds + 4096 + lane * 16;
  const f32x4* xr = (const f32x4*)(lds + 4096) + lane;
  char* otile = lds + 102400;
  {
    switch (w) {
      case 0: {
        acc_dswrite4<32, 0>(xw + 0);
        acc_dswrite4<36, 1024>(xw + 0);
        acc_dswrite4<40, 2048>(xw + 0);
        acc_dswrite4<44, 3072>(xw + 0);
        acc_dswrite4<160, 4096>(xw + 0);
        acc_dswrite4<164, 5120>(xw + 0);
        acc_dswrite4<168, 6144>(xw + 0);
        acc_dswrite4<172, 7168>(xw + 0);
        acc_dswrite4<64, 8192>(xw + 0);
        acc_dswrite4<68, 9216>(xw + 0);
        acc_dswrite4<72, 10240>(xw + 0);
        acc_dswrite4<76, 11264>(xw + 0);
        acc_dswrite4<192, 12288>(xw + 0);
        acc_dswrite4<196, 13312>(xw + 0);
        acc_dswrite4<200, 14336>(xw + 0);
        acc_dswrite4<204, 15360>(xw + 0);
        acc_dswrite4<96, 16384>(xw + 0);
        acc_dswrite4<100, 17408>(xw + 0);
        acc_dswrite4<104, 18432>(xw + 0);
        acc_dswrite4<108, 19456>(xw + 0);
        acc_dswrite4<224, 20480>(xw + 0);
        acc_dswrite4<228, 21504>(xw + 0);
        acc_dswrite4<232, 22528>(xw + 0);
        acc_dswrite4<236, 23552>(xw + 0);
      } break;
      case 1: {
        acc_dswrite4<0, 24576>(xw + 0);
        acc_dswrite4<4, 25600>(xw + 0);
        acc_dswrite4<8, 26624>(xw + 0);
        acc_dswrite4<12, 27648>(xw + 0);
        acc_dswrite4<128, 28672>(xw + 0);
        acc_dswrite4<132, 29696>(xw + 0);
        acc_dswrite4<136, 30720>(xw + 0);
        acc_dswrite4<140, 31744>(xw + 0);
        acc_dswrite4<64, 32768>(xw + 0);
        acc_dswrite4<68, 33792>(xw + 0);
        acc_dswrite4<72, 34816>(xw + 0);
        acc_dswrite4<76, 35840>(xw + 0);
        acc_dswrite4<192, 36864>(xw + 0);
        acc_dswrite4<196, 37888>(xw + 0);
        acc_dswrite4<200, 38912>(xw + 0);
        acc_dswrite4<204, 39936>(xw + 0);
        acc_dswrite4<96, 40960>(xw + 0);
        acc_dswrite4<100, 41984>(xw + 0);
        acc_dswrite4<104, 43008>(xw + 0);
        acc_dswrite4<108, 44032>(xw + 0);
        acc_dswrite4<224, 45056>(xw + 0);
        acc_dswrite4<228, 46080>(xw + 0);
        acc_dswrite4<232, 47104>(xw + 0);
        acc_dswrite4<236, 48128>(xw + 0);
      } break;
      case 2: {
        acc_dswrite4<0, 49152>(xw + 0);
        acc_dswrite4<4, 50176>(xw + 0);
        acc_dswrite4<8, 51200>(xw + 0);
        acc_dswrite4<12, 52224>(xw + 0);
        acc_dswrite4<128, 53248>(xw + 0);
        acc_dswrite4<132, 54272>(xw + 0);
        acc_dswrite4<136, 55296>(xw + 0);
        acc_dswrite4<140, 56320>(xw + 0);
        acc_dswrite4<32, 57344>(xw + 0);
        acc_dswrite4<36, 58368>(xw + 0);
        acc_dswrite4<40, 59392>(xw + 0);
        acc_dswrite4<44, 60416>(xw + 0);
        acc_dswrite4<160, 61440>(xw + 0);
        acc_dswrite4<164, 62464>(xw + 0);
        acc_dswrite4<168, 63488>(xw + 0);
        acc_dswrite4<172, 64512>(xw + 0);
        acc_dswrite4<96, 0>(xw + 65536);
        acc_dswrite4<100, 1024>(xw + 65536);
        acc_dswrite4<104, 2048>(xw + 65536);
        acc_dswrite4<108, 3072>(xw + 65536);
        acc_dswrite4<224, 4096>(xw + 65536);
        acc_dswrite4<228, 5120>(xw + 65536);
        acc_dswrite4<232, 6144>(xw + 65536);
        acc_dswrite4<236, 7168>(xw + 65536);
      } break;
      case 3: {
        acc_dswrite4<0, 8192>(xw + 65536);
        acc_dswrite4<4, 9216>(xw + 65536);
        acc_dswrite4<8, 10240>(xw + 65536);
        acc_dswrite4<12, 11264>(xw + 65536);
        acc_dswrite4<128, 12288>(xw + 65536);
        acc_dswrite4<132, 13312>(xw + 65536);
        acc_dswrite4<136, 14336>(xw + 65536);
        acc_dswrite4<140, 15360>(xw + 65536);
        acc_dswrite4<32, 16384>(xw + 65536);
        acc_dswrite4<36, 17408>(xw + 65536);
        acc_dswrite4<40, 18432>(xw + 65536);
        acc_dswrite4<44, 19456>(xw + 65536);
        acc_dswrite4<160, 20480>(xw + 65536);
        acc_dswrite4<164, 21504>(xw + 65536);
        acc_dswrite4<168, 22528>(xw + 65536);
        acc_dswrite4<172, 23552>(xw + 65536);
        acc_dswrite4<64, 24576>(xw + 65536);
        acc_dswrite4<68, 25600>(xw + 65536);
        acc_dswrite4<72, 26624>(xw + 65536);
        acc_dswrite4<76, 27648>(xw + 65536);
        acc_dswrite4<192, 28672>(xw + 65536);
        acc_dswrite4<196, 29696>(xw + 65536);
        acc_dswrite4<200, 30720>(xw + 65536);
        acc_dswrite4<204, 31744>(xw + 65536);
      } break;
    }
    WAITL0();
    __builtin_amdgcn_s_barrier();
    switch (w) {
      case 0: {
        { f32x4 sum = acc_read4<0>(); sum *= fw[0][0];
          sum += xr[1536] * fw[0][1];
          sum += xr[3072] * fw[0][2];
          sum += xr[4608] * fw[0][3];
          u32x2 v = {cvtpk(sum[0], sum[1]), cvtpk(sum[2], sum[3])};
          *(u32x2*)(otile + (0 + r) * 512 + (((0) ^ ((0 + r) & 31)) * 16) + 8 * h) = v; }
        { f32x4 sum = acc_read4<4>(); sum *= fw[0][0];
          sum += xr[1600] * fw[0][1];
          sum += xr[3136] * fw[0][2];
          sum += xr[4672] * fw[0][3];
          u32x2 v = {cvtpk(sum[0], sum[1]), cvtpk(sum[2], sum[3])};
          *(u32x2*)(otile + (0 + r) * 512 + (((1) ^ ((0 + r) & 31)) * 16) + 8 * h) = v; }
        { f32x4 sum = acc_read4<8>(); sum *= fw[0][0];
          sum += xr[1664] * fw[0][1];
          sum += xr[3200] * fw[0][2];
          sum += xr[4736] * fw[0][3];
          u32x2 v = {cvtpk(sum[0], sum[1]), cvtpk(sum[2], sum[3])};
          *(u32x2*)(otile + (0 + r) * 512 + (((2) ^ ((0 + r) & 31)) * 16) + 8 * h) = v; }
        { f32x4 sum = acc_read4<12>(); sum *= fw[0][0];
          sum += xr[1728] * fw[0][1];
          sum += xr[3264] * fw[0][2];
          sum += xr[4800] * fw[0][3];
          u32x2 v = {cvtpk(sum[0], sum[1]), cvtpk(sum[2], sum[3])};
          *(u32x2*)(otile + (0 + r) * 512 + (((3) ^ ((0 + r) & 31)) * 16) + 8 * h) = v; }
        { f32x4 sum = acc_read4<128>(); sum *= fw[1][0];
          sum += xr[1792] * fw[1][1];
          sum += xr[3328] * fw[1][2];
          sum += xr[4864] * fw[1][3];
          u32x2 v = {cvtpk(sum[0], sum[1]), cvtpk(sum[2], sum[3])};
          *(u32x2*)(otile + (32 + r) * 512 + (((0) ^ ((32 + r) & 31)) * 16) + 8 * h) = v; }
        { f32x4 sum = acc_read4<132>(); sum *= fw[1][0];
          sum += xr[1856] * fw[1][1];
          sum += xr[3392] * fw[1][2];
          sum += xr[4928] * fw[1][3];
          u32x2 v = {cvtpk(sum[0], sum[1]), cvtpk(sum[2], sum[3])};
          *(u32x2*)(otile + (32 + r) * 512 + (((1) ^ ((32 + r) & 31)) * 16) + 8 * h) = v; }
        { f32x4 sum = acc_read4<136>(); sum *= fw[1][0];
          sum += xr[1920] * fw[1][1];
          sum += xr[3456] * fw[1][2];
          sum += xr[4992] * fw[1][3];
          u32x2 v = {cvtpk(sum[0], sum[1]), cvtpk(sum[2], sum[3])};
          *(u32x2*)(otile + (32 + r) * 512 + (((2) ^ ((32 + r) & 31)) * 16) + 8 * h) = v; }
        { f32x4 sum = acc_read4<140>(); sum *= fw[1][0];
          sum += xr[1984] * fw[1][1];
          sum += xr[3520] * fw[1][2];
          sum += xr[5056] * fw[1][3];
          u32x2 v = {cvtpk(sum[0], sum[1]), cvtpk(sum[2], sum[3])};
          *(u32x2*)(otile + (32 + r) * 512 + (((3) ^ ((32 + r) & 31)) * 16) + 8 * h) = v; }
      } break;
      case 1: {
        { f32x4 sum = acc_read4<32>(); sum *= fw[0][1];
          sum += xr[0] * fw[0][0];
          sum += xr[3584] * fw[0][2];
          sum += xr[5120] * fw[0][3];
          u32x2 v = {cvtpk(sum[0], sum[1]), cvtpk(sum[2], sum[3])};
          *(u32x2*)(otile + (0 + r) * 512 + (((8) ^ ((0 + r) & 31)) * 16) + 8 * h) = v; }
        { f32x4 sum = acc_read4<36>(); sum *= fw[0][1];
          sum += xr[64] * fw[0][0];
          sum += xr[3648] * fw[0][2];
          sum += xr[5184] * fw[0][3];
          u32x2 v = {cvtpk(sum[0], sum[1]), cvtpk(sum[2], sum[3])};
          *(u32x2*)(otile + (0 + r) * 512 + (((9) ^ ((0 + r) & 31)) * 16) + 8 * h) = v; }
        { f32x4 sum = acc_read4<40>(); sum *= fw[0][1];
          sum += xr[128] * fw[0][0];
          sum += xr[3712] * fw[0][2];
          sum += xr[5248] * fw[0][3];
          u32x2 v = {cvtpk(sum[0], sum[1]), cvtpk(sum[2], sum[3])};
          *(u32x2*)(otile + (0 + r) * 512 + (((10) ^ ((0 + r) & 31)) * 16) + 8 * h) = v; }
        { f32x4 sum = acc_read4<44>(); sum *= fw[0][1];
          sum += xr[192] * fw[0][0];
          sum += xr[3776] * fw[0][2];
          sum += xr[5312] * fw[0][3];
          u32x2 v = {cvtpk(sum[0], sum[1]), cvtpk(sum[2], sum[3])};
          *(u32x2*)(otile + (0 + r) * 512 + (((11) ^ ((0 + r) & 31)) * 16) + 8 * h) = v; }
        { f32x4 sum = acc_read4<160>(); sum *= fw[1][1];
          sum += xr[256] * fw[1][0];
          sum += xr[3840] * fw[1][2];
          sum += xr[5376] * fw[1][3];
          u32x2 v = {cvtpk(sum[0], sum[1]), cvtpk(sum[2], sum[3])};
          *(u32x2*)(otile + (32 + r) * 512 + (((8) ^ ((32 + r) & 31)) * 16) + 8 * h) = v; }
        { f32x4 sum = acc_read4<164>(); sum *= fw[1][1];
          sum += xr[320] * fw[1][0];
          sum += xr[3904] * fw[1][2];
          sum += xr[5440] * fw[1][3];
          u32x2 v = {cvtpk(sum[0], sum[1]), cvtpk(sum[2], sum[3])};
          *(u32x2*)(otile + (32 + r) * 512 + (((9) ^ ((32 + r) & 31)) * 16) + 8 * h) = v; }
        { f32x4 sum = acc_read4<168>(); sum *= fw[1][1];
          sum += xr[384] * fw[1][0];
          sum += xr[3968] * fw[1][2];
          sum += xr[5504] * fw[1][3];
          u32x2 v = {cvtpk(sum[0], sum[1]), cvtpk(sum[2], sum[3])};
          *(u32x2*)(otile + (32 + r) * 512 + (((10) ^ ((32 + r) & 31)) * 16) + 8 * h) = v; }
        { f32x4 sum = acc_read4<172>(); sum *= fw[1][1];
          sum += xr[448] * fw[1][0];
          sum += xr[4032] * fw[1][2];
          sum += xr[5568] * fw[1][3];
          u32x2 v = {cvtpk(sum[0], sum[1]), cvtpk(sum[2], sum[3])};
          *(u32x2*)(otile + (32 + r) * 512 + (((11) ^ ((32 + r) & 31)) * 16) + 8 * h) = v; }
      } break;
      case 2: {
        { f32x4 sum = acc_read4<64>(); sum *= fw[0][2];
          sum += xr[512] * fw[0][0];
          sum += xr[2048] * fw[0][1];
          sum += xr[5632] * fw[0][3];
          u32x2 v = {cvtpk(sum[0], sum[1]), cvtpk(sum[2], sum[3])};
          *(u32x2*)(otile + (0 + r) * 512 + (((16) ^ ((0 + r) & 31)) * 16) + 8 * h) = v; }
        { f32x4 sum = acc_read4<68>(); sum *= fw[0][2];
          sum += xr[576] * fw[0][0];
          sum += xr[2112] * fw[0][1];
          sum += xr[5696] * fw[0][3];
          u32x2 v = {cvtpk(sum[0], sum[1]), cvtpk(sum[2], sum[3])};
          *(u32x2*)(otile + (0 + r) * 512 + (((17) ^ ((0 + r) & 31)) * 16) + 8 * h) = v; }
        { f32x4 sum = acc_read4<72>(); sum *= fw[0][2];
          sum += xr[640] * fw[0][0];
          sum += xr[2176] * fw[0][1];
          sum += xr[5760] * fw[0][3];
          u32x2 v = {cvtpk(sum[0], sum[1]), cvtpk(sum[2], sum[3])};
          *(u32x2*)(otile + (0 + r) * 512 + (((18) ^ ((0 + r) & 31)) * 16) + 8 * h) = v; }
        { f32x4 sum = acc_read4<76>(); sum *= fw[0][2];
          sum += xr[704] * fw[0][0];
          sum += xr[2240] * fw[0][1];
          sum += xr[5824] * fw[0][3];
          u32x2 v = {cvtpk(sum[0], sum[1]), cvtpk(sum[2], sum[3])};
          *(u32x2*)(otile + (0 + r) * 512 + (((19) ^ ((0 + r) & 31)) * 16) + 8 * h) = v; }
        { f32x4 sum = acc_read4<192>(); sum *= fw[1][2];
          sum += xr[768] * fw[1][0];
          sum += xr[2304] * fw[1][1];
          sum += xr[5888] * fw[1][3];
          u32x2 v = {cvtpk(sum[0], sum[1]), cvtpk(sum[2], sum[3])};
          *(u32x2*)(otile + (32 + r) * 512 + (((16) ^ ((32 + r) & 31)) * 16) + 8 * h) = v; }
        { f32x4 sum = acc_read4<196>(); sum *= fw[1][2];
          sum += xr[832] * fw[1][0];
          sum += xr[2368] * fw[1][1];
          sum += xr[5952] * fw[1][3];
          u32x2 v = {cvtpk(sum[0], sum[1]), cvtpk(sum[2], sum[3])};
          *(u32x2*)(otile + (32 + r) * 512 + (((17) ^ ((32 + r) & 31)) * 16) + 8 * h) = v; }
        { f32x4 sum = acc_read4<200>(); sum *= fw[1][2];
          sum += xr[896] * fw[1][0];
          sum += xr[2432] * fw[1][1];
          sum += xr[6016] * fw[1][3];
          u32x2 v = {cvtpk(sum[0], sum[1]), cvtpk(sum[2], sum[3])};
          *(u32x2*)(otile + (32 + r) * 512 + (((18) ^ ((32 + r) & 31)) * 16) + 8 * h) = v; }
        { f32x4 sum = acc_read4<204>(); sum *= fw[1][2];
          sum += xr[960] * fw[1][0];
          sum += xr[2496] * fw[1][1];
          sum += xr[6080] * fw[1][3];
          u32x2 v = {cvtpk(sum[0], sum[1]), cvtpk(sum[2], sum[3])};
          *(u32x2*)(otile + (32 + r) * 512 + (((19) ^ ((32 + r) & 31)) * 16) + 8 * h) = v; }
      } break;
      case 3: {
        { f32x4 sum = acc_read4<96>(); sum *= fw[0][3];
          sum += xr[1024] * fw[0][0];
          sum += xr[2560] * fw[0][1];
          sum += xr[4096] * fw[0][2];
          u32x2 v = {cvtpk(sum[0], sum[1]), cvtpk(sum[2], sum[3])};
          *(u32x2*)(otile + (0 + r) * 512 + (((24) ^ ((0 + r) & 31)) * 16) + 8 * h) = v; }
        { f32x4 sum = acc_read4<100>(); sum *= fw[0][3];
          sum += xr[1088] * fw[0][0];
          sum += xr[2624] * fw[0][1];
          sum += xr[4160] * fw[0][2];
          u32x2 v = {cvtpk(sum[0], sum[1]), cvtpk(sum[2], sum[3])};
          *(u32x2*)(otile + (0 + r) * 512 + (((25) ^ ((0 + r) & 31)) * 16) + 8 * h) = v; }
        { f32x4 sum = acc_read4<104>(); sum *= fw[0][3];
          sum += xr[1152] * fw[0][0];
          sum += xr[2688] * fw[0][1];
          sum += xr[4224] * fw[0][2];
          u32x2 v = {cvtpk(sum[0], sum[1]), cvtpk(sum[2], sum[3])};
          *(u32x2*)(otile + (0 + r) * 512 + (((26) ^ ((0 + r) & 31)) * 16) + 8 * h) = v; }
        { f32x4 sum = acc_read4<108>(); sum *= fw[0][3];
          sum += xr[1216] * fw[0][0];
          sum += xr[2752] * fw[0][1];
          sum += xr[4288] * fw[0][2];
          u32x2 v = {cvtpk(sum[0], sum[1]), cvtpk(sum[2], sum[3])};
          *(u32x2*)(otile + (0 + r) * 512 + (((27) ^ ((0 + r) & 31)) * 16) + 8 * h) = v; }
        { f32x4 sum = acc_read4<224>(); sum *= fw[1][3];
          sum += xr[1280] * fw[1][0];
          sum += xr[2816] * fw[1][1];
          sum += xr[4352] * fw[1][2];
          u32x2 v = {cvtpk(sum[0], sum[1]), cvtpk(sum[2], sum[3])};
          *(u32x2*)(otile + (32 + r) * 512 + (((24) ^ ((32 + r) & 31)) * 16) + 8 * h) = v; }
        { f32x4 sum = acc_read4<228>(); sum *= fw[1][3];
          sum += xr[1344] * fw[1][0];
          sum += xr[2880] * fw[1][1];
          sum += xr[4416] * fw[1][2];
          u32x2 v = {cvtpk(sum[0], sum[1]), cvtpk(sum[2], sum[3])};
          *(u32x2*)(otile + (32 + r) * 512 + (((25) ^ ((32 + r) & 31)) * 16) + 8 * h) = v; }
        { f32x4 sum = acc_read4<232>(); sum *= fw[1][3];
          sum += xr[1408] * fw[1][0];
          sum += xr[2944] * fw[1][1];
          sum += xr[4480] * fw[1][2];
          u32x2 v = {cvtpk(sum[0], sum[1]), cvtpk(sum[2], sum[3])};
          *(u32x2*)(otile + (32 + r) * 512 + (((26) ^ ((32 + r) & 31)) * 16) + 8 * h) = v; }
        { f32x4 sum = acc_read4<236>(); sum *= fw[1][3];
          sum += xr[1472] * fw[1][0];
          sum += xr[3008] * fw[1][1];
          sum += xr[4544] * fw[1][2];
          u32x2 v = {cvtpk(sum[0], sum[1]), cvtpk(sum[2], sum[3])};
          *(u32x2*)(otile + (32 + r) * 512 + (((27) ^ ((32 + r) & 31)) * 16) + 8 * h) = v; }
      } break;
    }
    WAITL0();
    __builtin_amdgcn_s_barrier();
  }
  {
    switch (w) {
      case 0: {
        acc_dswrite4<48, 0>(xw + 0);
        acc_dswrite4<52, 1024>(xw + 0);
        acc_dswrite4<56, 2048>(xw + 0);
        acc_dswrite4<60, 3072>(xw + 0);
        acc_dswrite4<176, 4096>(xw + 0);
        acc_dswrite4<180, 5120>(xw + 0);
        acc_dswrite4<184, 6144>(xw + 0);
        acc_dswrite4<188, 7168>(xw + 0);
        acc_dswrite4<80, 8192>(xw + 0);
        acc_dswrite4<84, 9216>(xw + 0);
        acc_dswrite4<88, 10240>(xw + 0);
        acc_dswrite4<92, 11264>(xw + 0);
        acc_dswrite4<208, 12288>(xw + 0);
        acc_dswrite4<212, 13312>(xw + 0);
        acc_dswrite4<216, 14336>(xw + 0);
        acc_dswrite4<220, 15360>(xw + 0);
        acc_dswrite4<112, 16384>(xw + 0);
        acc_dswrite4<116, 17408>(xw + 0);
        acc_dswrite4<120, 18432>(xw + 0);
        acc_dswrite4<124, 19456>(xw + 0);
        acc_dswrite4<240, 20480>(xw + 0);
        acc_dswrite4<244, 21504>(xw + 0);
        acc_dswrite4<248, 22528>(xw + 0);
        acc_dswrite4<252, 23552>(xw + 0);
      } break;
      case 1: {
        acc_dswrite4<16, 24576>(xw + 0);
        acc_dswrite4<20, 25600>(xw + 0);
        acc_dswrite4<24, 26624>(xw + 0);
        acc_dswrite4<28, 27648>(xw + 0);
        acc_dswrite4<144, 28672>(xw + 0);
        acc_dswrite4<148, 29696>(xw + 0);
        acc_dswrite4<152, 30720>(xw + 0);
        acc_dswrite4<156, 31744>(xw + 0);
        acc_dswrite4<80, 32768>(xw + 0);
        acc_dswrite4<84, 33792>(xw + 0);
        acc_dswrite4<88, 34816>(xw + 0);
        acc_dswrite4<92, 35840>(xw + 0);
        acc_dswrite4<208, 36864>(xw + 0);
        acc_dswrite4<212, 37888>(xw + 0);
        acc_dswrite4<216, 38912>(xw + 0);
        acc_dswrite4<220, 39936>(xw + 0);
        acc_dswrite4<112, 40960>(xw + 0);
        acc_dswrite4<116, 41984>(xw + 0);
        acc_dswrite4<120, 43008>(xw + 0);
        acc_dswrite4<124, 44032>(xw + 0);
        acc_dswrite4<240, 45056>(xw + 0);
        acc_dswrite4<244, 46080>(xw + 0);
        acc_dswrite4<248, 47104>(xw + 0);
        acc_dswrite4<252, 48128>(xw + 0);
      } break;
      case 2: {
        acc_dswrite4<16, 49152>(xw + 0);
        acc_dswrite4<20, 50176>(xw + 0);
        acc_dswrite4<24, 51200>(xw + 0);
        acc_dswrite4<28, 52224>(xw + 0);
        acc_dswrite4<144, 53248>(xw + 0);
        acc_dswrite4<148, 54272>(xw + 0);
        acc_dswrite4<152, 55296>(xw + 0);
        acc_dswrite4<156, 56320>(xw + 0);
        acc_dswrite4<48, 57344>(xw + 0);
        acc_dswrite4<52, 58368>(xw + 0);
        acc_dswrite4<56, 59392>(xw + 0);
        acc_dswrite4<60, 60416>(xw + 0);
        acc_dswrite4<176, 61440>(xw + 0);
        acc_dswrite4<180, 62464>(xw + 0);
        acc_dswrite4<184, 63488>(xw + 0);
        acc_dswrite4<188, 64512>(xw + 0);
        acc_dswrite4<112, 0>(xw + 65536);
        acc_dswrite4<116, 1024>(xw + 65536);
        acc_dswrite4<120, 2048>(xw + 65536);
        acc_dswrite4<124, 3072>(xw + 65536);
        acc_dswrite4<240, 4096>(xw + 65536);
        acc_dswrite4<244, 5120>(xw + 65536);
        acc_dswrite4<248, 6144>(xw + 65536);
        acc_dswrite4<252, 7168>(xw + 65536);
      } break;
      case 3: {
        acc_dswrite4<16, 8192>(xw + 65536);
        acc_dswrite4<20, 9216>(xw + 65536);
        acc_dswrite4<24, 10240>(xw + 65536);
        acc_dswrite4<28, 11264>(xw + 65536);
        acc_dswrite4<144, 12288>(xw + 65536);
        acc_dswrite4<148, 13312>(xw + 65536);
        acc_dswrite4<152, 14336>(xw + 65536);
        acc_dswrite4<156, 15360>(xw + 65536);
        acc_dswrite4<48, 16384>(xw + 65536);
        acc_dswrite4<52, 17408>(xw + 65536);
        acc_dswrite4<56, 18432>(xw + 65536);
        acc_dswrite4<60, 19456>(xw + 65536);
        acc_dswrite4<176, 20480>(xw + 65536);
        acc_dswrite4<180, 21504>(xw + 65536);
        acc_dswrite4<184, 22528>(xw + 65536);
        acc_dswrite4<188, 23552>(xw + 65536);
        acc_dswrite4<80, 24576>(xw + 65536);
        acc_dswrite4<84, 25600>(xw + 65536);
        acc_dswrite4<88, 26624>(xw + 65536);
        acc_dswrite4<92, 27648>(xw + 65536);
        acc_dswrite4<208, 28672>(xw + 65536);
        acc_dswrite4<212, 29696>(xw + 65536);
        acc_dswrite4<216, 30720>(xw + 65536);
        acc_dswrite4<220, 31744>(xw + 65536);
      } break;
    }
    WAITL0();
    __builtin_amdgcn_s_barrier();
    switch (w) {
      case 0: {
        { f32x4 sum = acc_read4<16>(); sum *= fw[0][0];
          sum += xr[1536] * fw[0][1];
          sum += xr[3072] * fw[0][2];
          sum += xr[4608] * fw[0][3];
          u32x2 v = {cvtpk(sum[0], sum[1]), cvtpk(sum[2], sum[3])};
          *(u32x2*)(otile + (0 + r) * 512 + (((4) ^ ((0 + r) & 31)) * 16) + 8 * h) = v; }
        { f32x4 sum = acc_read4<20>(); sum *= fw[0][0];
          sum += xr[1600] * fw[0][1];
          sum += xr[3136] * fw[0][2];
          sum += xr[4672] * fw[0][3];
          u32x2 v = {cvtpk(sum[0], sum[1]), cvtpk(sum[2], sum[3])};
          *(u32x2*)(otile + (0 + r) * 512 + (((5) ^ ((0 + r) & 31)) * 16) + 8 * h) = v; }
        { f32x4 sum = acc_read4<24>(); sum *= fw[0][0];
          sum += xr[1664] * fw[0][1];
          sum += xr[3200] * fw[0][2];
          sum += xr[4736] * fw[0][3];
          u32x2 v = {cvtpk(sum[0], sum[1]), cvtpk(sum[2], sum[3])};
          *(u32x2*)(otile + (0 + r) * 512 + (((6) ^ ((0 + r) & 31)) * 16) + 8 * h) = v; }
        { f32x4 sum = acc_read4<28>(); sum *= fw[0][0];
          sum += xr[1728] * fw[0][1];
          sum += xr[3264] * fw[0][2];
          sum += xr[4800] * fw[0][3];
          u32x2 v = {cvtpk(sum[0], sum[1]), cvtpk(sum[2], sum[3])};
          *(u32x2*)(otile + (0 + r) * 512 + (((7) ^ ((0 + r) & 31)) * 16) + 8 * h) = v; }
        { f32x4 sum = acc_read4<144>(); sum *= fw[1][0];
          sum += xr[1792] * fw[1][1];
          sum += xr[3328] * fw[1][2];
          sum += xr[4864] * fw[1][3];
          u32x2 v = {cvtpk(sum[0], sum[1]), cvtpk(sum[2], sum[3])};
          *(u32x2*)(otile + (32 + r) * 512 + (((4) ^ ((32 + r) & 31)) * 16) + 8 * h) = v; }
        { f32x4 sum = acc_read4<148>(); sum *= fw[1][0];
          sum += xr[1856] * fw[1][1];
          sum += xr[3392] * fw[1][2];
          sum += xr[4928] * fw[1][3];
          u32x2 v = {cvtpk(sum[0], sum[1]), cvtpk(sum[2], sum[3])};
          *(u32x2*)(otile + (32 + r) * 512 + (((5) ^ ((32 + r) & 31)) * 16) + 8 * h) = v; }
        { f32x4 sum = acc_read4<152>(); sum *= fw[1][0];
          sum += xr[1920] * fw[1][1];
          sum += xr[3456] * fw[1][2];
          sum += xr[4992] * fw[1][3];
          u32x2 v = {cvtpk(sum[0], sum[1]), cvtpk(sum[2], sum[3])};
          *(u32x2*)(otile + (32 + r) * 512 + (((6) ^ ((32 + r) & 31)) * 16) + 8 * h) = v; }
        { f32x4 sum = acc_read4<156>(); sum *= fw[1][0];
          sum += xr[1984] * fw[1][1];
          sum += xr[3520] * fw[1][2];
          sum += xr[5056] * fw[1][3];
          u32x2 v = {cvtpk(sum[0], sum[1]), cvtpk(sum[2], sum[3])};
          *(u32x2*)(otile + (32 + r) * 512 + (((7) ^ ((32 + r) & 31)) * 16) + 8 * h) = v; }
      } break;
      case 1: {
        { f32x4 sum = acc_read4<48>(); sum *= fw[0][1];
          sum += xr[0] * fw[0][0];
          sum += xr[3584] * fw[0][2];
          sum += xr[5120] * fw[0][3];
          u32x2 v = {cvtpk(sum[0], sum[1]), cvtpk(sum[2], sum[3])};
          *(u32x2*)(otile + (0 + r) * 512 + (((12) ^ ((0 + r) & 31)) * 16) + 8 * h) = v; }
        { f32x4 sum = acc_read4<52>(); sum *= fw[0][1];
          sum += xr[64] * fw[0][0];
          sum += xr[3648] * fw[0][2];
          sum += xr[5184] * fw[0][3];
          u32x2 v = {cvtpk(sum[0], sum[1]), cvtpk(sum[2], sum[3])};
          *(u32x2*)(otile + (0 + r) * 512 + (((13) ^ ((0 + r) & 31)) * 16) + 8 * h) = v; }
        { f32x4 sum = acc_read4<56>(); sum *= fw[0][1];
          sum += xr[128] * fw[0][0];
          sum += xr[3712] * fw[0][2];
          sum += xr[5248] * fw[0][3];
          u32x2 v = {cvtpk(sum[0], sum[1]), cvtpk(sum[2], sum[3])};
          *(u32x2*)(otile + (0 + r) * 512 + (((14) ^ ((0 + r) & 31)) * 16) + 8 * h) = v; }
        { f32x4 sum = acc_read4<60>(); sum *= fw[0][1];
          sum += xr[192] * fw[0][0];
          sum += xr[3776] * fw[0][2];
          sum += xr[5312] * fw[0][3];
          u32x2 v = {cvtpk(sum[0], sum[1]), cvtpk(sum[2], sum[3])};
          *(u32x2*)(otile + (0 + r) * 512 + (((15) ^ ((0 + r) & 31)) * 16) + 8 * h) = v; }
        { f32x4 sum = acc_read4<176>(); sum *= fw[1][1];
          sum += xr[256] * fw[1][0];
          sum += xr[3840] * fw[1][2];
          sum += xr[5376] * fw[1][3];
          u32x2 v = {cvtpk(sum[0], sum[1]), cvtpk(sum[2], sum[3])};
          *(u32x2*)(otile + (32 + r) * 512 + (((12) ^ ((32 + r) & 31)) * 16) + 8 * h) = v; }
        { f32x4 sum = acc_read4<180>(); sum *= fw[1][1];
          sum += xr[320] * fw[1][0];
          sum += xr[3904] * fw[1][2];
          sum += xr[5440] * fw[1][3];
          u32x2 v = {cvtpk(sum[0], sum[1]), cvtpk(sum[2], sum[3])};
          *(u32x2*)(otile + (32 + r) * 512 + (((13) ^ ((32 + r) & 31)) * 16) + 8 * h) = v; }
        { f32x4 sum = acc_read4<184>(); sum *= fw[1][1];
          sum += xr[384] * fw[1][0];
          sum += xr[3968] * fw[1][2];
          sum += xr[5504] * fw[1][3];
          u32x2 v = {cvtpk(sum[0], sum[1]), cvtpk(sum[2], sum[3])};
          *(u32x2*)(otile + (32 + r) * 512 + (((14) ^ ((32 + r) & 31)) * 16) + 8 * h) = v; }
        { f32x4 sum = acc_read4<188>(); sum *= fw[1][1];
          sum += xr[448] * fw[1][0];
          sum += xr[4032] * fw[1][2];
          sum += xr[5568] * fw[1][3];
          u32x2 v = {cvtpk(sum[0], sum[1]), cvtpk(sum[2], sum[3])};
          *(u32x2*)(otile + (32 + r) * 512 + (((15) ^ ((32 + r) & 31)) * 16) + 8 * h) = v; }
      } break;
      case 2: {
        { f32x4 sum = acc_read4<80>(); sum *= fw[0][2];
          sum += xr[512] * fw[0][0];
          sum += xr[2048] * fw[0][1];
          sum += xr[5632] * fw[0][3];
          u32x2 v = {cvtpk(sum[0], sum[1]), cvtpk(sum[2], sum[3])};
          *(u32x2*)(otile + (0 + r) * 512 + (((20) ^ ((0 + r) & 31)) * 16) + 8 * h) = v; }
        { f32x4 sum = acc_read4<84>(); sum *= fw[0][2];
          sum += xr[576] * fw[0][0];
          sum += xr[2112] * fw[0][1];
          sum += xr[5696] * fw[0][3];
          u32x2 v = {cvtpk(sum[0], sum[1]), cvtpk(sum[2], sum[3])};
          *(u32x2*)(otile + (0 + r) * 512 + (((21) ^ ((0 + r) & 31)) * 16) + 8 * h) = v; }
        { f32x4 sum = acc_read4<88>(); sum *= fw[0][2];
          sum += xr[640] * fw[0][0];
          sum += xr[2176] * fw[0][1];
          sum += xr[5760] * fw[0][3];
          u32x2 v = {cvtpk(sum[0], sum[1]), cvtpk(sum[2], sum[3])};
          *(u32x2*)(otile + (0 + r) * 512 + (((22) ^ ((0 + r) & 31)) * 16) + 8 * h) = v; }
        { f32x4 sum = acc_read4<92>(); sum *= fw[0][2];
          sum += xr[704] * fw[0][0];
          sum += xr[2240] * fw[0][1];
          sum += xr[5824] * fw[0][3];
          u32x2 v = {cvtpk(sum[0], sum[1]), cvtpk(sum[2], sum[3])};
          *(u32x2*)(otile + (0 + r) * 512 + (((23) ^ ((0 + r) & 31)) * 16) + 8 * h) = v; }
        { f32x4 sum = acc_read4<208>(); sum *= fw[1][2];
          sum += xr[768] * fw[1][0];
          sum += xr[2304] * fw[1][1];
          sum += xr[5888] * fw[1][3];
          u32x2 v = {cvtpk(sum[0], sum[1]), cvtpk(sum[2], sum[3])};
          *(u32x2*)(otile + (32 + r) * 512 + (((20) ^ ((32 + r) & 31)) * 16) + 8 * h) = v; }
        { f32x4 sum = acc_read4<212>(); sum *= fw[1][2];
          sum += xr[832] * fw[1][0];
          sum += xr[2368] * fw[1][1];
          sum += xr[5952] * fw[1][3];
          u32x2 v = {cvtpk(sum[0], sum[1]), cvtpk(sum[2], sum[3])};
          *(u32x2*)(otile + (32 + r) * 512 + (((21) ^ ((32 + r) & 31)) * 16) + 8 * h) = v; }
        { f32x4 sum = acc_read4<216>(); sum *= fw[1][2];
          sum += xr[896] * fw[1][0];
          sum += xr[2432] * fw[1][1];
          sum += xr[6016] * fw[1][3];
          u32x2 v = {cvtpk(sum[0], sum[1]), cvtpk(sum[2], sum[3])};
          *(u32x2*)(otile + (32 + r) * 512 + (((22) ^ ((32 + r) & 31)) * 16) + 8 * h) = v; }
        { f32x4 sum = acc_read4<220>(); sum *= fw[1][2];
          sum += xr[960] * fw[1][0];
          sum += xr[2496] * fw[1][1];
          sum += xr[6080] * fw[1][3];
          u32x2 v = {cvtpk(sum[0], sum[1]), cvtpk(sum[2], sum[3])};
          *(u32x2*)(otile + (32 + r) * 512 + (((23) ^ ((32 + r) & 31)) * 16) + 8 * h) = v; }
      } break;
      case 3: {
        { f32x4 sum = acc_read4<112>(); sum *= fw[0][3];
          sum += xr[1024] * fw[0][0];
          sum += xr[2560] * fw[0][1];
          sum += xr[4096] * fw[0][2];
          u32x2 v = {cvtpk(sum[0], sum[1]), cvtpk(sum[2], sum[3])};
          *(u32x2*)(otile + (0 + r) * 512 + (((28) ^ ((0 + r) & 31)) * 16) + 8 * h) = v; }
        { f32x4 sum = acc_read4<116>(); sum *= fw[0][3];
          sum += xr[1088] * fw[0][0];
          sum += xr[2624] * fw[0][1];
          sum += xr[4160] * fw[0][2];
          u32x2 v = {cvtpk(sum[0], sum[1]), cvtpk(sum[2], sum[3])};
          *(u32x2*)(otile + (0 + r) * 512 + (((29) ^ ((0 + r) & 31)) * 16) + 8 * h) = v; }
        { f32x4 sum = acc_read4<120>(); sum *= fw[0][3];
          sum += xr[1152] * fw[0][0];
          sum += xr[2688] * fw[0][1];
          sum += xr[4224] * fw[0][2];
          u32x2 v = {cvtpk(sum[0], sum[1]), cvtpk(sum[2], sum[3])};
          *(u32x2*)(otile + (0 + r) * 512 + (((30) ^ ((0 + r) & 31)) * 16) + 8 * h) = v; }
        { f32x4 sum = acc_read4<124>(); sum *= fw[0][3];
          sum += xr[1216] * fw[0][0];
          sum += xr[2752] * fw[0][1];
          sum += xr[4288] * fw[0][2];
          u32x2 v = {cvtpk(sum[0], sum[1]), cvtpk(sum[2], sum[3])};
          *(u32x2*)(otile + (0 + r) * 512 + (((31) ^ ((0 + r) & 31)) * 16) + 8 * h) = v; }
        { f32x4 sum = acc_read4<240>(); sum *= fw[1][3];
          sum += xr[1280] * fw[1][0];
          sum += xr[2816] * fw[1][1];
          sum += xr[4352] * fw[1][2];
          u32x2 v = {cvtpk(sum[0], sum[1]), cvtpk(sum[2], sum[3])};
          *(u32x2*)(otile + (32 + r) * 512 + (((28) ^ ((32 + r) & 31)) * 16) + 8 * h) = v; }
        { f32x4 sum = acc_read4<244>(); sum *= fw[1][3];
          sum += xr[1344] * fw[1][0];
          sum += xr[2880] * fw[1][1];
          sum += xr[4416] * fw[1][2];
          u32x2 v = {cvtpk(sum[0], sum[1]), cvtpk(sum[2], sum[3])};
          *(u32x2*)(otile + (32 + r) * 512 + (((29) ^ ((32 + r) & 31)) * 16) + 8 * h) = v; }
        { f32x4 sum = acc_read4<248>(); sum *= fw[1][3];
          sum += xr[1408] * fw[1][0];
          sum += xr[2944] * fw[1][1];
          sum += xr[4480] * fw[1][2];
          u32x2 v = {cvtpk(sum[0], sum[1]), cvtpk(sum[2], sum[3])};
          *(u32x2*)(otile + (32 + r) * 512 + (((30) ^ ((32 + r) & 31)) * 16) + 8 * h) = v; }
        { f32x4 sum = acc_read4<252>(); sum *= fw[1][3];
          sum += xr[1472] * fw[1][0];
          sum += xr[3008] * fw[1][1];
          sum += xr[4544] * fw[1][2];
          u32x2 v = {cvtpk(sum[0], sum[1]), cvtpk(sum[2], sum[3])};
          *(u32x2*)(otile + (32 + r) * 512 + (((31) ^ ((32 + r) & 31)) * 16) + 8 * h) = v; }
      } break;
    }
  }
  __syncthreads();
  asm volatile("s_waitcnt vmcnt(0)" :: "v"(c.pfd) : "memory");
  STAMP(15);
  {
    float* pout = (float*)o;
    char* wr = lds + 4096 + w * 24576;
    __amdgpu_buffer_rsrc_t rwo = __builtin_amdgcn_make_buffer_rsrc((void*)(wo + (size_t)(head * DH) * DM + 128 * w), 0,
                                                                    (DH * DM - 128 * w) * 2, 0x00020000);
    const int wrow = lane >> 4;
    const int wvo = wrow * (DM * 2) + (((lane & 15) ^ ((wrow & 3) << 2)) * 16);
    auto wissue2 = [&](int ch) {
#pragma unroll
      for (int p = 0; p < 8; ++p)
        __builtin_amdgcn_raw_ptr_buffer_load_lds(rwo, (lds_vp)(wr + (ch % 3) * 8192 + p * 1024), 16, wvo, (32 * ch + 4 * p) * (DM * 2), 0, 0);
    };
    wissue2(0); wissue2(1); wissue2(2);
    const int g2 = lane >> 4, q2 = (lane >> 2) & 3, p2 = lane & 3;
    unsigned tb[4];
#pragma unroll
    for (int cb = 0; cb < 4; ++cb)
      tb[cb] = (unsigned)(size_t)wr + (8 * (g2 >> 1) + q2) * 256 + ((64 * cb + 32 * (g2 & 1) + 8 * p2) ^ (q2 << 6));
    f32x16 pa[2][4];
#pragma unroll
    for (int a2 = 0; a2 < 2; ++a2)
#pragma unroll
      for (int b2 = 0; b2 < 4; ++b2)
#pragma unroll
        for (int i = 0; i < 16; ++i) pa[a2][b2][i] = 0.f;
#pragma unroll
    for (int ch = 0; ch < 8; ++ch) {
      if (ch <= 5) WAITV(16); else if (ch == 6) WAITV(8); else WAITV(0);
#pragma unroll
      for (int s2 = 0; s2 < 2; ++s2) {
        s16x4 a0[4], a1[4];
#pragma unroll
        for (int cb = 0; cb < 4; ++cb) {
          a0[cb] = trread(tb[cb] + (ch % 3) * 8192 + s2 * 4096);
          a1[cb] = trread(tb[cb] + (ch % 3) * 8192 + s2 * 4096 + 1024);
        }
        bf16x8 ob[2];
#pragma unroll
        for (int rb2 = 0; rb2 < 2; ++rb2) {
          const int R = 32 * rb2 + r;
          asm volatile("ds_read_b128 %0, %1" : "=v"(ob[rb2]) : "v"((unsigned)(size_t)(otile + R * 512 + (((2 * (2 * ch + s2) + h) ^ (R & 31)) * 16))) : "memory");
        }
        asm volatile("s_waitcnt lgkmcnt(0)"
                     : "+v"(a0[0]), "+v"(a0[1]), "+v"(a0[2]), "+v"(a0[3]), "+v"(a1[0]), "+v"(a1[1]), "+v"(a1[2]),
                       "+v"(a1[3]), "+v"(ob[0]), "+v"(ob[1])::"memory");
        if (s2 == 1 && ch + 3 < 8) wissue2(ch + 3);
#pragma unroll
        for (int cb = 0; cb < 4; ++cb) {
          const bf16x8 A = cat8(a0[cb], a1[cb]);
          pa[0][cb] = __builtin_amdgcn_mfma_f32_32x32x16_bf16(A, ob[0], pa[0][cb], 0, 0, 0);
          pa[1][cb] = __builtin_amdgcn_mfma_f32_32x32x16_bf16(A, ob[1], pa[1][cb], 0, 0, 0);
        }
      }
    }
#pragma unroll
    for (int rb2 = 0; rb2 < 2; ++rb2) {
      const int R = 32 * rb2 + r;
      if (R < 56) {
        float* dst = pout + ((size_t)head * NROW + row0 + R) * DM + 128 * w + 4 * h;
#pragma unroll
        for (int cb = 0; cb < 4; ++cb) {
          if (128 * w + 32 * cb < DM) {
#pragma unroll
            for (int gg = 0; gg < 4; ++gg) {
              f32x4 v = {pa[rb2][cb][4 * gg], pa[rb2][cb][4 * gg + 1], pa[rb2][cb][4 * gg + 2], pa[rb2][cb][4 * gg + 3]};
              *(f32x4*)(dst + 32 * cb + 8 * gg) = v;
            }
          }
        }
      }
    }
  }
  STAMP(10);
}

__global__ __launch_bounds__(256) void headsum_kernel(const float* __restrict__ pout, u16* __restrict__ out) {
  const int i = blockIdx.x * 256 + threadIdx.x;
  if (i >= NROW * DM / 4) return;
  f32x4 s = {0.f, 0.f, 0.f, 0.f};
#pragma unroll
  for (int hh = 0; hh < HQ; ++hh) s += *(const f32x4*)(pout + (size_t)hh * NROW * DM + (size_t)i * 4);
  u32x2 v = {cvtpk(s[0], s[1]), cvtpk(s[2], s[3])};
  *(u32x2*)(out + (size_t)i * 4) = v;
}
__global__ __launch_bounds__(256, 1) void qproj5_kernel(const u16* __restrict__ x, const u16* __restrict__ wq,
                                                         u16* __restrict__ qfrag, unsigned* __restrict__ cnt) {
  extern __shared__ __attribute__((aligned(16))) char lds[];
  const int tid = threadIdx.x, lane = tid & 63;
  const int w = __builtin_amdgcn_readfirstlane(tid >> 6);
  const int r = lane & 31, h = lane >> 5;
  const int bid = blockIdx.x;
  if (bid == 0 && tid < 64) cnt[tid] = 0u;
  const int xcd = bid & 7, j = bid >> 3;
  const int grp = (j >> 2) * 8 + xcd, sub = j & 3;
  const int b = grp >> 3, kvh = grp & 7;
  const int hl = sub >> 1, head = kvh * 2 + hl, rhalf = sub & 1;
  const int row0 = b * SQ + rhalf * 56;
  char* ring = lds + w * 40960;
  {
    __amdgpu_buffer_rsrc_t rx =
        __builtin_amdgcn_make_buffer_rsrc((void*)(x + (size_t)row0 * DM), 0, 56 * DM * 2, 0x00020000);
#pragma unroll
    for (int p = 0; p < 14; ++p) {
      const int ci = 64 * p + lane;
      const int lr = ci / 56, cp = ci - lr * 56;
      int gr = 16 * w + lr; gr = gr > 55 ? 55 : gr;
      const int cs = (cp & ~7) | ((cp ^ (lr >> 1)) & 7);
      __builtin_amdgcn_raw_ptr_buffer_load_lds(rx, (lds_vp)(ring + 3 * 8192 + p * 1024), 16, gr * (DM * 2) + cs * 16, 0, 0, 0);
    }
  }
  const int colw = head * DH + 64 * w;
  __amdgpu_buffer_rsrc_t rw = __builtin_amdgcn_make_buffer_rsrc((void*)(wq + colw), 0, (DM * NQ - colw) * 2, 0x00020000);
  const int drow = lane >> 3;
  const int wvoff = drow * (NQ * 2) + (((lane & 7) ^ (((drow >> 1) & 1) << 2)) * 16);
  auto wpiece = [&](int ch, int p) {
    __builtin_amdgcn_raw_ptr_buffer_load_lds(rw, (lds_vp)(ring + (ch % 3) * 8192 + p * 1024), 16, wvoff,
                                             (64 * ch + 8 * p) * (NQ * 2), 0, 0);
  };
#pragma unroll
  for (int ch = 0; ch < 3; ++ch)
#pragma unroll
    for (int p = 0; p < 8; ++p) wpiece(ch, p);
  const int g = lane >> 4, qq = (lane >> 2) & 3, pp = lane & 3;
  const unsigned trb = (unsigned)(size_t)ring + (8 * (g >> 1) + qq) * 128 + ((32 * (g & 1) + 8 * pp) ^ ((qq >> 1) << 6));
  const char* xa[2][4];
#pragma unroll
  for (int qb = 0; qb < 2; ++qb) {
    const int R = 32 * qb + r, lr = R & 15;
#pragma unroll
    for (int v = 0; v < 4; ++v)
      xa[qb][v] = lds + (R >> 4) * 40960 + 3 * 8192 + lr * 896 + ((((2 * v + h) ^ (lr >> 1)) & 7) * 16);
  }
  f32x16 qa[2][2];
#pragma unroll
  for (int a2 = 0; a2 < 2; ++a2)
#pragma unroll
    for (int b2 = 0; b2 < 2; ++b2)
#pragma unroll
      for (int i = 0; i < 16; ++i) qa[a2][b2][i] = 0.f;
  s16x4 fa[2][4];
  bf16x8 fx[2][2];
  auto rdstep = [&](int i, int bsel) {
    const int ch = i >> 2, s4 = i & 3;
    const unsigned tb = trb + (ch % 3) * 8192 + s4 * 2048;
    fa[bsel][0] = trread(tb);
    fa[bsel][1] = trread(tb + 512);
    fa[bsel][2] = trread(tb ^ 64);
    fa[bsel][3] = trread((tb + 512) ^ 64);
    asm volatile("ds_read_b128 %0, %1" : "=v"(fx[bsel][0]) : "v"((unsigned)(size_t)(xa[0][s4] + ch * 128)) : "memory");
    asm volatile("ds_read_b128 %0, %1" : "=v"(fx[bsel][1]) : "v"((unsigned)(size_t)(xa[1][s4] + ch * 128)) : "memory");
  };
  WAITV(16);
  __builtin_amdgcn_s_barrier();
  rdstep(0, 0);
#pragma unroll
  for (int i = 0; i < 28; ++i) {
    const int bs = i & 1, ch = i >> 2, s4 = i & 3;
    if (i + 1 < 28) {
      if (s4 == 3) { if (ch == 0) WAITV(8); else if (ch <= 4) WAITV(6); else WAITV(0); }
      rdstep(i + 1, bs ^ 1);
      asm volatile("s_waitcnt lgkmcnt(6)" : "+v"(fa[bs][0]), "+v"(fa[bs][1]), "+v"(fa[bs][2]), "+v"(fa[bs][3]), "+v"(fx[bs][0]), "+v"(fx[bs][1])::"memory");
    } else {
      asm volatile("s_waitcnt lgkmcnt(0)" : "+v"(fa[bs][0]), "+v"(fa[bs][1]), "+v"(fa[bs][2]), "+v"(fa[bs][3]), "+v"(fx[bs][0]), "+v"(fx[bs][1])::"memory");
    }
    bf16x8 A0 = cat8(fa[bs][0], fa[bs][1]), A1 = cat8(fa[bs][2], fa[bs][3]);
    qa[0][0] = __builtin_amdgcn_mfma_f32_32x32x16_bf16(A0, fx[bs][0], qa[0][0], 0, 0, 0);
    GAP();
    if (ch >= 1 && ch <= 4) wpiece(ch + 2, 2 * s4);
    GAP();
    qa[0][1] = __builtin_amdgcn_mfma_f32_32x32x16_bf16(A1, fx[bs][0], qa[0][1], 0, 0, 0);
    qa[1][0] = __builtin_amdgcn_mfma_f32_32x32x16_bf16(A0, fx[bs][1], qa[1][0], 0, 0, 0);
    GAP();
    if (ch >= 1 && ch <= 4) wpiece(ch + 2, 2 * s4 + 1);
    GAP();
    qa[1][1] = __builtin_amdgcn_mfma_f32_32x32x16_bf16(A1, fx[bs][1], qa[1][1], 0, 0, 0);
  }
#pragma unroll
  for (int qb = 0; qb < 2; ++qb) {
    const int lrow = 32 * qb + r;
    if (lrow < 56) {
      const int R = hl * 112 + rhalf * 56 + lrow;
      u16* base = qfrag + ((size_t)(grp * 8 + (R >> 5)) * 16 * 64 + (R & 31)) * 8 + 4 * h;
#pragma unroll
      for (int cb = 0; cb < 2; ++cb)
#pragma unroll
        for (int gg = 0; gg < 4; ++gg) {
          const int s = 4 * w + 2 * cb + (gg >> 1), hp = gg & 1;
          u32x2 v = {cvtpk(qa[qb][cb][4 * gg], qa[qb][cb][4 * gg + 1]), cvtpk(qa[qb][cb][4 * gg + 2], qa[qb][cb][4 * gg + 3])};
          *(u32x2*)(base + (s * 64 + 32 * hp) * 8) = v;
        }
    }
  }
}

template <int N> DI void dma5_unit(const Ctx4& c) {
  if constexpr (N < 32) {
    constexpr int slot = N % 16, t = unit_tile(N), hf = unit_half(N);
    if constexpr (!unit_is_v(N)) {
      __builtin_amdgcn_raw_ptr_buffer_load_lds(c.rk, (lds_vp)(c.ring + slot * 8192), 16, c.koff[0], 32 * t * 512 + hf * 256, 0, 0);
      __builtin_amdgcn_raw_ptr_buffer_load_lds(c.rk, (lds_vp)(c.ring + slot * 8192 + 1024), 16, c.koff[1], 32 * t * 512 + hf * 256, 0, 0);
    } else {
      __builtin_amdgcn_raw_ptr_buffer_load_lds(c.rv, (lds_vp)(c.ring + slot * 8192), 16, c.voff[0], (32 * t + 16 * hf) * 512, 0, 0);
      __builtin_amdgcn_raw_ptr_buffer_load_lds(c.rv, (lds_vp)(c.ring + slot * 8192 + 1024), 16, c.voff[1], (32 * t + 16 * hf) * 512, 0, 0);
    }
  }
}
template <int T, int J> DI bf16x8 kread5(const Ctx4& c) {
  constexpr int n = unit_k(T, J >> 3), slot = n % 16;
  bf16x8 r;
  asm volatile("ds_read_b128 %0, %1 offset:%2" : "=v"(r) : "v"(c.kaddr[J & 7] + (slot >> 3) * 65536), "i"((slot & 7) * 8192) : "memory");
  return r;
}
template <int T, int J> DI void phaseA5_step(Ctx4& c, bf16x8 (&kf)[4]) {
  if constexpr (J + 3 < 16) asm volatile("s_waitcnt lgkmcnt(3)" : "+v"(kf[J & 3])::"memory");
  else if constexpr (J + 3 == 16) asm volatile("s_waitcnt lgkmcnt(2)" : "+v"(kf[J & 3])::"memory");
  else if constexpr (J + 3 == 17) asm volatile("s_waitcnt lgkmcnt(1)" : "+v"(kf[J & 3])::"memory");
  else asm volatile("s_waitcnt lgkmcnt(0)" : "+v"(kf[J & 3])::"memory");
  qk_mfma<J == 0>(c.s[0], kf[J & 3], c.qf[0][J]);
  if constexpr (T >= 1 && (J & 3) == 1 && J < 16) dma5_unit<4 * T + 10 + (J >> 2)>(c);
  GAP();
  qk_mfma<J == 0>(c.s[1], kf[J & 3], c.qf[1][J]);
  if constexpr (J + 4 < 16) kf[J & 3] = kread5<T, J + 4>(c);
  GAP();
}
template <int T, int... Js> DI void phaseA5_(Ctx4& c, std::integer_sequence<int, Js...>) {
  bf16x8 kf[4];
  {
    constexpr int need = 4 * T + 1 > 31 ? 31 : 4 * T + 1;
    constexpr int lastiss = (T == 0 ? 13 : 4 * T + 9) > 31 ? 31 : (T == 0 ? 13 : 4 * T + 9);
    constexpr int after = 2 * (lastiss - need);
    asm volatile("s_waitcnt vmcnt(%0)" ::"i"(after) : "memory");
    __builtin_amdgcn_s_barrier();
  }
  kf[0] = kread5<T, 0>(c); kf[1] = kread5<T, 1>(c); kf[2] = kread5<T, 2>(c); kf[3] = kread5<T, 3>(c);
  GAP();
  (phaseA5_step<T, Js>(c, kf), ...);
}
template <int T> DI void phaseA5(Ctx4& c) { phaseA5_<T>(c, std::make_integer_sequence<int, 16>{}); }

template <int TV, int S> DI void vread5_set(const Ctx4& c, s16x4 (&v)[4]) {
  constexpr int ks = S >> 2, dbp = S & 3;
  constexpr int n = unit_v(TV, ks), slot = n % 16;
  constexpr int db0 = 2 * dbp, db1 = 2 * dbp + 1;
  constexpr int o0 = (slot & 7) * 8192 + (db0 >> 2) * 256, o1 = (slot & 7) * 8192 + (db1 >> 2) * 256;
  constexpr unsigned hi = (slot >> 3) * 65536;
  v[0] = trread_o<o0>(c.vaddr[db0 & 3] + hi);
  v[1] = trread_o<o0 + 4096>(c.vaddr[db0 & 3] + hi);
  v[2] = trread_o<o1>(c.vaddr[db1 & 3] + hi);
  v[3] = trread_o<o1 + 4096>(c.vaddr[db1 & 3] + hi);
}
template <int T, bool HASPV, bool HASSM, int M> DI void phaseB5_gap(Ctx4& c, s16x4 (&va)[4], s16x4 (&vb)[4]) {
  constexpr int TV = T - 1;
  constexpr int S = M >> 2, ks = S >> 2, dbp = S & 3, dd = (M >> 1) & 1, qb = M & 1;
  if constexpr (HASPV) {
    if constexpr ((M & 3) == 0) {
      if constexpr (S + 1 < 8) {
        if constexpr (S & 1) asm volatile("s_waitcnt lgkmcnt(4)" : "+v"(vb[0]), "+v"(vb[1]), "+v"(vb[2]), "+v"(vb[3])::"memory");
        else asm volatile("s_waitcnt lgkmcnt(4)" : "+v"(va[0]), "+v"(va[1]), "+v"(va[2]), "+v"(va[3])::"memory");
      } else {
        asm volatile("s_waitcnt lgkmcnt(0)" : "+v"(vb[0]), "+v"(vb[1]), "+v"(vb[2]), "+v"(vb[3])::"memory");
      }
    }
    {
      constexpr int db = 2 * dbp + dd;
      const bf16x8 vf = (S & 1) ? cat8(vb[2 * dd], vb[2 * dd + 1]) : cat8(va[2 * dd], va[2 * dd + 1]);
      pv_mfma<(qb * 8 + db) * 16, (TV == 0 && ks == 0)>(vf, c.pf[qb][ks]);
    }
    if constexpr ((M & 3) == 3 && S + 2 < 8) {
      if constexpr (S & 1) vread5_set<TV, S + 2>(c, vb); else vread5_set<TV, S + 2>(c, va);
    }
  }
  if constexpr (HASSM) sm_slice<T, M>(c);
  GAP();
}
template <int T, bool HASPV, bool HASSM, int... Ms> DI void phaseB5_(Ctx4& c, std::integer_sequence<int, Ms...>) {
  constexpr int TV = T - 1;
  s16x4 va[4], vb[4];
  if constexpr (!HASSM) {
    asm volatile("s_waitcnt vmcnt(0)" ::: "memory");
    __builtin_amdgcn_s_barrier();
  }
  if constexpr (HASPV) {
    vread5_set<TV, 0>(c, va);
    vread5_set<TV, 1>(c, vb);
  }
  if constexpr (HASSM) asm volatile("s_nop 7\n\ts_nop 7" : "+v"(c.s[0]), "+v"(c.s[1]));
  GAP();
  (phaseB5_gap<T, HASPV, HASSM, Ms>(c, va, vb), ...);
  if constexpr (HASSM) {
    c.l[0] += c.rs[0]; c.l[1] += c.rs[1];
#pragma unroll
    for (int qb = 0; qb < 2; ++qb)
#pragma unroll
      for (int ks = 0; ks < 2; ++ks) {
        u32x4 wv = {c.pn[qb][4 * ks], c.pn[qb][4 * ks + 1], c.pn[qb][4 * ks + 2], c.pn[qb][4 * ks + 3]};
        c.pf[qb][ks] = __builtin_bit_cast(bf16x8, wv);
      }
  }
  if constexpr (HASPV && HASSM) {
    if (__builtin_expect(c.pend, 0)) {
      float t0, t1;
      asm volatile("s_nop 7\n\ts_nop 7\n\t"
                   "v_accvgpr_read_b32 %0, a0\n\tv_mul_f32 %0, %2, %0\n\tv_accvgpr_write_b32 a0, %0\n\t"
                   "v_accvgpr_read_b32 %1, a1\n\tv_mul_f32 %1, %2, %1\n\tv_accvgpr_write_b32 a1, %1\n\t"
                   "v_accvgpr_read_b32 %0, a2\n\tv_mul_f32 %0, %2, %0\n\tv_accvgpr_write_b32 a2, %0\n\t"
                   "v_accvgpr_read_b32 %1, a3\n\tv_mul_f32 %1, %2, %1\n\tv_accvgpr_write_b32 a3, %1\n\t"
                   "v_accvgpr_read_b32 %0, a4\n\tv_mul_f32 %0, %2, %0\n\tv_accvgpr_write_b32 a4, %0\n\t"
                   "v_accvgpr_read_b32 %1, a5\n\tv_mul_f32 %1, %2, %1\n\tv_accvgpr_write_b32 a5, %1\n\t"
                   "v_accvgpr_read_b32 %0, a6\n\tv_mul_f32 %0, %2, %0\n\tv_accvgpr_write_b32 a6, %0\n\t"
                   "v_accvgpr_read_b32 %1, a7\n\tv_mul_f32 %1, %2, %1\n\tv_accvgpr_write_b32 a7, %1\n\t"
                   "v_accvgpr_read_b32 %0, a8\n\tv_mul_f32 %0, %2, %0\n\tv_accvgpr_write_b32 a8, %0\n\t"
                   "v_accvgpr_read_b32 %1, a9\n\tv_mul_f32 %1, %2, %1\n\tv_accvgpr_write_b32 a9, %1\n\t"
                   "v_accvgpr_read_b32 %0, a10\n\tv_mul_f32 %0, %2, %0\n\tv_accvgpr_write_b32 a10, %0\n\t"
                   "v_accvgpr_read_b32 %1, a11\n\tv_mul_f32 %1, %2, %1\n\tv_accvgpr_write_b32 a11, %1\n\t"
                   "v_accvgpr_read_b32 %0, a12\n\tv_mul_f32 %0, %2, %0\n\tv_accvgpr_write_b32 a12, %0\n\t"
                   "v_accvgpr_read_b32 %1, a13\n\tv_mul_f32 %1, %2, %1\n\tv_accvgpr_write_b32 a13, %1\n\t"
                   "v_accvgpr_read_b32 %0, a14\n\tv_mul_f32 %0, %2, %0\n\tv_accvgpr_write_b32 a14, %0\n\t"
                   "v_accvgpr_read_b32 %1, a15\n\tv_mul_f32 %1, %2, %1\n\tv_accvgpr_write_b32 a15, %1\n\t"
                   "v_accvgpr_read_b32 %0, a16\n\tv_mul_f32 %0, %2, %0\n\tv_accvgpr_write_b32 a16, %0\n\t"
                   "v_accvgpr_read_b32 %1, a17\n\tv_mul_f32 %1, %2, %1\n\tv_accvgpr_write_b32 a17, %1\n\t"
                   "v_accvgpr_read_b32 %0, a18\n\tv_mul_f32 %0, %2, %0\n\tv_accvgpr_write_b32 a18, %0\n\t"
                   "v_accvgpr_read_b32 %1, a19\n\tv_mul_f32 %1, %2, %1\n\tv_accvgpr_write_b32 a19, %1\n\t"
                   "v_accvgpr_read_b32 %0, a20\n\tv_mul_f32 %0, %2, %0\n\tv_accvgpr_write_b32 a20, %0\n\t"
                   "v_accvgpr_read_b32 %1, a21\n\tv_mul_f32 %1, %2, %1\n\tv_accvgpr_write_b32 a21, %1\n\t"
                   "v_accvgpr_read_b32 %0, a22\n\tv_mul_f32 %0, %2, %0\n\tv_accvgpr_write_b32 a22, %0\n\t"
                   "v_accvgpr_read_b32 %1, a23\n\tv_mul_f32 %1, %2, %1\n\tv_accvgpr_write_b32 a23, %1\n\t"
                   "v_accvgpr_read_b32 %0, a24\n\tv_mul_f32 %0, %2, %0\n\tv_accvgpr_write_b32 a24, %0\n\t"
                   "v_accvgpr_read_b32 %1, a25\n\tv_mul_f32 %1, %2, %1\n\tv_accvgpr_write_b32 a25, %1\n\t"
                   "v_accvgpr_read_b32 %0, a26\n\tv_mul_f32 %0, %2, %0\n\tv_accvgpr_write_b32 a26, %0\n\t"
                   "v_accvgpr_read_b32 %1, a27\n\tv_mul_f32 %1, %2, %1\n\tv_accvgpr_write_b32 a27, %1\n\t"
                   "v_accvgpr_read_b32 %0, a28\n\tv_mul_f32 %0, %2, %0\n\tv_accvgpr_write_b32 a28, %0\n\t"
                   "v_accvgpr_read_b32 %1, a29\n\tv_mul_f32 %1, %2, %1\n\tv_accvgpr_write_b32 a29, %1\n\t"
                   "v_accvgpr_read_b32 %0, a30\n\tv_mul_f32 %0, %2, %0\n\tv_accvgpr_write_b32 a30, %0\n\t"
                   "v_accvgpr_read_b32 %1, a31\n\tv_mul_f32 %1, %2, %1\n\tv_accvgpr_write_b32 a31, %1\n\t"
                   "v_accvgpr_read_b32 %0, a32\n\tv_mul_f32 %0, %2, %0\n\tv_accvgpr_write_b32 a32, %0\n\t"
                   "v_accvgpr_read_b32 %1, a33\n\tv_mul_f32 %1, %2, %1\n\tv_accvgpr_write_b32 a33, %1\n\t"
                   "v_accvgpr_read_b32 %0, a34\n\tv_mul_f32 %0, %2, %0\n\tv_accvgpr_write_b32 a34, %0\n\t"
                   "v_accvgpr_read_b32 %1, a35\n\tv_mul_f32 %1, %2, %1\n\tv_accvgpr_write_b32 a35, %1\n\t"
                   "v_accvgpr_read_b32 %0, a36\n\tv_mul_f32 %0, %2, %0\n\tv_accvgpr_write_b32 a36, %0\n\t"
                   "v_accvgpr_read_b32 %1, a37\n\tv_mul_f32 %1, %2, %1\n\tv_accvgpr_write_b32 a37, %1\n\t"
                   "v_accvgpr_read_b32 %0, a38\n\tv_mul_f32 %0, %2, %0\n\tv_accvgpr_write_b32 a38, %0\n\t"
                   "v_accvgpr_read_b32 %1, a39\n\tv_mul_f32 %1, %2, %1\n\tv_accvgpr_write_b32 a39, %1\n\t"
                   "v_accvgpr_read_b32 %0, a40\n\tv_mul_f32 %0, %2, %0\n\tv_accvgpr_write_b32 a40, %0\n\t"
                   "v_accvgpr_read_b32 %1, a41\n\tv_mul_f32 %1, %2, %1\n\tv_accvgpr_write_b32 a41, %1\n\t"
                   "v_accvgpr_read_b32 %0, a42\n\tv_mul_f32 %0, %2, %0\n\tv_accvgpr_write_b32 a42, %0\n\t"
                   "v_accvgpr_read_b32 %1, a43\n\tv_mul_f32 %1, %2, %1\n\tv_accvgpr_write_b32 a43, %1\n\t"
                   "v_accvgpr_read_b32 %0, a44\n\tv_mul_f32 %0, %2, %0\n\tv_accvgpr_write_b32 a44, %0\n\t"
                   "v_accvgpr_read_b32 %1, a45\n\tv_mul_f32 %1, %2, %1\n\tv_accvgpr_write_b32 a45, %1\n\t"
                   "v_accvgpr_read_b32 %0, a46\n\tv_mul_f32 %0, %2, %0\n\tv_accvgpr_write_b32 a46, %0\n\t"
                   "v_accvgpr_read_b32 %1, a47\n\tv_mul_f32 %1, %2, %1\n\tv_accvgpr_write_b32 a47, %1\n\t"
                   "v_accvgpr_read_b32 %0, a48\n\tv_mul_f32 %0, %2, %0\n\tv_accvgpr_write_b32 a48, %0\n\t"
                   "v_accvgpr_read_b32 %1, a49\n\tv_mul_f32 %1, %2, %1\n\tv_accvgpr_write_b32 a49, %1\n\t"
                   "v_accvgpr_read_b32 %0, a50\n\tv_mul_f32 %0, %2, %0\n\tv_accvgpr_write_b32 a50, %0\n\t"
                   "v_accvgpr_read_b32 %1, a51\n\tv_mul_f32 %1, %2, %1\n\tv_accvgpr_write_b32 a51, %1\n\t"
                   "v_accvgpr_read_b32 %0, a52\n\tv_mul_f32 %0, %2, %0\n\tv_accvgpr_write_b32 a52, %0\n\t"
                   "v_accvgpr_read_b32 %1, a53\n\tv_mul_f32 %1, %2, %1\n\tv_accvgpr_write_b32 a53, %1\n\t"
                   "v_accvgpr_read_b32 %0, a54\n\tv_mul_f32 %0, %2, %0\n\tv_accvgpr_write_b32 a54, %0\n\t"
                   "v_accvgpr_read_b32 %1, a55\n\tv_mul_f32 %1, %2, %1\n\tv_accvgpr_write_b32 a55, %1\n\t"
                   "v_accvgpr_read_b32 %0, a56\n\tv_mul_f32 %0, %2, %0\n\tv_accvgpr_write_b32 a56, %0\n\t"
                   "v_accvgpr_read_b32 %1, a57\n\tv_mul_f32 %1, %2, %1\n\tv_accvgpr_write_b32 a57, %1\n\t"
                   "v_accvgpr_read_b32 %0, a58\n\tv_mul_f32 %0, %2, %0\n\tv_accvgpr_write_b32 a58, %0\n\t"
                   "v_accvgpr_read_b32 %1, a59\n\tv_mul_f32 %1, %2, %1\n\tv_accvgpr_write_b32 a59, %1\n\t"
                   "v_accvgpr_read_b32 %0, a60\n\tv_mul_f32 %0, %2, %0\n\tv_accvgpr_write_b32 a60, %0\n\t"
                   "v_accvgpr_read_b32 %1, a61\n\tv_mul_f32 %1, %2, %1\n\tv_accvgpr_write_b32 a61, %1\n\t"
                   "v_accvgpr_read_b32 %0, a62\n\tv_mul_f32 %0, %2, %0\n\tv_accvgpr_write_b32 a62, %0\n\t"
                   "v_accvgpr_read_b32 %1, a63\n\tv_mul_f32 %1, %2, %1\n\tv_accvgpr_write_b32 a63, %1\n\t"
                   "v_accvgpr_read_b32 %0, a64\n\tv_mul_f32 %0, %2, %0\n\tv_accvgpr_write_b32 a64, %0\n\t"
                   "v_accvgpr_read_b32 %1, a65\n\tv_mul_f32 %1, %2, %1\n\tv_accvgpr_write_b32 a65, %1\n\t"
                   "v_accvgpr_read_b32 %0, a66\n\tv_mul_f32 %0, %2, %0\n\tv_accvgpr_write_b32 a66, %0\n\t"
                   "v_accvgpr_read_b32 %1, a67\n\tv_mul_f32 %1, %2, %1\n\tv_accvgpr_write_b32 a67, %1\n\t"
                   "v_accvgpr_read_b32 %0, a68\n\tv_mul_f32 %0, %2, %0\n\tv_accvgpr_write_b32 a68, %0\n\t"
                   "v_accvgpr_read_b32 %1, a69\n\tv_mul_f32 %1, %2, %1\n\tv_accvgpr_write_b32 a69, %1\n\t"
                   "v_accvgpr_read_b32 %0, a70\n\tv_mul_f32 %0, %2, %0\n\tv_accvgpr_write_b32 a70, %0\n\t"
                   "v_accvgpr_read_b32 %1, a71\n\tv_mul_f32 %1, %2, %1\n\tv_accvgpr_write_b32 a71, %1\n\t"
                   "v_accvgpr_read_b32 %0, a72\n\tv_mul_f32 %0, %2, %0\n\tv_accvgpr_write_b32 a72, %0\n\t"
                   "v_accvgpr_read_b32 %1, a73\n\tv_mul_f32 %1, %2, %1\n\tv_accvgpr_write_b32 a73, %1\n\t"
                   "v_accvgpr_read_b32 %0, a74\n\tv_mul_f32 %0, %2, %0\n\tv_accvgpr_write_b32 a74, %0\n\t"
                   "v_accvgpr_read_b32 %1, a75\n\tv_mul_f32 %1, %2, %1\n\tv_accvgpr_write_b32 a75, %1\n\t"
                   "v_accvgpr_read_b32 %0, a76\n\tv_mul_f32 %0, %2, %0\n\tv_accvgpr_write_b32 a76, %0\n\t"
                   "v_accvgpr_read_b32 %1, a77\n\tv_mul_f32 %1, %2, %1\n\tv_accvgpr_write_b32 a77, %1\n\t"
                   "v_accvgpr_read_b32 %0, a78\n\tv_mul_f32 %0, %2, %0\n\tv_accvgpr_write_b32 a78, %0\n\t"
                   "v_accvgpr_read_b32 %1, a79\n\tv_mul_f32 %1, %2, %1\n\tv_accvgpr_write_b32 a79, %1\n\t"
                   "v_accvgpr_read_b32 %0, a80\n\tv_mul_f32 %0, %2, %0\n\tv_accvgpr_write_b32 a80, %0\n\t"
                   "v_accvgpr_read_b32 %1, a81\n\tv_mul_f32 %1, %2, %1\n\tv_accvgpr_write_b32 a81, %1\n\t"
                   "v_accvgpr_read_b32 %0, a82\n\tv_mul_f32 %0, %2, %0\n\tv_accvgpr_write_b32 a82, %0\n\t"
                   "v_accvgpr_read_b32 %1, a83\n\tv_mul_f32 %1, %2, %1\n\tv_accvgpr_write_b32 a83, %1\n\t"
                   "v_accvgpr_read_b32 %0, a84\n\tv_mul_f32 %0, %2, %0\n\tv_accvgpr_write_b32 a84, %0\n\t"
                   "v_accvgpr_read_b32 %1, a85\n\tv_mul_f32 %1, %2, %1\n\tv_accvgpr_write_b32 a85, %1\n\t"
                   "v_accvgpr_read_b32 %0, a86\n\tv_mul_f32 %0, %2, %0\n\tv_accvgpr_write_b32 a86, %0\n\t"
                   "v_accvgpr_read_b32 %1, a87\n\tv_mul_f32 %1, %2, %1\n\tv_accvgpr_write_b32 a87, %1\n\t"
                   "v_accvgpr_read_b32 %0, a88\n\tv_mul_f32 %0, %2, %0\n\tv_accvgpr_write_b32 a88, %0\n\t"
                   "v_accvgpr_read_b32 %1, a89\n\tv_mul_f32 %1, %2, %1\n\tv_accvgpr_write_b32 a89, %1\n\t"
                   "v_accvgpr_read_b32 %0, a90\n\tv_mul_f32 %0, %2, %0\n\tv_accvgpr_write_b32 a90, %0\n\t"
                   "v_accvgpr_read_b32 %1, a91\n\tv_mul_f32 %1, %2, %1\n\tv_accvgpr_write_b32 a91, %1\n\t"
                   "v_accvgpr_read_b32 %0, a92\n\tv_mul_f32 %0, %2, %0\n\tv_accvgpr_write_b32 a92, %0\n\t"
                   "v_accvgpr_read_b32 %1, a93\n\tv_mul_f32 %1, %2, %1\n\tv_accvgpr_write_b32 a93, %1\n\t"
                   "v_accvgpr_read_b32 %0, a94\n\tv_mul_f32 %0, %2, %0\n\tv_accvgpr_write_b32 a94, %0\n\t"
                   "v_accvgpr_read_b32 %1, a95\n\tv_mul_f32 %1, %2, %1\n\tv_accvgpr_write_b32 a95, %1\n\t"
                   "v_accvgpr_read_b32 %0, a96\n\tv_mul_f32 %0, %2, %0\n\tv_accvgpr_write_b32 a96, %0\n\t"
                   "v_accvgpr_read_b32 %1, a97\n\tv_mul_f32 %1, %2, %1\n\tv_accvgpr_write_b32 a97, %1\n\t"
                   "v_accvgpr_read_b32 %0, a98\n\tv_mul_f32 %0, %2, %0\n\tv_accvgpr_write_b32 a98, %0\n\t"
                   "v_accvgpr_read_b32 %1, a99\n\tv_mul_f32 %1, %2, %1\n\tv_accvgpr_write_b32 a99, %1\n\t"
                   "v_accvgpr_read_b32 %0, a100\n\tv_mul_f32 %0, %2, %0\n\tv_accvgpr_write_b32 a100, %0\n\t"
                   "v_accvgpr_read_b32 %1, a101\n\tv_mul_f32 %1, %2, %1\n\tv_accvgpr_write_b32 a101, %1\n\t"
                   "v_accvgpr_read_b32 %0, a102\n\tv_mul_f32 %0, %2, %0\n\tv_accvgpr_write_b32 a102, %0\n\t"
                   "v_accvgpr_read_b32 %1, a103\n\tv_mul_f32 %1, %2, %1\n\tv_accvgpr_write_b32 a103, %1\n\t"
                   "v_accvgpr_read_b32 %0, a104\n\tv_mul_f32 %0, %2, %0\n\tv_accvgpr_write_b32 a104, %0\n\t"
                   "v_accvgpr_read_b32 %1, a105\n\tv_mul_f32 %1, %2, %1\n\tv_accvgpr_write_b32 a105, %1\n\t"
                   "v_accvgpr_read_b32 %0, a106\n\tv_mul_f32 %0, %2, %0\n\tv_accvgpr_write_b32 a106, %0\n\t"
                   "v_accvgpr_read_b32 %1, a107\n\tv_mul_f32 %1, %2, %1\n\tv_accvgpr_write_b32 a107, %1\n\t"
                   "v_accvgpr_read_b32 %0, a108\n\tv_mul_f32 %0, %2, %0\n\tv_accvgpr_write_b32 a108, %0\n\t"
                   "v_accvgpr_read_b32 %1, a109\n\tv_mul_f32 %1, %2, %1\n\tv_accvgpr_write_b32 a109, %1\n\t"
                   "v_accvgpr_read_b32 %0, a110\n\tv_mul_f32 %0, %2, %0\n\tv_accvgpr_write_b32 a110, %0\n\t"
                   "v_accvgpr_read_b32 %1, a111\n\tv_mul_f32 %1, %2, %1\n\tv_accvgpr_write_b32 a111, %1\n\t"
                   "v_accvgpr_read_b32 %0, a112\n\tv_mul_f32 %0, %2, %0\n\tv_accvgpr_write_b32 a112, %0\n\t"
                   "v_accvgpr_read_b32 %1, a113\n\tv_mul_f32 %1, %2, %1\n\tv_accvgpr_write_b32 a113, %1\n\t"
                   "v_accvgpr_read_b32 %0, a114\n\tv_mul_f32 %0, %2, %0\n\tv_accvgpr_write_b32 a114, %0\n\t"
                   "v_accvgpr_read_b32 %1, a115\n\tv_mul_f32 %1, %2, %1\n\tv_accvgpr_write_b32 a115, %1\n\t"
                   "v_accvgpr_read_b32 %0, a116\n\tv_mul_f32 %0, %2, %0\n\tv_accvgpr_write_b32 a116, %0\n\t"
                   "v_accvgpr_read_b32 %1, a117\n\tv_mul_f32 %1, %2, %1\n\tv_accvgpr_write_b32 a117, %1\n\t"
                   "v_accvgpr_read_b32 %0, a118\n\tv_mul_f32 %0, %2, %0\n\tv_accvgpr_write_b32 a118, %0\n\t"
                   "v_accvgpr_read_b32 %1, a119\n\tv_mul_f32 %1, %2, %1\n\tv_accvgpr_write_b32 a119, %1\n\t"
                   "v_accvgpr_read_b32 %0, a120\n\tv_mul_f32 %0, %2, %0\n\tv_accvgpr_write_b32 a120, %0\n\t"
                   "v_accvgpr_read_b32 %1, a121\n\tv_mul_f32 %1, %2, %1\n\tv_accvgpr_write_b32 a121, %1\n\t"
                   "v_accvgpr_read_b32 %0, a122\n\tv_mul_f32 %0, %2, %0\n\tv_accvgpr_write_b32 a122, %0\n\t"
                   "v_accvgpr_read_b32 %1, a123\n\tv_mul_f32 %1, %2, %1\n\tv_accvgpr_write_b32 a123, %1\n\t"
                   "v_accvgpr_read_b32 %0, a124\n\tv_mul_f32 %0, %2, %0\n\tv_accvgpr_write_b32 a124, %0\n\t"
                   "v_accvgpr_read_b32 %1, a125\n\tv_mul_f32 %1, %2, %1\n\tv_accvgpr_write_b32 a125, %1\n\t"
                   "v_accvgpr_read_b32 %0, a126\n\tv_mul_f32 %0, %2, %0\n\tv_accvgpr_write_b32 a126, %0\n\t"
                   "v_accvgpr_read_b32 %1, a127\n\tv_mul_f32 %1, %2, %1\n\tv_accvgpr_write_b32 a127, %1\n\t"
                   "v_accvgpr_read_b32 %0, a128\n\tv_mul_f32 %0, %3, %0\n\tv_accvgpr_write_b32 a128, %0\n\t"
                   "v_accvgpr_read_b32 %1, a129\n\tv_mul_f32 %1, %3, %1\n\tv_accvgpr_write_b32 a129, %1\n\t"
                   "v_accvgpr_read_b32 %0, a130\n\tv_mul_f32 %0, %3, %0\n\tv_accvgpr_write_b32 a130, %0\n\t"
                   "v_accvgpr_read_b32 %1, a131\n\tv_mul_f32 %1, %3, %1\n\tv_accvgpr_write_b32 a131, %1\n\t"
                   "v_accvgpr_read_b32 %0, a132\n\tv_mul_f32 %0, %3, %0\n\tv_accvgpr_write_b32 a132, %0\n\t"
                   "v_accvgpr_read_b32 %1, a133\n\tv_mul_f32 %1, %3, %1\n\tv_accvgpr_write_b32 a133, %1\n\t"
                   "v_accvgpr_read_b32 %0, a134\n\tv_mul_f32 %0, %3, %0\n\tv_accvgpr_write_b32 a134, %0\n\t"
                   "v_accvgpr_read_b32 %1, a135\n\tv_mul_f32 %1, %3, %1\n\tv_accvgpr_write_b32 a135, %1\n\t"
                   "v_accvgpr_read_b32 %0, a136\n\tv_mul_f32 %0, %3, %0\n\tv_accvgpr_write_b32 a136, %0\n\t"
                   "v_accvgpr_read_b32 %1, a137\n\tv_mul_f32 %1, %3, %1\n\tv_accvgpr_write_b32 a137, %1\n\t"
                   "v_accvgpr_read_b32 %0, a138\n\tv_mul_f32 %0, %3, %0\n\tv_accvgpr_write_b32 a138, %0\n\t"
                   "v_accvgpr_read_b32 %1, a139\n\tv_mul_f32 %1, %3, %1\n\tv_accvgpr_write_b32 a139, %1\n\t"
                   "v_accvgpr_read_b32 %0, a140\n\tv_mul_f32 %0, %3, %0\n\tv_accvgpr_write_b32 a140, %0\n\t"
                   "v_accvgpr_read_b32 %1, a141\n\tv_mul_f32 %1, %3, %1\n\tv_accvgpr_write_b32 a141, %1\n\t"
                   "v_accvgpr_read_b32 %0, a142\n\tv_mul_f32 %0, %3, %0\n\tv_accvgpr_write_b32 a142, %0\n\t"
                   "v_accvgpr_read_b32 %1, a143\n\tv_mul_f32 %1, %3, %1\n\tv_accvgpr_write_b32 a143, %1\n\t"
                   "v_accvgpr_read_b32 %0, a144\n\tv_mul_f32 %0, %3, %0\n\tv_accvgpr_write_b32 a144, %0\n\t"
                   "v_accvgpr_read_b32 %1, a145\n\tv_mul_f32 %1, %3, %1\n\tv_accvgpr_write_b32 a145, %1\n\t"
                   "v_accvgpr_read_b32 %0, a146\n\tv_mul_f32 %0, %3, %0\n\tv_accvgpr_write_b32 a146, %0\n\t"
                   "v_accvgpr_read_b32 %1, a147\n\tv_mul_f32 %1, %3, %1\n\tv_accvgpr_write_b32 a147, %1\n\t"
                   "v_accvgpr_read_b32 %0, a148\n\tv_mul_f32 %0, %3, %0\n\tv_accvgpr_write_b32 a148, %0\n\t"
                   "v_accvgpr_read_b32 %1, a149\n\tv_mul_f32 %1, %3, %1\n\tv_accvgpr_write_b32 a149, %1\n\t"
                   "v_accvgpr_read_b32 %0, a150\n\tv_mul_f32 %0, %3, %0\n\tv_accvgpr_write_b32 a150, %0\n\t"
                   "v_accvgpr_read_b32 %1, a151\n\tv_mul_f32 %1, %3, %1\n\tv_accvgpr_write_b32 a151, %1\n\t"
                   "v_accvgpr_read_b32 %0, a152\n\tv_mul_f32 %0, %3, %0\n\tv_accvgpr_write_b32 a152, %0\n\t"
                   "v_accvgpr_read_b32 %1, a153\n\tv_mul_f32 %1, %3, %1\n\tv_accvgpr_write_b32 a153, %1\n\t"
                   "v_accvgpr_read_b32 %0, a154\n\tv_mul_f32 %0, %3, %0\n\tv_accvgpr_write_b32 a154, %0\n\t"
                   "v_accvgpr_read_b32 %1, a155\n\tv_mul_f32 %1, %3, %1\n\tv_accvgpr_write_b32 a155, %1\n\t"
                   "v_accvgpr_read_b32 %0, a156\n\tv_mul_f32 %0, %3, %0\n\tv_accvgpr_write_b32 a156, %0\n\t"
                   "v_accvgpr_read_b32 %1, a157\n\tv_mul_f32 %1, %3, %1\n\tv_accvgpr_write_b32 a157, %1\n\t"
                   "v_accvgpr_read_b32 %0, a158\n\tv_mul_f32 %0, %3, %0\n\tv_accvgpr_write_b32 a158, %0\n\t"
                   "v_accvgpr_read_b32 %1, a159\n\tv_mul_f32 %1, %3, %1\n\tv_accvgpr_write_b32 a159, %1\n\t"
                   "v_accvgpr_read_b32 %0, a160\n\tv_mul_f32 %0, %3, %0\n\tv_accvgpr_write_b32 a160, %0\n\t"
                   "v_accvgpr_read_b32 %1, a161\n\tv_mul_f32 %1, %3, %1\n\tv_accvgpr_write_b32 a161, %1\n\t"
                   "v_accvgpr_read_b32 %0, a162\n\tv_mul_f32 %0, %3, %0\n\tv_accvgpr_write_b32 a162, %0\n\t"
                   "v_accvgpr_read_b32 %1, a163\n\tv_mul_f32 %1, %3, %1\n\tv_accvgpr_write_b32 a163, %1\n\t"
                   "v_accvgpr_read_b32 %0, a164\n\tv_mul_f32 %0, %3, %0\n\tv_accvgpr_write_b32 a164, %0\n\t"
                   "v_accvgpr_read_b32 %1, a165\n\tv_mul_f32 %1, %3, %1\n\tv_accvgpr_write_b32 a165, %1\n\t"
                   "v_accvgpr_read_b32 %0, a166\n\tv_mul_f32 %0, %3, %0\n\tv_accvgpr_write_b32 a166, %0\n\t"
                   "v_accvgpr_read_b32 %1, a167\n\tv_mul_f32 %1, %3, %1\n\tv_accvgpr_write_b32 a167, %1\n\t"
                   "v_accvgpr_read_b32 %0, a168\n\tv_mul_f32 %0, %3, %0\n\tv_accvgpr_write_b32 a168, %0\n\t"
                   "v_accvgpr_read_b32 %1, a169\n\tv_mul_f32 %1, %3, %1\n\tv_accvgpr_write_b32 a169, %1\n\t"
                   "v_accvgpr_read_b32 %0, a170\n\tv_mul_f32 %0, %3, %0\n\tv_accvgpr_write_b32 a170, %0\n\t"
                   "v_accvgpr_read_b32 %1, a171\n\tv_mul_f32 %1, %3, %1\n\tv_accvgpr_write_b32 a171, %1\n\t"
                   "v_accvgpr_read_b32 %0, a172\n\tv_mul_f32 %0, %3, %0\n\tv_accvgpr_write_b32 a172, %0\n\t"
                   "v_accvgpr_read_b32 %1, a173\n\tv_mul_f32 %1, %3, %1\n\tv_accvgpr_write_b32 a173, %1\n\t"
                   "v_accvgpr_read_b32 %0, a174\n\tv_mul_f32 %0, %3, %0\n\tv_accvgpr_write_b32 a174, %0\n\t"
                   "v_accvgpr_read_b32 %1, a175\n\tv_mul_f32 %1, %3, %1\n\tv_accvgpr_write_b32 a175, %1\n\t"
                   "v_accvgpr_read_b32 %0, a176\n\tv_mul_f32 %0, %3, %0\n\tv_accvgpr_write_b32 a176, %0\n\t"
                   "v_accvgpr_read_b32 %1, a177\n\tv_mul_f32 %1, %3, %1\n\tv_accvgpr_write_b32 a177, %1\n\t"
                   "v_accvgpr_read_b32 %0, a178\n\tv_mul_f32 %0, %3, %0\n\tv_accvgpr_write_b32 a178, %0\n\t"
                   "v_accvgpr_read_b32 %1, a179\n\tv_mul_f32 %1, %3, %1\n\tv_accvgpr_write_b32 a179, %1\n\t"
                   "v_accvgpr_read_b32 %0, a180\n\tv_mul_f32 %0, %3, %0\n\tv_accvgpr_write_b32 a180, %0\n\t"
                   "v_accvgpr_read_b32 %1, a181\n\tv_mul_f32 %1, %3, %1\n\tv_accvgpr_write_b32 a181, %1\n\t"
                   "v_accvgpr_read_b32 %0, a182\n\tv_mul_f32 %0, %3, %0\n\tv_accvgpr_write_b32 a182, %0\n\t"
                   "v_accvgpr_read_b32 %1, a183\n\tv_mul_f32 %1, %3, %1\n\tv_accvgpr_write_b32 a183, %1\n\t"
                   "v_accvgpr_read_b32 %0, a184\n\tv_mul_f32 %0, %3, %0\n\tv_accvgpr_write_b32 a184, %0\n\t"
                   "v_accvgpr_read_b32 %1, a185\n\tv_mul_f32 %1, %3, %1\n\tv_accvgpr_write_b32 a185, %1\n\t"
                   "v_accvgpr_read_b32 %0, a186\n\tv_mul_f32 %0, %3, %0\n\tv_accvgpr_write_b32 a186, %0\n\t"
                   "v_accvgpr_read_b32 %1, a187\n\tv_mul_f32 %1, %3, %1\n\tv_accvgpr_write_b32 a187, %1\n\t"
                   "v_accvgpr_read_b32 %0, a188\n\tv_mul_f32 %0, %3, %0\n\tv_accvgpr_write_b32 a188, %0\n\t"
                   "v_accvgpr_read_b32 %1, a189\n\tv_mul_f32 %1, %3, %1\n\tv_accvgpr_write_b32 a189, %1\n\t"
                   "v_accvgpr_read_b32 %0, a190\n\tv_mul_f32 %0, %3, %0\n\tv_accvgpr_write_b32 a190, %0\n\t"
                   "v_accvgpr_read_b32 %1, a191\n\tv_mul_f32 %1, %3, %1\n\tv_accvgpr_write_b32 a191, %1\n\t"
                   "v_accvgpr_read_b32 %0, a192\n\tv_mul_f32 %0, %3, %0\n\tv_accvgpr_write_b32 a192, %0\n\t"
                   "v_accvgpr_read_b32 %1, a193\n\tv_mul_f32 %1, %3, %1\n\tv_accvgpr_write_b32 a193, %1\n\t"
                   "v_accvgpr_read_b32 %0, a194\n\tv_mul_f32 %0, %3, %0\n\tv_accvgpr_write_b32 a194, %0\n\t"
                   "v_accvgpr_read_b32 %1, a195\n\tv_mul_f32 %1, %3, %1\n\tv_accvgpr_write_b32 a195, %1\n\t"
                   "v_accvgpr_read_b32 %0, a196\n\tv_mul_f32 %0, %3, %0\n\tv_accvgpr_write_b32 a196, %0\n\t"
                   "v_accvgpr_read_b32 %1, a197\n\tv_mul_f32 %1, %3, %1\n\tv_accvgpr_write_b32 a197, %1\n\t"
                   "v_accvgpr_read_b32 %0, a198\n\tv_mul_f32 %0, %3, %0\n\tv_accvgpr_write_b32 a198, %0\n\t"
                   "v_accvgpr_read_b32 %1, a199\n\tv_mul_f32 %1, %3, %1\n\tv_accvgpr_write_b32 a199, %1\n\t"
                   "v_accvgpr_read_b32 %0, a200\n\tv_mul_f32 %0, %3, %0\n\tv_accvgpr_write_b32 a200, %0\n\t"
                   "v_accvgpr_read_b32 %1, a201\n\tv_mul_f32 %1, %3, %1\n\tv_accvgpr_write_b32 a201, %1\n\t"
                   "v_accvgpr_read_b32 %0, a202\n\tv_mul_f32 %0, %3, %0\n\tv_accvgpr_write_b32 a202, %0\n\t"
                   "v_accvgpr_read_b32 %1, a203\n\tv_mul_f32 %1, %3, %1\n\tv_accvgpr_write_b32 a203, %1\n\t"
                   "v_accvgpr_read_b32 %0, a204\n\tv_mul_f32 %0, %3, %0\n\tv_accvgpr_write_b32 a204, %0\n\t"
                   "v_accvgpr_read_b32 %1, a205\n\tv_mul_f32 %1, %3, %1\n\tv_accvgpr_write_b32 a205, %1\n\t"
                   "v_accvgpr_read_b32 %0, a206\n\tv_mul_f32 %0, %3, %0\n\tv_accvgpr_write_b32 a206, %0\n\t"
                   "v_accvgpr_read_b32 %1, a207\n\tv_mul_f32 %1, %3, %1\n\tv_accvgpr_write_b32 a207, %1\n\t"
                   "v_accvgpr_read_b32 %0, a208\n\tv_mul_f32 %0, %3, %0\n\tv_accvgpr_write_b32 a208, %0\n\t"
                   "v_accvgpr_read_b32 %1, a209\n\tv_mul_f32 %1, %3, %1\n\tv_accvgpr_write_b32 a209, %1\n\t"
                   "v_accvgpr_read_b32 %0, a210\n\tv_mul_f32 %0, %3, %0\n\tv_accvgpr_write_b32 a210, %0\n\t"
                   "v_accvgpr_read_b32 %1, a211\n\tv_mul_f32 %1, %3, %1\n\tv_accvgpr_write_b32 a211, %1\n\t"
                   "v_accvgpr_read_b32 %0, a212\n\tv_mul_f32 %0, %3, %0\n\tv_accvgpr_write_b32 a212, %0\n\t"
                   "v_accvgpr_read_b32 %1, a213\n\tv_mul_f32 %1, %3, %1\n\tv_accvgpr_write_b32 a213, %1\n\t"
                   "v_accvgpr_read_b32 %0, a214\n\tv_mul_f32 %0, %3, %0\n\tv_accvgpr_write_b32 a214, %0\n\t"
                   "v_accvgpr_read_b32 %1, a215\n\tv_mul_f32 %1, %3, %1\n\tv_accvgpr_write_b32 a215, %1\n\t"
                   "v_accvgpr_read_b32 %0, a216\n\tv_mul_f32 %0, %3, %0\n\tv_accvgpr_write_b32 a216, %0\n\t"
                   "v_accvgpr_read_b32 %1, a217\n\tv_mul_f32 %1, %3, %1\n\tv_accvgpr_write_b32 a217, %1\n\t"
                   "v_accvgpr_read_b32 %0, a218\n\tv_mul_f32 %0, %3, %0\n\tv_accvgpr_write_b32 a218, %0\n\t"
                   "v_accvgpr_read_b32 %1, a219\n\tv_mul_f32 %1, %3, %1\n\tv_accvgpr_write_b32 a219, %1\n\t"
                   "v_accvgpr_read_b32 %0, a220\n\tv_mul_f32 %0, %3, %0\n\tv_accvgpr_write_b32 a220, %0\n\t"
                   "v_accvgpr_read_b32 %1, a221\n\tv_mul_f32 %1, %3, %1\n\tv_accvgpr_write_b32 a221, %1\n\t"
                   "v_accvgpr_read_b32 %0, a222\n\tv_mul_f32 %0, %3, %0\n\tv_accvgpr_write_b32 a222, %0\n\t"
                   "v_accvgpr_read_b32 %1, a223\n\tv_mul_f32 %1, %3, %1\n\tv_accvgpr_write_b32 a223, %1\n\t"
                   "v_accvgpr_read_b32 %0, a224\n\tv_mul_f32 %0, %3, %0\n\tv_accvgpr_write_b32 a224, %0\n\t"
                   "v_accvgpr_read_b32 %1, a225\n\tv_mul_f32 %1, %3, %1\n\tv_accvgpr_write_b32 a225, %1\n\t"
                   "v_accvgpr_read_b32 %0, a226\n\tv_mul_f32 %0, %3, %0\n\tv_accvgpr_write_b32 a226, %0\n\t"
                   "v_accvgpr_read_b32 %1, a227\n\tv_mul_f32 %1, %3, %1\n\tv_accvgpr_write_b32 a227, %1\n\t"
                   "v_accvgpr_read_b32 %0, a228\n\tv_mul_f32 %0, %3, %0\n\tv_accvgpr_write_b32 a228, %0\n\t"
                   "v_accvgpr_read_b32 %1, a229\n\tv_mul_f32 %1, %3, %1\n\tv_accvgpr_write_b32 a229, %1\n\t"
                   "v_accvgpr_read_b32 %0, a230\n\tv_mul_f32 %0, %3, %0\n\tv_accvgpr_write_b32 a230, %0\n\t"
                   "v_accvgpr_read_b32 %1, a231\n\tv_mul_f32 %1, %3, %1\n\tv_accvgpr_write_b32 a231, %1\n\t"
                   "v_accvgpr_read_b32 %0, a232\n\tv_mul_f32 %0, %3, %0\n\tv_accvgpr_write_b32 a232, %0\n\t"
                   "v_accvgpr_read_b32 %1, a233\n\tv_mul_f32 %1, %3, %1\n\tv_accvgpr_write_b32 a233, %1\n\t"
                   "v_accvgpr_read_b32 %0, a234\n\tv_mul_f32 %0, %3, %0\n\tv_accvgpr_write_b32 a234, %0\n\t"
                   "v_accvgpr_read_b32 %1, a235\n\tv_mul_f32 %1, %3, %1\n\tv_accvgpr_write_b32 a235, %1\n\t"
                   "v_accvgpr_read_b32 %0, a236\n\tv_mul_f32 %0, %3, %0\n\tv_accvgpr_write_b32 a236, %0\n\t"
                   "v_accvgpr_read_b32 %1, a237\n\tv_mul_f32 %1, %3, %1\n\tv_accvgpr_write_b32 a237, %1\n\t"
                   "v_accvgpr_read_b32 %0, a238\n\tv_mul_f32 %0, %3, %0\n\tv_accvgpr_write_b32 a238, %0\n\t"
                   "v_accvgpr_read_b32 %1, a239\n\tv_mul_f32 %1, %3, %1\n\tv_accvgpr_write_b32 a239, %1\n\t"
                   "v_accvgpr_read_b32 %0, a240\n\tv_mul_f32 %0, %3, %0\n\tv_accvgpr_write_b32 a240, %0\n\t"
                   "v_accvgpr_read_b32 %1, a241\n\tv_mul_f32 %1, %3, %1\n\tv_accvgpr_write_b32 a241, %1\n\t"
                   "v_accvgpr_read_b32 %0, a242\n\tv_mul_f32 %0, %3, %0\n\tv_accvgpr_write_b32 a242, %0\n\t"
                   "v_accvgpr_read_b32 %1, a243\n\tv_mul_f32 %1, %3, %1\n\tv_accvgpr_write_b32 a243, %1\n\t"
                   "v_accvgpr_read_b32 %0, a244\n\tv_mul_f32 %0, %3, %0\n\tv_accvgpr_write_b32 a244, %0\n\t"
                   "v_accvgpr_read_b32 %1, a245\n\tv_mul_f32 %1, %3, %1\n\tv_accvgpr_write_b32 a245, %1\n\t"
                   "v_accvgpr_read_b32 %0, a246\n\tv_mul_f32 %0, %3, %0\n\tv_accvgpr_write_b32 a246, %0\n\t"
                   "v_accvgpr_read_b32 %1, a247\n\tv_mul_f32 %1, %3, %1\n\tv_accvgpr_write_b32 a247, %1\n\t"
                   "v_accvgpr_read_b32 %0, a248\n\tv_mul_f32 %0, %3, %0\n\tv_accvgpr_write_b32 a248, %0\n\t"
                   "v_accvgpr_read_b32 %1, a249\n\tv_mul_f32 %1, %3, %1\n\tv_accvgpr_write_b32 a249, %1\n\t"
                   "v_accvgpr_read_b32 %0, a250\n\tv_mul_f32 %0, %3, %0\n\tv_accvgpr_write_b32 a250, %0\n\t"
                   "v_accvgpr_read_b32 %1, a251\n\tv_mul_f32 %1, %3, %1\n\tv_accvgpr_write_b32 a251, %1\n\t"
                   "v_accvgpr_read_b32 %0, a252\n\tv_mul_f32 %0, %3, %0\n\tv_accvgpr_write_b32 a252, %0\n\t"
                   "v_accvgpr_read_b32 %1, a253\n\tv_mul_f32 %1, %3, %1\n\tv_accvgpr_write_b32 a253, %1\n\t"
                   "v_accvgpr_read_b32 %0, a254\n\tv_mul_f32 %0, %3, %0\n\tv_accvgpr_write_b32 a254, %0\n\t"
                   "v_accvgpr_read_b32 %1, a255\n\tv_mul_f32 %1, %3, %1\n\tv_accvgpr_write_b32 a255, %1\n\t"
                   "s_nop 1"
                   : "=&v"(t0), "=&v"(t1) : "v"(c.fct[0]), "v"(c.fct[1]) : ACLOB);
      c.pend = false;
    }
  }
  asm volatile("s_nop 1" ::: "memory");
  GAP();
}
template <int T, bool HASPV, bool HASSM> DI void phaseB5(Ctx4& c) {
  phaseB5_<T, HASPV, HASSM>(c, std::make_integer_sequence<int, 32>{});
}

__global__ __launch_bounds__(256, 1) void attn5_kernel(const u16* __restrict__ qfrag, const u16* __restrict__ kc,
                                                       const u16* __restrict__ vc, const u16* __restrict__ wo,
                                                       u16* __restrict__ o, u16* __restrict__ part,
                                                       float* __restrict__ mlg, unsigned* __restrict__ cnt DBGARG) {
  extern __shared__ __attribute__((aligned(16))) char lds[];
  STAMP(0);
  const int tid = threadIdx.x, lane = tid & 63;
  const int w = __builtin_amdgcn_readfirstlane(tid >> 6);
  const int r = lane & 31, h = lane >> 5;
  const int bid = blockIdx.x;
  const int xcd = bid & 7, j = bid >> 3;
  const int grp = (j >> 2) * 8 + xcd, sp = j & 3;
  const int b = grp >> 3, kvh = grp & 7;

  const u16* Kw = kc + ((size_t)(b * HKV + kvh) * SK + 256 * sp) * DH;
  const u16* Vw = vc + ((size_t)(b * HKV + kvh) * SK + 256 * sp) * DH;

  Ctx4 c;
  c.pfd = 0;
  c.ring = lds + w * 2048;
  c.rk = __builtin_amdgcn_make_buffer_rsrc((void*)Kw, 0, 256 * DH * 2, 0x00020000);
  c.rv = __builtin_amdgcn_make_buffer_rsrc((void*)Vw, 0, 256 * DH * 2, 0x00020000);
#pragma unroll
  for (int pi = 0; pi < 2; ++pi) {
    const int krow = 4 * (2 * w + pi) + (lane >> 4);
    c.koff[pi] = krow * 512 + (((lane & 15) ^ (krow & 15)) * 16);
    const int vrow = 2 * (2 * w + pi) + (lane >> 5);
    c.voff[pi] = vrow * 512 + (((lane & 31) ^ ((vrow & 3) << 2)) * 16);
  }
  {
    const u16* qp = qfrag + ((size_t)(grp * 8 + 2 * w) * 16 * 64 + lane) * 8;
#pragma unroll
    for (int qb = 0; qb < 2; ++qb)
#pragma unroll
      for (int s = 0; s < 16; ++s) c.qf[qb][s] = *(const bf16x8*)(qp + (size_t)(qb * 16 + s) * 512);
  }
  GAP();
  dma5_unit<0>(c); dma5_unit<1>(c); dma5_unit<2>(c); dma5_unit<3>(c); dma5_unit<4>(c); dma5_unit<5>(c); dma5_unit<6>(c);
  dma5_unit<7>(c); dma5_unit<8>(c); dma5_unit<9>(c); dma5_unit<10>(c); dma5_unit<11>(c); dma5_unit<12>(c); dma5_unit<13>(c);
#pragma unroll
  for (int qb = 0; qb < 2; ++qb)
#pragma unroll
    for (int s = 0; s < 16; ++s) asm volatile("" : "+v"(c.qf[qb][s]));
  if (w == 3) {
#pragma unroll
    for (int s = 0; s < 16; ++s) c.qf[1][s] = bf16x8{0, 0, 0, 0, 0, 0, 0, 0};
  }
  STAMP(1);
  {
    const unsigned rb = (unsigned)(size_t)lds;
#pragma unroll
    for (int ss = 0; ss < 8; ++ss) c.kaddr[ss] = rb + r * 256 + ((((2 * ss + h) ^ (r & 15))) * 16);
    const int g = lane >> 4, qq = (lane >> 2) & 3, pp = lane & 3;
#pragma unroll
    for (int d = 0; d < 4; ++d)
      c.vaddr[d] = rb + (4 * (g >> 1) + qq) * 512 + ((64 * d + 32 * (g & 1) + 8 * pp) ^ (qq << 6));
  }
  c.pend = false;
  c.fct[0] = 1.f; c.fct[1] = 1.f;

  phaseA5<0>(c); phaseB5<0, false, true>(c); STAMP(2);
  phaseA5<1>(c); phaseB5<1, true, true>(c); STAMP(3);
  phaseA5<2>(c); phaseB5<2, true, true>(c); STAMP(4);
  phaseA5<3>(c); phaseB5<3, true, true>(c); STAMP(5);
  phaseA5<4>(c); phaseB5<4, true, true>(c); STAMP(6);
  phaseA5<5>(c); phaseB5<5, true, true>(c); STAMP(7);
  phaseA5<6>(c); phaseB5<6, true, true>(c); STAMP(8);
  {
    int li = (bid * 4 + w) * 28 + (lane < 28 ? lane : 27);
    asm volatile("global_load_dword %0, %1, off" : "+v"(c.pfd) : "v"(wo + (size_t)li * 64) : "memory");
  }
  phaseA5<7>(c); phaseB5<7, true, true>(c); STAMP(9);
  phaseB5<8, true, false>(c);
  asm volatile("s_nop 7\n\ts_nop 7" :: "v"(c.pfd) : "memory");
  STAMP(13);

  float lt[2], inv[2];
#pragma unroll
  for (int qb = 0; qb < 2; ++qb) {
    auto rr = __builtin_amdgcn_permlane32_swap(__float_as_uint(c.l[qb]), __float_as_uint(c.l[qb]), false, false);
    lt[qb] = __uint_as_float(rr[0]) + __uint_as_float(rr[1]);
    inv[qb] = 1.f / lt[qb];
  }
  __amdgpu_buffer_rsrc_t rml = __builtin_amdgcn_make_buffer_rsrc((void*)(mlg + (size_t)grp * 4 * 512), 0, 4 * 512 * 4, 0x00020000);
  __amdgpu_buffer_rsrc_t rpt = __builtin_amdgcn_make_buffer_rsrc((void*)(part + (size_t)grp * 16 * 16384), 0, 16 * 32768, 0x00020000);
  if (h == 0) {
#pragma unroll
    for (int qb = 0; qb < 2; ++qb) {
      u32x2 v = {__float_as_uint(c.m[qb]), __float_as_uint(lt[qb])};
      __builtin_amdgcn_raw_buffer_store_b64(v, rml, (sp * 256 + 64 * w + 32 * qb + r) * 8, 0, 16);
    }
  }
  switch (sp) {
    case 0: {
      { f32x4 a = acc_read4<32>(), b2 = acc_read4<36>(); a *= inv[0]; b2 *= inv[0];
        u32x4 v = {cvtpk(a[0], a[1]), cvtpk(a[2], a[3]), cvtpk(b2[0], b2[1]), cvtpk(b2[2], b2[3])};
        __builtin_amdgcn_raw_buffer_store_b128(v, rpt, 32768 + w * 8192 + 0 + lane * 16, 0, 16); }
      { f32x4 a = acc_read4<40>(), b2 = acc_read4<44>(); a *= inv[0]; b2 *= inv[0];
        u32x4 v = {cvtpk(a[0], a[1]), cvtpk(a[2], a[3]), cvtpk(b2[0], b2[1]), cvtpk(b2[2], b2[3])};
        __builtin_amdgcn_raw_buffer_store_b128(v, rpt, 32768 + w * 8192 + 1024 + lane * 16, 0, 16); }
      { f32x4 a = acc_read4<48>(), b2 = acc_read4<52>(); a *= inv[0]; b2 *= inv[0];
        u32x4 v = {cvtpk(a[0], a[1]), cvtpk(a[2], a[3]), cvtpk(b2[0], b2[1]), cvtpk(b2[2], b2[3])};
        __builtin_amdgcn_raw_buffer_store_b128(v, rpt, 32768 + w * 8192 + 2048 + lane * 16, 0, 16); }
      { f32x4 a = acc_read4<56>(), b2 = acc_read4<60>(); a *= inv[0]; b2 *= inv[0];
        u32x4 v = {cvtpk(a[0], a[1]), cvtpk(a[2], a[3]), cvtpk(b2[0], b2[1]), cvtpk(b2[2], b2[3])};
        __builtin_amdgcn_raw_buffer_store_b128(v, rpt, 32768 + w * 8192 + 3072 + lane * 16, 0, 16); }
      { f32x4 a = acc_read4<160>(), b2 = acc_read4<164>(); a *= inv[1]; b2 *= inv[1];
        u32x4 v = {cvtpk(a[0], a[1]), cvtpk(a[2], a[3]), cvtpk(b2[0], b2[1]), cvtpk(b2[2], b2[3])};
        __builtin_amdgcn_raw_buffer_store_b128(v, rpt, 32768 + w * 8192 + 4096 + lane * 16, 0, 16); }
      { f32x4 a = acc_read4<168>(), b2 = acc_read4<172>(); a *= inv[1]; b2 *= inv[1];
        u32x4 v = {cvtpk(a[0], a[1]), cvtpk(a[2], a[3]), cvtpk(b2[0], b2[1]), cvtpk(b2[2], b2[3])};
        __builtin_amdgcn_raw_buffer_store_b128(v, rpt, 32768 + w * 8192 + 5120 + lane * 16, 0, 16); }
      { f32x4 a = acc_read4<176>(), b2 = acc_read4<180>(); a *= inv[1]; b2 *= inv[1];
        u32x4 v = {cvtpk(a[0], a[1]), cvtpk(a[2], a[3]), cvtpk(b2[0], b2[1]), cvtpk(b2[2], b2[3])};
        __builtin_amdgcn_raw_buffer_store_b128(v, rpt, 32768 + w * 8192 + 6144 + lane * 16, 0, 16); }
      { f32x4 a = acc_read4<184>(), b2 = acc_read4<188>(); a *= inv[1]; b2 *= inv[1];
        u32x4 v = {cvtpk(a[0], a[1]), cvtpk(a[2], a[3]), cvtpk(b2[0], b2[1]), cvtpk(b2[2], b2[3])};
        __builtin_amdgcn_raw_buffer_store_b128(v, rpt, 32768 + w * 8192 + 7168 + lane * 16, 0, 16); }
      { f32x4 a = acc_read4<64>(), b2 = acc_read4<68>(); a *= inv[0]; b2 *= inv[0];
        u32x4 v = {cvtpk(a[0], a[1]), cvtpk(a[2], a[3]), cvtpk(b2[0], b2[1]), cvtpk(b2[2], b2[3])};
        __builtin_amdgcn_raw_buffer_store_b128(v, rpt, 65536 + w * 8192 + 0 + lane * 16, 0, 16); }
      { f32x4 a = acc_read4<72>(), b2 = acc_read4<76>(); a *= inv[0]; b2 *= inv[0];
        u32x4 v = {cvtpk(a[0], a[1]), cvtpk(a[2], a[3]), cvtpk(b2[0], b2[1]), cvtpk(b2[2], b2[3])};
        __builtin_amdgcn_raw_buffer_store_b128(v, rpt, 65536 + w * 8192 + 1024 + lane * 16, 0, 16); }
      { f32x4 a = acc_read4<80>(), b2 = acc_read4<84>(); a *= inv[0]; b2 *= inv[0];
        u32x4 v = {cvtpk(a[0], a[1]), cvtpk(a[2], a[3]), cvtpk(b2[0], b2[1]), cvtpk(b2[2], b2[3])};
        __builtin_amdgcn_raw_buffer_store_b128(v, rpt, 65536 + w * 8192 + 2048 + lane * 16, 0, 16); }
      { f32x4 a = acc_read4<88>(), b2 = acc_read4<92>(); a *= inv[0]; b2 *= inv[0];
        u32x4 v = {cvtpk(a[0], a[1]), cvtpk(a[2], a[3]), cvtpk(b2[0], b2[1]), cvtpk(b2[2], b2[3])};
        __builtin_amdgcn_raw_buffer_store_b128(v, rpt, 65536 + w * 8192 + 3072 + lane * 16, 0, 16); }
      { f32x4 a = acc_read4<192>(), b2 = acc_read4<196>(); a *= inv[1]; b2 *= inv[1];
        u32x4 v = {cvtpk(a[0], a[1]), cvtpk(a[2], a[3]), cvtpk(b2[0], b2[1]), cvtpk(b2[2], b2[3])};
        __builtin_amdgcn_raw_buffer_store_b128(v, rpt, 65536 + w * 8192 + 4096 + lane * 16, 0, 16); }
      { f32x4 a = acc_read4<200>(), b2 = acc_read4<204>(); a *= inv[1]; b2 *= inv[1];
        u32x4 v = {cvtpk(a[0], a[1]), cvtpk(a[2], a[3]), cvtpk(b2[0], b2[1]), cvtpk(b2[2], b2[3])};
        __builtin_amdgcn_raw_buffer_store_b128(v, rpt, 65536 + w * 8192 + 5120 + lane * 16, 0, 16); }
      { f32x4 a = acc_read4<208>(), b2 = acc_read4<212>(); a *= inv[1]; b2 *= inv[1];
        u32x4 v = {cvtpk(a[0], a[1]), cvtpk(a[2], a[3]), cvtpk(b2[0], b2[1]), cvtpk(b2[2], b2[3])};
        __builtin_amdgcn_raw_buffer_store_b128(v, rpt, 65536 + w * 8192 + 6144 + lane * 16, 0, 16); }
      { f32x4 a = acc_read4<216>(), b2 = acc_read4<220>(); a *= inv[1]; b2 *= inv[1];
        u32x4 v = {cvtpk(a[0], a[1]), cvtpk(a[2], a[3]), cvtpk(b2[0], b2[1]), cvtpk(b2[2], b2[3])};
        __builtin_amdgcn_raw_buffer_store_b128(v, rpt, 65536 + w * 8192 + 7168 + lane * 16, 0, 16); }
      { f32x4 a = acc_read4<96>(), b2 = acc_read4<100>(); a *= inv[0]; b2 *= inv[0];
        u32x4 v = {cvtpk(a[0], a[1]), cvtpk(a[2], a[3]), cvtpk(b2[0], b2[1]), cvtpk(b2[2], b2[3])};
        __builtin_amdgcn_raw_buffer_store_b128(v, rpt, 98304 + w * 8192 + 0 + lane * 16, 0, 16); }
      { f32x4 a = acc_read4<104>(), b2 = acc_read4<108>(); a *= inv[0]; b2 *= inv[0];
        u32x4 v = {cvtpk(a[0], a[1]), cvtpk(a[2], a[3]), cvtpk(b2[0], b2[1]), cvtpk(b2[2], b2[3])};
        __builtin_amdgcn_raw_buffer_store_b128(v, rpt, 98304 + w * 8192 + 1024 + lane * 16, 0, 16); }
      { f32x4 a = acc_read4<112>(), b2 = acc_read4<116>(); a *= inv[0]; b2 *= inv[0];
        u32x4 v = {cvtpk(a[0], a[1]), cvtpk(a[2], a[3]), cvtpk(b2[0], b2[1]), cvtpk(b2[2], b2[3])};
        __builtin_amdgcn_raw_buffer_store_b128(v, rpt, 98304 + w * 8192 + 2048 + lane * 16, 0, 16); }
      { f32x4 a = acc_read4<120>(), b2 = acc_read4<124>(); a *= inv[0]; b2 *= inv[0];
        u32x4 v = {cvtpk(a[0], a[1]), cvtpk(a[2], a[3]), cvtpk(b2[0], b2[1]), cvtpk(b2[2], b2[3])};
        __builtin_amdgcn_raw_buffer_store_b128(v, rpt, 98304 + w * 8192 + 3072 + lane * 16, 0, 16); }
      { f32x4 a = acc_read4<224>(), b2 = acc_read4<228>(); a *= inv[1]; b2 *= inv[1];
        u32x4 v = {cvtpk(a[0], a[1]), cvtpk(a[2], a[3]), cvtpk(b2[0], b2[1]), cvtpk(b2[2], b2[3])};
        __builtin_amdgcn_raw_buffer_store_b128(v, rpt, 98304 + w * 8192 + 4096 + lane * 16, 0, 16); }
      { f32x4 a = acc_read4<232>(), b2 = acc_read4<236>(); a *= inv[1]; b2 *= inv[1];
        u32x4 v = {cvtpk(a[0], a[1]), cvtpk(a[2], a[3]), cvtpk(b2[0], b2[1]), cvtpk(b2[2], b2[3])};
        __builtin_amdgcn_raw_buffer_store_b128(v, rpt, 98304 + w * 8192 + 5120 + lane * 16, 0, 16); }
      { f32x4 a = acc_read4<240>(), b2 = acc_read4<244>(); a *= inv[1]; b2 *= inv[1];
        u32x4 v = {cvtpk(a[0], a[1]), cvtpk(a[2], a[3]), cvtpk(b2[0], b2[1]), cvtpk(b2[2], b2[3])};
        __builtin_amdgcn_raw_buffer_store_b128(v, rpt, 98304 + w * 8192 + 6144 + lane * 16, 0, 16); }
      { f32x4 a = acc_read4<248>(), b2 = acc_read4<252>(); a *= inv[1]; b2 *= inv[1];
        u32x4 v = {cvtpk(a[0], a[1]), cvtpk(a[2], a[3]), cvtpk(b2[0], b2[1]), cvtpk(b2[2], b2[3])};
        __builtin_amdgcn_raw_buffer_store_b128(v, rpt, 98304 + w * 8192 + 7168 + lane * 16, 0, 16); }
    } break;
    case 1: {
      { f32x4 a = acc_read4<0>(), b2 = acc_read4<4>(); a *= inv[0]; b2 *= inv[0];
        u32x4 v = {cvtpk(a[0], a[1]), cvtpk(a[2], a[3]), cvtpk(b2[0], b2[1]), cvtpk(b2[2], b2[3])};
        __builtin_amdgcn_raw_buffer_store_b128(v, rpt, 131072 + w * 8192 + 0 + lane * 16, 0, 16); }
      { f32x4 a = acc_read4<8>(), b2 = acc_read4<12>(); a *= inv[0]; b2 *= inv[0];
        u32x4 v = {cvtpk(a[0], a[1]), cvtpk(a[2], a[3]), cvtpk(b2[0], b2[1]), cvtpk(b2[2], b2[3])};
        __builtin_amdgcn_raw_buffer_store_b128(v, rpt, 131072 + w * 8192 + 1024 + lane * 16, 0, 16); }
      { f32x4 a = acc_read4<16>(), b2 = acc_read4<20>(); a *= inv[0]; b2 *= inv[0];
        u32x4 v = {cvtpk(a[0], a[1]), cvtpk(a[2], a[3]), cvtpk(b2[0], b2[1]), cvtpk(b2[2], b2[3])};
        __builtin_amdgcn_raw_buffer_store_b128(v, rpt, 131072 + w * 8192 + 2048 + lane * 16, 0, 16); }
      { f32x4 a = acc_read4<24>(), b2 = acc_read4<28>(); a *= inv[0]; b2 *= inv[0];
        u32x4 v = {cvtpk(a[0], a[1]), cvtpk(a[2], a[3]), cvtpk(b2[0], b2[1]), cvtpk(b2[2], b2[3])};
        __builtin_amdgcn_raw_buffer_store_b128(v, rpt, 131072 + w * 8192 + 3072 + lane * 16, 0, 16); }
      { f32x4 a = acc_read4<128>(), b2 = acc_read4<132>(); a *= inv[1]; b2 *= inv[1];
        u32x4 v = {cvtpk(a[0], a[1]), cvtpk(a[2], a[3]), cvtpk(b2[0], b2[1]), cvtpk(b2[2], b2[3])};
        __builtin_amdgcn_raw_buffer_store_b128(v, rpt, 131072 + w * 8192 + 4096 + lane * 16, 0, 16); }
      { f32x4 a = acc_read4<136>(), b2 = acc_read4<140>(); a *= inv[1]; b2 *= inv[1];
        u32x4 v = {cvtpk(a[0], a[1]), cvtpk(a[2], a[3]), cvtpk(b2[0], b2[1]), cvtpk(b2[2], b2[3])};
        __builtin_amdgcn_raw_buffer_store_b128(v, rpt, 131072 + w * 8192 + 5120 + lane * 16, 0, 16); }
      { f32x4 a = acc_read4<144>(), b2 = acc_read4<148>(); a *= inv[1]; b2 *= inv[1];
        u32x4 v = {cvtpk(a[0], a[1]), cvtpk(a[2], a[3]), cvtpk(b2[0], b2[1]), cvtpk(b2[2], b2[3])};
        __builtin_amdgcn_raw_buffer_store_b128(v, rpt, 131072 + w * 8192 + 6144 + lane * 16, 0, 16); }
      { f32x4 a = acc_read4<152>(), b2 = acc_read4<156>(); a *= inv[1]; b2 *= inv[1];
        u32x4 v = {cvtpk(a[0], a[1]), cvtpk(a[2], a[3]), cvtpk(b2[0], b2[1]), cvtpk(b2[2], b2[3])};
        __builtin_amdgcn_raw_buffer_store_b128(v, rpt, 131072 + w * 8192 + 7168 + lane * 16, 0, 16); }
      { f32x4 a = acc_read4<64>(), b2 = acc_read4<68>(); a *= inv[0]; b2 *= inv[0];
        u32x4 v = {cvtpk(a[0], a[1]), cvtpk(a[2], a[3]), cvtpk(b2[0], b2[1]), cvtpk(b2[2], b2[3])};
        __builtin_amdgcn_raw_buffer_store_b128(v, rpt, 196608 + w * 8192 + 0 + lane * 16, 0, 16); }
      { f32x4 a = acc_read4<72>(), b2 = acc_read4<76>(); a *= inv[0]; b2 *= inv[0];
        u32x4 v = {cvtpk(a[0], a[1]), cvtpk(a[2], a[3]), cvtpk(b2[0], b2[1]), cvtpk(b2[2], b2[3])};
        __builtin_amdgcn_raw_buffer_store_b128(v, rpt, 196608 + w * 8192 + 1024 + lane * 16, 0, 16); }
      { f32x4 a = acc_read4<80>(), b2 = acc_read4<84>(); a *= inv[0]; b2 *= inv[0];
        u32x4 v = {cvtpk(a[0], a[1]), cvtpk(a[2], a[3]), cvtpk(b2[0], b2[1]), cvtpk(b2[2], b2[3])};
        __builtin_amdgcn_raw_buffer_store_b128(v, rpt, 196608 + w * 8192 + 2048 + lane * 16, 0, 16); }
      { f32x4 a = acc_read4<88>(), b2 = acc_read4<92>(); a *= inv[0]; b2 *= inv[0];
        u32x4 v = {cvtpk(a[0], a[1]), cvtpk(a[2], a[3]), cvtpk(b2[0], b2[1]), cvtpk(b2[2], b2[3])};
        __builtin_amdgcn_raw_buffer_store_b128(v, rpt, 196608 + w * 8192 + 3072 + lane * 16, 0, 16); }
      { f32x4 a = acc_read4<192>(), b2 = acc_read4<196>(); a *= inv[1]; b2 *= inv[1];
        u32x4 v = {cvtpk(a[0], a[1]), cvtpk(a[2], a[3]), cvtpk(b2[0], b2[1]), cvtpk(b2[2], b2[3])};
        __builtin_amdgcn_raw_buffer_store_b128(v, rpt, 196608 + w * 8192 + 4096 + lane * 16, 0, 16); }
      { f32x4 a = acc_read4<200>(), b2 = acc_read4<204>(); a *= inv[1]; b2 *= inv[1];
        u32x4 v = {cvtpk(a[0], a[1]), cvtpk(a[2], a[3]), cvtpk(b2[0], b2[1]), cvtpk(b2[2], b2[3])};
        __builtin_amdgcn_raw_buffer_store_b128(v, rpt, 196608 + w * 8192 + 5120 + lane * 16, 0, 16); }
      { f32x4 a = acc_read4<208>(), b2 = acc_read4<212>(); a *= inv[1]; b2 *= inv[1];
        u32x4 v = {cvtpk(a[0], a[1]), cvtpk(a[2], a[3]), cvtpk(b2[0], b2[1]), cvtpk(b2[2], b2[3])};
        __builtin_amdgcn_raw_buffer_store_b128(v, rpt, 196608 + w * 8192 + 6144 + lane * 16, 0, 16); }
      { f32x4 a = acc_read4<216>(), b2 = acc_read4<220>(); a *= inv[1]; b2 *= inv[1];
        u32x4 v = {cvtpk(a[0], a[1]), cvtpk(a[2], a[3]), cvtpk(b2[0], b2[1]), cvtpk(b2[2], b2[3])};
        __builtin_amdgcn_raw_buffer_store_b128(v, rpt, 196608 + w * 8192 + 7168 + lane * 16, 0, 16); }
      { f32x4 a = acc_read4<96>(), b2 = acc_read4<100>(); a *= inv[0]; b2 *= inv[0];
        u32x4 v = {cvtpk(a[0], a[1]), cvtpk(a[2], a[3]), cvtpk(b2[0], b2[1]), cvtpk(b2[2], b2[3])};
        __builtin_amdgcn_raw_buffer_store_b128(v, rpt, 229376 + w * 8192 + 0 + lane * 16, 0, 16); }
      { f32x4 a = acc_read4<104>(), b2 = acc_read4<108>(); a *= inv[0]; b2 *= inv[0];
        u32x4 v = {cvtpk(a[0], a[1]), cvtpk(a[2], a[3]), cvtpk(b2[0], b2[1]), cvtpk(b2[2], b2[3])};
        __builtin_amdgcn_raw_buffer_store_b128(v, rpt, 229376 + w * 8192 + 1024 + lane * 16, 0, 16); }
      { f32x4 a = acc_read4<112>(), b2 = acc_read4<116>(); a *= inv[0]; b2 *= inv[0];
        u32x4 v = {cvtpk(a[0], a[1]), cvtpk(a[2], a[3]), cvtpk(b2[0], b2[1]), cvtpk(b2[2], b2[3])};
        __builtin_amdgcn_raw_buffer_store_b128(v, rpt, 229376 + w * 8192 + 2048 + lane * 16, 0, 16); }
      { f32x4 a = acc_read4<120>(), b2 = acc_read4<124>(); a *= inv[0]; b2 *= inv[0];
        u32x4 v = {cvtpk(a[0], a[1]), cvtpk(a[2], a[3]), cvtpk(b2[0], b2[1]), cvtpk(b2[2], b2[3])};
        __builtin_amdgcn_raw_buffer_store_b128(v, rpt, 229376 + w * 8192 + 3072 + lane * 16, 0, 16); }
      { f32x4 a = acc_read4<224>(), b2 = acc_read4<228>(); a *= inv[1]; b2 *= inv[1];
        u32x4 v = {cvtpk(a[0], a[1]), cvtpk(a[2], a[3]), cvtpk(b2[0], b2[1]), cvtpk(b2[2], b2[3])};
        __builtin_amdgcn_raw_buffer_store_b128(v, rpt, 229376 + w * 8192 + 4096 + lane * 16, 0, 16); }
      { f32x4 a = acc_read4<232>(), b2 = acc_read4<236>(); a *= inv[1]; b2 *= inv[1];
        u32x4 v = {cvtpk(a[0], a[1]), cvtpk(a[2], a[3]), cvtpk(b2[0], b2[1]), cvtpk(b2[2], b2[3])};
        __builtin_amdgcn_raw_buffer_store_b128(v, rpt, 229376 + w * 8192 + 5120 + lane * 16, 0, 16); }
      { f32x4 a = acc_read4<240>(), b2 = acc_read4<244>(); a *= inv[1]; b2 *= inv[1];
        u32x4 v = {cvtpk(a[0], a[1]), cvtpk(a[2], a[3]), cvtpk(b2[0], b2[1]), cvtpk(b2[2], b2[3])};
        __builtin_amdgcn_raw_buffer_store_b128(v, rpt, 229376 + w * 8192 + 6144 + lane * 16, 0, 16); }
      { f32x4 a = acc_read4<248>(), b2 = acc_read4<252>(); a *= inv[1]; b2 *= inv[1];
        u32x4 v = {cvtpk(a[0], a[1]), cvtpk(a[2], a[3]), cvtpk(b2[0], b2[1]), cvtpk(b2[2], b2[3])};
        __builtin_amdgcn_raw_buffer_store_b128(v, rpt, 229376 + w * 8192 + 7168 + lane * 16, 0, 16); }
    } break;
    case 2: {
      { f32x4 a = acc_read4<0>(), b2 = acc_read4<4>(); a *= inv[0]; b2 *= inv[0];
        u32x4 v = {cvtpk(a[0], a[1]), cvtpk(a[2], a[3]), cvtpk(b2[0], b2[1]), cvtpk(b2[2], b2[3])};
        __builtin_amdgcn_raw_buffer_store_b128(v, rpt, 262144 + w * 8192 + 0 + lane * 16, 0, 16); }
      { f32x4 a = acc_read4<8>(), b2 = acc_read4<12>(); a *= inv[0]; b2 *= inv[0];
        u32x4 v = {cvtpk(a[0], a[1]), cvtpk(a[2], a[3]), cvtpk(b2[0], b2[1]), cvtpk(b2[2], b2[3])};
        __builtin_amdgcn_raw_buffer_store_b128(v, rpt, 262144 + w * 8192 + 1024 + lane * 16, 0, 16); }
      { f32x4 a = acc_read4<16>(), b2 = acc_read4<20>(); a *= inv[0]; b2 *= inv[0];
        u32x4 v = {cvtpk(a[0], a[1]), cvtpk(a[2], a[3]), cvtpk(b2[0], b2[1]), cvtpk(b2[2], b2[3])};
        __builtin_amdgcn_raw_buffer_store_b128(v, rpt, 262144 + w * 8192 + 2048 + lane * 16, 0, 16); }
      { f32x4 a = acc_read4<24>(), b2 = acc_read4<28>(); a *= inv[0]; b2 *= inv[0];
        u32x4 v = {cvtpk(a[0], a[1]), cvtpk(a[2], a[3]), cvtpk(b2[0], b2[1]), cvtpk(b2[2], b2[3])};
        __builtin_amdgcn_raw_buffer_store_b128(v, rpt, 262144 + w * 8192 + 3072 + lane * 16, 0, 16); }
      { f32x4 a = acc_read4<128>(), b2 = acc_read4<132>(); a *= inv[1]; b2 *= inv[1];
        u32x4 v = {cvtpk(a[0], a[1]), cvtpk(a[2], a[3]), cvtpk(b2[0], b2[1]), cvtpk(b2[2], b2[3])};
        __builtin_amdgcn_raw_buffer_store_b128(v, rpt, 262144 + w * 8192 + 4096 + lane * 16, 0, 16); }
      { f32x4 a = acc_read4<136>(), b2 = acc_read4<140>(); a *= inv[1]; b2 *= inv[1];
        u32x4 v = {cvtpk(a[0], a[1]), cvtpk(a[2], a[3]), cvtpk(b2[0], b2[1]), cvtpk(b2[2], b2[3])};
        __builtin_amdgcn_raw_buffer_store_b128(v, rpt, 262144 + w * 8192 + 5120 + lane * 16, 0, 16); }
      { f32x4 a = acc_read4<144>(), b2 = acc_read4<148>(); a *= inv[1]; b2 *= inv[1];
        u32x4 v = {cvtpk(a[0], a[1]), cvtpk(a[2], a[3]), cvtpk(b2[0], b2[1]), cvtpk(b2[2], b2[3])};
        __builtin_amdgcn_raw_buffer_store_b128(v, rpt, 262144 + w * 8192 + 6144 + lane * 16, 0, 16); }
      { f32x4 a = acc_read4<152>(), b2 = acc_read4<156>(); a *= inv[1]; b2 *= inv[1];
        u32x4 v = {cvtpk(a[0], a[1]), cvtpk(a[2], a[3]), cvtpk(b2[0], b2[1]), cvtpk(b2[2], b2[3])};
        __builtin_amdgcn_raw_buffer_store_b128(v, rpt, 262144 + w * 8192 + 7168 + lane * 16, 0, 16); }
      { f32x4 a = acc_read4<32>(), b2 = acc_read4<36>(); a *= inv[0]; b2 *= inv[0];
        u32x4 v = {cvtpk(a[0], a[1]), cvtpk(a[2], a[3]), cvtpk(b2[0], b2[1]), cvtpk(b2[2], b2[3])};
        __builtin_amdgcn_raw_buffer_store_b128(v, rpt, 294912 + w * 8192 + 0 + lane * 16, 0, 16); }
      { f32x4 a = acc_read4<40>(), b2 = acc_read4<44>(); a *= inv[0]; b2 *= inv[0];
        u32x4 v = {cvtpk(a[0], a[1]), cvtpk(a[2], a[3]), cvtpk(b2[0], b2[1]), cvtpk(b2[2], b2[3])};
        __builtin_amdgcn_raw_buffer_store_b128(v, rpt, 294912 + w * 8192 + 1024 + lane * 16, 0, 16); }
      { f32x4 a = acc_read4<48>(), b2 = acc_read4<52>(); a *= inv[0]; b2 *= inv[0];
        u32x4 v = {cvtpk(a[0], a[1]), cvtpk(a[2], a[3]), cvtpk(b2[0], b2[1]), cvtpk(b2[2], b2[3])};
        __builtin_amdgcn_raw_buffer_store_b128(v, rpt, 294912 + w * 8192 + 2048 + lane * 16, 0, 16); }
      { f32x4 a = acc_read4<56>(), b2 = acc_read4<60>(); a *= inv[0]; b2 *= inv[0];
        u32x4 v = {cvtpk(a[0], a[1]), cvtpk(a[2], a[3]), cvtpk(b2[0], b2[1]), cvtpk(b2[2], b2[3])};
        __builtin_amdgcn_raw_buffer_store_b128(v, rpt, 294912 + w * 8192 + 3072 + lane * 16, 0, 16); }
      { f32x4 a = acc_read4<160>(), b2 = acc_read4<164>(); a *= inv[1]; b2 *= inv[1];
        u32x4 v = {cvtpk(a[0], a[1]), cvtpk(a[2], a[3]), cvtpk(b2[0], b2[1]), cvtpk(b2[2], b2[3])};
        __builtin_amdgcn_raw_buffer_store_b128(v, rpt, 294912 + w * 8192 + 4096 + lane * 16, 0, 16); }
      { f32x4 a = acc_read4<168>(), b2 = acc_read4<172>(); a *= inv[1]; b2 *= inv[1];
        u32x4 v = {cvtpk(a[0], a[1]), cvtpk(a[2], a[3]), cvtpk(b2[0], b2[1]), cvtpk(b2[2], b2[3])};
        __builtin_amdgcn_raw_buffer_store_b128(v, rpt, 294912 + w * 8192 + 5120 + lane * 16, 0, 16); }
      { f32x4 a = acc_read4<176>(), b2 = acc_read4<180>(); a *= inv[1]; b2 *= inv[1];
        u32x4 v = {cvtpk(a[0], a[1]), cvtpk(a[2], a[3]), cvtpk(b2[0], b2[1]), cvtpk(b2[2], b2[3])};
        __builtin_amdgcn_raw_buffer_store_b128(v, rpt, 294912 + w * 8192 + 6144 + lane * 16, 0, 16); }
      { f32x4 a = acc_read4<184>(), b2 = acc_read4<188>(); a *= inv[1]; b2 *= inv[1];
        u32x4 v = {cvtpk(a[0], a[1]), cvtpk(a[2], a[3]), cvtpk(b2[0], b2[1]), cvtpk(b2[2], b2[3])};
        __builtin_amdgcn_raw_buffer_store_b128(v, rpt, 294912 + w * 8192 + 7168 + lane * 16, 0, 16); }
      { f32x4 a = acc_read4<96>(), b2 = acc_read4<100>(); a *= inv[0]; b2 *= inv[0];
        u32x4 v = {cvtpk(a[0], a[1]), cvtpk(a[2], a[3]), cvtpk(b2[0], b2[1]), cvtpk(b2[2], b2[3])};
        __builtin_amdgcn_raw_buffer_store_b128(v, rpt, 360448 + w * 8192 + 0 + lane * 16, 0, 16); }
      { f32x4 a = acc_read4<104>(), b2 = acc_read4<108>(); a *= inv[0]; b2 *= inv[0];
        u32x4 v = {cvtpk(a[0], a[1]), cvtpk(a[2], a[3]), cvtpk(b2[0], b2[1]), cvtpk(b2[2], b2[3])};
        __builtin_amdgcn_raw_buffer_store_b128(v, rpt, 360448 + w * 8192 + 1024 + lane * 16, 0, 16); }
      { f32x4 a = acc_read4<112>(), b2 = acc_read4<116>(); a *= inv[0]; b2 *= inv[0];
        u32x4 v = {cvtpk(a[0], a[1]), cvtpk(a[2], a[3]), cvtpk(b2[0], b2[1]), cvtpk(b2[2], b2[3])};
        __builtin_amdgcn_raw_buffer_store_b128(v, rpt, 360448 + w * 8192 + 2048 + lane * 16, 0, 16); }
      { f32x4 a = acc_read4<120>(), b2 = acc_read4<124>(); a *= inv[0]; b2 *= inv[0];
        u32x4 v = {cvtpk(a[0], a[1]), cvtpk(a[2], a[3]), cvtpk(b2[0], b2[1]), cvtpk(b2[2], b2[3])};
        __builtin_amdgcn_raw_buffer_store_b128(v, rpt, 360448 + w * 8192 + 3072 + lane * 16, 0, 16); }
      { f32x4 a = acc_read4<224>(), b2 = acc_read4<228>(); a *= inv[1]; b2 *= inv[1];
        u32x4 v = {cvtpk(a[0], a[1]), cvtpk(a[2], a[3]), cvtpk(b2[0], b2[1]), cvtpk(b2[2], b2[3])};
        __builtin_amdgcn_raw_buffer_store_b128(v, rpt, 360448 + w * 8192 + 4096 + lane * 16, 0, 16); }
      { f32x4 a = acc_read4<232>(), b2 = acc_read4<236>(); a *= inv[1]; b2 *= inv[1];
        u32x4 v = {cvtpk(a[0], a[1]), cvtpk(a[2], a[3]), cvtpk(b2[0], b2[1]), cvtpk(b2[2], b2[3])};
        __builtin_amdgcn_raw_buffer_store_b128(v, rpt, 360448 + w * 8192 + 5120 + lane * 16, 0, 16); }
      { f32x4 a = acc_read4<240>(), b2 = acc_read4<244>(); a *= inv[1]; b2 *= inv[1];
        u32x4 v = {cvtpk(a[0], a[1]), cvtpk(a[2], a[3]), cvtpk(b2[0], b2[1]), cvtpk(b2[2], b2[3])};
        __builtin_amdgcn_raw_buffer_store_b128(v, rpt, 360448 + w * 8192 + 6144 + lane * 16, 0, 16); }
      { f32x4 a = acc_read4<248>(), b2 = acc_read4<252>(); a *= inv[1]; b2 *= inv[1];
        u32x4 v = {cvtpk(a[0], a[1]), cvtpk(a[2], a[3]), cvtpk(b2[0], b2[1]), cvtpk(b2[2], b2[3])};
        __builtin_amdgcn_raw_buffer_store_b128(v, rpt, 360448 + w * 8192 + 7168 + lane * 16, 0, 16); }
    } break;
    case 3: {
      { f32x4 a = acc_read4<0>(), b2 = acc_read4<4>(); a *= inv[0]; b2 *= inv[0];
        u32x4 v = {cvtpk(a[0], a[1]), cvtpk(a[2], a[3]), cvtpk(b2[0], b2[1]), cvtpk(b2[2], b2[3])};
        __builtin_amdgcn_raw_buffer_store_b128(v, rpt, 393216 + w * 8192 + 0 + lane * 16, 0, 16); }
      { f32x4 a = acc_read4<8>(), b2 = acc_read4<12>(); a *= inv[0]; b2 *= inv[0];
        u32x4 v = {cvtpk(a[0], a[1]), cvtpk(a[2], a[3]), cvtpk(b2[0], b2[1]), cvtpk(b2[2], b2[3])};
        __builtin_amdgcn_raw_buffer_store_b128(v, rpt, 393216 + w * 8192 + 1024 + lane * 16, 0, 16); }
      { f32x4 a = acc_read4<16>(), b2 = acc_read4<20>(); a *= inv[0]; b2 *= inv[0];
        u32x4 v = {cvtpk(a[0], a[1]), cvtpk(a[2], a[3]), cvtpk(b2[0], b2[1]), cvtpk(b2[2], b2[3])};
        __builtin_amdgcn_raw_buffer_store_b128(v, rpt, 393216 + w * 8192 + 2048 + lane * 16, 0, 16); }
      { f32x4 a = acc_read4<24>(), b2 = acc_read4<28>(); a *= inv[0]; b2 *= inv[0];
        u32x4 v = {cvtpk(a[0], a[1]), cvtpk(a[2], a[3]), cvtpk(b2[0], b2[1]), cvtpk(b2[2], b2[3])};
        __builtin_amdgcn_raw_buffer_store_b128(v, rpt, 393216 + w * 8192 + 3072 + lane * 16, 0, 16); }
      { f32x4 a = acc_read4<128>(), b2 = acc_read4<132>(); a *= inv[1]; b2 *= inv[1];
        u32x4 v = {cvtpk(a[0], a[1]), cvtpk(a[2], a[3]), cvtpk(b2[0], b2[1]), cvtpk(b2[2], b2[3])};
        __builtin_amdgcn_raw_buffer_store_b128(v, rpt, 393216 + w * 8192 + 4096 + lane * 16, 0, 16); }
      { f32x4 a = acc_read4<136>(), b2 = acc_read4<140>(); a *= inv[1]; b2 *= inv[1];
        u32x4 v = {cvtpk(a[0], a[1]), cvtpk(a[2], a[3]), cvtpk(b2[0], b2[1]), cvtpk(b2[2], b2[3])};
        __builtin_amdgcn_raw_buffer_store_b128(v, rpt, 393216 + w * 8192 + 5120 + lane * 16, 0, 16); }
      { f32x4 a = acc_read4<144>(), b2 = acc_read4<148>(); a *= inv[1]; b2 *= inv[1];
        u32x4 v = {cvtpk(a[0], a[1]), cvtpk(a[2], a[3]), cvtpk(b2[0], b2[1]), cvtpk(b2[2], b2[3])};
        __builtin_amdgcn_raw_buffer_store_b128(v, rpt, 393216 + w * 8192 + 6144 + lane * 16, 0, 16); }
      { f32x4 a = acc_read4<152>(), b2 = acc_read4<156>(); a *= inv[1]; b2 *= inv[1];
        u32x4 v = {cvtpk(a[0], a[1]), cvtpk(a[2], a[3]), cvtpk(b2[0], b2[1]), cvtpk(b2[2], b2[3])};
        __builtin_amdgcn_raw_buffer_store_b128(v, rpt, 393216 + w * 8192 + 7168 + lane * 16, 0, 16); }
      { f32x4 a = acc_read4<32>(), b2 = acc_read4<36>(); a *= inv[0]; b2 *= inv[0];
        u32x4 v = {cvtpk(a[0], a[1]), cvtpk(a[2], a[3]), cvtpk(b2[0], b2[1]), cvtpk(b2[2], b2[3])};
        __builtin_amdgcn_raw_buffer_store_b128(v, rpt, 425984 + w * 8192 + 0 + lane * 16, 0, 16); }
      { f32x4 a = acc_read4<40>(), b2 = acc_read4<44>(); a *= inv[0]; b2 *= inv[0];
        u32x4 v = {cvtpk(a[0], a[1]), cvtpk(a[2], a[3]), cvtpk(b2[0], b2[1]), cvtpk(b2[2], b2[3])};
        __builtin_amdgcn_raw_buffer_store_b128(v, rpt, 425984 + w * 8192 + 1024 + lane * 16, 0, 16); }
      { f32x4 a = acc_read4<48>(), b2 = acc_read4<52>(); a *= inv[0]; b2 *= inv[0];
        u32x4 v = {cvtpk(a[0], a[1]), cvtpk(a[2], a[3]), cvtpk(b2[0], b2[1]), cvtpk(b2[2], b2[3])};
        __builtin_amdgcn_raw_buffer_store_b128(v, rpt, 425984 + w * 8192 + 2048 + lane * 16, 0, 16); }
      { f32x4 a = acc_read4<56>(), b2 = acc_read4<60>(); a *= inv[0]; b2 *= inv[0];
        u32x4 v = {cvtpk(a[0], a[1]), cvtpk(a[2], a[3]), cvtpk(b2[0], b2[1]), cvtpk(b2[2], b2[3])};
        __builtin_amdgcn_raw_buffer_store_b128(v, rpt, 425984 + w * 8192 + 3072 + lane * 16, 0, 16); }
      { f32x4 a = acc_read4<160>(), b2 = acc_read4<164>(); a *= inv[1]; b2 *= inv[1];
        u32x4 v = {cvtpk(a[0], a[1]), cvtpk(a[2], a[3]), cvtpk(b2[0], b2[1]), cvtpk(b2[2], b2[3])};
        __builtin_amdgcn_raw_buffer_store_b128(v, rpt, 425984 + w * 8192 + 4096 + lane * 16, 0, 16); }
      { f32x4 a = acc_read4<168>(), b2 = acc_read4<172>(); a *= inv[1]; b2 *= inv[1];
        u32x4 v = {cvtpk(a[0], a[1]), cvtpk(a[2], a[3]), cvtpk(b2[0], b2[1]), cvtpk(b2[2], b2[3])};
        __builtin_amdgcn_raw_buffer_store_b128(v, rpt, 425984 + w * 8192 + 5120 + lane * 16, 0, 16); }
      { f32x4 a = acc_read4<176>(), b2 = acc_read4<180>(); a *= inv[1]; b2 *= inv[1];
        u32x4 v = {cvtpk(a[0], a[1]), cvtpk(a[2], a[3]), cvtpk(b2[0], b2[1]), cvtpk(b2[2], b2[3])};
        __builtin_amdgcn_raw_buffer_store_b128(v, rpt, 425984 + w * 8192 + 6144 + lane * 16, 0, 16); }
      { f32x4 a = acc_read4<184>(), b2 = acc_read4<188>(); a *= inv[1]; b2 *= inv[1];
        u32x4 v = {cvtpk(a[0], a[1]), cvtpk(a[2], a[3]), cvtpk(b2[0], b2[1]), cvtpk(b2[2], b2[3])};
        __builtin_amdgcn_raw_buffer_store_b128(v, rpt, 425984 + w * 8192 + 7168 + lane * 16, 0, 16); }
      { f32x4 a = acc_read4<64>(), b2 = acc_read4<68>(); a *= inv[0]; b2 *= inv[0];
        u32x4 v = {cvtpk(a[0], a[1]), cvtpk(a[2], a[3]), cvtpk(b2[0], b2[1]), cvtpk(b2[2], b2[3])};
        __builtin_amdgcn_raw_buffer_store_b128(v, rpt, 458752 + w * 8192 + 0 + lane * 16, 0, 16); }
      { f32x4 a = acc_read4<72>(), b2 = acc_read4<76>(); a *= inv[0]; b2 *= inv[0];
        u32x4 v = {cvtpk(a[0], a[1]), cvtpk(a[2], a[3]), cvtpk(b2[0], b2[1]), cvtpk(b2[2], b2[3])};
        __builtin_amdgcn_raw_buffer_store_b128(v, rpt, 458752 + w * 8192 + 1024 + lane * 16, 0, 16); }
      { f32x4 a = acc_read4<80>(), b2 = acc_read4<84>(); a *= inv[0]; b2 *= inv[0];
        u32x4 v = {cvtpk(a[0], a[1]), cvtpk(a[2], a[3]), cvtpk(b2[0], b2[1]), cvtpk(b2[2], b2[3])};
        __builtin_amdgcn_raw_buffer_store_b128(v, rpt, 458752 + w * 8192 + 2048 + lane * 16, 0, 16); }
      { f32x4 a = acc_read4<88>(), b2 = acc_read4<92>(); a *= inv[0]; b2 *= inv[0];
        u32x4 v = {cvtpk(a[0], a[1]), cvtpk(a[2], a[3]), cvtpk(b2[0], b2[1]), cvtpk(b2[2], b2[3])};
        __builtin_amdgcn_raw_buffer_store_b128(v, rpt, 458752 + w * 8192 + 3072 + lane * 16, 0, 16); }
      { f32x4 a = acc_read4<192>(), b2 = acc_read4<196>(); a *= inv[1]; b2 *= inv[1];
        u32x4 v = {cvtpk(a[0], a[1]), cvtpk(a[2], a[3]), cvtpk(b2[0], b2[1]), cvtpk(b2[2], b2[3])};
        __builtin_amdgcn_raw_buffer_store_b128(v, rpt, 458752 + w * 8192 + 4096 + lane * 16, 0, 16); }
      { f32x4 a = acc_read4<200>(), b2 = acc_read4<204>(); a *= inv[1]; b2 *= inv[1];
        u32x4 v = {cvtpk(a[0], a[1]), cvtpk(a[2], a[3]), cvtpk(b2[0], b2[1]), cvtpk(b2[2], b2[3])};
        __builtin_amdgcn_raw_buffer_store_b128(v, rpt, 458752 + w * 8192 + 5120 + lane * 16, 0, 16); }
      { f32x4 a = acc_read4<208>(), b2 = acc_read4<212>(); a *= inv[1]; b2 *= inv[1];
        u32x4 v = {cvtpk(a[0], a[1]), cvtpk(a[2], a[3]), cvtpk(b2[0], b2[1]), cvtpk(b2[2], b2[3])};
        __builtin_amdgcn_raw_buffer_store_b128(v, rpt, 458752 + w * 8192 + 6144 + lane * 16, 0, 16); }
      { f32x4 a = acc_read4<216>(), b2 = acc_read4<220>(); a *= inv[1]; b2 *= inv[1];
        u32x4 v = {cvtpk(a[0], a[1]), cvtpk(a[2], a[3]), cvtpk(b2[0], b2[1]), cvtpk(b2[2], b2[3])};
        __builtin_amdgcn_raw_buffer_store_b128(v, rpt, 458752 + w * 8192 + 7168 + lane * 16, 0, 16); }
    } break;
  }
  asm volatile("s_waitcnt vmcnt(0)" ::: "memory");
  __syncthreads();
  STAMP(14);
  if (tid == 0) {
    __hip_atomic_fetch_add(cnt + grp, 1u, __ATOMIC_RELAXED, __HIP_MEMORY_SCOPE_AGENT);
    for (int it = 0; it < (1 << 24); ++it) {
      if (__hip_atomic_load(cnt + grp, __ATOMIC_RELAXED, __HIP_MEMORY_SCOPE_AGENT) >= 4u) break;
      __builtin_amdgcn_s_sleep(1);
    }
  }
  __syncthreads();
  STAMP(15);
  float fw[2][4];
#pragma unroll
  for (int qb = 0; qb < 2; ++qb) {
    float mm[4], ll[4];
#pragma unroll
    for (int s4 = 0; s4 < 4; ++s4) {
      u32x2 v = __builtin_amdgcn_raw_buffer_load_b64(rml, (s4 * 256 + 64 * w + 32 * qb + r) * 8, 0, 16);
      mm[s4] = __uint_as_float(v[0]); ll[s4] = __uint_as_float(v[1]);
    }
    const float M = fmaxf(fmaxf(mm[0], mm[1]), fmaxf(mm[2], mm[3]));
    float e[4], L = 0.f;
#pragma unroll
    for (int s4 = 0; s4 < 4; ++s4) { e[s4] = __builtin_amdgcn_exp2f(mm[s4] - M) * ll[s4]; L += e[s4]; }
    const float iL = 1.f / L;
#pragma unroll
    for (int s4 = 0; s4 < 4; ++s4) fw[qb][s4] = e[s4] * iL;
  }
  switch (sp) {
    case 0: {
      { const int R = 64 * w + 0 + r; const int hl = R >= 112 ? 1 : 0; const int orow = b * SQ + R - 112 * hl;
        { f32x4 a = acc_read4<0>(), b2 = acc_read4<4>(); const float fo = fw[0][0] * inv[0]; a *= fo; b2 *= fo;
          { u32x4 v = __builtin_amdgcn_raw_buffer_load_b128(rpt, 131072 + w * 8192 + 0 + lane * 16, 0, 16); const float f = fw[0][1];
            a[0] += f * __uint_as_float(v[0] << 16); a[1] += f * __uint_as_float(v[0] & 0xffff0000u); a[2] += f * __uint_as_float(v[1] << 16); a[3] += f * __uint_as_float(v[1] & 0xffff0000u);
            b2[0] += f * __uint_as_float(v[2] << 16); b2[1] += f * __uint_as_float(v[2] & 0xffff0000u); b2[2] += f * __uint_as_float(v[3] << 16); b2[3] += f * __uint_as_float(v[3] & 0xffff0000u); }
          { u32x4 v = __builtin_amdgcn_raw_buffer_load_b128(rpt, 262144 + w * 8192 + 0 + lane * 16, 0, 16); const float f = fw[0][2];
            a[0] += f * __uint_as_float(v[0] << 16); a[1] += f * __uint_as_float(v[0] & 0xffff0000u); a[2] += f * __uint_as_float(v[1] << 16); a[3] += f * __uint_as_float(v[1] & 0xffff0000u);
            b2[0] += f * __uint_as_float(v[2] << 16); b2[1] += f * __uint_as_float(v[2] & 0xffff0000u); b2[2] += f * __uint_as_float(v[3] << 16); b2[3] += f * __uint_as_float(v[3] & 0xffff0000u); }
          { u32x4 v = __builtin_amdgcn_raw_buffer_load_b128(rpt, 393216 + w * 8192 + 0 + lane * 16, 0, 16); const float f = fw[0][3];
            a[0] += f * __uint_as_float(v[0] << 16); a[1] += f * __uint_as_float(v[0] & 0xffff0000u); a[2] += f * __uint_as_float(v[1] << 16); a[3] += f * __uint_as_float(v[1] & 0xffff0000u);
            b2[0] += f * __uint_as_float(v[2] << 16); b2[1] += f * __uint_as_float(v[2] & 0xffff0000u); b2[2] += f * __uint_as_float(v[3] << 16); b2[3] += f * __uint_as_float(v[3] & 0xffff0000u); }
          if (R < 224) { u16* dst = o + ((size_t)((kvh * 2 + hl) * 8 + 0) * NROW + orow) * 32 + 0 + 4 * h;
            u32x2 v0 = {cvtpk(a[0], a[1]), cvtpk(a[2], a[3])}; u32x2 v1 = {cvtpk(b2[0], b2[1]), cvtpk(b2[2], b2[3])};
            *(u32x2*)dst = v0; *(u32x2*)(dst + 8) = v1; } }
        { f32x4 a = acc_read4<8>(), b2 = acc_read4<12>(); const float fo = fw[0][0] * inv[0]; a *= fo; b2 *= fo;
          { u32x4 v = __builtin_amdgcn_raw_buffer_load_b128(rpt, 131072 + w * 8192 + 1024 + lane * 16, 0, 16); const float f = fw[0][1];
            a[0] += f * __uint_as_float(v[0] << 16); a[1] += f * __uint_as_float(v[0] & 0xffff0000u); a[2] += f * __uint_as_float(v[1] << 16); a[3] += f * __uint_as_float(v[1] & 0xffff0000u);
            b2[0] += f * __uint_as_float(v[2] << 16); b2[1] += f * __uint_as_float(v[2] & 0xffff0000u); b2[2] += f * __uint_as_float(v[3] << 16); b2[3] += f * __uint_as_float(v[3] & 0xffff0000u); }
          { u32x4 v = __builtin_amdgcn_raw_buffer_load_b128(rpt, 262144 + w * 8192 + 1024 + lane * 16, 0, 16); const float f = fw[0][2];
            a[0] += f * __uint_as_float(v[0] << 16); a[1] += f * __uint_as_float(v[0] & 0xffff0000u); a[2] += f * __uint_as_float(v[1] << 16); a[3] += f * __uint_as_float(v[1] & 0xffff0000u);
            b2[0] += f * __uint_as_float(v[2] << 16); b2[1] += f * __uint_as_float(v[2] & 0xffff0000u); b2[2] += f * __uint_as_float(v[3] << 16); b2[3] += f * __uint_as_float(v[3] & 0xffff0000u); }
          { u32x4 v = __builtin_amdgcn_raw_buffer_load_b128(rpt, 393216 + w * 8192 + 1024 + lane * 16, 0, 16); const float f = fw[0][3];
            a[0] += f * __uint_as_float(v[0] << 16); a[1] += f * __uint_as_float(v[0] & 0xffff0000u); a[2] += f * __uint_as_float(v[1] << 16); a[3] += f * __uint_as_float(v[1] & 0xffff0000u);
            b2[0] += f * __uint_as_float(v[2] << 16); b2[1] += f * __uint_as_float(v[2] & 0xffff0000u); b2[2] += f * __uint_as_float(v[3] << 16); b2[3] += f * __uint_as_float(v[3] & 0xffff0000u); }
          if (R < 224) { u16* dst = o + ((size_t)((kvh * 2 + hl) * 8 + 0) * NROW + orow) * 32 + 16 + 4 * h;
            u32x2 v0 = {cvtpk(a[0], a[1]), cvtpk(a[2], a[3])}; u32x2 v1 = {cvtpk(b2[0], b2[1]), cvtpk(b2[2], b2[3])};
            *(u32x2*)dst = v0; *(u32x2*)(dst + 8) = v1; } }
        { f32x4 a = acc_read4<16>(), b2 = acc_read4<20>(); const float fo = fw[0][0] * inv[0]; a *= fo; b2 *= fo;
          { u32x4 v = __builtin_amdgcn_raw_buffer_load_b128(rpt, 131072 + w * 8192 + 2048 + lane * 16, 0, 16); const float f = fw[0][1];
            a[0] += f * __uint_as_float(v[0] << 16); a[1] += f * __uint_as_float(v[0] & 0xffff0000u); a[2] += f * __uint_as_float(v[1] << 16); a[3] += f * __uint_as_float(v[1] & 0xffff0000u);
            b2[0] += f * __uint_as_float(v[2] << 16); b2[1] += f * __uint_as_float(v[2] & 0xffff0000u); b2[2] += f * __uint_as_float(v[3] << 16); b2[3] += f * __uint_as_float(v[3] & 0xffff0000u); }
          { u32x4 v = __builtin_amdgcn_raw_buffer_load_b128(rpt, 262144 + w * 8192 + 2048 + lane * 16, 0, 16); const float f = fw[0][2];
            a[0] += f * __uint_as_float(v[0] << 16); a[1] += f * __uint_as_float(v[0] & 0xffff0000u); a[2] += f * __uint_as_float(v[1] << 16); a[3] += f * __uint_as_float(v[1] & 0xffff0000u);
            b2[0] += f * __uint_as_float(v[2] << 16); b2[1] += f * __uint_as_float(v[2] & 0xffff0000u); b2[2] += f * __uint_as_float(v[3] << 16); b2[3] += f * __uint_as_float(v[3] & 0xffff0000u); }
          { u32x4 v = __builtin_amdgcn_raw_buffer_load_b128(rpt, 393216 + w * 8192 + 2048 + lane * 16, 0, 16); const float f = fw[0][3];
            a[0] += f * __uint_as_float(v[0] << 16); a[1] += f * __uint_as_float(v[0] & 0xffff0000u); a[2] += f * __uint_as_float(v[1] << 16); a[3] += f * __uint_as_float(v[1] & 0xffff0000u);
            b2[0] += f * __uint_as_float(v[2] << 16); b2[1] += f * __uint_as_float(v[2] & 0xffff0000u); b2[2] += f * __uint_as_float(v[3] << 16); b2[3] += f * __uint_as_float(v[3] & 0xffff0000u); }
          if (R < 224) { u16* dst = o + ((size_t)((kvh * 2 + hl) * 8 + 1) * NROW + orow) * 32 + 0 + 4 * h;
            u32x2 v0 = {cvtpk(a[0], a[1]), cvtpk(a[2], a[3])}; u32x2 v1 = {cvtpk(b2[0], b2[1]), cvtpk(b2[2], b2[3])};
            *(u32x2*)dst = v0; *(u32x2*)(dst + 8) = v1; } }
        { f32x4 a = acc_read4<24>(), b2 = acc_read4<28>(); const float fo = fw[0][0] * inv[0]; a *= fo; b2 *= fo;
          { u32x4 v = __builtin_amdgcn_raw_buffer_load_b128(rpt, 131072 + w * 8192 + 3072 + lane * 16, 0, 16); const float f = fw[0][1];
            a[0] += f * __uint_as_float(v[0] << 16); a[1] += f * __uint_as_float(v[0] & 0xffff0000u); a[2] += f * __uint_as_float(v[1] << 16); a[3] += f * __uint_as_float(v[1] & 0xffff0000u);
            b2[0] += f * __uint_as_float(v[2] << 16); b2[1] += f * __uint_as_float(v[2] & 0xffff0000u); b2[2] += f * __uint_as_float(v[3] << 16); b2[3] += f * __uint_as_float(v[3] & 0xffff0000u); }
          { u32x4 v = __builtin_amdgcn_raw_buffer_load_b128(rpt, 262144 + w * 8192 + 3072 + lane * 16, 0, 16); const float f = fw[0][2];
            a[0] += f * __uint_as_float(v[0] << 16); a[1] += f * __uint_as_float(v[0] & 0xffff0000u); a[2] += f * __uint_as_float(v[1] << 16); a[3] += f * __uint_as_float(v[1] & 0xffff0000u);
            b2[0] += f * __uint_as_float(v[2] << 16); b2[1] += f * __uint_as_float(v[2] & 0xffff0000u); b2[2] += f * __uint_as_float(v[3] << 16); b2[3] += f * __uint_as_float(v[3] & 0xffff0000u); }
          { u32x4 v = __builtin_amdgcn_raw_buffer_load_b128(rpt, 393216 + w * 8192 + 3072 + lane * 16, 0, 16); const float f = fw[0][3];
            a[0] += f * __uint_as_float(v[0] << 16); a[1] += f * __uint_as_float(v[0] & 0xffff0000u); a[2] += f * __uint_as_float(v[1] << 16); a[3] += f * __uint_as_float(v[1] & 0xffff0000u);
            b2[0] += f * __uint_as_float(v[2] << 16); b2[1] += f * __uint_as_float(v[2] & 0xffff0000u); b2[2] += f * __uint_as_float(v[3] << 16); b2[3] += f * __uint_as_float(v[3] & 0xffff0000u); }
          if (R < 224) { u16* dst = o + ((size_t)((kvh * 2 + hl) * 8 + 1) * NROW + orow) * 32 + 16 + 4 * h;
            u32x2 v0 = {cvtpk(a[0], a[1]), cvtpk(a[2], a[3])}; u32x2 v1 = {cvtpk(b2[0], b2[1]), cvtpk(b2[2], b2[3])};
            *(u32x2*)dst = v0; *(u32x2*)(dst + 8) = v1; } }
      }
      { const int R = 64 * w + 32 + r; const int hl = R >= 112 ? 1 : 0; const int orow = b * SQ + R - 112 * hl;
        { f32x4 a = acc_read4<128>(), b2 = acc_read4<132>(); const float fo = fw[1][0] * inv[1]; a *= fo; b2 *= fo;
          { u32x4 v = __builtin_amdgcn_raw_buffer_load_b128(rpt, 131072 + w * 8192 + 4096 + lane * 16, 0, 16); const float f = fw[1][1];
            a[0] += f * __uint_as_float(v[0] << 16); a[1] += f * __uint_as_float(v[0] & 0xffff0000u); a[2] += f * __uint_as_float(v[1] << 16); a[3] += f * __uint_as_float(v[1] & 0xffff0000u);
            b2[0] += f * __uint_as_float(v[2] << 16); b2[1] += f * __uint_as_float(v[2] & 0xffff0000u); b2[2] += f * __uint_as_float(v[3] << 16); b2[3] += f * __uint_as_float(v[3] & 0xffff0000u); }
          { u32x4 v = __builtin_amdgcn_raw_buffer_load_b128(rpt, 262144 + w * 8192 + 4096 + lane * 16, 0, 16); const float f = fw[1][2];
            a[0] += f * __uint_as_float(v[0] << 16); a[1] += f * __uint_as_float(v[0] & 0xffff0000u); a[2] += f * __uint_as_float(v[1] << 16); a[3] += f * __uint_as_float(v[1] & 0xffff0000u);
            b2[0] += f * __uint_as_float(v[2] << 16); b2[1] += f * __uint_as_float(v[2] & 0xffff0000u); b2[2] += f * __uint_as_float(v[3] << 16); b2[3] += f * __uint_as_float(v[3] & 0xffff0000u); }
          { u32x4 v = __builtin_amdgcn_raw_buffer_load_b128(rpt, 393216 + w * 8192 + 4096 + lane * 16, 0, 16); const float f = fw[1][3];
            a[0] += f * __uint_as_float(v[0] << 16); a[1] += f * __uint_as_float(v[0] & 0xffff0000u); a[2] += f * __uint_as_float(v[1] << 16); a[3] += f * __uint_as_float(v[1] & 0xffff0000u);
            b2[0] += f * __uint_as_float(v[2] << 16); b2[1] += f * __uint_as_float(v[2] & 0xffff0000u); b2[2] += f * __uint_as_float(v[3] << 16); b2[3] += f * __uint_as_float(v[3] & 0xffff0000u); }
          if (R < 224) { u16* dst = o + ((size_t)((kvh * 2 + hl) * 8 + 0) * NROW + orow) * 32 + 0 + 4 * h;
            u32x2 v0 = {cvtpk(a[0], a[1]), cvtpk(a[2], a[3])}; u32x2 v1 = {cvtpk(b2[0], b2[1]), cvtpk(b2[2], b2[3])};
            *(u32x2*)dst = v0; *(u32x2*)(dst + 8) = v1; } }
        { f32x4 a = acc_read4<136>(), b2 = acc_read4<140>(); const float fo = fw[1][0] * inv[1]; a *= fo; b2 *= fo;
          { u32x4 v = __builtin_amdgcn_raw_buffer_load_b128(rpt, 131072 + w * 8192 + 5120 + lane * 16, 0, 16); const float f = fw[1][1];
            a[0] += f * __uint_as_float(v[0] << 16); a[1] += f * __uint_as_float(v[0] & 0xffff0000u); a[2] += f * __uint_as_float(v[1] << 16); a[3] += f * __uint_as_float(v[1] & 0xffff0000u);
            b2[0] += f * __uint_as_float(v[2] << 16); b2[1] += f * __uint_as_float(v[2] & 0xffff0000u); b2[2] += f * __uint_as_float(v[3] << 16); b2[3] += f * __uint_as_float(v[3] & 0xffff0000u); }
          { u32x4 v = __builtin_amdgcn_raw_buffer_load_b128(rpt, 262144 + w * 8192 + 5120 + lane * 16, 0, 16); const float f = fw[1][2];
            a[0] += f * __uint_as_float(v[0] << 16); a[1] += f * __uint_as_float(v[0] & 0xffff0000u); a[2] += f * __uint_as_float(v[1] << 16); a[3] += f * __uint_as_float(v[1] & 0xffff0000u);
            b2[0] += f * __uint_as_float(v[2] << 16); b2[1] += f * __uint_as_float(v[2] & 0xffff0000u); b2[2] += f * __uint_as_float(v[3] << 16); b2[3] += f * __uint_as_float(v[3] & 0xffff0000u); }
          { u32x4 v = __builtin_amdgcn_raw_buffer_load_b128(rpt, 393216 + w * 8192 + 5120 + lane * 16, 0, 16); const float f = fw[1][3];
            a[0] += f * __uint_as_float(v[0] << 16); a[1] += f * __uint_as_float(v[0] & 0xffff0000u); a[2] += f * __uint_as_float(v[1] << 16); a[3] += f * __uint_as_float(v[1] & 0xffff0000u);
            b2[0] += f * __uint_as_float(v[2] << 16); b2[1] += f * __uint_as_float(v[2] & 0xffff0000u); b2[2] += f * __uint_as_float(v[3] << 16); b2[3] += f * __uint_as_float(v[3] & 0xffff0000u); }
          if (R < 224) { u16* dst = o + ((size_t)((kvh * 2 + hl) * 8 + 0) * NROW + orow) * 32 + 16 + 4 * h;
            u32x2 v0 = {cvtpk(a[0], a[1]), cvtpk(a[2], a[3])}; u32x2 v1 = {cvtpk(b2[0], b2[1]), cvtpk(b2[2], b2[3])};
            *(u32x2*)dst = v0; *(u32x2*)(dst + 8) = v1; } }
        { f32x4 a = acc_read4<144>(), b2 = acc_read4<148>(); const float fo = fw[1][0] * inv[1]; a *= fo; b2 *= fo;
          { u32x4 v = __builtin_amdgcn_raw_buffer_load_b128(rpt, 131072 + w * 8192 + 6144 + lane * 16, 0, 16); const float f = fw[1][1];
            a[0] += f * __uint_as_float(v[0] << 16); a[1] += f * __uint_as_float(v[0] & 0xffff0000u); a[2] += f * __uint_as_float(v[1] << 16); a[3] += f * __uint_as_float(v[1] & 0xffff0000u);
            b2[0] += f * __uint_as_float(v[2] << 16); b2[1] += f * __uint_as_float(v[2] & 0xffff0000u); b2[2] += f * __uint_as_float(v[3] << 16); b2[3] += f * __uint_as_float(v[3] & 0xffff0000u); }
          { u32x4 v = __builtin_amdgcn_raw_buffer_load_b128(rpt, 262144 + w * 8192 + 6144 + lane * 16, 0, 16); const float f = fw[1][2];
            a[0] += f * __uint_as_float(v[0] << 16); a[1] += f * __uint_as_float(v[0] & 0xffff0000u); a[2] += f * __uint_as_float(v[1] << 16); a[3] += f * __uint_as_float(v[1] & 0xffff0000u);
            b2[0] += f * __uint_as_float(v[2] << 16); b2[1] += f * __uint_as_float(v[2] & 0xffff0000u); b2[2] += f * __uint_as_float(v[3] << 16); b2[3] += f * __uint_as_float(v[3] & 0xffff0000u); }
          { u32x4 v = __builtin_amdgcn_raw_buffer_load_b128(rpt, 393216 + w * 8192 + 6144 + lane * 16, 0, 16); const float f = fw[1][3];
            a[0] += f * __uint_as_float(v[0] << 16); a[1] += f * __uint_as_float(v[0] & 0xffff0000u); a[2] += f * __uint_as_float(v[1] << 16); a[3] += f * __uint_as_float(v[1] & 0xffff0000u);
            b2[0] += f * __uint_as_float(v[2] << 16); b2[1] += f * __uint_as_float(v[2] & 0xffff0000u); b2[2] += f * __uint_as_float(v[3] << 16); b2[3] += f * __uint_as_float(v[3] & 0xffff0000u); }
          if (R < 224) { u16* dst = o + ((size_t)((kvh * 2 + hl) * 8 + 1) * NROW + orow) * 32 + 0 + 4 * h;
            u32x2 v0 = {cvtpk(a[0], a[1]), cvtpk(a[2], a[3])}; u32x2 v1 = {cvtpk(b2[0], b2[1]), cvtpk(b2[2], b2[3])};
            *(u32x2*)dst = v0; *(u32x2*)(dst + 8) = v1; } }
        { f32x4 a = acc_read4<152>(), b2 = acc_read4<156>(); const float fo = fw[1][0] * inv[1]; a *= fo; b2 *= fo;
          { u32x4 v = __builtin_amdgcn_raw_buffer_load_b128(rpt, 131072 + w * 8192 + 7168 + lane * 16, 0, 16); const float f = fw[1][1];
            a[0] += f * __uint_as_float(v[0] << 16); a[1] += f * __uint_as_float(v[0] & 0xffff0000u); a[2] += f * __uint_as_float(v[1] << 16); a[3] += f * __uint_as_float(v[1] & 0xffff0000u);
            b2[0] += f * __uint_as_float(v[2] << 16); b2[1] += f * __uint_as_float(v[2] & 0xffff0000u); b2[2] += f * __uint_as_float(v[3] << 16); b2[3] += f * __uint_as_float(v[3] & 0xffff0000u); }
          { u32x4 v = __builtin_amdgcn_raw_buffer_load_b128(rpt, 262144 + w * 8192 + 7168 + lane * 16, 0, 16); const float f = fw[1][2];
            a[0] += f * __uint_as_float(v[0] << 16); a[1] += f * __uint_as_float(v[0] & 0xffff0000u); a[2] += f * __uint_as_float(v[1] << 16); a[3] += f * __uint_as_float(v[1] & 0xffff0000u);
            b2[0] += f * __uint_as_float(v[2] << 16); b2[1] += f * __uint_as_float(v[2] & 0xffff0000u); b2[2] += f * __uint_as_float(v[3] << 16); b2[3] += f * __uint_as_float(v[3] & 0xffff0000u); }
          { u32x4 v = __builtin_amdgcn_raw_buffer_load_b128(rpt, 393216 + w * 8192 + 7168 + lane * 16, 0, 16); const float f = fw[1][3];
            a[0] += f * __uint_as_float(v[0] << 16); a[1] += f * __uint_as_float(v[0] & 0xffff0000u); a[2] += f * __uint_as_float(v[1] << 16); a[3] += f * __uint_as_float(v[1] & 0xffff0000u);
            b2[0] += f * __uint_as_float(v[2] << 16); b2[1] += f * __uint_as_float(v[2] & 0xffff0000u); b2[2] += f * __uint_as_float(v[3] << 16); b2[3] += f * __uint_as_float(v[3] & 0xffff0000u); }
          if (R < 224) { u16* dst = o + ((size_t)((kvh * 2 + hl) * 8 + 1) * NROW + orow) * 32 + 16 + 4 * h;
            u32x2 v0 = {cvtpk(a[0], a[1]), cvtpk(a[2], a[3])}; u32x2 v1 = {cvtpk(b2[0], b2[1]), cvtpk(b2[2], b2[3])};
            *(u32x2*)dst = v0; *(u32x2*)(dst + 8) = v1; } }
      }
    } break;
    case 1: {
      { const int R = 64 * w + 0 + r; const int hl = R >= 112 ? 1 : 0; const int orow = b * SQ + R - 112 * hl;
        { f32x4 a = acc_read4<32>(), b2 = acc_read4<36>(); const float fo = fw[0][1] * inv[0]; a *= fo; b2 *= fo;
          { u32x4 v = __builtin_amdgcn_raw_buffer_load_b128(rpt, 32768 + w * 8192 + 0 + lane * 16, 0, 16); const float f = fw[0][0];
            a[0] += f * __uint_as_float(v[0] << 16); a[1] += f * __uint_as_float(v[0] & 0xffff0000u); a[2] += f * __uint_as_float(v[1] << 16); a[3] += f * __uint_as_float(v[1] & 0xffff0000u);
            b2[0] += f * __uint_as_float(v[2] << 16); b2[1] += f * __uint_as_float(v[2] & 0xffff0000u); b2[2] += f * __uint_as_float(v[3] << 16); b2[3] += f * __uint_as_float(v[3] & 0xffff0000u); }
          { u32x4 v = __builtin_amdgcn_raw_buffer_load_b128(rpt, 294912 + w * 8192 + 0 + lane * 16, 0, 16); const float f = fw[0][2];
            a[0] += f * __uint_as_float(v[0] << 16); a[1] += f * __uint_as_float(v[0] & 0xffff0000u); a[2] += f * __uint_as_float(v[1] << 16); a[3] += f * __uint_as_float(v[1] & 0xffff0000u);
            b2[0] += f * __uint_as_float(v[2] << 16); b2[1] += f * __uint_as_float(v[2] & 0xffff0000u); b2[2] += f * __uint_as_float(v[3] << 16); b2[3] += f * __uint_as_float(v[3] & 0xffff0000u); }
          { u32x4 v = __builtin_amdgcn_raw_buffer_load_b128(rpt, 425984 + w * 8192 + 0 + lane * 16, 0, 16); const float f = fw[0][3];
            a[0] += f * __uint_as_float(v[0] << 16); a[1] += f * __uint_as_float(v[0] & 0xffff0000u); a[2] += f * __uint_as_float(v[1] << 16); a[3] += f * __uint_as_float(v[1] & 0xffff0000u);
            b2[0] += f * __uint_as_float(v[2] << 16); b2[1] += f * __uint_as_float(v[2] & 0xffff0000u); b2[2] += f * __uint_as_float(v[3] << 16); b2[3] += f * __uint_as_float(v[3] & 0xffff0000u); }
          if (R < 224) { u16* dst = o + ((size_t)((kvh * 2 + hl) * 8 + 2) * NROW + orow) * 32 + 0 + 4 * h;
            u32x2 v0 = {cvtpk(a[0], a[1]), cvtpk(a[2], a[3])}; u32x2 v1 = {cvtpk(b2[0], b2[1]), cvtpk(b2[2], b2[3])};
            *(u32x2*)dst = v0; *(u32x2*)(dst + 8) = v1; } }
        { f32x4 a = acc_read4<40>(), b2 = acc_read4<44>(); const float fo = fw[0][1] * inv[0]; a *= fo; b2 *= fo;
          { u32x4 v = __builtin_amdgcn_raw_buffer_load_b128(rpt, 32768 + w * 8192 + 1024 + lane * 16, 0, 16); const float f = fw[0][0];
            a[0] += f * __uint_as_float(v[0] << 16); a[1] += f * __uint_as_float(v[0] & 0xffff0000u); a[2] += f * __uint_as_float(v[1] << 16); a[3] += f * __uint_as_float(v[1] & 0xffff0000u);
            b2[0] += f * __uint_as_float(v[2] << 16); b2[1] += f * __uint_as_float(v[2] & 0xffff0000u); b2[2] += f * __uint_as_float(v[3] << 16); b2[3] += f * __uint_as_float(v[3] & 0xffff0000u); }
          { u32x4 v = __builtin_amdgcn_raw_buffer_load_b128(rpt, 294912 + w * 8192 + 1024 + lane * 16, 0, 16); const float f = fw[0][2];
            a[0] += f * __uint_as_float(v[0] << 16); a[1] += f * __uint_as_float(v[0] & 0xffff0000u); a[2] += f * __uint_as_float(v[1] << 16); a[3] += f * __uint_as_float(v[1] & 0xffff0000u);
            b2[0] += f * __uint_as_float(v[2] << 16); b2[1] += f * __uint_as_float(v[2] & 0xffff0000u); b2[2] += f * __uint_as_float(v[3] << 16); b2[3] += f * __uint_as_float(v[3] & 0xffff0000u); }
          { u32x4 v = __builtin_amdgcn_raw_buffer_load_b128(rpt, 425984 + w * 8192 + 1024 + lane * 16, 0, 16); const float f = fw[0][3];
            a[0] += f * __uint_as_float(v[0] << 16); a[1] += f * __uint_as_float(v[0] & 0xffff0000u); a[2] += f * __uint_as_float(v[1] << 16); a[3] += f * __uint_as_float(v[1] & 0xffff0000u);
            b2[0] += f * __uint_as_float(v[2] << 16); b2[1] += f * __uint_as_float(v[2] & 0xffff0000u); b2[2] += f * __uint_as_float(v[3] << 16); b2[3] += f * __uint_as_float(v[3] & 0xffff0000u); }
          if (R < 224) { u16* dst = o + ((size_t)((kvh * 2 + hl) * 8 + 2) * NROW + orow) * 32 + 16 + 4 * h;
            u32x2 v0 = {cvtpk(a[0], a[1]), cvtpk(a[2], a[3])}; u32x2 v1 = {cvtpk(b2[0], b2[1]), cvtpk(b2[2], b2[3])};
            *(u32x2*)dst = v0; *(u32x2*)(dst + 8) = v1; } }
        { f32x4 a = acc_read4<48>(), b2 = acc_read4<52>(); const float fo = fw[0][1] * inv[0]; a *= fo; b2 *= fo;
          { u32x4 v = __builtin_amdgcn_raw_buffer_load_b128(rpt, 32768 + w * 8192 + 2048 + lane * 16, 0, 16); const float f = fw[0][0];
            a[0] += f * __uint_as_float(v[0] << 16); a[1] += f * __uint_as_float(v[0] & 0xffff0000u); a[2] += f * __uint_as_float(v[1] << 16); a[3] += f * __uint_as_float(v[1] & 0xffff0000u);
            b2[0] += f * __uint_as_float(v[2] << 16); b2[1] += f * __uint_as_float(v[2] & 0xffff0000u); b2[2] += f * __uint_as_float(v[3] << 16); b2[3] += f * __uint_as_float(v[3] & 0xffff0000u); }
          { u32x4 v = __builtin_amdgcn_raw_buffer_load_b128(rpt, 294912 + w * 8192 + 2048 + lane * 16, 0, 16); const float f = fw[0][2];
            a[0] += f * __uint_as_float(v[0] << 16); a[1] += f * __uint_as_float(v[0] & 0xffff0000u); a[2] += f * __uint_as_float(v[1] << 16); a[3] += f * __uint_as_float(v[1] & 0xffff0000u);
            b2[0] += f * __uint_as_float(v[2] << 16); b2[1] += f * __uint_as_float(v[2] & 0xffff0000u); b2[2] += f * __uint_as_float(v[3] << 16); b2[3] += f * __uint_as_float(v[3] & 0xffff0000u); }
          { u32x4 v = __builtin_amdgcn_raw_buffer_load_b128(rpt, 425984 + w * 8192 + 2048 + lane * 16, 0, 16); const float f = fw[0][3];
            a[0] += f * __uint_as_float(v[0] << 16); a[1] += f * __uint_as_float(v[0] & 0xffff0000u); a[2] += f * __uint_as_float(v[1] << 16); a[3] += f * __uint_as_float(v[1] & 0xffff0000u);
            b2[0] += f * __uint_as_float(v[2] << 16); b2[1] += f * __uint_as_float(v[2] & 0xffff0000u); b2[2] += f * __uint_as_float(v[3] << 16); b2[3] += f * __uint_as_float(v[3] & 0xffff0000u); }
          if (R < 224) { u16* dst = o + ((size_t)((kvh * 2 + hl) * 8 + 3) * NROW + orow) * 32 + 0 + 4 * h;
            u32x2 v0 = {cvtpk(a[0], a[1]), cvtpk(a[2], a[3])}; u32x2 v1 = {cvtpk(b2[0], b2[1]), cvtpk(b2[2], b2[3])};
            *(u32x2*)dst = v0; *(u32x2*)(dst + 8) = v1; } }
        { f32x4 a = acc_read4<56>(), b2 = acc_read4<60>(); const float fo = fw[0][1] * inv[0]; a *= fo; b2 *= fo;
          { u32x4 v = __builtin_amdgcn_raw_buffer_load_b128(rpt, 32768 + w * 8192 + 3072 + lane * 16, 0, 16); const float f = fw[0][0];
            a[0] += f * __uint_as_float(v[0] << 16); a[1] += f * __uint_as_float(v[0] & 0xffff0000u); a[2] += f * __uint_as_float(v[1] << 16); a[3] += f * __uint_as_float(v[1] & 0xffff0000u);
            b2[0] += f * __uint_as_float(v[2] << 16); b2[1] += f * __uint_as_float(v[2] & 0xffff0000u); b2[2] += f * __uint_as_float(v[3] << 16); b2[3] += f * __uint_as_float(v[3] & 0xffff0000u); }
          { u32x4 v = __builtin_amdgcn_raw_buffer_load_b128(rpt, 294912 + w * 8192 + 3072 + lane * 16, 0, 16); const float f = fw[0][2];
            a[0] += f * __uint_as_float(v[0] << 16); a[1] += f * __uint_as_float(v[0] & 0xffff0000u); a[2] += f * __uint_as_float(v[1] << 16); a[3] += f * __uint_as_float(v[1] & 0xffff0000u);
            b2[0] += f * __uint_as_float(v[2] << 16); b2[1] += f * __uint_as_float(v[2] & 0xffff0000u); b2[2] += f * __uint_as_float(v[3] << 16); b2[3] += f * __uint_as_float(v[3] & 0xffff0000u); }
          { u32x4 v = __builtin_amdgcn_raw_buffer_load_b128(rpt, 425984 + w * 8192 + 3072 + lane * 16, 0, 16); const float f = fw[0][3];
            a[0] += f * __uint_as_float(v[0] << 16); a[1] += f * __uint_as_float(v[0] & 0xffff0000u); a[2] += f * __uint_as_float(v[1] << 16); a[3] += f * __uint_as_float(v[1] & 0xffff0000u);
            b2[0] += f * __uint_as_float(v[2] << 16); b2[1] += f * __uint_as_float(v[2] & 0xffff0000u); b2[2] += f * __uint_as_float(v[3] << 16); b2[3] += f * __uint_as_float(v[3] & 0xffff0000u); }
          if (R < 224) { u16* dst = o + ((size_t)((kvh * 2 + hl) * 8 + 3) * NROW + orow) * 32 + 16 + 4 * h;
            u32x2 v0 = {cvtpk(a[0], a[1]), cvtpk(a[2], a[3])}; u32x2 v1 = {cvtpk(b2[0], b2[1]), cvtpk(b2[2], b2[3])};
            *(u32x2*)dst = v0; *(u32x2*)(dst + 8) = v1; } }
      }
      { const int R = 64 * w + 32 + r; const int hl = R >= 112 ? 1 : 0; const int orow = b * SQ + R - 112 * hl;
        { f32x4 a = acc_read4<160>(), b2 = acc_read4<164>(); const float fo = fw[1][1] * inv[1]; a *= fo; b2 *= fo;
          { u32x4 v = __builtin_amdgcn_raw_buffer_load_b128(rpt, 32768 + w * 8192 + 4096 + lane * 16, 0, 16); const float f = fw[1][0];
            a[0] += f * __uint_as_float(v[0] << 16); a[1] += f * __uint_as_float(v[0] & 0xffff0000u); a[2] += f * __uint_as_float(v[1] << 16); a[3] += f * __uint_as_float(v[1] & 0xffff0000u);
            b2[0] += f * __uint_as_float(v[2] << 16); b2[1] += f * __uint_as_float(v[2] & 0xffff0000u); b2[2] += f * __uint_as_float(v[3] << 16); b2[3] += f * __uint_as_float(v[3] & 0xffff0000u); }
          { u32x4 v = __builtin_amdgcn_raw_buffer_load_b128(rpt, 294912 + w * 8192 + 4096 + lane * 16, 0, 16); const float f = fw[1][2];
            a[0] += f * __uint_as_float(v[0] << 16); a[1] += f * __uint_as_float(v[0] & 0xffff0000u); a[2] += f * __uint_as_float(v[1] << 16); a[3] += f * __uint_as_float(v[1] & 0xffff0000u);
            b2[0] += f * __uint_as_float(v[2] << 16); b2[1] += f * __uint_as_float(v[2] & 0xffff0000u); b2[2] += f * __uint_as_float(v[3] << 16); b2[3] += f * __uint_as_float(v[3] & 0xffff0000u); }
          { u32x4 v = __builtin_amdgcn_raw_buffer_load_b128(rpt, 425984 + w * 8192 + 4096 + lane * 16, 0, 16); const float f = fw[1][3];
            a[0] += f * __uint_as_float(v[0] << 16); a[1] += f * __uint_as_float(v[0] & 0xffff0000u); a[2] += f * __uint_as_float(v[1] << 16); a[3] += f * __uint_as_float(v[1] & 0xffff0000u);
            b2[0] += f * __uint_as_float(v[2] << 16); b2[1] += f * __uint_as_float(v[2] & 0xffff0000u); b2[2] += f * __uint_as_float(v[3] << 16); b2[3] += f * __uint_as_float(v[3] & 0xffff0000u); }
          if (R < 224) { u16* dst = o + ((size_t)((kvh * 2 + hl) * 8 + 2) * NROW + orow) * 32 + 0 + 4 * h;
            u32x2 v0 = {cvtpk(a[0], a[1]), cvtpk(a[2], a[3])}; u32x2 v1 = {cvtpk(b2[0], b2[1]), cvtpk(b2[2], b2[3])};
            *(u32x2*)dst = v0; *(u32x2*)(dst + 8) = v1; } }
        { f32x4 a = acc_read4<168>(), b2 = acc_read4<172>(); const float fo = fw[1][1] * inv[1]; a *= fo; b2 *= fo;
          { u32x4 v = __builtin_amdgcn_raw_buffer_load_b128(rpt, 32768 + w * 8192 + 5120 + lane * 16, 0, 16); const float f = fw[1][0];
            a[0] += f * __uint_as_float(v[0] << 16); a[1] += f * __uint_as_float(v[0] & 0xffff0000u); a[2] += f * __uint_as_float(v[1] << 16); a[3] += f * __uint_as_float(v[1] & 0xffff0000u);
            b2[0] += f * __uint_as_float(v[2] << 16); b2[1] += f * __uint_as_float(v[2] & 0xffff0000u); b2[2] += f * __uint_as_float(v[3] << 16); b2[3] += f * __uint_as_float(v[3] & 0xffff0000u); }
          { u32x4 v = __builtin_amdgcn_raw_buffer_load_b128(rpt, 294912 + w * 8192 + 5120 + lane * 16, 0, 16); const float f = fw[1][2];
            a[0] += f * __uint_as_float(v[0] << 16); a[1] += f * __uint_as_float(v[0] & 0xffff0000u); a[2] += f * __uint_as_float(v[1] << 16); a[3] += f * __uint_as_float(v[1] & 0xffff0000u);
            b2[0] += f * __uint_as_float(v[2] << 16); b2[1] += f * __uint_as_float(v[2] & 0xffff0000u); b2[2] += f * __uint_as_float(v[3] << 16); b2[3] += f * __uint_as_float(v[3] & 0xffff0000u); }
          { u32x4 v = __builtin_amdgcn_raw_buffer_load_b128(rpt, 425984 + w * 8192 + 5120 + lane * 16, 0, 16); const float f = fw[1][3];
            a[0] += f * __uint_as_float(v[0] << 16); a[1] += f * __uint_as_float(v[0] & 0xffff0000u); a[2] += f * __uint_as_float(v[1] << 16); a[3] += f * __uint_as_float(v[1] & 0xffff0000u);
            b2[0] += f * __uint_as_float(v[2] << 16); b2[1] += f * __uint_as_float(v[2] & 0xffff0000u); b2[2] += f * __uint_as_float(v[3] << 16); b2[3] += f * __uint_as_float(v[3] & 0xffff0000u); }
          if (R < 224) { u16* dst = o + ((size_t)((kvh * 2 + hl) * 8 + 2) * NROW + orow) * 32 + 16 + 4 * h;
            u32x2 v0 = {cvtpk(a[0], a[1]), cvtpk(a[2], a[3])}; u32x2 v1 = {cvtpk(b2[0], b2[1]), cvtpk(b2[2], b2[3])};
            *(u32x2*)dst = v0; *(u32x2*)(dst + 8) = v1; } }
        { f32x4 a = acc_read4<176>(), b2 = acc_read4<180>(); const float fo = fw[1][1] * inv[1]; a *= fo; b2 *= fo;
          { u32x4 v = __builtin_amdgcn_raw_buffer_load_b128(rpt, 32768 + w * 8192 + 6144 + lane * 16, 0, 16); const float f = fw[1][0];
            a[0] += f * __uint_as_float(v[0] << 16); a[1] += f * __uint_as_float(v[0] & 0xffff0000u); a[2] += f * __uint_as_float(v[1] << 16); a[3] += f * __uint_as_float(v[1] & 0xffff0000u);
            b2[0] += f * __uint_as_float(v[2] << 16); b2[1] += f * __uint_as_float(v[2] & 0xffff0000u); b2[2] += f * __uint_as_float(v[3] << 16); b2[3] += f * __uint_as_float(v[3] & 0xffff0000u); }
          { u32x4 v = __builtin_amdgcn_raw_buffer_load_b128(rpt, 294912 + w * 8192 + 6144 + lane * 16, 0, 16); const float f = fw[1][2];
            a[0] += f * __uint_as_float(v[0] << 16); a[1] += f * __uint_as_float(v[0] & 0xffff0000u); a[2] += f * __uint_as_float(v[1] << 16); a[3] += f * __uint_as_float(v[1] & 0xffff0000u);
            b2[0] += f * __uint_as_float(v[2] << 16); b2[1] += f * __uint_as_float(v[2] & 0xffff0000u); b2[2] += f * __uint_as_float(v[3] << 16); b2[3] += f * __uint_as_float(v[3] & 0xffff0000u); }
          { u32x4 v = __builtin_amdgcn_raw_buffer_load_b128(rpt, 425984 + w * 8192 + 6144 + lane * 16, 0, 16); const float f = fw[1][3];
            a[0] += f * __uint_as_float(v[0] << 16); a[1] += f * __uint_as_float(v[0] & 0xffff0000u); a[2] += f * __uint_as_float(v[1] << 16); a[3] += f * __uint_as_float(v[1] & 0xffff0000u);
            b2[0] += f * __uint_as_float(v[2] << 16); b2[1] += f * __uint_as_float(v[2] & 0xffff0000u); b2[2] += f * __uint_as_float(v[3] << 16); b2[3] += f * __uint_as_float(v[3] & 0xffff0000u); }
          if (R < 224) { u16* dst = o + ((size_t)((kvh * 2 + hl) * 8 + 3) * NROW + orow) * 32 + 0 + 4 * h;
            u32x2 v0 = {cvtpk(a[0], a[1]), cvtpk(a[2], a[3])}; u32x2 v1 = {cvtpk(b2[0], b2[1]), cvtpk(b2[2], b2[3])};
            *(u32x2*)dst = v0; *(u32x2*)(dst + 8) = v1; } }
        { f32x4 a = acc_read4<184>(), b2 = acc_read4<188>(); const float fo = fw[1][1] * inv[1]; a *= fo; b2 *= fo;
          { u32x4 v = __builtin_amdgcn_raw_buffer_load_b128(rpt, 32768 + w * 8192 + 7168 + lane * 16, 0, 16); const float f = fw[1][0];
            a[0] += f * __uint_as_float(v[0] << 16); a[1] += f * __uint_as_float(v[0] & 0xffff0000u); a[2] += f * __uint_as_float(v[1] << 16); a[3] += f * __uint_as_float(v[1] & 0xffff0000u);
            b2[0] += f * __uint_as_float(v[2] << 16); b2[1] += f * __uint_as_float(v[2] & 0xffff0000u); b2[2] += f * __uint_as_float(v[3] << 16); b2[3] += f * __uint_as_float(v[3] & 0xffff0000u); }
          { u32x4 v = __builtin_amdgcn_raw_buffer_load_b128(rpt, 294912 + w * 8192 + 7168 + lane * 16, 0, 16); const float f = fw[1][2];
            a[0] += f * __uint_as_float(v[0] << 16); a[1] += f * __uint_as_float(v[0] & 0xffff0000u); a[2] += f * __uint_as_float(v[1] << 16); a[3] += f * __uint_as_float(v[1] & 0xffff0000u);
            b2[0] += f * __uint_as_float(v[2] << 16); b2[1] += f * __uint_as_float(v[2] & 0xffff0000u); b2[2] += f * __uint_as_float(v[3] << 16); b2[3] += f * __uint_as_float(v[3] & 0xffff0000u); }
          { u32x4 v = __builtin_amdgcn_raw_buffer_load_b128(rpt, 425984 + w * 8192 + 7168 + lane * 16, 0, 16); const float f = fw[1][3];
            a[0] += f * __uint_as_float(v[0] << 16); a[1] += f * __uint_as_float(v[0] & 0xffff0000u); a[2] += f * __uint_as_float(v[1] << 16); a[3] += f * __uint_as_float(v[1] & 0xffff0000u);
            b2[0] += f * __uint_as_float(v[2] << 16); b2[1] += f * __uint_as_float(v[2] & 0xffff0000u); b2[2] += f * __uint_as_float(v[3] << 16); b2[3] += f * __uint_as_float(v[3] & 0xffff0000u); }
          if (R < 224) { u16* dst = o + ((size_t)((kvh * 2 + hl) * 8 + 3) * NROW + orow) * 32 + 16 + 4 * h;
            u32x2 v0 = {cvtpk(a[0], a[1]), cvtpk(a[2], a[3])}; u32x2 v1 = {cvtpk(b2[0], b2[1]), cvtpk(b2[2], b2[3])};
            *(u32x2*)dst = v0; *(u32x2*)(dst + 8) = v1; } }
      }
    } break;
    case 2: {
      { const int R = 64 * w + 0 + r; const int hl = R >= 112 ? 1 : 0; const int orow = b * SQ + R - 112 * hl;
        { f32x4 a = acc_read4<64>(), b2 = acc_read4<68>(); const float fo = fw[0][2] * inv[0]; a *= fo; b2 *= fo;
          { u32x4 v = __builtin_amdgcn_raw_buffer_load_b128(rpt, 65536 + w * 8192 + 0 + lane * 16, 0, 16); const float f = fw[0][0];
            a[0] += f * __uint_as_float(v[0] << 16); a[1] += f * __uint_as_float(v[0] & 0xffff0000u); a[2] += f * __uint_as_float(v[1] << 16); a[3] += f * __uint_as_float(v[1] & 0xffff0000u);
            b2[0] += f * __uint_as_float(v[2] << 16); b2[1] += f * __uint_as_float(v[2] & 0xffff0000u); b2[2] += f * __uint_as_float(v[3] << 16); b2[3] += f * __uint_as_float(v[3] & 0xffff0000u); }
          { u32x4 v = __builtin_amdgcn_raw_buffer_load_b128(rpt, 196608 + w * 8192 + 0 + lane * 16, 0, 16); const float f = fw[0][1];
            a[0] += f * __uint_as_float(v[0] << 16); a[1] += f * __uint_as_float(v[0] & 0xffff0000u); a[2] += f * __uint_as_float(v[1] << 16); a[3] += f * __uint_as_float(v[1] & 0xffff0000u);
            b2[0] += f * __uint_as_float(v[2] << 16); b2[1] += f * __uint_as_float(v[2] & 0xffff0000u); b2[2] += f * __uint_as_float(v[3] << 16); b2[3] += f * __uint_as_float(v[3] & 0xffff0000u); }
          { u32x4 v = __builtin_amdgcn_raw_buffer_load_b128(rpt, 458752 + w * 8192 + 0 + lane * 16, 0, 16); const float f = fw[0][3];
            a[0] += f * __uint_as_float(v[0] << 16); a[1] += f * __uint_as_float(v[0] & 0xffff0000u); a[2] += f * __uint_as_float(v[1] << 16); a[3] += f * __uint_as_float(v[1] & 0xffff0000u);
            b2[0] += f * __uint_as_float(v[2] << 16); b2[1] += f * __uint_as_float(v[2] & 0xffff0000u); b2[2] += f * __uint_as_float(v[3] << 16); b2[3] += f * __uint_as_float(v[3] & 0xffff0000u); }
          if (R < 224) { u16* dst = o + ((size_t)((kvh * 2 + hl) * 8 + 4) * NROW + orow) * 32 + 0 + 4 * h;
            u32x2 v0 = {cvtpk(a[0], a[1]), cvtpk(a[2], a[3])}; u32x2 v1 = {cvtpk(b2[0], b2[1]), cvtpk(b2[2], b2[3])};
            *(u32x2*)dst = v0; *(u32x2*)(dst + 8) = v1; } }
        { f32x4 a = acc_read4<72>(), b2 = acc_read4<76>(); const float fo = fw[0][2] * inv[0]; a *= fo; b2 *= fo;
          { u32x4 v = __builtin_amdgcn_raw_buffer_load_b128(rpt, 65536 + w * 8192 + 1024 + lane * 16, 0, 16); const float f = fw[0][0];
            a[0] += f * __uint_as_float(v[0] << 16); a[1] += f * __uint_as_float(v[0] & 0xffff0000u); a[2] += f * __uint_as_float(v[1] << 16); a[3] += f * __uint_as_float(v[1] & 0xffff0000u);
            b2[0] += f * __uint_as_float(v[2] << 16); b2[1] += f * __uint_as_float(v[2] & 0xffff0000u); b2[2] += f * __uint_as_float(v[3] << 16); b2[3] += f * __uint_as_float(v[3] & 0xffff0000u); }
          { u32x4 v = __builtin_amdgcn_raw_buffer_load_b128(rpt, 196608 + w * 8192 + 1024 + lane * 16, 0, 16); const float f = fw[0][1];
            a[0] += f * __uint_as_float(v[0] << 16); a[1] += f * __uint_as_float(v[0] & 0xffff0000u); a[2] += f * __uint_as_float(v[1] << 16); a[3] += f * __uint_as_float(v[1] & 0xffff0000u);
            b2[0] += f * __uint_as_float(v[2] << 16); b2[1] += f * __uint_as_float(v[2] & 0xffff0000u); b2[2] += f * __uint_as_float(v[3] << 16); b2[3] += f * __uint_as_float(v[3] & 0xffff0000u); }
          { u32x4 v = __builtin_amdgcn_raw_buffer_load_b128(rpt, 458752 + w * 8192 + 1024 + lane * 16, 0, 16); const float f = fw[0][3];
            a[0] += f * __uint_as_float(v[0] << 16); a[1] += f * __uint_as_float(v[0] & 0xffff0000u); a[2] += f * __uint_as_float(v[1] << 16); a[3] += f * __uint_as_float(v[1] & 0xffff0000u);
            b2[0] += f * __uint_as_float(v[2] << 16); b2[1] += f * __uint_as_float(v[2] & 0xffff0000u); b2[2] += f * __uint_as_float(v[3] << 16); b2[3] += f * __uint_as_float(v[3] & 0xffff0000u); }
          if (R < 224) { u16* dst = o + ((size_t)((kvh * 2 + hl) * 8 + 4) * NROW + orow) * 32 + 16 + 4 * h;
            u32x2 v0 = {cvtpk(a[0], a[1]), cvtpk(a[2], a[3])}; u32x2 v1 = {cvtpk(b2[0], b2[1]), cvtpk(b2[2], b2[3])};
            *(u32x2*)dst = v0; *(u32x2*)(dst + 8) = v1; } }
        { f32x4 a = acc_read4<80>(), b2 = acc_read4<84>(); const float fo = fw[0][2] * inv[0]; a *= fo; b2 *= fo;
          { u32x4 v = __builtin_amdgcn_raw_buffer_load_b128(rpt, 65536 + w * 8192 + 2048 + lane * 16, 0, 16); const float f = fw[0][0];
            a[0] += f * __uint_as_float(v[0] << 16); a[1] += f * __uint_as_float(v[0] & 0xffff0000u); a[2] += f * __uint_as_float(v[1] << 16); a[3] += f * __uint_as_float(v[1] & 0xffff0000u);
            b2[0] += f * __uint_as_float(v[2] << 16); b2[1] += f * __uint_as_float(v[2] & 0xffff0000u); b2[2] += f * __uint_as_float(v[3] << 16); b2[3] += f * __uint_as_float(v[3] & 0xffff0000u); }
          { u32x4 v = __builtin_amdgcn_raw_buffer_load_b128(rpt, 196608 + w * 8192 + 2048 + lane * 16, 0, 16); const float f = fw[0][1];
            a[0] += f * __uint_as_float(v[0] << 16); a[1] += f * __uint_as_float(v[0] & 0xffff0000u); a[2] += f * __uint_as_float(v[1] << 16); a[3] += f * __uint_as_float(v[1] & 0xffff0000u);
            b2[0] += f * __uint_as_float(v[2] << 16); b2[1] += f * __uint_as_float(v[2] & 0xffff0000u); b2[2] += f * __uint_as_float(v[3] << 16); b2[3] += f * __uint_as_float(v[3] & 0xffff0000u); }
          { u32x4 v = __builtin_amdgcn_raw_buffer_load_b128(rpt, 458752 + w * 8192 + 2048 + lane * 16, 0, 16); const float f = fw[0][3];
            a[0] += f * __uint_as_float(v[0] << 16); a[1] += f * __uint_as_float(v[0] & 0xffff0000u); a[2] += f * __uint_as_float(v[1] << 16); a[3] += f * __uint_as_float(v[1] & 0xffff0000u);
            b2[0] += f * __uint_as_float(v[2] << 16); b2[1] += f * __uint_as_float(v[2] & 0xffff0000u); b2[2] += f * __uint_as_float(v[3] << 16); b2[3] += f * __uint_as_float(v[3] & 0xffff0000u); }
          if (R < 224) { u16* dst = o + ((size_t)((kvh * 2 + hl) * 8 + 5) * NROW + orow) * 32 + 0 + 4 * h;
            u32x2 v0 = {cvtpk(a[0], a[1]), cvtpk(a[2], a[3])}; u32x2 v1 = {cvtpk(b2[0], b2[1]), cvtpk(b2[2], b2[3])};
            *(u32x2*)dst = v0; *(u32x2*)(dst + 8) = v1; } }
        { f32x4 a = acc_read4<88>(), b2 = acc_read4<92>(); const float fo = fw[0][2] * inv[0]; a *= fo; b2 *= fo;
          { u32x4 v = __builtin_amdgcn_raw_buffer_load_b128(rpt, 65536 + w * 8192 + 3072 + lane * 16, 0, 16); const float f = fw[0][0];
            a[0] += f * __uint_as_float(v[0] << 16); a[1] += f * __uint_as_float(v[0] & 0xffff0000u); a[2] += f * __uint_as_float(v[1] << 16); a[3] += f * __uint_as_float(v[1] & 0xffff0000u);
            b2[0] += f * __uint_as_float(v[2] << 16); b2[1] += f * __uint_as_float(v[2] & 0xffff0000u); b2[2] += f * __uint_as_float(v[3] << 16); b2[3] += f * __uint_as_float(v[3] & 0xffff0000u); }
          { u32x4 v = __builtin_amdgcn_raw_buffer_load_b128(rpt, 196608 + w * 8192 + 3072 + lane * 16, 0, 16); const float f = fw[0][1];
            a[0] += f * __uint_as_float(v[0] << 16); a[1] += f * __uint_as_float(v[0] & 0xffff0000u); a[2] += f * __uint_as_float(v[1] << 16); a[3] += f * __uint_as_float(v[1] & 0xffff0000u);
            b2[0] += f * __uint_as_float(v[2] << 16); b2[1] += f * __uint_as_float(v[2] & 0xffff0000u); b2[2] += f * __uint_as_float(v[3] << 16); b2[3] += f * __uint_as_float(v[3] & 0xffff0000u); }
          { u32x4 v = __builtin_amdgcn_raw_buffer_load_b128(rpt, 458752 + w * 8192 + 3072 + lane * 16, 0, 16); const float f = fw[0][3];
            a[0] += f * __uint_as_float(v[0] << 16); a[1] += f * __uint_as_float(v[0] & 0xffff0000u); a[2] += f * __uint_as_float(v[1] << 16); a[3] += f * __uint_as_float(v[1] & 0xffff0000u);
            b2[0] += f * __uint_as_float(v[2] << 16); b2[1] += f * __uint_as_float(v[2] & 0xffff0000u); b2[2] += f * __uint_as_float(v[3] << 16); b2[3] += f * __uint_as_float(v[3] & 0xffff0000u); }
          if (R < 224) { u16* dst = o + ((size_t)((kvh * 2 + hl) * 8 + 5) * NROW + orow) * 32 + 16 + 4 * h;
            u32x2 v0 = {cvtpk(a[0], a[1]), cvtpk(a[2], a[3])}; u32x2 v1 = {cvtpk(b2[0], b2[1]), cvtpk(b2[2], b2[3])};
            *(u32x2*)dst = v0; *(u32x2*)(dst + 8) = v1; } }
      }
      { const int R = 64 * w + 32 + r; const int hl = R >= 112 ? 1 : 0; const int orow = b * SQ + R - 112 * hl;
        { f32x4 a = acc_read4<192>(), b2 = acc_read4<196>(); const float fo = fw[1][2] * inv[1]; a *= fo; b2 *= fo;
          { u32x4 v = __builtin_amdgcn_raw_buffer_load_b128(rpt, 65536 + w * 8192 + 4096 + lane * 16, 0, 16); const float f = fw[1][0];
            a[0] += f * __uint_as_float(v[0] << 16); a[1] += f * __uint_as_float(v[0] & 0xffff0000u); a[2] += f * __uint_as_float(v[1] << 16); a[3] += f * __uint_as_float(v[1] & 0xffff0000u);
            b2[0] += f * __uint_as_float(v[2] << 16); b2[1] += f * __uint_as_float(v[2] & 0xffff0000u); b2[2] += f * __uint_as_float(v[3] << 16); b2[3] += f * __uint_as_float(v[3] & 0xffff0000u); }
          { u32x4 v = __builtin_amdgcn_raw_buffer_load_b128(rpt, 196608 + w * 8192 + 4096 + lane * 16, 0, 16); const float f = fw[1][1];
            a[0] += f * __uint_as_float(v[0] << 16); a[1] += f * __uint_as_float(v[0] & 0xffff0000u); a[2] += f * __uint_as_float(v[1] << 16); a[3] += f * __uint_as_float(v[1] & 0xffff0000u);
            b2[0] += f * __uint_as_float(v[2] << 16); b2[1] += f * __uint_as_float(v[2] & 0xffff0000u); b2[2] += f * __uint_as_float(v[3] << 16); b2[3] += f * __uint_as_float(v[3] & 0xffff0000u); }
          { u32x4 v = __builtin_amdgcn_raw_buffer_load_b128(rpt, 458752 + w * 8192 + 4096 + lane * 16, 0, 16); const float f = fw[1][3];
            a[0] += f * __uint_as_float(v[0] << 16); a[1] += f * __uint_as_float(v[0] & 0xffff0000u); a[2] += f * __uint_as_float(v[1] << 16); a[3] += f * __uint_as_float(v[1] & 0xffff0000u);
            b2[0] += f * __uint_as_float(v[2] << 16); b2[1] += f * __uint_as_float(v[2] & 0xffff0000u); b2[2] += f * __uint_as_float(v[3] << 16); b2[3] += f * __uint_as_float(v[3] & 0xffff0000u); }
          if (R < 224) { u16* dst = o + ((size_t)((kvh * 2 + hl) * 8 + 4) * NROW + orow) * 32 + 0 + 4 * h;
            u32x2 v0 = {cvtpk(a[0], a[1]), cvtpk(a[2], a[3])}; u32x2 v1 = {cvtpk(b2[0], b2[1]), cvtpk(b2[2], b2[3])};
            *(u32x2*)dst = v0; *(u32x2*)(dst + 8) = v1; } }
        { f32x4 a = acc_read4<200>(), b2 = acc_read4<204>(); const float fo = fw[1][2] * inv[1]; a *= fo; b2 *= fo;
          { u32x4 v = __builtin_amdgcn_raw_buffer_load_b128(rpt, 65536 + w * 8192 + 5120 + lane * 16, 0, 16); const float f = fw[1][0];
            a[0] += f * __uint_as_float(v[0] << 16); a[1] += f * __uint_as_float(v[0] & 0xffff0000u); a[2] += f * __uint_as_float(v[1] << 16); a[3] += f * __uint_as_float(v[1] & 0xffff0000u);
            b2[0] += f * __uint_as_float(v[2] << 16); b2[1] += f * __uint_as_float(v[2] & 0xffff0000u); b2[2] += f * __uint_as_float(v[3] << 16); b2[3] += f * __uint_as_float(v[3] & 0xffff0000u); }
          { u32x4 v = __builtin_amdgcn_raw_buffer_load_b128(rpt, 196608 + w * 8192 + 5120 + lane * 16, 0, 16); const float f = fw[1][1];
            a[0] += f * __uint_as_float(v[0] << 16); a[1] += f * __uint_as_float(v[0] & 0xffff0000u); a[2] += f * __uint_as_float(v[1] << 16); a[3] += f * __uint_as_float(v[1] & 0xffff0000u);
            b2[0] += f * __uint_as_float(v[2] << 16); b2[1] += f * __uint_as_float(v[2] & 0xffff0000u); b2[2] += f * __uint_as_float(v[3] << 16); b2[3] += f * __uint_as_float(v[3] & 0xffff0000u); }
          { u32x4 v = __builtin_amdgcn_raw_buffer_load_b128(rpt, 458752 + w * 8192 + 5120 + lane * 16, 0, 16); const float f = fw[1][3];
            a[0] += f * __uint_as_float(v[0] << 16); a[1] += f * __uint_as_float(v[0] & 0xffff0000u); a[2] += f * __uint_as_float(v[1] << 16); a[3] += f * __uint_as_float(v[1] & 0xffff0000u);
            b2[0] += f * __uint_as_float(v[2] << 16); b2[1] += f * __uint_as_float(v[2] & 0xffff0000u); b2[2] += f * __uint_as_float(v[3] << 16); b2[3] += f * __uint_as_float(v[3] & 0xffff0000u); }
          if (R < 224) { u16* dst = o + ((size_t)((kvh * 2 + hl) * 8 + 4) * NROW + orow) * 32 + 16 + 4 * h;
            u32x2 v0 = {cvtpk(a[0], a[1]), cvtpk(a[2], a[3])}; u32x2 v1 = {cvtpk(b2[0], b2[1]), cvtpk(b2[2], b2[3])};
            *(u32x2*)dst = v0; *(u32x2*)(dst + 8) = v1; } }
        { f32x4 a = acc_read4<208>(), b2 = acc_read4<212>(); const float fo = fw[1][2] * inv[1]; a *= fo; b2 *= fo;
          { u32x4 v = __builtin_amdgcn_raw_buffer_load_b128(rpt, 65536 + w * 8192 + 6144 + lane * 16, 0, 16); const float f = fw[1][0];
            a[0] += f * __uint_as_float(v[0] << 16); a[1] += f * __uint_as_float(v[0] & 0xffff0000u); a[2] += f * __uint_as_float(v[1] << 16); a[3] += f * __uint_as_float(v[1] & 0xffff0000u);
            b2[0] += f * __uint_as_float(v[2] << 16); b2[1] += f * __uint_as_float(v[2] & 0xffff0000u); b2[2] += f * __uint_as_float(v[3] << 16); b2[3] += f * __uint_as_float(v[3] & 0xffff0000u); }
          { u32x4 v = __builtin_amdgcn_raw_buffer_load_b128(rpt, 196608 + w * 8192 + 6144 + lane * 16, 0, 16); const float f = fw[1][1];
            a[0] += f * __uint_as_float(v[0] << 16); a[1] += f * __uint_as_float(v[0] & 0xffff0000u); a[2] += f * __uint_as_float(v[1] << 16); a[3] += f * __uint_as_float(v[1] & 0xffff0000u);
            b2[0] += f * __uint_as_float(v[2] << 16); b2[1] += f * __uint_as_float(v[2] & 0xffff0000u); b2[2] += f * __uint_as_float(v[3] << 16); b2[3] += f * __uint_as_float(v[3] & 0xffff0000u); }
          { u32x4 v = __builtin_amdgcn_raw_buffer_load_b128(rpt, 458752 + w * 8192 + 6144 + lane * 16, 0, 16); const float f = fw[1][3];
            a[0] += f * __uint_as_float(v[0] << 16); a[1] += f * __uint_as_float(v[0] & 0xffff0000u); a[2] += f * __uint_as_float(v[1] << 16); a[3] += f * __uint_as_float(v[1] & 0xffff0000u);
            b2[0] += f * __uint_as_float(v[2] << 16); b2[1] += f * __uint_as_float(v[2] & 0xffff0000u); b2[2] += f * __uint_as_float(v[3] << 16); b2[3] += f * __uint_as_float(v[3] & 0xffff0000u); }
          if (R < 224) { u16* dst = o + ((size_t)((kvh * 2 + hl) * 8 + 5) * NROW + orow) * 32 + 0 + 4 * h;
            u32x2 v0 = {cvtpk(a[0], a[1]), cvtpk(a[2], a[3])}; u32x2 v1 = {cvtpk(b2[0], b2[1]), cvtpk(b2[2], b2[3])};
            *(u32x2*)dst = v0; *(u32x2*)(dst + 8) = v1; } }
        { f32x4 a = acc_read4<216>(), b2 = acc_read4<220>(); const float fo = fw[1][2] * inv[1]; a *= fo; b2 *= fo;
          { u32x4 v = __builtin_amdgcn_raw_buffer_load_b128(rpt, 65536 + w * 8192 + 7168 + lane * 16, 0, 16); const float f = fw[1][0];
            a[0] += f * __uint_as_float(v[0] << 16); a[1] += f * __uint_as_float(v[0] & 0xffff0000u); a[2] += f * __uint_as_float(v[1] << 16); a[3] += f * __uint_as_float(v[1] & 0xffff0000u);
            b2[0] += f * __uint_as_float(v[2] << 16); b2[1] += f * __uint_as_float(v[2] & 0xffff0000u); b2[2] += f * __uint_as_float(v[3] << 16); b2[3] += f * __uint_as_float(v[3] & 0xffff0000u); }
          { u32x4 v = __builtin_amdgcn_raw_buffer_load_b128(rpt, 196608 + w * 8192 + 7168 + lane * 16, 0, 16); const float f = fw[1][1];
            a[0] += f * __uint_as_float(v[0] << 16); a[1] += f * __uint_as_float(v[0] & 0xffff0000u); a[2] += f * __uint_as_float(v[1] << 16); a[3] += f * __uint_as_float(v[1] & 0xffff0000u);
            b2[0] += f * __uint_as_float(v[2] << 16); b2[1] += f * __uint_as_float(v[2] & 0xffff0000u); b2[2] += f * __uint_as_float(v[3] << 16); b2[3] += f * __uint_as_float(v[3] & 0xffff0000u); }
          { u32x4 v = __builtin_amdgcn_raw_buffer_load_b128(rpt, 458752 + w * 8192 + 7168 + lane * 16, 0, 16); const float f = fw[1][3];
            a[0] += f * __uint_as_float(v[0] << 16); a[1] += f * __uint_as_float(v[0] & 0xffff0000u); a[2] += f * __uint_as_float(v[1] << 16); a[3] += f * __uint_as_float(v[1] & 0xffff0000u);
            b2[0] += f * __uint_as_float(v[2] << 16); b2[1] += f * __uint_as_float(v[2] & 0xffff0000u); b2[2] += f * __uint_as_float(v[3] << 16); b2[3] += f * __uint_as_float(v[3] & 0xffff0000u); }
          if (R < 224) { u16* dst = o + ((size_t)((kvh * 2 + hl) * 8 + 5) * NROW + orow) * 32 + 16 + 4 * h;
            u32x2 v0 = {cvtpk(a[0], a[1]), cvtpk(a[2], a[3])}; u32x2 v1 = {cvtpk(b2[0], b2[1]), cvtpk(b2[2], b2[3])};
            *(u32x2*)dst = v0; *(u32x2*)(dst + 8) = v1; } }
      }
    } break;
    case 3: {
      { const int R = 64 * w + 0 + r; const int hl = R >= 112 ? 1 : 0; const int orow = b * SQ + R - 112 * hl;
        { f32x4 a = acc_read4<96>(), b2 = acc_read4<100>(); const float fo = fw[0][3] * inv[0]; a *= fo; b2 *= fo;
          { u32x4 v = __builtin_amdgcn_raw_buffer_load_b128(rpt, 98304 + w * 8192 + 0 + lane * 16, 0, 16); const float f = fw[0][0];
            a[0] += f * __uint_as_float(v[0] << 16); a[1] += f * __uint_as_float(v[0] & 0xffff0000u); a[2] += f * __uint_as_float(v[1] << 16); a[3] += f * __uint_as_float(v[1] & 0xffff0000u);
            b2[0] += f * __uint_as_float(v[2] << 16); b2[1] += f * __uint_as_float(v[2] & 0xffff0000u); b2[2] += f * __uint_as_float(v[3] << 16); b2[3] += f * __uint_as_float(v[3] & 0xffff0000u); }
          { u32x4 v = __builtin_amdgcn_raw_buffer_load_b128(rpt, 229376 + w * 8192 + 0 + lane * 16, 0, 16); const float f = fw[0][1];
            a[0] += f * __uint_as_float(v[0] << 16); a[1] += f * __uint_as_float(v[0] & 0xffff0000u); a[2] += f * __uint_as_float(v[1] << 16); a[3] += f * __uint_as_float(v[1] & 0xffff0000u);
            b2[0] += f * __uint_as_float(v[2] << 16); b2[1] += f * __uint_as_float(v[2] & 0xffff0000u); b2[2] += f * __uint_as_float(v[3] << 16); b2[3] += f * __uint_as_float(v[3] & 0xffff0000u); }
          { u32x4 v = __builtin_amdgcn_raw_buffer_load_b128(rpt, 360448 + w * 8192 + 0 + lane * 16, 0, 16); const float f = fw[0][2];
            a[0] += f * __uint_as_float(v[0] << 16); a[1] += f * __uint_as_float(v[0] & 0xffff0000u); a[2] += f * __uint_as_float(v[1] << 16); a[3] += f * __uint_as_float(v[1] & 0xffff0000u);
            b2[0] += f * __uint_as_float(v[2] << 16); b2[1] += f * __uint_as_float(v[2] & 0xffff0000u); b2[2] += f * __uint_as_float(v[3] << 16); b2[3] += f * __uint_as_float(v[3] & 0xffff0000u); }
          if (R < 224) { u16* dst = o + ((size_t)((kvh * 2 + hl) * 8 + 6) * NROW + orow) * 32 + 0 + 4 * h;
            u32x2 v0 = {cvtpk(a[0], a[1]), cvtpk(a[2], a[3])}; u32x2 v1 = {cvtpk(b2[0], b2[1]), cvtpk(b2[2], b2[3])};
            *(u32x2*)dst = v0; *(u32x2*)(dst + 8) = v1; } }
        { f32x4 a = acc_read4<104>(), b2 = acc_read4<108>(); const float fo = fw[0][3] * inv[0]; a *= fo; b2 *= fo;
          { u32x4 v = __builtin_amdgcn_raw_buffer_load_b128(rpt, 98304 + w * 8192 + 1024 + lane * 16, 0, 16); const float f = fw[0][0];
            a[0] += f * __uint_as_float(v[0] << 16); a[1] += f * __uint_as_float(v[0] & 0xffff0000u); a[2] += f * __uint_as_float(v[1] << 16); a[3] += f * __uint_as_float(v[1] & 0xffff0000u);
            b2[0] += f * __uint_as_float(v[2] << 16); b2[1] += f * __uint_as_float(v[2] & 0xffff0000u); b2[2] += f * __uint_as_float(v[3] << 16); b2[3] += f * __uint_as_float(v[3] & 0xffff0000u); }
          { u32x4 v = __builtin_amdgcn_raw_buffer_load_b128(rpt, 229376 + w * 8192 + 1024 + lane * 16, 0, 16); const float f = fw[0][1];
            a[0] += f * __uint_as_float(v[0] << 16); a[1] += f * __uint_as_float(v[0] & 0xffff0000u); a[2] += f * __uint_as_float(v[1] << 16); a[3] += f * __uint_as_float(v[1] & 0xffff0000u);
            b2[0] += f * __uint_as_float(v[2] << 16); b2[1] += f * __uint_as_float(v[2] & 0xffff0000u); b2[2] += f * __uint_as_float(v[3] << 16); b2[3] += f * __uint_as_float(v[3] & 0xffff0000u); }
          { u32x4 v = __builtin_amdgcn_raw_buffer_load_b128(rpt, 360448 + w * 8192 + 1024 + lane * 16, 0, 16); const float f = fw[0][2];
            a[0] += f * __uint_as_float(v[0] << 16); a[1] += f * __uint_as_float(v[0] & 0xffff0000u); a[2] += f * __uint_as_float(v[1] << 16); a[3] += f * __uint_as_float(v[1] & 0xffff0000u);
            b2[0] += f * __uint_as_float(v[2] << 16); b2[1] += f * __uint_as_float(v[2] & 0xffff0000u); b2[2] += f * __uint_as_float(v[3] << 16); b2[3] += f * __uint_as_float(v[3] & 0xffff0000u); }
          if (R < 224) { u16* dst = o + ((size_t)((kvh * 2 + hl) * 8 + 6) * NROW + orow) * 32 + 16 + 4 * h;
            u32x2 v0 = {cvtpk(a[0], a[1]), cvtpk(a[2], a[3])}; u32x2 v1 = {cvtpk(b2[0], b2[1]), cvtpk(b2[2], b2[3])};
            *(u32x2*)dst = v0; *(u32x2*)(dst + 8) = v1; } }
        { f32x4 a = acc_read4<112>(), b2 = acc_read4<116>(); const float fo = fw[0][3] * inv[0]; a *= fo; b2 *= fo;
          { u32x4 v = __builtin_amdgcn_raw_buffer_load_b128(rpt, 98304 + w * 8192 + 2048 + lane * 16, 0, 16); const float f = fw[0][0];
            a[0] += f * __uint_as_float(v[0] << 16); a[1] += f * __uint_as_float(v[0] & 0xffff0000u); a[2] += f * __uint_as_float(v[1] << 16); a[3] += f * __uint_as_float(v[1] & 0xffff0000u);
            b2[0] += f * __uint_as_float(v[2] << 16); b2[1] += f * __uint_as_float(v[2] & 0xffff0000u); b2[2] += f * __uint_as_float(v[3] << 16); b2[3] += f * __uint_as_float(v[3] & 0xffff0000u); }
          { u32x4 v = __builtin_amdgcn_raw_buffer_load_b128(rpt, 229376 + w * 8192 + 2048 + lane * 16, 0, 16); const float f = fw[0][1];
            a[0] += f * __uint_as_float(v[0] << 16); a[1] += f * __uint_as_float(v[0] & 0xffff0000u); a[2] += f * __uint_as_float(v[1] << 16); a[3] += f * __uint_as_float(v[1] & 0xffff0000u);
            b2[0] += f * __uint_as_float(v[2] << 16); b2[1] += f * __uint_as_float(v[2] & 0xffff0000u); b2[2] += f * __uint_as_float(v[3] << 16); b2[3] += f * __uint_as_float(v[3] & 0xffff0000u); }
          { u32x4 v = __builtin_amdgcn_raw_buffer_load_b128(rpt, 360448 + w * 8192 + 2048 + lane * 16, 0, 16); const float f = fw[0][2];
            a[0] += f * __uint_as_float(v[0] << 16); a[1] += f * __uint_as_float(v[0] & 0xffff0000u); a[2] += f * __uint_as_float(v[1] << 16); a[3] += f * __uint_as_float(v[1] & 0xffff0000u);
            b2[0] += f * __uint_as_float(v[2] << 16); b2[1] += f * __uint_as_float(v[2] & 0xffff0000u); b2[2] += f * __uint_as_float(v[3] << 16); b2[3] += f * __uint_as_float(v[3] & 0xffff0000u); }
          if (R < 224) { u16* dst = o + ((size_t)((kvh * 2 + hl) * 8 + 7) * NROW + orow) * 32 + 0 + 4 * h;
            u32x2 v0 = {cvtpk(a[0], a[1]), cvtpk(a[2], a[3])}; u32x2 v1 = {cvtpk(b2[0], b2[1]), cvtpk(b2[2], b2[3])};
            *(u32x2*)dst = v0; *(u32x2*)(dst + 8) = v1; } }
        { f32x4 a = acc_read4<120>(), b2 = acc_read4<124>(); const float fo = fw[0][3] * inv[0]; a *= fo; b2 *= fo;
          { u32x4 v = __builtin_amdgcn_raw_buffer_load_b128(rpt, 98304 + w * 8192 + 3072 + lane * 16, 0, 16); const float f = fw[0][0];
            a[0] += f * __uint_as_float(v[0] << 16); a[1] += f * __uint_as_float(v[0] & 0xffff0000u); a[2] += f * __uint_as_float(v[1] << 16); a[3] += f * __uint_as_float(v[1] & 0xffff0000u);
            b2[0] += f * __uint_as_float(v[2] << 16); b2[1] += f * __uint_as_float(v[2] & 0xffff0000u); b2[2] += f * __uint_as_float(v[3] << 16); b2[3] += f * __uint_as_float(v[3] & 0xffff0000u); }
          { u32x4 v = __builtin_amdgcn_raw_buffer_load_b128(rpt, 229376 + w * 8192 + 3072 + lane * 16, 0, 16); const float f = fw[0][1];
            a[0] += f * __uint_as_float(v[0] << 16); a[1] += f * __uint_as_float(v[0] & 0xffff0000u); a[2] += f * __uint_as_float(v[1] << 16); a[3] += f * __uint_as_float(v[1] & 0xffff0000u);
            b2[0] += f * __uint_as_float(v[2] << 16); b2[1] += f * __uint_as_float(v[2] & 0xffff0000u); b2[2] += f * __uint_as_float(v[3] << 16); b2[3] += f * __uint_as_float(v[3] & 0xffff0000u); }
          { u32x4 v = __builtin_amdgcn_raw_buffer_load_b128(rpt, 360448 + w * 8192 + 3072 + lane * 16, 0, 16); const float f = fw[0][2];
            a[0] += f * __uint_as_float(v[0] << 16); a[1] += f * __uint_as_float(v[0] & 0xffff0000u); a[2] += f * __uint_as_float(v[1] << 16); a[3] += f * __uint_as_float(v[1] & 0xffff0000u);
            b2[0] += f * __uint_as_float(v[2] << 16); b2[1] += f * __uint_as_float(v[2] & 0xffff0000u); b2[2] += f * __uint_as_float(v[3] << 16); b2[3] += f * __uint_as_float(v[3] & 0xffff0000u); }
          if (R < 224) { u16* dst = o + ((size_t)((kvh * 2 + hl) * 8 + 7) * NROW + orow) * 32 + 16 + 4 * h;
            u32x2 v0 = {cvtpk(a[0], a[1]), cvtpk(a[2], a[3])}; u32x2 v1 = {cvtpk(b2[0], b2[1]), cvtpk(b2[2], b2[3])};
            *(u32x2*)dst = v0; *(u32x2*)(dst + 8) = v1; } }
      }
      { const int R = 64 * w + 32 + r; const int hl = R >= 112 ? 1 : 0; const int orow = b * SQ + R - 112 * hl;
        { f32x4 a = acc_read4<224>(), b2 = acc_read4<228>(); const float fo = fw[1][3] * inv[1]; a *= fo; b2 *= fo;
          { u32x4 v = __builtin_amdgcn_raw_buffer_load_b128(rpt, 98304 + w * 8192 + 4096 + lane * 16, 0, 16); const float f = fw[1][0];
            a[0] += f * __uint_as_float(v[0] << 16); a[1] += f * __uint_as_float(v[0] & 0xffff0000u); a[2] += f * __uint_as_float(v[1] << 16); a[3] += f * __uint_as_float(v[1] & 0xffff0000u);
            b2[0] += f * __uint_as_float(v[2] << 16); b2[1] += f * __uint_as_float(v[2] & 0xffff0000u); b2[2] += f * __uint_as_float(v[3] << 16); b2[3] += f * __uint_as_float(v[3] & 0xffff0000u); }
          { u32x4 v = __builtin_amdgcn_raw_buffer_load_b128(rpt, 229376 + w * 8192 + 4096 + lane * 16, 0, 16); const float f = fw[1][1];
            a[0] += f * __uint_as_float(v[0] << 16); a[1] += f * __uint_as_float(v[0] & 0xffff0000u); a[2] += f * __uint_as_float(v[1] << 16); a[3] += f * __uint_as_float(v[1] & 0xffff0000u);
            b2[0] += f * __uint_as_float(v[2] << 16); b2[1] += f * __uint_as_float(v[2] & 0xffff0000u); b2[2] += f * __uint_as_float(v[3] << 16); b2[3] += f * __uint_as_float(v[3] & 0xffff0000u); }
          { u32x4 v = __builtin_amdgcn_raw_buffer_load_b128(rpt, 360448 + w * 8192 + 4096 + lane * 16, 0, 16); const float f = fw[1][2];
            a[0] += f * __uint_as_float(v[0] << 16); a[1] += f * __uint_as_float(v[0] & 0xffff0000u); a[2] += f * __uint_as_float(v[1] << 16); a[3] += f * __uint_as_float(v[1] & 0xffff0000u);
            b2[0] += f * __uint_as_float(v[2] << 16); b2[1] += f * __uint_as_float(v[2] & 0xffff0000u); b2[2] += f * __uint_as_float(v[3] << 16); b2[3] += f * __uint_as_float(v[3] & 0xffff0000u); }
          if (R < 224) { u16* dst = o + ((size_t)((kvh * 2 + hl) * 8 + 6) * NROW + orow) * 32 + 0 + 4 * h;
            u32x2 v0 = {cvtpk(a[0], a[1]), cvtpk(a[2], a[3])}; u32x2 v1 = {cvtpk(b2[0], b2[1]), cvtpk(b2[2], b2[3])};
            *(u32x2*)dst = v0; *(u32x2*)(dst + 8) = v1; } }
        { f32x4 a = acc_read4<232>(), b2 = acc_read4<236>(); const float fo = fw[1][3] * inv[1]; a *= fo; b2 *= fo;
          { u32x4 v = __builtin_amdgcn_raw_buffer_load_b128(rpt, 98304 + w * 8192 + 5120 + lane * 16, 0, 16); const float f = fw[1][0];
            a[0] += f * __uint_as_float(v[0] << 16); a[1] += f * __uint_as_float(v[0] & 0xffff0000u); a[2] += f * __uint_as_float(v[1] << 16); a[3] += f * __uint_as_float(v[1] & 0xffff0000u);
            b2[0] += f * __uint_as_float(v[2] << 16); b2[1] += f * __uint_as_float(v[2] & 0xffff0000u); b2[2] += f * __uint_as_float(v[3] << 16); b2[3] += f * __uint_as_float(v[3] & 0xffff0000u); }
          { u32x4 v = __builtin_amdgcn_raw_buffer_load_b128(rpt, 229376 + w * 8192 + 5120 + lane * 16, 0, 16); const float f = fw[1][1];
            a[0] += f * __uint_as_float(v[0] << 16); a[1] += f * __uint_as_float(v[0] & 0xffff0000u); a[2] += f * __uint_as_float(v[1] << 16); a[3] += f * __uint_as_float(v[1] & 0xffff0000u);
            b2[0] += f * __uint_as_float(v[2] << 16); b2[1] += f * __uint_as_float(v[2] & 0xffff0000u); b2[2] += f * __uint_as_float(v[3] << 16); b2[3] += f * __uint_as_float(v[3] & 0xffff0000u); }
          { u32x4 v = __builtin_amdgcn_raw_buffer_load_b128(rpt, 360448 + w * 8192 + 5120 + lane * 16, 0, 16); const float f = fw[1][2];
            a[0] += f * __uint_as_float(v[0] << 16); a[1] += f * __uint_as_float(v[0] & 0xffff0000u); a[2] += f * __uint_as_float(v[1] << 16); a[3] += f * __uint_as_float(v[1] & 0xffff0000u);
            b2[0] += f * __uint_as_float(v[2] << 16); b2[1] += f * __uint_as_float(v[2] & 0xffff0000u); b2[2] += f * __uint_as_float(v[3] << 16); b2[3] += f * __uint_as_float(v[3] & 0xffff0000u); }
          if (R < 224) { u16* dst = o + ((size_t)((kvh * 2 + hl) * 8 + 6) * NROW + orow) * 32 + 16 + 4 * h;
            u32x2 v0 = {cvtpk(a[0], a[1]), cvtpk(a[2], a[3])}; u32x2 v1 = {cvtpk(b2[0], b2[1]), cvtpk(b2[2], b2[3])};
            *(u32x2*)dst = v0; *(u32x2*)(dst + 8) = v1; } }
        { f32x4 a = acc_read4<240>(), b2 = acc_read4<244>(); const float fo = fw[1][3] * inv[1]; a *= fo; b2 *= fo;
          { u32x4 v = __builtin_amdgcn_raw_buffer_load_b128(rpt, 98304 + w * 8192 + 6144 + lane * 16, 0, 16); const float f = fw[1][0];
            a[0] += f * __uint_as_float(v[0] << 16); a[1] += f * __uint_as_float(v[0] & 0xffff0000u); a[2] += f * __uint_as_float(v[1] << 16); a[3] += f * __uint_as_float(v[1] & 0xffff0000u);
            b2[0] += f * __uint_as_float(v[2] << 16); b2[1] += f * __uint_as_float(v[2] & 0xffff0000u); b2[2] += f * __uint_as_float(v[3] << 16); b2[3] += f * __uint_as_float(v[3] & 0xffff0000u); }
          { u32x4 v = __builtin_amdgcn_raw_buffer_load_b128(rpt, 229376 + w * 8192 + 6144 + lane * 16, 0, 16); const float f = fw[1][1];
            a[0] += f * __uint_as_float(v[0] << 16); a[1] += f * __uint_as_float(v[0] & 0xffff0000u); a[2] += f * __uint_as_float(v[1] << 16); a[3] += f * __uint_as_float(v[1] & 0xffff0000u);
            b2[0] += f * __uint_as_float(v[2] << 16); b2[1] += f * __uint_as_float(v[2] & 0xffff0000u); b2[2] += f * __uint_as_float(v[3] << 16); b2[3] += f * __uint_as_float(v[3] & 0xffff0000u); }
          { u32x4 v = __builtin_amdgcn_raw_buffer_load_b128(rpt, 360448 + w * 8192 + 6144 + lane * 16, 0, 16); const float f = fw[1][2];
            a[0] += f * __uint_as_float(v[0] << 16); a[1] += f * __uint_as_float(v[0] & 0xffff0000u); a[2] += f * __uint_as_float(v[1] << 16); a[3] += f * __uint_as_float(v[1] & 0xffff0000u);
            b2[0] += f * __uint_as_float(v[2] << 16); b2[1] += f * __uint_as_float(v[2] & 0xffff0000u); b2[2] += f * __uint_as_float(v[3] << 16); b2[3] += f * __uint_as_float(v[3] & 0xffff0000u); }
          if (R < 224) { u16* dst = o + ((size_t)((kvh * 2 + hl) * 8 + 7) * NROW + orow) * 32 + 0 + 4 * h;
            u32x2 v0 = {cvtpk(a[0], a[1]), cvtpk(a[2], a[3])}; u32x2 v1 = {cvtpk(b2[0], b2[1]), cvtpk(b2[2], b2[3])};
            *(u32x2*)dst = v0; *(u32x2*)(dst + 8) = v1; } }
        { f32x4 a = acc_read4<248>(), b2 = acc_read4<252>(); const float fo = fw[1][3] * inv[1]; a *= fo; b2 *= fo;
          { u32x4 v = __builtin_amdgcn_raw_buffer_load_b128(rpt, 98304 + w * 8192 + 7168 + lane * 16, 0, 16); const float f = fw[1][0];
            a[0] += f * __uint_as_float(v[0] << 16); a[1] += f * __uint_as_float(v[0] & 0xffff0000u); a[2] += f * __uint_as_float(v[1] << 16); a[3] += f * __uint_as_float(v[1] & 0xffff0000u);
            b2[0] += f * __uint_as_float(v[2] << 16); b2[1] += f * __uint_as_float(v[2] & 0xffff0000u); b2[2] += f * __uint_as_float(v[3] << 16); b2[3] += f * __uint_as_float(v[3] & 0xffff0000u); }
          { u32x4 v = __builtin_amdgcn_raw_buffer_load_b128(rpt, 229376 + w * 8192 + 7168 + lane * 16, 0, 16); const float f = fw[1][1];
            a[0] += f * __uint_as_float(v[0] << 16); a[1] += f * __uint_as_float(v[0] & 0xffff0000u); a[2] += f * __uint_as_float(v[1] << 16); a[3] += f * __uint_as_float(v[1] & 0xffff0000u);
            b2[0] += f * __uint_as_float(v[2] << 16); b2[1] += f * __uint_as_float(v[2] & 0xffff0000u); b2[2] += f * __uint_as_float(v[3] << 16); b2[3] += f * __uint_as_float(v[3] & 0xffff0000u); }
          { u32x4 v = __builtin_amdgcn_raw_buffer_load_b128(rpt, 360448 + w * 8192 + 7168 + lane * 16, 0, 16); const float f = fw[1][2];
            a[0] += f * __uint_as_float(v[0] << 16); a[1] += f * __uint_as_float(v[0] & 0xffff0000u); a[2] += f * __uint_as_float(v[1] << 16); a[3] += f * __uint_as_float(v[1] & 0xffff0000u);
            b2[0] += f * __uint_as_float(v[2] << 16); b2[1] += f * __uint_as_float(v[2] & 0xffff0000u); b2[2] += f * __uint_as_float(v[3] << 16); b2[3] += f * __uint_as_float(v[3] & 0xffff0000u); }
          if (R < 224) { u16* dst = o + ((size_t)((kvh * 2 + hl) * 8 + 7) * NROW + orow) * 32 + 16 + 4 * h;
            u32x2 v0 = {cvtpk(a[0], a[1]), cvtpk(a[2], a[3])}; u32x2 v1 = {cvtpk(b2[0], b2[1]), cvtpk(b2[2], b2[3])};
            *(u32x2*)dst = v0; *(u32x2*)(dst + 8) = v1; } }
      }
    } break;
  }
  STAMP(10);
}
__global__ __launch_bounds__(512, 2) void oproj2_kernel(const u16* __restrict__ o, const u16* __restrict__ wo,
                                                        u16* __restrict__ out) {
  extern __shared__ __attribute__((aligned(16))) char lds[];
  const int tid = threadIdx.x, lane = tid & 63;
  const int w = __builtin_amdgcn_readfirstlane(tid >> 6);
  const int r = lane & 31, h = lane >> 5;
  const int xcd = blockIdx.x & 7, idx = blockIdx.x >> 3;
  const int rt = xcd + 8 * (idx / 7), ct = idx % 7;
  if (rt >= 28) return;
  const int col0 = ct * 64;
  char* ring = lds + w * 18432;
  __amdgpu_buffer_rsrc_t rw = __builtin_amdgcn_make_buffer_rsrc((void*)(wo + (size_t)(512 * w) * DM + col0), 0,
                                                                 (512 * DM - col0) * 2, 0x00020000);
  __amdgpu_buffer_rsrc_t ra = __builtin_amdgcn_make_buffer_rsrc((void*)(o + ((size_t)(16 * w) * NROW + rt * 32) * 32), 0,
                                                                 (16 * NROW - rt * 32) * 64, 0x00020000);
  const int drow = lane >> 3;
  const int wvoff = drow * (DM * 2) + (((lane & 7) ^ (((drow >> 1) & 1) << 2)) * 16);
  const int arow = lane >> 2;
  const int avoff = arow * 64 + (((lane & 3) ^ ((arow >> 2) & 3)) * 16);
  auto issue = [&](int u) {
    char* dst = ring + (u % 3) * 6144;
#pragma unroll
    for (int p = 0; p < 4; ++p)
      __builtin_amdgcn_raw_ptr_buffer_load_lds(rw, (lds_vp)(dst + p * 1024), 16, wvoff, (32 * u + 8 * p) * (DM * 2), 0, 0);
#pragma unroll
    for (int p = 0; p < 2; ++p)
      __builtin_amdgcn_raw_ptr_buffer_load_lds(ra, (lds_vp)(dst + 4096 + p * 1024), 16, avoff, 1024 * p + u * (NROW * 64), 0, 0);
  };
  issue(0); issue(1); issue(2);
  const int g = lane >> 4, qq = (lane >> 2) & 3, pp = lane & 3;
  const unsigned rb = (unsigned)(size_t)ring;
  const unsigned trb = rb + (8 * (g >> 1) + qq) * 128 + ((32 * (g & 1) + 8 * pp) ^ ((qq >> 1) << 6));
  const unsigned ab0 = rb + 4096 + r * 64 + (((0 + h) ^ ((r >> 2) & 3)) * 16);
  const unsigned ab1 = rb + 4096 + r * 64 + (((2 + h) ^ ((r >> 2) & 3)) * 16);
  f32x16 acc[2];
#pragma unroll
  for (int b2 = 0; b2 < 2; ++b2)
#pragma unroll
    for (int i = 0; i < 16; ++i) acc[b2][i] = 0.f;
#pragma unroll
  for (int u = 0; u < 16; ++u) {
    if (u <= 13) WAITV(12); else if (u == 14) WAITV(6); else WAITV(0);
    const unsigned so = (u % 3) * 6144;
    s16x4 a[2][4];
    bf16x8 xb[2];
#pragma unroll
    for (int s = 0; s < 2; ++s) {
      a[s][0] = trread(trb + so + s * 2048);
      a[s][1] = trread(trb + so + s * 2048 + 512);
      a[s][2] = trread((trb + so + s * 2048) ^ 64);
      a[s][3] = trread((trb + so + s * 2048 + 512) ^ 64);
    }
    asm volatile("ds_read_b128 %0, %1" : "=v"(xb[0]) : "v"(ab0 + so) : "memory");
    asm volatile("ds_read_b128 %0, %1" : "=v"(xb[1]) : "v"(ab1 + so) : "memory");
    asm volatile("s_waitcnt lgkmcnt(0)"
                 : "+v"(a[0][0]), "+v"(a[0][1]), "+v"(a[0][2]), "+v"(a[0][3]), "+v"(a[1][0]), "+v"(a[1][1]),
                   "+v"(a[1][2]), "+v"(a[1][3]), "+v"(xb[0]), "+v"(xb[1])::"memory");
    if (u + 3 < 16) issue(u + 3);
#pragma unroll
    for (int s = 0; s < 2; ++s) {
      acc[0] = __builtin_amdgcn_mfma_f32_32x32x16_bf16(cat8(a[s][0], a[s][1]), xb[s], acc[0], 0, 0, 0);
      acc[1] = __builtin_amdgcn_mfma_f32_32x32x16_bf16(cat8(a[s][2], a[s][3]), xb[s], acc[1], 0, 0, 0);
    }
  }
  __syncthreads();
  float* red = (float*)lds;
#pragma unroll
  for (int cb = 0; cb < 2; ++cb)
#pragma unroll
    for (int gg = 0; gg < 4; ++gg) {
      f32x4 v = {acc[cb][4 * gg], acc[cb][4 * gg + 1], acc[cb][4 * gg + 2], acc[cb][4 * gg + 3]};
      *(f32x4*)(red + ((w * 8 + cb * 4 + gg) * 64 + lane) * 4) = v;
    }
  __syncthreads();
  {
    const int cg = w, cb = cg >> 2, gg = cg & 3;
    f32x4 s = {0.f, 0.f, 0.f, 0.f};
#pragma unroll
    for (int ww = 0; ww < 8; ++ww) s += *(const f32x4*)(red + ((ww * 8 + cg) * 64 + lane) * 4);
    u32x2 v = {cvtpk(s[0], s[1]), cvtpk(s[2], s[3])};
    *(u32x2*)(out + (size_t)(rt * 32 + r) * DM + col0 + 32 * cb + 8 * gg + 4 * h) = v;
  }
}
__global__ __launch_bounds__(512, 2) void oproj3_kernel(const u16* __restrict__ o, const u16* __restrict__ wo,
                                                        u16* __restrict__ out, float* __restrict__ slab,
                                                        unsigned* __restrict__ cnt DBGARG) {
  extern __shared__ __attribute__((aligned(16))) char lds[];
  STAMP(0);
  const int tid = threadIdx.x, lane = tid & 63;
  const int w = __builtin_amdgcn_readfirstlane(tid >> 6);
  const int r = lane & 31, h = lane >> 5;
  const int xcd = blockIdx.x & 7, idx = blockIdx.x >> 3;
  const int rt = xcd + 8 * (idx / 14), ct = (idx % 14) >> 1, kh = idx & 1;
  if (rt >= 14) return;
  const int tile = rt * 7 + ct;
  const int col0 = ct * 64, k0 = kh * 2048 + 256 * w;
  char* ring = lds + w * 16384;
  __amdgpu_buffer_rsrc_t rw = __builtin_amdgcn_make_buffer_rsrc((void*)(wo + (size_t)k0 * DM + col0), 0,
                                                                 (256 * DM - col0) * 2, 0x00020000);
  __amdgpu_buffer_rsrc_t ra = __builtin_amdgcn_make_buffer_rsrc((void*)(o + ((size_t)(k0 >> 5) * NROW + rt * 64) * 32), 0,
                                                                 (8 * NROW - rt * 64) * 64, 0x00020000);
  const int drow = lane >> 3;
  const int wvoff = drow * (DM * 2) + (((lane & 7) ^ (((drow >> 1) & 1) << 2)) * 16);
  const int arow = lane >> 2;
  const int avoff = arow * 64 + (((lane & 3) ^ ((arow >> 2) & 3)) * 16);
  auto issue = [&](int u) {
    char* dst = ring + (u & 1) * 8192;
#pragma unroll
    for (int p = 0; p < 4; ++p)
      __builtin_amdgcn_raw_ptr_buffer_load_lds(rw, (lds_vp)(dst + p * 1024), 16, wvoff, (32 * u + 8 * p) * (DM * 2), 0, 0);
#pragma unroll
    for (int p = 0; p < 4; ++p)
      __builtin_amdgcn_raw_ptr_buffer_load_lds(ra, (lds_vp)(dst + 4096 + p * 1024), 16, avoff, 1024 * p + u * (NROW * 64), 0, 0);
  };
  issue(0); issue(1);
  const int g = lane >> 4, qq = (lane >> 2) & 3, pp = lane & 3;
  const unsigned rb = (unsigned)(size_t)ring;
  const unsigned trb = rb + (8 * (g >> 1) + qq) * 128 + ((32 * (g & 1) + 8 * pp) ^ ((qq >> 1) << 6));
  const unsigned ab0 = rb + 4096 + r * 64 + (((0 + h) ^ ((r >> 2) & 3)) * 16);
  const unsigned ab1 = rb + 4096 + r * 64 + (((2 + h) ^ ((r >> 2) & 3)) * 16);
  f32x16 acc[2][2];
#pragma unroll
  for (int a2 = 0; a2 < 2; ++a2)
#pragma unroll
    for (int b2 = 0; b2 < 2; ++b2)
#pragma unroll
      for (int i = 0; i < 16; ++i) acc[a2][b2][i] = 0.f;
#pragma unroll
  for (int u = 0; u < 8; ++u) {
    if (u <= 6) WAITV(8); else WAITV(0);
    const unsigned so = (u & 1) * 8192;
    s16x4 a[2][4];
    bf16x8 xb[2][2];
#pragma unroll
    for (int s = 0; s < 2; ++s) {
      a[s][0] = trread(trb + so + s * 2048);
      a[s][1] = trread(trb + so + s * 2048 + 512);
      a[s][2] = trread((trb + so + s * 2048) ^ 64);
      a[s][3] = trread((trb + so + s * 2048 + 512) ^ 64);
    }
    asm volatile("ds_read_b128 %0, %1" : "=v"(xb[0][0]) : "v"(ab0 + so) : "memory");
    asm volatile("ds_read_b128 %0, %1" : "=v"(xb[1][0]) : "v"(ab1 + so) : "memory");
    asm volatile("ds_read_b128 %0, %1 offset:2048" : "=v"(xb[0][1]) : "v"(ab0 + so) : "memory");
    asm volatile("ds_read_b128 %0, %1 offset:2048" : "=v"(xb[1][1]) : "v"(ab1 + so) : "memory");
    asm volatile("s_waitcnt lgkmcnt(0)"
                 : "+v"(a[0][0]), "+v"(a[0][1]), "+v"(a[0][2]), "+v"(a[0][3]), "+v"(a[1][0]), "+v"(a[1][1]),
                   "+v"(a[1][2]), "+v"(a[1][3]), "+v"(xb[0][0]), "+v"(xb[1][0]), "+v"(xb[0][1]), "+v"(xb[1][1])::"memory");
    if (u + 2 < 8) issue(u + 2);
#pragma unroll
    for (int s = 0; s < 2; ++s)
#pragma unroll
      for (int rb2 = 0; rb2 < 2; ++rb2) {
        acc[rb2][0] = __builtin_amdgcn_mfma_f32_32x32x16_bf16(cat8(a[s][0], a[s][1]), xb[s][rb2], acc[rb2][0], 0, 0, 0);
        acc[rb2][1] = __builtin_amdgcn_mfma_f32_32x32x16_bf16(cat8(a[s][2], a[s][3]), xb[s][rb2], acc[rb2][1], 0, 0, 0);
      }
  }
  STAMP(1);
  __syncthreads();
  float* red = (float*)lds;
#pragma unroll
  for (int rb2 = 0; rb2 < 2; ++rb2)
#pragma unroll
    for (int cb = 0; cb < 2; ++cb)
#pragma unroll
      for (int gg = 0; gg < 4; ++gg) {
        f32x4 v = {acc[rb2][cb][4 * gg], acc[rb2][cb][4 * gg + 1], acc[rb2][cb][4 * gg + 2], acc[rb2][cb][4 * gg + 3]};
        *(f32x4*)(red + ((w * 16 + rb2 * 8 + cb * 4 + gg) * 64 + lane) * 4) = v;
      }
  __syncthreads();
  f32x4 sum[2];
#pragma unroll
  for (int jj = 0; jj < 2; ++jj) {
    const int grp = 2 * w + jj;
    f32x4 s = {0.f, 0.f, 0.f, 0.f};
#pragma unroll
    for (int ww = 0; ww < 8; ++ww) s += *(const f32x4*)(red + ((ww * 16 + grp) * 64 + lane) * 4);
    sum[jj] = s;
  }
  STAMP(2);
  __amdgpu_buffer_rsrc_t rs = __builtin_amdgcn_make_buffer_rsrc((void*)(slab + (size_t)tile * 2 * 4096), 0, 2 * 4096 * 4, 0x00020000);
#pragma unroll
  for (int jj = 0; jj < 2; ++jj)
    __builtin_amdgcn_raw_buffer_store_b128(__builtin_bit_cast(u32x4, sum[jj]), rs, (kh * 16 + 2 * w + jj) * 1024 + lane * 16, 0, 16);
  asm volatile("s_waitcnt vmcnt(0)" ::: "memory");
  __syncthreads();
  unsigned* flag = (unsigned*)(lds + 131072);
  if (tid == 0) *flag = __hip_atomic_fetch_add(cnt + tile, 1u, __ATOMIC_RELAXED, __HIP_MEMORY_SCOPE_AGENT);
  __syncthreads();
  STAMP(3);
  if (*flag == 1u) {
#pragma unroll
    for (int jj = 0; jj < 2; ++jj) {
      u32x4 pv = __builtin_amdgcn_raw_buffer_load_b128(rs, ((kh ^ 1) * 16 + 2 * w + jj) * 1024 + lane * 16, 0, 16);
      f32x4 t = sum[jj] + __builtin_bit_cast(f32x4, pv);
      const int grp = 2 * w + jj, rb2 = grp >> 3, cb = (grp >> 2) & 1, gg = grp & 3;
      u32x2 v = {cvtpk(t[0], t[1]), cvtpk(t[2], t[3])};
      *(u32x2*)(out + (size_t)(rt * 64 + 32 * rb2 + r) * DM + col0 + 32 * cb + 8 * gg + 4 * h) = v;
    }
  }
  STAMP(4);
}
extern "C" void kernel_launch(void* const* d_in, const int* in_sizes, int n_in, void* d_out, int out_size,
                              void* d_ws, size_t ws_size, hipStream_t stream) {
  const u16* x = (const u16*)d_in[0];
  const u16* kc = (const u16*)d_in[1];
  const u16* vc = (const u16*)d_in[2];
  const u16* wq = (const u16*)d_in[3];
  const u16* wo = (const u16*)d_in[4];
  u16* out = (u16*)d_out;
  u16* qws = (u16*)d_ws;
  u16* ows = (u16*)((char*)d_ws + (8  __H0__ ));
  float* slab = (float*)((char*)d_ws + (40 << 20));
  unsigned* cnt = (unsigned*)((char*)d_ws + (48 << 20));
  (void)hipFuncSetAttribute((const void*)attn4_kernel, hipFuncAttributeMaxDynamicSharedMemorySize, 163840);
  hipLaunchKernelGGL(attn4_kernel, dim3(256), dim3(256), 163840, stream, x, wq, kc, vc, wo, ows, cnt
  );
  hipLaunchKernelGGL(headsum_kernel, dim3(392), dim3(256), 0, stream, (const float*)ows, out);
  return;
  (void)hipFuncSetAttribute((const void*)oproj2_kernel, hipFuncAttributeMaxDynamicSharedMemorySize, 163840);
  hipLaunchKernelGGL(oproj2_kernel, dim3(8 * 28), dim3(512), 163840, stream, (const u16*)ows, wo, out);
  return;
  (void)hipFuncSetAttribute((const void*)oproj3_kernel, hipFuncAttributeMaxDynamicSharedMemorySize, 131328);
  hipLaunchKernelGGL(oproj3_kernel, dim3(8 * 28), dim3(512), 131328, stream, (const u16*)ows, wo, out, slab, cnt
  );
}
```

```cpp
#include <hip/hip_runtime.h>
#include <hip/hip_bf16.h>
#include <utility>

typedef unsigned short u16;
typedef __attribute__((ext_vector_type(8))) short bf16x8;
typedef __attribute__((ext_vector_type(4))) short s16x4;
typedef __attribute__((ext_vector_type(16))) float f32x16;
typedef __attribute__((ext_vector_type(4))) float f32x4;
typedef __attribute__((ext_vector_type(2))) float f32x2;
typedef __attribute__((ext_vector_type(2))) __bf16 bf16x2v;
typedef __attribute__((ext_vector_type(2))) unsigned u32x2;
typedef __attribute__((ext_vector_type(4))) unsigned u32x4;
#define DI __device__ __forceinline__

constexpr int NB = 8, SQ = 112, DM = 448, HQ = 16, HKV = 8, SK = 1024, DH = 256;
constexpr int NROW = NB * SQ;
constexpr int NQ = HQ * DH;

DI unsigned cvtpk(float lo, float hi) {
  f32x2 v = {lo, hi};
  bf16x2v b = __builtin_convertvector(v, bf16x2v);
  return __builtin_bit_cast(unsigned, b);
}
#define WAITV(n) asm volatile("s_waitcnt vmcnt(" #n ")" ::: "memory")
#define WAITL0() asm volatile("s_waitcnt lgkmcnt(0)" ::: "memory")

DI s16x4 trread(unsigned addr) {
  s16x4 r;
  asm volatile("ds_read_b64_tr_b16 %0, %1" : "=v"(r) : "v"(addr) : "memory");
  return r;
}
template <int OFF> DI s16x4 trread_o(unsigned addr) {
  s16x4 r;
  asm volatile("ds_read_b64_tr_b16 %0, %1 offset:%2" : "=v"(r) : "v"(addr), "i"(OFF) : "memory");
  return r;
}
DI bf16x8 cat8(s16x4 a, s16x4 b) { return __builtin_shufflevector(a, b, 0, 1, 2, 3, 4, 5, 6, 7); }

constexpr float SCL = 0.0625f * 1.4426950408889634f;
constexpr float THR = 8.0f;
typedef __attribute__((address_space(3))) void* lds_vp;
#define STAMP(i)
#define DBGARG

#define GAP() __builtin_amdgcn_sched_barrier(0)
#define KVAUX 0
#define ACLOB "a0","a1","a2","a3","a4","a5","a6","a7","a8","a9","a10","a11","a12","a13","a14","a15","a16","a17","a18","a19","a20","a21","a22","a23","a24","a25","a26","a27","a28","a29","a30","a31","a32","a33","a34","a35","a36","a37","a38","a39","a40","a41","a42","a43","a44","a45","a46","a47","a48","a49","a50","a51","a52","a53","a54","a55","a56","a57","a58","a59","a60","a61","a62","a63","a64","a65","a66","a67","a68","a69","a70","a71","a72","a73","a74","a75","a76","a77","a78","a79","a80","a81","a82","a83","a84","a85","a86","a87","a88","a89","a90","a91","a92","a93","a94","a95","a96","a97","a98","a99","a100","a101","a102","a103","a104","a105","a106","a107","a108","a109","a110","a111","a112","a113","a114","a115","a116","a117","a118","a119","a120","a121","a122","a123","a124","a125","a126","a127","a128","a129","a130","a131","a132","a133","a134","a135","a136","a137","a138","a139","a140","a141","a142","a143","a144","a145","a146","a147","a148","a149","a150","a151","a152","a153","a154","a155","a156","a157","a158","a159","a160","a161","a162","a163","a164","a165","a166","a167","a168","a169","a170","a171","a172","a173","a174","a175","a176","a177","a178","a179","a180","a181","a182","a183","a184","a185","a186","a187","a188","a189","a190","a191","a192","a193","a194","a195","a196","a197","a198","a199","a200","a201","a202","a203","a204","a205","a206","a207","a208","a209","a210","a211","a212","a213","a214","a215","a216","a217","a218","a219","a220","a221","a222","a223","a224","a225","a226","a227","a228","a229","a230","a231","a232","a233","a234","a235","a236","a237","a238","a239","a240","a241","a242","a243","a244","a245","a246","a247","a248","a249","a250","a251","a252","a253","a254","a255"

template <int AO, bool ZERO> DI void pv_mfma(const bf16x8& vf, const bf16x8& pf) {
  if constexpr (ZERO)
    asm volatile("v_mfma_f32_32x32x16_bf16 a[%c2:%c3], %0, %1, 0" ::"v"(vf), "v"(pf), "i"(AO), "i"(AO + 15) : ACLOB);
  else
    asm volatile("v_mfma_f32_32x32x16_bf16 a[%c2:%c3], %0, %1, a[%c2:%c3]" ::"v"(vf), "v"(pf), "i"(AO), "i"(AO + 15)
                 : ACLOB);
}
template <bool FIRST> DI void qk_mfma(f32x16& s, const bf16x8& kf, const bf16x8& qf) {
  if constexpr (FIRST)
    asm volatile("v_mfma_f32_32x32x16_bf16 %0, %1, %2, 0" : "=&v"(s) : "v"(kf), "v"(qf) : ACLOB);
  else
    asm volatile("v_mfma_f32_32x32x16_bf16 %0, %1, %2, %0" : "+v"(s) : "v"(kf), "v"(qf) : ACLOB);
}
DI float max3f(float a, float b, float c) { float r; asm("v_max3_f32 %0, %1, %2, %3" : "=v"(r) : "v"(a), "v"(b), "v"(c)); return r; }
DI float max2f(float a, float b) { float r; asm("v_max_f32_e32 %0, %1, %2" : "=v"(r) : "v"(a), "v"(b)); return r; }
DI float fadd_s(float a, float b) { float r; asm("v_add_f32_e32 %0, %1, %2" : "=v"(r) : "v"(a), "v"(b)); return r; }
DI float ffms_s(float a, float b, float c) { float r; asm("v_fma_f32 %0, %1, %2, -%3" : "=v"(r) : "v"(a), "v"(b), "v"(c)); return r; }
DI float ffma_s(float a, float b, float c) { float r; asm("v_fma_f32 %0, %1, %2, %3" : "=v"(r) : "v"(a), "v"(b), "v"(c)); return r; }

constexpr int unit_is_v(int n) { return n < 2 ? 0 : (n >= 30 ? 1 : (((n - 2) & 3) >= 2)); }
constexpr int unit_tile(int n) { return n < 2 ? 0 : (n >= 30 ? 7 : (((n - 2) & 3) >= 2 ? (n - 2) / 4 : (n - 2) / 4 + 1)); }
constexpr int unit_half(int n) { return n & 1; }
constexpr int unit_k(int t, int hf) { return t == 0 ? hf : 4 * (t - 1) + 2 + hf; }
constexpr int unit_v(int t, int hf) { return t == 7 ? 30 + hf : 4 * t + 4 + hf; }

struct Ctx4 {
  __amdgpu_buffer_rsrc_t rk, rv;
  int koff[4], voff[2];
  char* ring;
  unsigned kaddr[8];
  unsigned vaddr[4];
  bf16x8 qf[2][16];
  f32x16 s[3];
  bf16x8 pf[2][2];
  unsigned pn[2][8];
  float m[2], l[2], nm[2], mx[2], fct[2];
  float rs[2];
  bool pend;
  const u16* Kp; const u16* Vp;
  unsigned pfoff;
  int sub;
  unsigned pfd;
};
DI void prefetch_unit(Ctx4& c, int n) {
}

template <int N, int P> DI void dma_piece(const Ctx4& c) {
  if constexpr (N < 32) {
    constexpr int slot = N % 5, t = unit_tile(N), hf = unit_half(N);
    if constexpr (!unit_is_v(N))
      __builtin_amdgcn_raw_ptr_buffer_load_lds(c.rk, (lds_vp)(c.ring + slot * 8192 + P * 1024), 16, c.koff[P & 3],
                                               (32 * t + 16 * (P >> 2)) * 512 + hf * 256, 0, KVAUX);
    else
      __builtin_amdgcn_raw_ptr_buffer_load_lds(c.rv, (lds_vp)(c.ring + slot * 8192 + P * 1024), 16, c.voff[P & 1],
                                               (32 * t + 16 * hf + 4 * (P >> 1)) * 512, 0, KVAUX);
  }
}
template <int N> DI void dma_unit(const Ctx4& c) {
  dma_piece<N, 0>(c); dma_piece<N, 1>(c); dma_piece<N, 2>(c); dma_piece<N, 3>(c);
  dma_piece<N, 4>(c); dma_piece<N, 5>(c); dma_piece<N, 6>(c); dma_piece<N, 7>(c);
}
template <int N, int AHEAD> DI void wait_unit4() {
  constexpr int last = N + AHEAD > 31 ? 31 : N + AHEAD;
  constexpr int after = last - N;
  if constexpr (after == 4) WAITV(32);
  else if constexpr (after == 3) WAITV(24);
  else if constexpr (after == 2) WAITV(16);
  else if constexpr (after == 1) WAITV(8);
  else WAITV(0);
}

template <int T, int J> DI bf16x8 kread(const Ctx4& c) {
  constexpr int n = unit_k(T, J >> 3), slot = n % 5;
  bf16x8 r;
  asm volatile("ds_read_b128 %0, %1 offset:%2" : "=v"(r) : "v"(c.kaddr[J & 7]), "i"(slot * 8192) : "memory");
  return r;
}
template <int T, int GA> DI void sm_sliceA(Ctx4& c);
template <int T, int J> DI void phaseA_step(Ctx4& c, bf16x8 (&kf)[4]) {
  constexpr int n0 = unit_k(T, 0), n1 = unit_k(T, 1);
  if constexpr (J + 3 < 16) asm volatile("s_waitcnt lgkmcnt(3)" : "+v"(kf[J & 3])::"memory");
  else if constexpr (J + 3 == 16) asm volatile("s_waitcnt lgkmcnt(2)" : "+v"(kf[J & 3])::"memory");
  else if constexpr (J + 3 == 17) asm volatile("s_waitcnt lgkmcnt(1)" : "+v"(kf[J & 3])::"memory");
  else asm volatile("s_waitcnt lgkmcnt(0)" : "+v"(kf[J & 3])::"memory");
  qk_mfma<J == 0>(c.s[0], kf[J & 3], c.qf[0][J]);
  if constexpr (J == 8 && T <= 4) prefetch_unit(c, 14 + 4 * T + c.sub);
  if constexpr (J >= 8) { dma_piece<n0 + 5, J - 8>(c); }
  GAP();
  qk_mfma<J == 0>(c.s[1 + (T & 1)], kf[J & 3], c.qf[1][J]);
  if constexpr (T >= 1) sm_sliceA<T - 1, J>(c);
  if constexpr (J + 4 == 8) wait_unit4<n1, 3>();
  if constexpr (J + 4 < 16) kf[J & 3] = kread<T, J + 4>(c);
  GAP();
}
template <int T, int... Js> DI void phaseA_(Ctx4& c, std::integer_sequence<int, Js...>) {
  bf16x8 kf[4];
  wait_unit4<unit_k(T, 0), 4>();
  kf[0] = kread<T, 0>(c); kf[1] = kread<T, 1>(c); kf[2] = kread<T, 2>(c); kf[3] = kread<T, 3>(c);
  GAP();
  (phaseA_step<T, Js>(c, kf), ...);
}
template <int T> DI void phaseA(Ctx4& c) { phaseA_<T>(c, std::make_integer_sequence<int, 16>{}); }

constexpr int egap(int e) { return 6 + (e * 25) / 32; }
constexpr int egap0(int e) { return 6 + (e * 25) / 16; }
template <int T, int G> DI void sm_slice(Ctx4& c) {
  constexpr int I1 = 1 + (T & 1);
  if constexpr (G < 4) {
    constexpr int qb = G >> 1, si = qb ? I1 : 0;
    if constexpr ((G & 1) == 0) {
      float a = max3f(c.s[si][0], c.s[si][1], c.s[si][2]);
      a = max3f(a, c.s[si][3], c.s[si][4]);
      a = max3f(a, c.s[si][5], c.s[si][6]);
      c.mx[qb] = max3f(a, c.s[si][7], c.s[si][8]);
    } else {
      float a = max3f(c.mx[qb], c.s[si][9], c.s[si][10]);
      a = max3f(a, c.s[si][11], c.s[si][12]);
      a = max3f(a, c.s[si][13], c.s[si][14]);
      c.mx[qb] = max2f(a, c.s[si][15]);
    }
  } else if constexpr (G == 4) {
#pragma unroll
    for (int qb = 0; qb < 2; ++qb) {
      auto rr = __builtin_amdgcn_permlane32_swap(__float_as_uint(c.mx[qb]), __float_as_uint(c.mx[qb]), false, false);
      c.mx[qb] = max2f(__uint_as_float(rr[0]), __uint_as_float(rr[1])) * SCL;
    }
  } else if constexpr (G == 5) {
    if constexpr (T == 0) {
      c.m[0] = c.mx[0]; c.m[1] = c.mx[1];
      c.l[0] = 0.f; c.l[1] = 0.f;
    } else {
      const bool grew = (c.mx[0] - c.m[0] > THR) | (c.mx[1] - c.m[1] > THR);
      if (__builtin_expect(__any((int)grew), 0)) {
#pragma unroll
        for (int qb = 0; qb < 2; ++qb) {
          const float mn = fmaxf(c.m[qb], c.mx[qb]);
          const float al = __builtin_amdgcn_exp2f(c.m[qb] - mn);
          c.m[qb] = mn;
          c.l[qb] *= al;
          c.fct[qb] = al;
        }
        c.pend = true;
      }
    }
    c.rs[0] = 0.f; c.rs[1] = 0.f;
  }
  if constexpr (G >= 6) {
    constexpr int NE = T == 7 ? 32 : 16;
#pragma unroll
    for (int e = 0; e < NE; ++e) {
      if ((T == 7 ? egap(e) : egap0(e)) == G) {
        const int qb = e >> 4, i = e & 15, si = qb ? I1 : 0;
        c.s[si][i] = __builtin_amdgcn_exp2f(ffms_s(c.s[si][i], SCL, c.m[qb]));
      }
    }
#pragma unroll
    for (int e = 0; e < NE; ++e) {
      const int ge = T == 7 ? egap(e) : egap0(e);
      if (ge == G - 1 || (G == 31 && ge == 31)) {
        const int qb = e >> 4, i = e & 15, si = qb ? I1 : 0;
        c.rs[qb] += c.s[si][i];
      }
    }
#pragma unroll
    for (int d = 0; d < NE / 2; ++d) {
      const int ge = T == 7 ? egap(2 * d + 1) : egap0(2 * d + 1);
      if (ge == G - 1 || (G == 31 && ge == 31)) {
        const int qb = d >> 3, i = (2 * d) & 15, si = qb ? I1 : 0;
        c.pn[qb][d & 7] = cvtpk(c.s[si][i], c.s[si][i + 1]);
      }
    }
  }
}
template <int T, int GA> DI void sm_sliceA(Ctx4& c) {
  constexpr int I1 = 1 + (T & 1);
  c.s[I1][GA] = __builtin_amdgcn_exp2f(ffms_s(c.s[I1][GA], SCL, c.m[1]));
  if constexpr (GA >= 1) c.rs[1] += c.s[I1][GA - 1];
  if constexpr (GA >= 2 && (GA & 1) == 0) c.pn[1][(GA - 2) >> 1] = cvtpk(c.s[I1][GA - 2], c.s[I1][GA - 1]);
  if constexpr (GA == 15) {
    c.rs[1] += c.s[I1][15];
    c.pn[1][7] = cvtpk(c.s[I1][14], c.s[I1][15]);
    c.l[1] += c.rs[1];
#pragma unroll
    for (int ks = 0; ks < 2; ++ks) {
      u32x4 wv = {c.pn[1][4 * ks], c.pn[1][4 * ks + 1], c.pn[1][4 * ks + 2], c.pn[1][4 * ks + 3]};
      c.pf[1][ks] = __builtin_bit_cast(bf16x8, wv);
    }
  }
}

template <int TV, int S> DI void vread_set(const Ctx4& c, s16x4 (&v)[4]) {
  constexpr int ks = S >> 2, dbp = S & 3;
  constexpr int n = unit_v(TV, ks), slot = n % 5;
  constexpr int db0 = 2 * dbp, db1 = 2 * dbp + 1;
  constexpr int o0 = slot * 8192 + (db0 >> 2) * 256, o1 = slot * 8192 + (db1 >> 2) * 256;
  v[0] = trread_o<o0>(c.vaddr[db0 & 3]);
  v[1] = trread_o<o0 + 4096>(c.vaddr[db0 & 3]);
  v[2] = trread_o<o1>(c.vaddr[db1 & 3]);
  v[3] = trread_o<o1 + 4096>(c.vaddr[db1 & 3]);
}
template <int T, bool HASPV, bool HASSM, int M> DI void phaseB_gap(Ctx4& c, s16x4 (&va)[4], s16x4 (&vb)[4]) {
  constexpr int TV = T - 1;
  constexpr int S = M >> 2, ks = S >> 2, dbp = S & 3, dd = (M >> 1) & 1, qb = M & 1;
  if constexpr (HASPV) {
    if constexpr ((M & 3) == 0) {
      if constexpr (S + 1 < 8) {
        if constexpr (S & 1) asm volatile("s_waitcnt lgkmcnt(4)" : "+v"(vb[0]), "+v"(vb[1]), "+v"(vb[2]), "+v"(vb[3])::"memory");
        else asm volatile("s_waitcnt lgkmcnt(4)" : "+v"(va[0]), "+v"(va[1]), "+v"(va[2]), "+v"(va[3])::"memory");
      } else {
        asm volatile("s_waitcnt lgkmcnt(0)" : "+v"(vb[0]), "+v"(vb[1]), "+v"(vb[2]), "+v"(vb[3])::"memory");
      }
    }
    {
      constexpr int db = 2 * dbp + dd;
      const bf16x8 vf = (S & 1) ? cat8(vb[2 * dd], vb[2 * dd + 1]) : cat8(va[2 * dd], va[2 * dd + 1]);
      pv_mfma<(qb * 8 + db) * 16, (TV == 0 && ks == 0)>(vf, c.pf[qb][ks]);
    }
    if constexpr ((M & 3) == 3 && S + 2 < 8) {
      if constexpr (S + 2 == 4) wait_unit4<unit_v(TV, 1), 3>();
      if constexpr (S & 1) vread_set<TV, S + 2>(c, vb); else vread_set<TV, S + 2>(c, va);
    }
    if constexpr (M >= 16 && M < 24) dma_piece<unit_v(TV, 0) + 5, M - 16>(c);
  }
  if constexpr (HASSM) sm_slice<T, M>(c);
  GAP();
}
template <int T, bool HASPV, bool HASSM, int... Ms> DI void phaseB_(Ctx4& c, std::integer_sequence<int, Ms...>) {
  constexpr int TV = T - 1;
  s16x4 va[4], vb[4];
  if constexpr (HASPV) {
    wait_unit4<unit_v(TV, 0), 3>();
    vread_set<TV, 0>(c, va);
    vread_set<TV, 1>(c, vb);
  }
  if constexpr (HASSM) dma_unit<unit_k(T, 1) + 5>(c);
  if constexpr (HASSM) {
    asm volatile("s_nop 7\n\ts_nop 7" : "+v"(c.s[0]), "+v"(c.s[1 + (T & 1)]));
  }
  GAP();
  (phaseB_gap<T, HASPV, HASSM, Ms>(c, va, vb), ...);
  if constexpr (HASPV) dma_unit<unit_v(TV, 1) + 5>(c);
  if constexpr (HASSM) {
    c.l[0] += c.rs[0];
    if constexpr (T == 7) c.l[1] += c.rs[1];
#pragma unroll
    for (int qb = 0; qb < (T == 7 ? 2 : 1); ++qb)
#pragma unroll
      for (int ks = 0; ks < 2; ++ks) {
        u32x4 wv = {c.pn[qb][4 * ks], c.pn[qb][4 * ks + 1], c.pn[qb][4 * ks + 2], c.pn[qb][4 * ks + 3]};
        c.pf[qb][ks] = __builtin_bit_cast(bf16x8, wv);
      }
  }
  if constexpr (HASPV && HASSM) {
    if (__builtin_expect(c.pend, 0)) {
      float t0, t1;
      asm volatile("s_nop 7\n\ts_nop 7\n\t"
                   "v_accvgpr_read_b32 %0, a0\n\tv_mul_f32 %0, %2, %0\n\tv_accvgpr_write_b32 a0, %0\n\t"
                   "v_accvgpr_read_b32 %1, a1\n\tv_mul_f32 %1, %2, %1\n\tv_accvgpr_write_b32 a1, %1\n\t"
                   "v_accvgpr_read_b32 %0, a2\n\tv_mul_f32 %0, %2, %0\n\tv_accvgpr_write_b32 a2, %0\n\t"
                   "v_accvgpr_read_b32 %1, a3\n\tv_mul_f32 %1, %2, %1\n\tv_accvgpr_write_b32 a3, %1\n\t"
                   "v_accvgpr_read_b32 %0, a4\n\tv_mul_f32 %0, %2, %0\n\tv_accvgpr_write_b32 a4, %0\n\t"
                   "v_accvgpr_read_b32 %1, a5\n\tv_mul_f32 %1, %2, %1\n\tv_accvgpr_write_b32 a5, %1\n\t"
                   "v_accvgpr_read_b32 %0, a6\n\tv_mul_f32 %0, %2, %0\n\tv_accvgpr_write_b32 a6, %0\n\t"
                   "v_accvgpr_read_b32 %1, a7\n\tv_mul_f32 %1, %2, %1\n\tv_accvgpr_write_b32 a7, %1\n\t"
                   "v_accvgpr_read_b32 %0, a8\n\tv_mul_f32 %0, %2, %0\n\tv_accvgpr_write_b32 a8, %0\n\t"
                   "v_accvgpr_read_b32 %1, a9\n\tv_mul_f32 %1, %2, %1\n\tv_accvgpr_write_b32 a9, %1\n\t"
                   "v_accvgpr_read_b32 %0, a10\n\tv_mul_f32 %0, %2, %0\n\tv_accvgpr_write_b32 a10, %0\n\t"
                   "v_accvgpr_read_b32 %1, a11\n\tv_mul_f32 %1, %2, %1\n\tv_accvgpr_write_b32 a11, %1\n\t"
                   "v_accvgpr_read_b32 %0, a12\n\tv_mul_f32 %0, %2, %0\n\tv_accvgpr_write_b32 a12, %0\n\t"
                   "v_accvgpr_read_b32 %1, a13\n\tv_mul_f32 %1, %2, %1\n\tv_accvgpr_write_b32 a13, %1\n\t"
                   "v_accvgpr_read_b32 %0, a14\n\tv_mul_f32 %0, %2, %0\n\tv_accvgpr_write_b32 a14, %0\n\t"
                   "v_accvgpr_read_b32 %1, a15\n\tv_mul_f32 %1, %2, %1\n\tv_accvgpr_write_b32 a15, %1\n\t"
                   "v_accvgpr_read_b32 %0, a16\n\tv_mul_f32 %0, %2, %0\n\tv_accvgpr_write_b32 a16, %0\n\t"
                   "v_accvgpr_read_b32 %1, a17\n\tv_mul_f32 %1, %2, %1\n\tv_accvgpr_write_b32 a17, %1\n\t"
                   "v_accvgpr_read_b32 %0, a18\n\tv_mul_f32 %0, %2, %0\n\tv_accvgpr_write_b32 a18, %0\n\t"
                   "v_accvgpr_read_b32 %1, a19\n\tv_mul_f32 %1, %2, %1\n\tv_accvgpr_write_b32 a19, %1\n\t"
                   "v_accvgpr_read_b32 %0, a20\n\tv_mul_f32 %0, %2, %0\n\tv_accvgpr_write_b32 a20, %0\n\t"
                   "v_accvgpr_read_b32 %1, a21\n\tv_mul_f32 %1, %2, %1\n\tv_accvgpr_write_b32 a21, %1\n\t"
                   "v_accvgpr_read_b32 %0, a22\n\tv_mul_f32 %0, %2, %0\n\tv_accvgpr_write_b32 a22, %0\n\t"
                   "v_accvgpr_read_b32 %1, a23\n\tv_mul_f32 %1, %2, %1\n\tv_accvgpr_write_b32 a23, %1\n\t"
                   "v_accvgpr_read_b32 %0, a24\n\tv_mul_f32 %0, %2, %0\n\tv_accvgpr_write_b32 a24, %0\n\t"
                   "v_accvgpr_read_b32 %1, a25\n\tv_mul_f32 %1, %2, %1\n\tv_accvgpr_write_b32 a25, %1\n\t"
                   "v_accvgpr_read_b32 %0, a26\n\tv_mul_f32 %0, %2, %0\n\tv_accvgpr_write_b32 a26, %0\n\t"
                   "v_accvgpr_read_b32 %1, a27\n\tv_mul_f32 %1, %2, %1\n\tv_accvgpr_write_b32 a27, %1\n\t"
                   "v_accvgpr_read_b32 %0, a28\n\tv_mul_f32 %0, %2, %0\n\tv_accvgpr_write_b32 a28, %0\n\t"
                   "v_accvgpr_read_b32 %1, a29\n\tv_mul_f32 %1, %2, %1\n\tv_accvgpr_write_b32 a29, %1\n\t"
                   "v_accvgpr_read_b32 %0, a30\n\tv_mul_f32 %0, %2, %0\n\tv_accvgpr_write_b32 a30, %0\n\t"
                   "v_accvgpr_read_b32 %1, a31\n\tv_mul_f32 %1, %2, %1\n\tv_accvgpr_write_b32 a31, %1\n\t"
                   "v_accvgpr_read_b32 %0, a32\n\tv_mul_f32 %0, %2, %0\n\tv_accvgpr_write_b32 a32, %0\n\t"
                   "v_accvgpr_read_b32 %1, a33\n\tv_mul_f32 %1, %2, %1\n\tv_accvgpr_write_b32 a33, %1\n\t"
                   "v_accvgpr_read_b32 %0, a34\n\tv_mul_f32 %0, %2, %0\n\tv_accvgpr_write_b32 a34, %0\n\t"
                   "v_accvgpr_read_b32 %1, a35\n\tv_mul_f32 %1, %2, %1\n\tv_accvgpr_write_b32 a35, %1\n\t"
                   "v_accvgpr_read_b32 %0, a36\n\tv_mul_f32 %0, %2, %0\n\tv_accvgpr_write_b32 a36, %0\n\t"
                   "v_accvgpr_read_b32 %1, a37\n\tv_mul_f32 %1, %2, %1\n\tv_accvgpr_write_b32 a37, %1\n\t"
                   "v_accvgpr_read_b32 %0, a38\n\tv_mul_f32 %0, %2, %0\n\tv_accvgpr_write_b32 a38, %0\n\t"
                   "v_accvgpr_read_b32 %1, a39\n\tv_mul_f32 %1, %2, %1\n\tv_accvgpr_write_b32 a39, %1\n\t"
                   "v_accvgpr_read_b32 %0, a40\n\tv_mul_f32 %0, %2, %0\n\tv_accvgpr_write_b32 a40, %0\n\t"
                   "v_accvgpr_read_b32 %1, a41\n\tv_mul_f32 %1, %2, %1\n\tv_accvgpr_write_b32 a41, %1\n\t"
                   "v_accvgpr_read_b32 %0, a42\n\tv_mul_f32 %0, %2, %0\n\tv_accvgpr_write_b32 a42, %0\n\t"
                   "v_accvgpr_read_b32 %1, a43\n\tv_mul_f32 %1, %2, %1\n\tv_accvgpr_write_b32 a43, %1\n\t"
                   "v_accvgpr_read_b32 %0, a44\n\tv_mul_f32 %0, %2, %0\n\tv_accvgpr_write_b32 a44, %0\n\t"
                   "v_accvgpr_read_b32 %1, a45\n\tv_mul_f32 %1, %2, %1\n\tv_accvgpr_write_b32 a45, %1\n\t"
                   "v_accvgpr_read_b32 %0, a46\n\tv_mul_f32 %0, %2, %0\n\tv_accvgpr_write_b32 a46, %0\n\t"
                   "v_accvgpr_read_b32 %1, a47\n\tv_mul_f32 %1, %2, %1\n\tv_accvgpr_write_b32 a47, %1\n\t"
                   "v_accvgpr_read_b32 %0, a48\n\tv_mul_f32 %0, %2, %0\n\tv_accvgpr_write_b32 a48, %0\n\t"
                   "v_accvgpr_read_b32 %1, a49\n\tv_mul_f32 %1, %2, %1\n\tv_accvgpr_write_b32 a49, %1\n\t"
                   "v_accvgpr_read_b32 %0, a50\n\tv_mul_f32 %0, %2, %0\n\tv_accvgpr_write_b32 a50, %0\n\t"
                   "v_accvgpr_read_b32 %1, a51\n\tv_mul_f32 %1, %2, %1\n\tv_accvgpr_write_b32 a51, %1\n\t"
                   "v_accvgpr_read_b32 %0, a52\n\tv_mul_f32 %0, %2, %0\n\tv_accvgpr_write_b32 a52, %0\n\t"
                   "v_accvgpr_read_b32 %1, a53\n\tv_mul_f32 %1, %2, %1\n\tv_accvgpr_write_b32 a53, %1\n\t"
                   "v_accvgpr_read_b32 %0, a54\n\tv_mul_f32 %0, %2, %0\n\tv_accvgpr_write_b32 a54, %0\n\t"
                   "v_accvgpr_read_b32 %1, a55\n\tv_mul_f32 %1, %2, %1\n\tv_accvgpr_write_b32 a55, %1\n\t"
                   "v_accvgpr_read_b32 %0, a56\n\tv_mul_f32 %0, %2, %0\n\tv_accvgpr_write_b32 a56, %0\n\t"
                   "v_accvgpr_read_b32 %1, a57\n\tv_mul_f32 %1, %2, %1\n\tv_accvgpr_write_b32 a57, %1\n\t"
                   "v_accvgpr_read_b32 %0, a58\n\tv_mul_f32 %0, %2, %0\n\tv_accvgpr_write_b32 a58, %0\n\t"
                   "v_accvgpr_read_b32 %1, a59\n\tv_mul_f32 %1, %2, %1\n\tv_accvgpr_write_b32 a59, %1\n\t"
                   "v_accvgpr_read_b32 %0, a60\n\tv_mul_f32 %0, %2, %0\n\tv_accvgpr_write_b32 a60, %0\n\t"
                   "v_accvgpr_read_b32 %1, a61\n\tv_mul_f32 %1, %2, %1\n\tv_accvgpr_write_b32 a61, %1\n\t"
                   "v_accvgpr_read_b32 %0, a62\n\tv_mul_f32 %0, %2, %0\n\tv_accvgpr_write_b32 a62, %0\n\t"
                   "v_accvgpr_read_b32 %1, a63\n\tv_mul_f32 %1, %2, %1\n\tv_accvgpr_write_b32 a63, %1\n\t"
                   "v_accvgpr_read_b32 %0, a64\n\tv_mul_f32 %0, %2, %0\n\tv_accvgpr_write_b32 a64, %0\n\t"
                   "v_accvgpr_read_b32 %1, a65\n\tv_mul_f32 %1, %2, %1\n\tv_accvgpr_write_b32 a65, %1\n\t"
                   "v_accvgpr_read_b32 %0, a66\n\tv_mul_f32 %0, %2, %0\n\tv_accvgpr_write_b32 a66, %0\n\t"
                   "v_accvgpr_read_b32 %1, a67\n\tv_mul_f32 %1, %2, %1\n\tv_accvgpr_write_b32 a67, %1\n\t"
                   "v_accvgpr_read_b32 %0, a68\n\tv_mul_f32 %0, %2, %0\n\tv_accvgpr_write_b32 a68, %0\n\t"
                   "v_accvgpr_read_b32 %1, a69\n\tv_mul_f32 %1, %2, %1\n\tv_accvgpr_write_b32 a69, %1\n\t"
                   "v_accvgpr_read_b32 %0, a70\n\tv_mul_f32 %0, %2, %0\n\tv_accvgpr_write_b32 a70, %0\n\t"
                   "v_accvgpr_read_b32 %1, a71\n\tv_mul_f32 %1, %2, %1\n\tv_accvgpr_write_b32 a71, %1\n\t"
                   "v_accvgpr_read_b32 %0, a72\n\tv_mul_f32 %0, %2, %0\n\tv_accvgpr_write_b32 a72, %0\n\t"
                   "v_accvgpr_read_b32 %1, a73\n\tv_mul_f32 %1, %2, %1\n\tv_accvgpr_write_b32 a73, %1\n\t"
                   "v_accvgpr_read_b32 %0, a74\n\tv_mul_f32 %0, %2, %0\n\tv_accvgpr_write_b32 a74, %0\n\t"
                   "v_accvgpr_read_b32 %1, a75\n\tv_mul_f32 %1, %2, %1\n\tv_accvgpr_write_b32 a75, %1\n\t"
                   "v_accvgpr_read_b32 %0, a76\n\tv_mul_f32 %0, %2, %0\n\tv_accvgpr_write_b32 a76, %0\n\t"
                   "v_accvgpr_read_b32 %1, a77\n\tv_mul_f32 %1, %2, %1\n\tv_accvgpr_write_b32 a77, %1\n\t"
                   "v_accvgpr_read_b32 %0, a78\n\tv_mul_f32 %0, %2, %0\n\tv_accvgpr_write_b32 a78, %0\n\t"
                   "v_accvgpr_read_b32 %1, a79\n\tv_mul_f32 %1, %2, %1\n\tv_accvgpr_write_b32 a79, %1\n\t"
                   "v_accvgpr_read_b32 %0, a80\n\tv_mul_f32 %0, %2, %0\n\tv_accvgpr_write_b32 a80, %0\n\t"
                   "v_accvgpr_read_b32 %1, a81\n\tv_mul_f32 %1, %2, %1\n\tv_accvgpr_write_b32 a81, %1\n\t"
                   "v_accvgpr_read_b32 %0, a82\n\tv_mul_f32 %0, %2, %0\n\tv_accvgpr_write_b32 a82, %0\n\t"
                   "v_accvgpr_read_b32 %1, a83\n\tv_mul_f32 %1, %2, %1\n\tv_accvgpr_write_b32 a83, %1\n\t"
                   "v_accvgpr_read_b32 %0, a84\n\tv_mul_f32 %0, %2, %0\n\tv_accvgpr_write_b32 a84, %0\n\t"
                   "v_accvgpr_read_b32 %1, a85\n\tv_mul_f32 %1, %2, %1\n\tv_accvgpr_write_b32 a85, %1\n\t"
                   "v_accvgpr_read_b32 %0, a86\n\tv_mul_f32 %0, %2, %0\n\tv_accvgpr_write_b32 a86, %0\n\t"
                   "v_accvgpr_read_b32 %1, a87\n\tv_mul_f32 %1, %2, %1\n\tv_accvgpr_write_b32 a87, %1\n\t"
                   "v_accvgpr_read_b32 %0, a88\n\tv_mul_f32 %0, %2, %0\n\tv_accvgpr_write_b32 a88, %0\n\t"
                   "v_accvgpr_read_b32 %1, a89\n\tv_mul_f32 %1, %2, %1\n\tv_accvgpr_write_b32 a89, %1\n\t"
                   "v_accvgpr_read_b32 %0, a90\n\tv_mul_f32 %0, %2, %0\n\tv_accvgpr_write_b32 a90, %0\n\t"
                   "v_accvgpr_read_b32 %1, a91\n\tv_mul_f32 %1, %2, %1\n\tv_accvgpr_write_b32 a91, %1\n\t"
                   "v_accvgpr_read_b32 %0, a92\n\tv_mul_f32 %0, %2, %0\n\tv_accvgpr_write_b32 a92, %0\n\t"
                   "v_accvgpr_read_b32 %1, a93\n\tv_mul_f32 %1, %2, %1\n\tv_accvgpr_write_b32 a93, %1\n\t"
                   "v_accvgpr_read_b32 %0, a94\n\tv_mul_f32 %0, %2, %0\n\tv_accvgpr_write_b32 a94, %0\n\t"
                   "v_accvgpr_read_b32 %1, a95\n\tv_mul_f32 %1, %2, %1\n\tv_accvgpr_write_b32 a95, %1\n\t"
                   "v_accvgpr_read_b32 %0, a96\n\tv_mul_f32 %0, %2, %0\n\tv_accvgpr_write_b32 a96, %0\n\t"
                   "v_accvgpr_read_b32 %1, a97\n\tv_mul_f32 %1, %2, %1\n\tv_accvgpr_write_b32 a97, %1\n\t"
                   "v_accvgpr_read_b32 %0, a98\n\tv_mul_f32 %0, %2, %0\n\tv_accvgpr_write_b32 a98, %0\n\t"
                   "v_accvgpr_read_b32 %1, a99\n\tv_mul_f32 %1, %2, %1\n\tv_accvgpr_write_b32 a99, %1\n\t"
                   "v_accvgpr_read_b32 %0, a100\n\tv_mul_f32 %0, %2, %0\n\tv_accvgpr_write_b32 a100, %0\n\t"
                   "v_accvgpr_read_b32 %1, a101\n\tv_mul_f32 %1, %2, %1\n\tv_accvgpr_write_b32 a101, %1\n\t"
                   "v_accvgpr_read_b32 %0, a102\n\tv_mul_f32 %0, %2, %0\n\tv_accvgpr_write_b32 a102, %0\n\t"
                   "v_accvgpr_read_b32 %1, a103\n\tv_mul_f32 %1, %2, %1\n\tv_accvgpr_write_b32 a103, %1\n\t"
                   "v_accvgpr_read_b32 %0, a104\n\tv_mul_f32 %0, %2, %0\n\tv_accvgpr_write_b32 a104, %0\n\t"
                   "v_accvgpr_read_b32 %1, a105\n\tv_mul_f32 %1, %2, %1\n\tv_accvgpr_write_b32 a105, %1\n\t"
                   "v_accvgpr_read_b32 %0, a106\n\tv_mul_f32 %0, %2, %0\n\tv_accvgpr_write_b32 a106, %0\n\t"
                   "v_accvgpr_read_b32 %1, a107\n\tv_mul_f32 %1, %2, %1\n\tv_accvgpr_write_b32 a107, %1\n\t"
                   "v_accvgpr_read_b32 %0, a108\n\tv_mul_f32 %0, %2, %0\n\tv_accvgpr_write_b32 a108, %0\n\t"
                   "v_accvgpr_read_b32 %1, a109\n\tv_mul_f32 %1, %2, %1\n\tv_accvgpr_write_b32 a109, %1\n\t"
                   "v_accvgpr_read_b32 %0, a110\n\tv_mul_f32 %0, %2, %0\n\tv_accvgpr_write_b32 a110, %0\n\t"
                   "v_accvgpr_read_b32 %1, a111\n\tv_mul_f32 %1, %2, %1\n\tv_accvgpr_write_b32 a111, %1\n\t"
                   "v_accvgpr_read_b32 %0, a112\n\tv_mul_f32 %0, %2, %0\n\tv_accvgpr_write_b32 a112, %0\n\t"
                   "v_accvgpr_read_b32 %1, a113\n\tv_mul_f32 %1, %2, %1\n\tv_accvgpr_write_b32 a113, %1\n\t"
                   "v_accvgpr_read_b32 %0, a114\n\tv_mul_f32 %0, %2, %0\n\tv_accvgpr_write_b32 a114, %0\n\t"
                   "v_accvgpr_read_b32 %1, a115\n\tv_mul_f32 %1, %2, %1\n\tv_accvgpr_write_b32 a115, %1\n\t"
                   "v_accvgpr_read_b32 %0, a116\n\tv_mul_f32 %0, %2, %0\n\tv_accvgpr_write_b32 a116, %0\n\t"
                   "v_accvgpr_read_b32 %1, a117\n\tv_mul_f32 %1, %2, %1\n\tv_accvgpr_write_b32 a117, %1\n\t"
                   "v_accvgpr_read_b32 %0, a118\n\tv_mul_f32 %0, %2, %0\n\tv_accvgpr_write_b32 a118, %0\n\t"
                   "v_accvgpr_read_b32 %1, a119\n\tv_mul_f32 %1, %2, %1\n\tv_accvgpr_write_b32 a119, %1\n\t"
                   "v_accvgpr_read_b32 %0, a120\n\tv_mul_f32 %0, %2, %0\n\tv_accvgpr_write_b32 a120, %0\n\t"
                   "v_accvgpr_read_b32 %1, a121\n\tv_mul_f32 %1, %2, %1\n\tv_accvgpr_write_b32 a121, %1\n\t"
                   "v_accvgpr_read_b32 %0, a122\n\tv_mul_f32 %0, %2, %0\n\tv_accvgpr_write_b32 a122, %0\n\t"
                   "v_accvgpr_read_b32 %1, a123\n\tv_mul_f32 %1, %2, %1\n\tv_accvgpr_write_b32 a123, %1\n\t"
                   "v_accvgpr_read_b32 %0, a124\n\tv_mul_f32 %0, %2, %0\n\tv_accvgpr_write_b32 a124, %0\n\t"
                   "v_accvgpr_read_b32 %1, a125\n\tv_mul_f32 %1, %2, %1\n\tv_accvgpr_write_b32 a125, %1\n\t"
                   "v_accvgpr_read_b32 %0, a126\n\tv_mul_f32 %0, %2, %0\n\tv_accvgpr_write_b32 a126, %0\n\t"
                   "v_accvgpr_read_b32 %1, a127\n\tv_mul_f32 %1, %2, %1\n\tv_accvgpr_write_b32 a127, %1\n\t"
                   "v_accvgpr_read_b32 %0, a128\n\tv_mul_f32 %0, %3, %0\n\tv_accvgpr_write_b32 a128, %0\n\t"
                   "v_accvgpr_read_b32 %1, a129\n\tv_mul_f32 %1, %3, %1\n\tv_accvgpr_write_b32 a129, %1\n\t"
                   "v_accvgpr_read_b32 %0, a130\n\tv_mul_f32 %0, %3, %0\n\tv_accvgpr_write_b32 a130, %0\n\t"
                   "v_accvgpr_read_b32 %1, a131\n\tv_mul_f32 %1, %3, %1\n\tv_accvgpr_write_b32 a131, %1\n\t"
                   "v_accvgpr_read_b32 %0, a132\n\tv_mul_f32 %0, %3, %0\n\tv_accvgpr_write_b32 a132, %0\n\t"
                   "v_accvgpr_read_b32 %1, a133\n\tv_mul_f32 %1, %3, %1\n\tv_accvgpr_write_b32 a133, %1\n\t"
                   "v_accvgpr_read_b32 %0, a134\n\tv_mul_f32 %0, %3, %0\n\tv_accvgpr_write_b32 a134, %0\n\t"
                   "v_accvgpr_read_b32 %1, a135\n\tv_mul_f32 %1, %3, %1\n\tv_accvgpr_write_b32 a135, %1\n\t"
                   "v_accvgpr_read_b32 %0, a136\n\tv_mul_f32 %0, %3, %0\n\tv_accvgpr_write_b32 a136, %0\n\t"
                   "v_accvgpr_read_b32 %1, a137\n\tv_mul_f32 %1, %3, %1\n\tv_accvgpr_write_b32 a137, %1\n\t"
                   "v_accvgpr_read_b32 %0, a138\n\tv_mul_f32 %0, %3, %0\n\tv_accvgpr_write_b32 a138, %0\n\t"
                   "v_accvgpr_read_b32 %1, a139\n\tv_mul_f32 %1, %3, %1\n\tv_accvgpr_write_b32 a139, %1\n\t"
                   "v_accvgpr_read_b32 %0, a140\n\tv_mul_f32 %0, %3, %0\n\tv_accvgpr_write_b32 a140, %0\n\t"
                   "v_accvgpr_read_b32 %1, a141\n\tv_mul_f32 %1, %3, %1\n\tv_accvgpr_write_b32 a141, %1\n\t"
                   "v_accvgpr_read_b32 %0, a142\n\tv_mul_f32 %0, %3, %0\n\tv_accvgpr_write_b32 a142, %0\n\t"
                   "v_accvgpr_read_b32 %1, a143\n\tv_mul_f32 %1, %3, %1\n\tv_accvgpr_write_b32 a143, %1\n\t"
                   "v_accvgpr_read_b32 %0, a144\n\tv_mul_f32 %0, %3, %0\n\tv_accvgpr_write_b32 a144, %0\n\t"
                   "v_accvgpr_read_b32 %1, a145\n\tv_mul_f32 %1, %3, %1\n\tv_accvgpr_write_b32 a145, %1\n\t"
                   "v_accvgpr_read_b32 %0, a146\n\tv_mul_f32 %0, %3, %0\n\tv_accvgpr_write_b32 a146, %0\n\t"
                   "v_accvgpr_read_b32 %1, a147\n\tv_mul_f32 %1, %3, %1\n\tv_accvgpr_write_b32 a147, %1\n\t"
                   "v_accvgpr_read_b32 %0, a148\n\tv_mul_f32 %0, %3, %0\n\tv_accvgpr_write_b32 a148, %0\n\t"
                   "v_accvgpr_read_b32 %1, a149\n\tv_mul_f32 %1, %3, %1\n\tv_accvgpr_write_b32 a149, %1\n\t"
                   "v_accvgpr_read_b32 %0, a150\n\tv_mul_f32 %0, %3, %0\n\tv_accvgpr_write_b32 a150, %0\n\t"
                   "v_accvgpr_read_b32 %1, a151\n\tv_mul_f32 %1, %3, %1\n\tv_accvgpr_write_b32 a151, %1\n\t"
                   "v_accvgpr_read_b32 %0, a152\n\tv_mul_f32 %0, %3, %0\n\tv_accvgpr_write_b32 a152, %0\n\t"
                   "v_accvgpr_read_b32 %1, a153\n\tv_mul_f32 %1, %3, %1\n\tv_accvgpr_write_b32 a153, %1\n\t"
                   "v_accvgpr_read_b32 %0, a154\n\tv_mul_f32 %0, %3, %0\n\tv_accvgpr_write_b32 a154, %0\n\t"
                   "v_accvgpr_read_b32 %1, a155\n\tv_mul_f32 %1, %3, %1\n\tv_accvgpr_write_b32 a155, %1\n\t"
                   "v_accvgpr_read_b32 %0, a156\n\tv_mul_f32 %0, %3, %0\n\tv_accvgpr_write_b32 a156, %0\n\t"
                   "v_accvgpr_read_b32 %1, a157\n\tv_mul_f32 %1, %3, %1\n\tv_accvgpr_write_b32 a157, %1\n\t"
                   "v_accvgpr_read_b32 %0, a158\n\tv_mul_f32 %0, %3, %0\n\tv_accvgpr_write_b32 a158, %0\n\t"
                   "v_accvgpr_read_b32 %1, a159\n\tv_mul_f32 %1, %3, %1\n\tv_accvgpr_write_b32 a159, %1\n\t"
                   "v_accvgpr_read_b32 %0, a160\n\tv_mul_f32 %0, %3, %0\n\tv_accvgpr_write_b32 a160, %0\n\t"
                   "v_accvgpr_read_b32 %1, a161\n\tv_mul_f32 %1, %3, %1\n\tv_accvgpr_write_b32 a161, %1\n\t"
                   "v_accvgpr_read_b32 %0, a162\n\tv_mul_f32 %0, %3, %0\n\tv_accvgpr_write_b32 a162, %0\n\t"
                   "v_accvgpr_read_b32 %1, a163\n\tv_mul_f32 %1, %3, %1\n\tv_accvgpr_write_b32 a163, %1\n\t"
                   "v_accvgpr_read_b32 %0, a164\n\tv_mul_f32 %0, %3, %0\n\tv_accvgpr_write_b32 a164, %0\n\t"
                   "v_accvgpr_read_b32 %1, a165\n\tv_mul_f32 %1, %3, %1\n\tv_accvgpr_write_b32 a165, %1\n\t"
                   "v_accvgpr_read_b32 %0, a166\n\tv_mul_f32 %0, %3, %0\n\tv_accvgpr_write_b32 a166, %0\n\t"
                   "v_accvgpr_read_b32 %1, a167\n\tv_mul_f32 %1, %3, %1\n\tv_accvgpr_write_b32 a167, %1\n\t"
                   "v_accvgpr_read_b32 %0, a168\n\tv_mul_f32 %0, %3, %0\n\tv_accvgpr_write_b32 a168, %0\n\t"
                   "v_accvgpr_read_b32 %1, a169\n\tv_mul_f32 %1, %3, %1\n\tv_accvgpr_write_b32 a169, %1\n\t"
                   "v_accvgpr_read_b32 %0, a170\n\tv_mul_f32 %0, %3, %0\n\tv_accvgpr_write_b32 a170, %0\n\t"
                   "v_accvgpr_read_b32 %1, a171\n\tv_mul_f32 %1, %3, %1\n\tv_accvgpr_write_b32 a171, %1\n\t"
                   "v_accvgpr_read_b32 %0, a172\n\tv_mul_f32 %0, %3, %0\n\tv_accvgpr_write_b32 a172, %0\n\t"
                   "v_accvgpr_read_b32 %1, a173\n\tv_mul_f32 %1, %3, %1\n\tv_accvgpr_write_b32 a173, %1\n\t"
                   "v_accvgpr_read_b32 %0, a174\n\tv_mul_f32 %0, %3, %0\n\tv_accvgpr_write_b32 a174, %0\n\t"
                   "v_accvgpr_read_b32 %1, a175\n\tv_mul_f32 %1, %3, %1\n\tv_accvgpr_write_b32 a175, %1\n\t"
                   "v_accvgpr_read_b32 %0, a176\n\tv_mul_f32 %0, %3, %0\n\tv_accvgpr_write_b32 a176, %0\n\t"
                   "v_accvgpr_read_b32 %1, a177\n\tv_mul_f32 %1, %3, %1\n\tv_accvgpr_write_b32 a177, %1\n\t"
                   "v_accvgpr_read_b32 %0, a178\n\tv_mul_f32 %0, %3, %0\n\tv_accvgpr_write_b32 a178, %0\n\t"
                   "v_accvgpr_read_b32 %1, a179\n\tv_mul_f32 %1, %3, %1\n\tv_accvgpr_write_b32 a179, %1\n\t"
                   "v_accvgpr_read_b32 %0, a180\n\tv_mul_f32 %0, %3, %0\n\tv_accvgpr_write_b32 a180, %0\n\t"
                   "v_accvgpr_read_b32 %1, a181\n\tv_mul_f32 %1, %3, %1\n\tv_accvgpr_write_b32 a181, %1\n\t"
                   "v_accvgpr_read_b32 %0, a182\n\tv_mul_f32 %0, %3, %0\n\tv_accvgpr_write_b32 a182, %0\n\t"
                   "v_accvgpr_read_b32 %1, a183\n\tv_mul_f32 %1, %3, %1\n\tv_accvgpr_write_b32 a183, %1\n\t"
                   "v_accvgpr_read_b32 %0, a184\n\tv_mul_f32 %0, %3, %0\n\tv_accvgpr_write_b32 a184, %0\n\t"
                   "v_accvgpr_read_b32 %1, a185\n\tv_mul_f32 %1, %3, %1\n\tv_accvgpr_write_b32 a185, %1\n\t"
                   "v_accvgpr_read_b32 %0, a186\n\tv_mul_f32 %0, %3, %0\n\tv_accvgpr_write_b32 a186, %0\n\t"
                   "v_accvgpr_read_b32 %1, a187\n\tv_mul_f32 %1, %3, %1\n\tv_accvgpr_write_b32 a187, %1\n\t"
                   "v_accvgpr_read_b32 %0, a188\n\tv_mul_f32 %0, %3, %0\n\tv_accvgpr_write_b32 a188, %0\n\t"
                   "v_accvgpr_read_b32 %1, a189\n\tv_mul_f32 %1, %3, %1\n\tv_accvgpr_write_b32 a189, %1\n\t"
                   "v_accvgpr_read_b32 %0, a190\n\tv_mul_f32 %0, %3, %0\n\tv_accvgpr_write_b32 a190, %0\n\t"
                   "v_accvgpr_read_b32 %1, a191\n\tv_mul_f32 %1, %3, %1\n\tv_accvgpr_write_b32 a191, %1\n\t"
                   "v_accvgpr_read_b32 %0, a192\n\tv_mul_f32 %0, %3, %0\n\tv_accvgpr_write_b32 a192, %0\n\t"
                   "v_accvgpr_read_b32 %1, a193\n\tv_mul_f32 %1, %3, %1\n\tv_accvgpr_write_b32 a193, %1\n\t"
                   "v_accvgpr_read_b32 %0, a194\n\tv_mul_f32 %0, %3, %0\n\tv_accvgpr_write_b32 a194, %0\n\t"
                   "v_accvgpr_read_b32 %1, a195\n\tv_mul_f32 %1, %3, %1\n\tv_accvgpr_write_b32 a195, %1\n\t"
                   "v_accvgpr_read_b32 %0, a196\n\tv_mul_f32 %0, %3, %0\n\tv_accvgpr_write_b32 a196, %0\n\t"
                   "v_accvgpr_read_b32 %1, a197\n\tv_mul_f32 %1, %3, %1\n\tv_accvgpr_write_b32 a197, %1\n\t"
                   "v_accvgpr_read_b32 %0, a198\n\tv_mul_f32 %0, %3, %0\n\tv_accvgpr_write_b32 a198, %0\n\t"
                   "v_accvgpr_read_b32 %1, a199\n\tv_mul_f32 %1, %3, %1\n\tv_accvgpr_write_b32 a199, %1\n\t"
                   "v_accvgpr_read_b32 %0, a200\n\tv_mul_f32 %0, %3, %0\n\tv_accvgpr_write_b32 a200, %0\n\t"
                   "v_accvgpr_read_b32 %1, a201\n\tv_mul_f32 %1, %3, %1\n\tv_accvgpr_write_b32 a201, %1\n\t"
                   "v_accvgpr_read_b32 %0, a202\n\tv_mul_f32 %0, %3, %0\n\tv_accvgpr_write_b32 a202, %0\n\t"
                   "v_accvgpr_read_b32 %1, a203\n\tv_mul_f32 %1, %3, %1\n\tv_accvgpr_write_b32 a203, %1\n\t"
                   "v_accvgpr_read_b32 %0, a204\n\tv_mul_f32 %0, %3, %0\n\tv_accvgpr_write_b32 a204, %0\n\t"
                   "v_accvgpr_read_b32 %1, a205\n\tv_mul_f32 %1, %3, %1\n\tv_accvgpr_write_b32 a205, %1\n\t"
                   "v_accvgpr_read_b32 %0, a206\n\tv_mul_f32 %0, %3, %0\n\tv_accvgpr_write_b32 a206, %0\n\t"
                   "v_accvgpr_read_b32 %1, a207\n\tv_mul_f32 %1, %3, %1\n\tv_accvgpr_write_b32 a207, %1\n\t"
                   "v_accvgpr_read_b32 %0, a208\n\tv_mul_f32 %0, %3, %0\n\tv_accvgpr_write_b32 a208, %0\n\t"
                   "v_accvgpr_read_b32 %1, a209\n\tv_mul_f32 %1, %3, %1\n\tv_accvgpr_write_b32 a209, %1\n\t"
                   "v_accvgpr_read_b32 %0, a210\n\tv_mul_f32 %0, %3, %0\n\tv_accvgpr_write_b32 a210, %0\n\t"
                   "v_accvgpr_read_b32 %1, a211\n\tv_mul_f32 %1, %3, %1\n\tv_accvgpr_write_b32 a211, %1\n\t"
                   "v_accvgpr_read_b32 %0, a212\n\tv_mul_f32 %0, %3, %0\n\tv_accvgpr_write_b32 a212, %0\n\t"
                   "v_accvgpr_read_b32 %1, a213\n\tv_mul_f32 %1, %3, %1\n\tv_accvgpr_write_b32 a213, %1\n\t"
                   "v_accvgpr_read_b32 %0, a214\n\tv_mul_f32 %0, %3, %0\n\tv_accvgpr_write_b32 a214, %0\n\t"
                   "v_accvgpr_read_b32 %1, a215\n\tv_mul_f32 %1, %3, %1\n\tv_accvgpr_write_b32 a215, %1\n\t"
                   "v_accvgpr_read_b32 %0, a216\n\tv_mul_f32 %0, %3, %0\n\tv_accvgpr_write_b32 a216, %0\n\t"
                   "v_accvgpr_read_b32 %1, a217\n\tv_mul_f32 %1, %3, %1\n\tv_accvgpr_write_b32 a217, %1\n\t"
                   "v_accvgpr_read_b32 %0, a218\n\tv_mul_f32 %0, %3, %0\n\tv_accvgpr_write_b32 a218, %0\n\t"
                   "v_accvgpr_read_b32 %1, a219\n\tv_mul_f32 %1, %3, %1\n\tv_accvgpr_write_b32 a219, %1\n\t"
                   "v_accvgpr_read_b32 %0, a220\n\tv_mul_f32 %0, %3, %0\n\tv_accvgpr_write_b32 a220, %0\n\t"
                   "v_accvgpr_read_b32 %1, a221\n\tv_mul_f32 %1, %3, %1\n\tv_accvgpr_write_b32 a221, %1\n\t"
                   "v_accvgpr_read_b32 %0, a222\n\tv_mul_f32 %0, %3, %0\n\tv_accvgpr_write_b32 a222, %0\n\t"
                   "v_accvgpr_read_b32 %1, a223\n\tv_mul_f32 %1, %3, %1\n\tv_accvgpr_write_b32 a223, %1\n\t"
                   "v_accvgpr_read_b32 %0, a224\n\tv_mul_f32 %0, %3, %0\n\tv_accvgpr_write_b32 a224, %0\n\t"
                   "v_accvgpr_read_b32 %1, a225\n\tv_mul_f32 %1, %3, %1\n\tv_accvgpr_write_b32 a225, %1\n\t"
                   "v_accvgpr_read_b32 %0, a226\n\tv_mul_f32 %0, %3, %0\n\tv_accvgpr_write_b32 a226, %0\n\t"
                   "v_accvgpr_read_b32 %1, a227\n\tv_mul_f32 %1, %3, %1\n\tv_accvgpr_write_b32 a227, %1\n\t"
                   "v_accvgpr_read_b32 %0, a228\n\tv_mul_f32 %0, %3, %0\n\tv_accvgpr_write_b32 a228, %0\n\t"
                   "v_accvgpr_read_b32 %1, a229\n\tv_mul_f32 %1, %3, %1\n\tv_accvgpr_write_b32 a229, %1\n\t"
                   "v_accvgpr_read_b32 %0, a230\n\tv_mul_f32 %0, %3, %0\n\tv_accvgpr_write_b32 a230, %0\n\t"
                   "v_accvgpr_read_b32 %1, a231\n\tv_mul_f32 %1, %3, %1\n\tv_accvgpr_write_b32 a231, %1\n\t"
                   "v_accvgpr_read_b32 %0, a232\n\tv_mul_f32 %0, %3, %0\n\tv_accvgpr_write_b32 a232, %0\n\t"
                   "v_accvgpr_read_b32 %1, a233\n\tv_mul_f32 %1, %3, %1\n\tv_accvgpr_write_b32 a233, %1\n\t"
                   "v_accvgpr_read_b32 %0, a234\n\tv_mul_f32 %0, %3, %0\n\tv_accvgpr_write_b32 a234, %0\n\t"
                   "v_accvgpr_read_b32 %1, a235\n\tv_mul_f32 %1, %3, %1\n\tv_accvgpr_write_b32 a235, %1\n\t"
                   "v_accvgpr_read_b32 %0, a236\n\tv_mul_f32 %0, %3, %0\n\tv_accvgpr_write_b32 a236, %0\n\t"
                   "v_accvgpr_read_b32 %1, a237\n\tv_mul_f32 %1, %3, %1\n\tv_accvgpr_write_b32 a237, %1\n\t"
                   "v_accvgpr_read_b32 %0, a238\n\tv_mul_f32 %0, %3, %0\n\tv_accvgpr_write_b32 a238, %0\n\t"
                   "v_accvgpr_read_b32 %1, a239\n\tv_mul_f32 %1, %3, %1\n\tv_accvgpr_write_b32 a239, %1\n\t"
                   "v_accvgpr_read_b32 %0, a240\n\tv_mul_f32 %0, %3, %0\n\tv_accvgpr_write_b32 a240, %0\n\t"
                   "v_accvgpr_read_b32 %1, a241\n\tv_mul_f32 %1, %3, %1\n\tv_accvgpr_write_b32 a241, %1\n\t"
                   "v_accvgpr_read_b32 %0, a242\n\tv_mul_f32 %0, %3, %0\n\tv_accvgpr_write_b32 a242, %0\n\t"
                   "v_accvgpr_read_b32 %1, a243\n\tv_mul_f32 %1, %3, %1\n\tv_accvgpr_write_b32 a243, %1\n\t"
                   "v_accvgpr_read_b32 %0, a244\n\tv_mul_f32 %0, %3, %0\n\tv_accvgpr_write_b32 a244, %0\n\t"
                   "v_accvgpr_read_b32 %1, a245\n\tv_mul_f32 %1, %3, %1\n\tv_accvgpr_write_b32 a245, %1\n\t"
                   "v_accvgpr_read_b32 %0, a246\n\tv_mul_f32 %0, %3, %0\n\tv_accvgpr_write_b32 a246, %0\n\t"
                   "v_accvgpr_read_b32 %1, a247\n\tv_mul_f32 %1, %3, %1\n\tv_accvgpr_write_b32 a247, %1\n\t"
                   "v_accvgpr_read_b32 %0, a248\n\tv_mul_f32 %0, %3, %0\n\tv_accvgpr_write_b32 a248, %0\n\t"
                   "v_accvgpr_read_b32 %1, a249\n\tv_mul_f32 %1, %3, %1\n\tv_accvgpr_write_b32 a249, %1\n\t"
                   "v_accvgpr_read_b32 %0, a250\n\tv_mul_f32 %0, %3, %0\n\tv_accvgpr_write_b32 a250, %0\n\t"
                   "v_accvgpr_read_b32 %1, a251\n\tv_mul_f32 %1, %3, %1\n\tv_accvgpr_write_b32 a251, %1\n\t"
                   "v_accvgpr_read_b32 %0, a252\n\tv_mul_f32 %0, %3, %0\n\tv_accvgpr_write_b32 a252, %0\n\t"
                   "v_accvgpr_read_b32 %1, a253\n\tv_mul_f32 %1, %3, %1\n\tv_accvgpr_write_b32 a253, %1\n\t"
                   "v_accvgpr_read_b32 %0, a254\n\tv_mul_f32 %0, %3, %0\n\tv_accvgpr_write_b32 a254, %0\n\t"
                   "v_accvgpr_read_b32 %1, a255\n\tv_mul_f32 %1, %3, %1\n\tv_accvgpr_write_b32 a255, %1\n\t"
                   "s_nop 1"
                   : "=&v"(t0), "=&v"(t1) : "v"(c.fct[0]), "v"(c.fct[1]) : ACLOB);
      c.pend = false;
    }
  }
  asm volatile("s_nop 1" ::: "memory");
  GAP();
}
template <int T, bool HASPV, bool HASSM> DI void phaseB(Ctx4& c) {
  phaseB_<T, HASPV, HASSM>(c, std::make_integer_sequence<int, 32>{});
}

template <int A, int OFF> DI void acc_dswrite4(unsigned addr) {
  asm volatile("ds_write_b128 %0, a[%c1:%c2] offset:%c3" ::"v"(addr), "i"(A), "i"(A + 3), "i"(OFF) : "memory", ACLOB);
}
template <int A> DI f32x4 acc_read4() {
  f32x4 r;
  asm volatile("v_accvgpr_read_b32 %0, a[%c4]\n\tv_accvgpr_read_b32 %1, a[%c5]\n\tv_accvgpr_read_b32 %2, a[%c6]\n\t"
               "v_accvgpr_read_b32 %3, a[%c7]"
               : "=v"(r[0]), "=v"(r[1]), "=v"(r[2]), "=v"(r[3]) : "i"(A), "i"(A + 1), "i"(A + 2), "i"(A + 3) : ACLOB);
  return r;
}

__global__ __launch_bounds__(256, 1) void attn4_kernel(const u16* __restrict__ x, const u16* __restrict__ wq,
                                                       const u16* __restrict__ kc, const u16* __restrict__ vc,
                                                       const u16* __restrict__ wo, u16* __restrict__ o,
                                                       unsigned* __restrict__ cnt DBGARG) {
  extern __shared__ __attribute__((aligned(16))) char lds[];
  STAMP(0);
  if (blockIdx.x == 0 && threadIdx.x < 128) cnt[threadIdx.x] = 0u;
  const int tid = threadIdx.x, lane = tid & 63;
  const int w = __builtin_amdgcn_readfirstlane(tid >> 6);
  const int r = lane & 31, h = lane >> 5;
  const int bid = blockIdx.x;
  const int xcd = bid & 7, j = bid >> 3;
  const int grp = (j >> 2) * 8 + xcd, sub = j & 3;
  const int b = grp >> 3, kvh = grp & 7;
  const int head = kvh * 2 + (sub >> 1), rhalf = sub & 1;
  const int row0 = b * SQ + rhalf * 56;

  const u16* Kw = kc + ((size_t)(b * HKV + kvh) * SK + 256 * w) * DH;
  const u16* Vw = vc + ((size_t)(b * HKV + kvh) * SK + 256 * w) * DH;

  Ctx4 c;
  c.pfd = 0; c.sub = sub;
  c.Kp = Kw; c.Vp = Vw; c.pfoff = lane * 128;
  c.ring = lds + w * 40960;
  c.rk = __builtin_amdgcn_make_buffer_rsrc((void*)Kw, 0, 256 * DH * 2, 0x00020000);
  c.rv = __builtin_amdgcn_make_buffer_rsrc((void*)Vw, 0, 256 * DH * 2, 0x00020000);
#pragma unroll
  for (int pi = 0; pi < 4; ++pi) {
    const int krow = 4 * pi + (lane >> 4);
    c.koff[pi] = krow * 512 + (((lane & 15) ^ (krow & 15)) * 16);
  }
#pragma unroll
  for (int pi = 0; pi < 2; ++pi) {
    const int vrow = 2 * pi + (lane >> 5);
    c.voff[pi] = vrow * 512 + (((lane & 31) ^ ((vrow & 3) << 2)) * 16);
  }
  {
    __amdgpu_buffer_rsrc_t rx =
        __builtin_amdgcn_make_buffer_rsrc((void*)(x + (size_t)row0 * DM), 0, 56 * DM * 2, 0x00020000);
#pragma unroll
    for (int p = 0; p < 14; ++p) {
      const int ci = 64 * p + lane;
      const int lr = ci / 56, cp = ci - lr * 56;
      int gr = 16 * w + lr; gr = gr > 55 ? 55 : gr;
      const int cs = (cp & ~7) | ((cp ^ (lr >> 1)) & 7);
      __builtin_amdgcn_raw_ptr_buffer_load_lds(rx, (lds_vp)(c.ring + 3 * 8192 + p * 1024), 16, gr * (DM * 2) + cs * 16,
                                               0, 0, 0);
    }
  }
  {
    const int colw = head * DH + 64 * w;
    __amdgpu_buffer_rsrc_t rw =
        __builtin_amdgcn_make_buffer_rsrc((void*)(wq + colw), 0, (DM * NQ - colw) * 2, 0x00020000);
    const int drow = lane >> 3;
    const int wvoff = drow * (NQ * 2) + (((lane & 7) ^ (((drow >> 1) & 1) << 2)) * 16);
    auto wissue = [&](int ch) {
#pragma unroll
      for (int p = 0; p < 8; ++p)
        __builtin_amdgcn_raw_ptr_buffer_load_lds(rw, (lds_vp)(c.ring + ((ch + 2) % 3) * 8192 + p * 1024), 16, wvoff,
                                                 (64 * ch + 8 * p) * (NQ * 2), 0, 0);
    };
    wissue(0); wissue(1); wissue(2);
    const int g = lane >> 4, qq = (lane >> 2) & 3, pp = lane & 3;
    const unsigned trb = (unsigned)(size_t)c.ring + (8 * (g >> 1) + qq) * 128 + ((32 * (g & 1) + 8 * pp) ^ ((qq >> 1) << 6));
    const char* xa[2][4];
#pragma unroll
    for (int qb = 0; qb < 2; ++qb) {
      const int R = 32 * qb + r, lr = R & 15;
#pragma unroll
      for (int v = 0; v < 4; ++v)
        xa[qb][v] = lds + (R >> 4) * 40960 + 3 * 8192 + lr * 896 + ((((2 * v + h) ^ (lr >> 1)) & 7) * 16);
    }
    f32x16 qa[2][2];
#pragma unroll
    for (int a2 = 0; a2 < 2; ++a2)
#pragma unroll
      for (int b2 = 0; b2 < 2; ++b2)
#pragma unroll
        for (int i = 0; i < 16; ++i) qa[a2][b2][i] = 0.f;
    s16x4 fa[2][4];
    bf16x8 fx[2][2];
    auto rdstep = [&](int i, int bsel) {
      const int ch = i >> 2, s4 = i & 3;
      const unsigned tb = trb + ((ch + 2) % 3) * 8192 + s4 * 2048;
      fa[bsel][0] = trread(tb);
      fa[bsel][1] = trread(tb + 512);
      fa[bsel][2] = trread(tb ^ 64);
      fa[bsel][3] = trread((tb + 512) ^ 64);
      asm volatile("ds_read_b128 %0, %1" : "=v"(fx[bsel][0]) : "v"((unsigned)(size_t)(xa[0][s4] + ch * 128)) : "memory");
      asm volatile("ds_read_b128 %0, %1" : "=v"(fx[bsel][1]) : "v"((unsigned)(size_t)(xa[1][s4] + ch * 128)) : "memory");
    };
    auto refill_piece = [&](int k, int p) {
      if (k <= 3) {
        __builtin_amdgcn_raw_ptr_buffer_load_lds(rw, (lds_vp)(c.ring + ((k + 5) % 3) * 8192 + p * 1024), 16, wvoff,
                                                 (64 * (k + 3) + 8 * p) * (NQ * 2), 0, 0);
      }
    };
    WAITV(16);
    __builtin_amdgcn_s_barrier();
    rdstep(0, 0);
#pragma unroll
    for (int i = 0; i < 28; ++i) {
      const int bs = i & 1, ch = i >> 2, s4 = i & 3;
      if (i + 1 < 28) {
        if (s4 == 3) { if (ch == 0) WAITV(8); else WAITV(6); }
        rdstep(i + 1, bs ^ 1);
        asm volatile("s_waitcnt lgkmcnt(6)" : "+v"(fa[bs][0]), "+v"(fa[bs][1]), "+v"(fa[bs][2]), "+v"(fa[bs][3]), "+v"(fx[bs][0]), "+v"(fx[bs][1])::"memory");
      } else {
        asm volatile("s_waitcnt lgkmcnt(0)" : "+v"(fa[bs][0]), "+v"(fa[bs][1]), "+v"(fa[bs][2]), "+v"(fa[bs][3]), "+v"(fx[bs][0]), "+v"(fx[bs][1])::"memory");
      }
      bf16x8 A0 = cat8(fa[bs][0], fa[bs][1]), A1 = cat8(fa[bs][2], fa[bs][3]);
      qa[0][0] = __builtin_amdgcn_mfma_f32_32x32x16_bf16(A0, fx[bs][0], qa[0][0], 0, 0, 0);
      GAP();
      if (ch == 5 && s4 == 0) { prefetch_unit(c, 6 + sub); prefetch_unit(c, 10 + sub); }
      if (ch >= 1) {
        if (ch - 1 <= 3) refill_piece(ch - 1, 2 * s4);
        else if (ch == 5) { if (s4 == 0) dma_piece<0, 0>(c); else if (s4 == 1) dma_piece<0, 2>(c); else if (s4 == 2) dma_piece<0, 4>(c); else dma_piece<0, 6>(c); }
        else { if (s4 == 0) dma_piece<1, 0>(c); else if (s4 == 1) dma_piece<1, 2>(c); else if (s4 == 2) dma_piece<1, 4>(c); else dma_piece<1, 6>(c); }
      }
      GAP();
      qa[0][1] = __builtin_amdgcn_mfma_f32_32x32x16_bf16(A1, fx[bs][0], qa[0][1], 0, 0, 0);
      qa[1][0] = __builtin_amdgcn_mfma_f32_32x32x16_bf16(A0, fx[bs][1], qa[1][0], 0, 0, 0);
      GAP();
      if (ch >= 1) {
        if (ch - 1 <= 3) refill_piece(ch - 1, 2 * s4 + 1);
        else if (ch == 5) { if (s4 == 0) dma_piece<0, 1>(c); else if (s4 == 1) dma_piece<0, 3>(c); else if (s4 == 2) dma_piece<0, 5>(c); else dma_piece<0, 7>(c); }
        else { if (s4 == 0) dma_piece<1, 1>(c); else if (s4 == 1) dma_piece<1, 3>(c); else if (s4 == 2) dma_piece<1, 5>(c); else dma_piece<1, 7>(c); }
      }
      GAP();
      qa[1][1] = __builtin_amdgcn_mfma_f32_32x32x16_bf16(A1, fx[bs][1], qa[1][1], 0, 0, 0);
    }
    dma_unit<2>(c);
    STAMP(12);
    __builtin_amdgcn_s_barrier();
#pragma unroll
    for (int qb = 0; qb < 2; ++qb) {
      const int R = 32 * qb + r;
      char* base = lds + (R >> 4) * 40960 + 4 * 8192 + (R & 15) * 512 + 8 * h;
#pragma unroll
      for (int cb = 0; cb < 2; ++cb)
#pragma unroll
        for (int gg = 0; gg < 4; ++gg) {
          u32x2 v = {cvtpk(qa[qb][cb][4 * gg], qa[qb][cb][4 * gg + 1]), cvtpk(qa[qb][cb][4 * gg + 2], qa[qb][cb][4 * gg + 3])};
          *(u32x2*)(base + (((8 * w + 4 * cb + gg) ^ (R & 15)) * 16)) = v;
        }
    }
    WAITL0();
    __builtin_amdgcn_s_barrier();
  }
  {
#pragma unroll
    for (int qb = 0; qb < 2; ++qb) {
      const int R = 32 * qb + r;
      const char* base = lds + (R >> 4) * 40960 + 4 * 8192 + (R & 15) * 512;
#pragma unroll
      for (int s = 0; s < 16; ++s) c.qf[qb][s] = *(const bf16x8*)(base + (((2 * s + h) ^ (R & 15)) * 16));
    }
#pragma unroll
    for (int qb = 0; qb < 2; ++qb)
#pragma unroll
      for (int s = 0; s < 16; ++s) asm volatile("" : "+v"(c.qf[qb][s]));
  }
  WAITL0();
  __builtin_amdgcn_s_barrier();
  dma_unit<3>(c);
  dma_unit<4>(c);
  STAMP(1);

  {
    const unsigned rb = (unsigned)(size_t)c.ring;
#pragma unroll
    for (int ss = 0; ss < 8; ++ss) c.kaddr[ss] = rb + r * 256 + ((((2 * ss + h) ^ (r & 15))) * 16);
    const int g = lane >> 4, qq = (lane >> 2) & 3, pp = lane & 3;
#pragma unroll
    for (int d = 0; d < 4; ++d)
      c.vaddr[d] = rb + (4 * (g >> 1) + qq) * 512 + ((64 * d + 32 * (g & 1) + 8 * pp) ^ (qq << 6));
  }
  c.pend = false;
  c.fct[0] = 1.f; c.fct[1] = 1.f;

  phaseA<0>(c); phaseB<0, false, true>(c); STAMP(2);
  phaseA<1>(c); phaseB<1, true, true>(c); STAMP(3);
  phaseA<2>(c); phaseB<2, true, true>(c); STAMP(4);
  phaseA<3>(c); STAMP(11); phaseB<3, true, true>(c); STAMP(5);
  phaseA<4>(c); phaseB<4, true, true>(c); STAMP(6);
  phaseA<5>(c); phaseB<5, true, true>(c); STAMP(7);
  phaseA<6>(c); phaseB<6, true, true>(c); STAMP(8);
  {
    int li = (bid * 4 + w) * 28 + (lane < 28 ? lane : 27);
    asm volatile("global_load_dword %0, %1, off" : "+v"(c.pfd) : "v"(wo + (size_t)li * 64) : "memory");
  }
  phaseA<7>(c); phaseB<7, true, true>(c); STAMP(9);
  phaseB<8, true, false>(c);
  asm volatile("s_nop 7\n\ts_nop 7" ::: "memory");
  STAMP(13);

  __syncthreads();
  float* ml = (float*)lds;
#pragma unroll
  for (int qb = 0; qb < 2; ++qb) {
    auto rr = __builtin_amdgcn_permlane32_swap(__float_as_uint(c.l[qb]), __float_as_uint(c.l[qb]), false, false);
    const float lt = __uint_as_float(rr[0]) + __uint_as_float(rr[1]);
    if (h == 0) {
      ml[((w * 2 + qb) * 32 + r) * 2 + 0] = c.m[qb];
      ml[((w * 2 + qb) * 32 + r) * 2 + 1] = lt;
    }
  }
  __syncthreads();
  float fw[2][4];
#pragma unroll
  for (int qb = 0; qb < 2; ++qb) {
    float mm[4], ll[4];
#pragma unroll
    for (int ww = 0; ww < 4; ++ww) {
      mm[ww] = ml[((ww * 2 + qb) * 32 + r) * 2 + 0];
      ll[ww] = ml[((ww * 2 + qb) * 32 + r) * 2 + 1];
    }
    const float M = fmaxf(fmaxf(mm[0], mm[1]), fmaxf(mm[2], mm[3]));
    float e[4], L = 0.f;
#pragma unroll
    for (int ww = 0; ww < 4; ++ww) { e[ww] = __builtin_amdgcn_exp2f(mm[ww] - M); L += e[ww] * ll[ww]; }
    const float inv = 1.f / L;
#pragma unroll
    for (int ww = 0; ww < 4; ++ww) fw[qb][ww] = e[ww] * inv;
  }
  STAMP(14);
  const unsigned xw = (unsigned)(size_t)lds + 4096 + lane * 16;
  const f32x4* xr = (const f32x4*)(lds + 4096) + lane;
  char* otile = lds + 102400;
  {
    switch (w) {
      case 0: {
        acc_dswrite4<32, 0>(xw + 0);
        acc_dswrite4<36, 1024>(xw + 0);
        acc_dswrite4<40, 2048>(xw + 0);
        acc_dswrite4<44, 3072>(xw + 0);
        acc_dswrite4<160, 4096>(xw + 0);
        acc_dswrite4<164, 5120>(xw + 0);
        acc_dswrite4<168, 6144>(xw + 0);
        acc_dswrite4<172, 7168>(xw + 0);
        acc_dswrite4<64, 8192>(xw + 0);
        acc_dswrite4<68, 9216>(xw + 0);
        acc_dswrite4<72, 10240>(xw + 0);
        acc_dswrite4<76, 11264>(xw + 0);
        acc_dswrite4<192, 12288>(xw + 0);
        acc_dswrite4<196, 13312>(xw + 0);
        acc_dswrite4<200, 14336>(xw + 0);
        acc_dswrite4<204, 15360>(xw + 0);
        acc_dswrite4<96, 16384>(xw + 0);
        acc_dswrite4<100, 17408>(xw + 0);
        acc_dswrite4<104, 18432>(xw + 0);
        acc_dswrite4<108, 19456>(xw + 0);
        acc_dswrite4<224, 20480>(xw + 0);
        acc_dswrite4<228, 21504>(xw + 0);
        acc_dswrite4<232, 22528>(xw + 0);
        acc_dswrite4<236, 23552>(xw + 0);
      } break;
      case 1: {
        acc_dswrite4<0, 24576>(xw + 0);
        acc_dswrite4<4, 25600>(xw + 0);
        acc_dswrite4<8, 26624>(xw + 0);
        acc_dswrite4<12, 27648>(xw + 0);
        acc_dswrite4<128, 28672>(xw + 0);
        acc_dswrite4<132, 29696>(xw + 0);
        acc_dswrite4<136, 30720>(xw + 0);
        acc_dswrite4<140, 31744>(xw + 0);
        acc_dswrite4<64, 32768>(xw + 0);
        acc_dswrite4<68, 33792>(xw + 0);
        acc_dswrite4<72, 34816>(xw + 0);
        acc_dswrite4<76, 35840>(xw + 0);
        acc_dswrite4<192, 36864>(xw + 0);
        acc_dswrite4<196, 37888>(xw + 0);
        acc_dswrite4<200, 38912>(xw + 0);
        acc_dswrite4<204, 39936>(xw + 0);
        acc_dswrite4<96, 40960>(xw + 0);
        acc_dswrite4<100, 41984>(xw + 0);
        acc_dswrite4<104, 43008>(xw + 0);
        acc_dswrite4<108, 44032>(xw + 0);
        acc_dswrite4<224, 45056>(xw + 0);
        acc_dswrite4<228, 46080>(xw + 0);
        acc_dswrite4<232, 47104>(xw + 0);
        acc_dswrite4<236, 48128>(xw + 0);
      } break;
      case 2: {
        acc_dswrite4<0, 49152>(xw + 0);
        acc_dswrite4<4, 50176>(xw + 0);
        acc_dswrite4<8, 51200>(xw + 0);
        acc_dswrite4<12, 52224>(xw + 0);
        acc_dswrite4<128, 53248>(xw + 0);
        acc_dswrite4<132, 54272>(xw + 0);
        acc_dswrite4<136, 55296>(xw + 0);
        acc_dswrite4<140, 56320>(xw + 0);
        acc_dswrite4<32, 57344>(xw + 0);
        acc_dswrite4<36, 58368>(xw + 0);
        acc_dswrite4<40, 59392>(xw + 0);
        acc_dswrite4<44, 60416>(xw + 0);
        acc_dswrite4<160, 61440>(xw + 0);
        acc_dswrite4<164, 62464>(xw + 0);
        acc_dswrite4<168, 63488>(xw + 0);
        acc_dswrite4<172, 64512>(xw + 0);
        acc_dswrite4<96, 0>(xw + 65536);
        acc_dswrite4<100, 1024>(xw + 65536);
        acc_dswrite4<104, 2048>(xw + 65536);
        acc_dswrite4<108, 3072>(xw + 65536);
        acc_dswrite4<224, 4096>(xw + 65536);
        acc_dswrite4<228, 5120>(xw + 65536);
        acc_dswrite4<232, 6144>(xw + 65536);
        acc_dswrite4<236, 7168>(xw + 65536);
      } break;
      case 3: {
        acc_dswrite4<0, 8192>(xw + 65536);
        acc_dswrite4<4, 9216>(xw + 65536);
        acc_dswrite4<8, 10240>(xw + 65536);
        acc_dswrite4<12, 11264>(xw + 65536);
        acc_dswrite4<128, 12288>(xw + 65536);
        acc_dswrite4<132, 13312>(xw + 65536);
        acc_dswrite4<136, 14336>(xw + 65536);
        acc_dswrite4<140, 15360>(xw + 65536);
        acc_dswrite4<32, 16384>(xw + 65536);
        acc_dswrite4<36, 17408>(xw + 65536);
        acc_dswrite4<40, 18432>(xw + 65536);
        acc_dswrite4<44, 19456>(xw + 65536);
        acc_dswrite4<160, 20480>(xw + 65536);
        acc_dswrite4<164, 21504>(xw + 65536);
        acc_dswrite4<168, 22528>(xw + 65536);
        acc_dswrite4<172, 23552>(xw + 65536);
        acc_dswrite4<64, 24576>(xw + 65536);
        acc_dswrite4<68, 25600>(xw + 65536);
        acc_dswrite4<72, 26624>(xw + 65536);
        acc_dswrite4<76, 27648>(xw + 65536);
        acc_dswrite4<192, 28672>(xw + 65536);
        acc_dswrite4<196, 29696>(xw + 65536);
        acc_dswrite4<200, 30720>(xw + 65536);
        acc_dswrite4<204, 31744>(xw + 65536);
      } break;
    }
    WAITL0();
    __builtin_amdgcn_s_barrier();
    switch (w) {
      case 0: {
        { f32x4 sum = acc_read4<0>(); sum *= fw[0][0];
          sum += xr[1536] * fw[0][1];
          sum += xr[3072] * fw[0][2];
          sum += xr[4608] * fw[0][3];
          u32x2 v = {cvtpk(sum[0], sum[1]), cvtpk(sum[2], sum[3])};
          *(u32x2*)(otile + (0 + r) * 512 + (((0) ^ ((0 + r) & 31)) * 16) + 8 * h) = v; }
        { f32x4 sum = acc_read4<4>(); sum *= fw[0][0];
          sum += xr[1600] * fw[0][1];
          sum += xr[3136] * fw[0][2];
          sum += xr[4672] * fw[0][3];
          u32x2 v = {cvtpk(sum[0], sum[1]), cvtpk(sum[2], sum[3])};
          *(u32x2*)(otile + (0 + r) * 512 + (((1) ^ ((0 + r) & 31)) * 16) + 8 * h) = v; }
        { f32x4 sum = acc_read4<8>(); sum *= fw[0][0];
          sum += xr[1664] * fw[0][1];
          sum += xr[3200] * fw[0][2];
          sum += xr[4736] * fw[0][3];
          u32x2 v = {cvtpk(sum[0], sum[1]), cvtpk(sum[2], sum[3])};
          *(u32x2*)(otile + (0 + r) * 512 + (((2) ^ ((0 + r) & 31)) * 16) + 8 * h) = v; }
        { f32x4 sum = acc_read4<12>(); sum *= fw[0][0];
          sum += xr[1728] * fw[0][1];
          sum += xr[3264] * fw[0][2];
          sum += xr[4800] * fw[0][3];
          u32x2 v = {cvtpk(sum[0], sum[1]), cvtpk(sum[2], sum[3])};
          *(u32x2*)(otile + (0 + r) * 512 + (((3) ^ ((0 + r) & 31)) * 16) + 8 * h) = v; }
        { f32x4 sum = acc_read4<128>(); sum *= fw[1][0];
          sum += xr[1792] * fw[1][1];
          sum += xr[3328] * fw[1][2];
          sum += xr[4864] * fw[1][3];
          u32x2 v = {cvtpk(sum[0], sum[1]), cvtpk(sum[2], sum[3])};
          *(u32x2*)(otile + (32 + r) * 512 + (((0) ^ ((32 + r) & 31)) * 16) + 8 * h) = v; }
        { f32x4 sum = acc_read4<132>(); sum *= fw[1][0];
          sum += xr[1856] * fw[1][1];
          sum += xr[3392] * fw[1][2];
          sum += xr[4928] * fw[1][3];
          u32x2 v = {cvtpk(sum[0], sum[1]), cvtpk(sum[2], sum[3])};
          *(u32x2*)(otile + (32 + r) * 512 + (((1) ^ ((32 + r) & 31)) * 16) + 8 * h) = v; }
        { f32x4 sum = acc_read4<136>(); sum *= fw[1][0];
          sum += xr[1920] * fw[1][1];
          sum += xr[3456] * fw[1][2];
          sum += xr[4992] * fw[1][3];
          u32x2 v = {cvtpk(sum[0], sum[1]), cvtpk(sum[2], sum[3])};
          *(u32x2*)(otile + (32 + r) * 512 + (((2) ^ ((32 + r) & 31)) * 16) + 8 * h) = v; }
        { f32x4 sum = acc_read4<140>(); sum *= fw[1][0];
          sum += xr[1984] * fw[1][1];
          sum += xr[3520] * fw[1][2];
          sum += xr[5056] * fw[1][3];
          u32x2 v = {cvtpk(sum[0], sum[1]), cvtpk(sum[2], sum[3])};
          *(u32x2*)(otile + (32 + r) * 512 + (((3) ^ ((32 + r) & 31)) * 16) + 8 * h) = v; }
      } break;
      case 1: {
        { f32x4 sum = acc_read4<32>(); sum *= fw[0][1];
          sum += xr[0] * fw[0][0];
          sum += xr[3584] * fw[0][2];
          sum += xr[5120] * fw[0][3];
          u32x2 v = {cvtpk(sum[0], sum[1]), cvtpk(sum[2], sum[3])};
          *(u32x2*)(otile + (0 + r) * 512 + (((8) ^ ((0 + r) & 31)) * 16) + 8 * h) = v; }
        { f32x4 sum = acc_read4<36>(); sum *= fw[0][1];
          sum += xr[64] * fw[0][0];
          sum += xr[3648] * fw[0][2];
          sum += xr[5184] * fw[0][3];
          u32x2 v = {cvtpk(sum[0], sum[1]), cvtpk(sum[2], sum[3])};
          *(u32x2*)(otile + (0 + r) * 512 + (((9) ^ ((0 + r) & 31)) * 16) + 8 * h) = v; }
        { f32x4 sum = acc_read4<40>(); sum *= fw[0][1];
          sum += xr[128] * fw[0][0];
          sum += xr[3712] * fw[0][2];
          sum += xr[5248] * fw[0][3];
          u32x2 v = {cvtpk(sum[0], sum[1]), cvtpk(sum[2], sum[3])};
          *(u32x2*)(otile + (0 + r) * 512 + (((10) ^ ((0 + r) & 31)) * 16) + 8 * h) = v; }
        { f32x4 sum = acc_read4<44>(); sum *= fw[0][1];
          sum += xr[192] * fw[0][0];
          sum += xr[3776] * fw[0][2];
          sum += xr[5312] * fw[0][3];
          u32x2 v = {cvtpk(sum[0], sum[1]), cvtpk(sum[2], sum[3])};
          *(u32x2*)(otile + (0 + r) * 512 + (((11) ^ ((0 + r) & 31)) * 16) + 8 * h) = v; }
        { f32x4 sum = acc_read4<160>(); sum *= fw[1][1];
          sum += xr[256] * fw[1][0];
          sum += xr[3840] * fw[1][2];
          sum += xr[5376] * fw[1][3];
          u32x2 v = {cvtpk(sum[0], sum[1]), cvtpk(sum[2], sum[3])};
          *(u32x2*)(otile + (32 + r) * 512 + (((8) ^ ((32 + r) & 31)) * 16) + 8 * h) = v; }
        { f32x4 sum = acc_read4<164>(); sum *= fw[1][1];
          sum += xr[320] * fw[1][0];
          sum += xr[3904] * fw[1][2];
          sum += xr[5440] * fw[1][3];
          u32x2 v = {cvtpk(sum[0], sum[1]), cvtpk(sum[2], sum[3])};
          *(u32x2*)(otile + (32 + r) * 512 + (((9) ^ ((32 + r) & 31)) * 16) + 8 * h) = v; }
        { f32x4 sum = acc_read4<168>(); sum *= fw[1][1];
          sum += xr[384] * fw[1][0];
          sum += xr[3968] * fw[1][2];
          sum += xr[5504] * fw[1][3];
          u32x2 v = {cvtpk(sum[0], sum[1]), cvtpk(sum[2], sum[3])};
          *(u32x2*)(otile + (32 + r) * 512 + (((10) ^ ((32 + r) & 31)) * 16) + 8 * h) = v; }
        { f32x4 sum = acc_read4<172>(); sum *= fw[1][1];
          sum += xr[448] * fw[1][0];
          sum += xr[4032] * fw[1][2];
          sum += xr[5568] * fw[1][3];
          u32x2 v = {cvtpk(sum[0], sum[1]), cvtpk(sum[2], sum[3])};
          *(u32x2*)(otile + (32 + r) * 512 + (((11) ^ ((32 + r) & 31)) * 16) + 8 * h) = v; }
      } break;
      case 2: {
        { f32x4 sum = acc_read4<64>(); sum *= fw[0][2];
          sum += xr[512] * fw[0][0];
          sum += xr[2048] * fw[0][1];
          sum += xr[5632] * fw[0][3];
          u32x2 v = {cvtpk(sum[0], sum[1]), cvtpk(sum[2], sum[3])};
          *(u32x2*)(otile + (0 + r) * 512 + (((16) ^ ((0 + r) & 31)) * 16) + 8 * h) = v; }
        { f32x4 sum = acc_read4<68>(); sum *= fw[0][2];
          sum += xr[576] * fw[0][0];
          sum += xr[2112] * fw[0][1];
          sum += xr[5696] * fw[0][3];
          u32x2 v = {cvtpk(sum[0], sum[1]), cvtpk(sum[2], sum[3])};
          *(u32x2*)(otile + (0 + r) * 512 + (((17) ^ ((0 + r) & 31)) * 16) + 8 * h) = v; }
        { f32x4 sum = acc_read4<72>(); sum *= fw[0][2];
          sum += xr[640] * fw[0][0];
          sum += xr[2176] * fw[0][1];
          sum += xr[5760] * fw[0][3];
          u32x2 v = {cvtpk(sum[0], sum[1]), cvtpk(sum[2], sum[3])};
          *(u32x2*)(otile + (0 + r) * 512 + (((18) ^ ((0 + r) & 31)) * 16) + 8 * h) = v; }
        { f32x4 sum = acc_read4<76>(); sum *= fw[0][2];
          sum += xr[704] * fw[0][0];
          sum += xr[2240] * fw[0][1];
          sum += xr[5824] * fw[0][3];
          u32x2 v = {cvtpk(sum[0], sum[1]), cvtpk(sum[2], sum[3])};
          *(u32x2*)(otile + (0 + r) * 512 + (((19) ^ ((0 + r) & 31)) * 16) + 8 * h) = v; }
        { f32x4 sum = acc_read4<192>(); sum *= fw[1][2];
          sum += xr[768] * fw[1][0];
          sum += xr[2304] * fw[1][1];
          sum += xr[5888] * fw[1][3];
          u32x2 v = {cvtpk(sum[0], sum[1]), cvtpk(sum[2], sum[3])};
          *(u32x2*)(otile + (32 + r) * 512 + (((16) ^ ((32 + r) & 31)) * 16) + 8 * h) = v; }
        { f32x4 sum = acc_read4<196>(); sum *= fw[1][2];
          sum += xr[832] * fw[1][0];
          sum += xr[2368] * fw[1][1];
          sum += xr[5952] * fw[1][3];
          u32x2 v = {cvtpk(sum[0], sum[1]), cvtpk(sum[2], sum[3])};
          *(u32x2*)(otile + (32 + r) * 512 + (((17) ^ ((32 + r) & 31)) * 16) + 8 * h) = v; }
        { f32x4 sum = acc_read4<200>(); sum *= fw[1][2];
          sum += xr[896] * fw[1][0];
          sum += xr[2432] * fw[1][1];
          sum += xr[6016] * fw[1][3];
          u32x2 v = {cvtpk(sum[0], sum[1]), cvtpk(sum[2], sum[3])};
          *(u32x2*)(otile + (32 + r) * 512 + (((18) ^ ((32 + r) & 31)) * 16) + 8 * h) = v; }
        { f32x4 sum = acc_read4<204>(); sum *= fw[1][2];
          sum += xr[960] * fw[1][0];
          sum += xr[2496] * fw[1][1];
          sum += xr[6080] * fw[1][3];
          u32x2 v = {cvtpk(sum[0], sum[1]), cvtpk(sum[2], sum[3])};
          *(u32x2*)(otile + (32 + r) * 512 + (((19) ^ ((32 + r) & 31)) * 16) + 8 * h) = v; }
      } break;
      case 3: {
        { f32x4 sum = acc_read4<96>(); sum *= fw[0][3];
          sum += xr[1024] * fw[0][0];
          sum += xr[2560] * fw[0][1];
          sum += xr[4096] * fw[0][2];
          u32x2 v = {cvtpk(sum[0], sum[1]), cvtpk(sum[2], sum[3])};
          *(u32x2*)(otile + (0 + r) * 512 + (((24) ^ ((0 + r) & 31)) * 16) + 8 * h) = v; }
        { f32x4 sum = acc_read4<100>(); sum *= fw[0][3];
          sum += xr[1088] * fw[0][0];
          sum += xr[2624] * fw[0][1];
          sum += xr[4160] * fw[0][2];
          u32x2 v = {cvtpk(sum[0], sum[1]), cvtpk(sum[2], sum[3])};
          *(u32x2*)(otile + (0 + r) * 512 + (((25) ^ ((0 + r) & 31)) * 16) + 8 * h) = v; }
        { f32x4 sum = acc_read4<104>(); sum *= fw[0][3];
          sum += xr[1152] * fw[0][0];
          sum += xr[2688] * fw[0][1];
          sum += xr[4224] * fw[0][2];
          u32x2 v = {cvtpk(sum[0], sum[1]), cvtpk(sum[2], sum[3])};
          *(u32x2*)(otile + (0 + r) * 512 + (((26) ^ ((0 + r) & 31)) * 16) + 8 * h) = v; }
        { f32x4 sum = acc_read4<108>(); sum *= fw[0][3];
          sum += xr[1216] * fw[0][0];
          sum += xr[2752] * fw[0][1];
          sum += xr[4288] * fw[0][2];
          u32x2 v = {cvtpk(sum[0], sum[1]), cvtpk(sum[2], sum[3])};
          *(u32x2*)(otile + (0 + r) * 512 + (((27) ^ ((0 + r) & 31)) * 16) + 8 * h) = v; }
        { f32x4 sum = acc_read4<224>(); sum *= fw[1][3];
          sum += xr[1280] * fw[1][0];
          sum += xr[2816] * fw[1][1];
          sum += xr[4352] * fw[1][2];
          u32x2 v = {cvtpk(sum[0], sum[1]), cvtpk(sum[2], sum[3])};
          *(u32x2*)(otile + (32 + r) * 512 + (((24) ^ ((32 + r) & 31)) * 16) + 8 * h) = v; }
        { f32x4 sum = acc_read4<228>(); sum *= fw[1][3];
          sum += xr[1344] * fw[1][0];
          sum += xr[2880] * fw[1][1];
          sum += xr[4416] * fw[1][2];
          u32x2 v = {cvtpk(sum[0], sum[1]), cvtpk(sum[2], sum[3])};
          *(u32x2*)(otile + (32 + r) * 512 + (((25) ^ ((32 + r) & 31)) * 16) + 8 * h) = v; }
        { f32x4 sum = acc_read4<232>(); sum *= fw[1][3];
          sum += xr[1408] * fw[1][0];
          sum += xr[2944] * fw[1][1];
          sum += xr[4480] * fw[1][2];
          u32x2 v = {cvtpk(sum[0], sum[1]), cvtpk(sum[2], sum[3])};
          *(u32x2*)(otile + (32 + r) * 512 + (((26) ^ ((32 + r) & 31)) * 16) + 8 * h) = v; }
        { f32x4 sum = acc_read4<236>(); sum *= fw[1][3];
          sum += xr[1472] * fw[1][0];
          sum += xr[3008] * fw[1][1];
          sum += xr[4544] * fw[1][2];
          u32x2 v = {cvtpk(sum[0], sum[1]), cvtpk(sum[2], sum[3])};
          *(u32x2*)(otile + (32 + r) * 512 + (((27) ^ ((32 + r) & 31)) * 16) + 8 * h) = v; }
      } break;
    }
    WAITL0();
    __builtin_amdgcn_s_barrier();
  }
  {
    switch (w) {
      case 0: {
        acc_dswrite4<48, 0>(xw + 0);
        acc_dswrite4<52, 1024>(xw + 0);
        acc_dswrite4<56, 2048>(xw + 0);
        acc_dswrite4<60, 3072>(xw + 0);
        acc_dswrite4<176, 4096>(xw + 0);
        acc_dswrite4<180, 5120>(xw + 0);
        acc_dswrite4<184, 6144>(xw + 0);
        acc_dswrite4<188, 7168>(xw + 0);
        acc_dswrite4<80, 8192>(xw + 0);
        acc_dswrite4<84, 9216>(xw + 0);
        acc_dswrite4<88, 10240>(xw + 0);
        acc_dswrite4<92, 11264>(xw + 0);
        acc_dswrite4<208, 12288>(xw + 0);
        acc_dswrite4<212, 13312>(xw + 0);
        acc_dswrite4<216, 14336>(xw + 0);
        acc_dswrite4<220, 15360>(xw + 0);
        acc_dswrite4<112, 16384>(xw + 0);
        acc_dswrite4<116, 17408>(xw + 0);
        acc_dswrite4<120, 18432>(xw + 0);
        acc_dswrite4<124, 19456>(xw + 0);
        acc_dswrite4<240, 20480>(xw + 0);
        acc_dswrite4<244, 21504>(xw + 0);
        acc_dswrite4<248, 22528>(xw + 0);
        acc_dswrite4<252, 23552>(xw + 0);
      } break;
      case 1: {
        acc_dswrite4<16, 24576>(xw + 0);
        acc_dswrite4<20, 25600>(xw + 0);
        acc_dswrite4<24, 26624>(xw + 0);
        acc_dswrite4<28, 27648>(xw + 0);
        acc_dswrite4<144, 28672>(xw + 0);
        acc_dswrite4<148, 29696>(xw + 0);
        acc_dswrite4<152, 30720>(xw + 0);
        acc_dswrite4<156, 31744>(xw + 0);
        acc_dswrite4<80, 32768>(xw + 0);
        acc_dswrite4<84, 33792>(xw + 0);
        acc_dswrite4<88, 34816>(xw + 0);
        acc_dswrite4<92, 35840>(xw + 0);
        acc_dswrite4<208, 36864>(xw + 0);
        acc_dswrite4<212, 37888>(xw + 0);
        acc_dswrite4<216, 38912>(xw + 0);
        acc_dswrite4<220, 39936>(xw + 0);
        acc_dswrite4<112, 40960>(xw + 0);
        acc_dswrite4<116, 41984>(xw + 0);
        acc_dswrite4<120, 43008>(xw + 0);
        acc_dswrite4<124, 44032>(xw + 0);
        acc_dswrite4<240, 45056>(xw + 0);
        acc_dswrite4<244, 46080>(xw + 0);
        acc_dswrite4<248, 47104>(xw + 0);
        acc_dswrite4<252, 48128>(xw + 0);
      } break;
      case 2: {
        acc_dswrite4<16, 49152>(xw + 0);
        acc_dswrite4<20, 50176>(xw + 0);
        acc_dswrite4<24, 51200>(xw + 0);
        acc_dswrite4<28, 52224>(xw + 0);
        acc_dswrite4<144, 53248>(xw + 0);
        acc_dswrite4<148, 54272>(xw + 0);
        acc_dswrite4<152, 55296>(xw + 0);
        acc_dswrite4<156, 56320>(xw + 0);
        acc_dswrite4<48, 57344>(xw + 0);
        acc_dswrite4<52, 58368>(xw + 0);
        acc_dswrite4<56, 59392>(xw + 0);
        acc_dswrite4<60, 60416>(xw + 0);
        acc_dswrite4<176, 61440>(xw + 0);
        acc_dswrite4<180, 62464>(xw + 0);
        acc_dswrite4<184, 63488>(xw + 0);
        acc_dswrite4<188, 64512>(xw + 0);
        acc_dswrite4<112, 0>(xw + 65536);
        acc_dswrite4<116, 1024>(xw + 65536);
        acc_dswrite4<120, 2048>(xw + 65536);
        acc_dswrite4<124, 3072>(xw + 65536);
        acc_dswrite4<240, 4096>(xw + 65536);
        acc_dswrite4<244, 5120>(xw + 65536);
        acc_dswrite4<248, 6144>(xw + 65536);
        acc_dswrite4<252, 7168>(xw + 65536);
      } break;
      case 3: {
        acc_dswrite4<16, 8192>(xw + 65536);
        acc_dswrite4<20, 9216>(xw + 65536);
        acc_dswrite4<24, 10240>(xw + 65536);
        acc_dswrite4<28, 11264>(xw + 65536);
        acc_dswrite4<144, 12288>(xw + 65536);
        acc_dswrite4<148, 13312>(xw + 65536);
        acc_dswrite4<152, 14336>(xw + 65536);
        acc_dswrite4<156, 15360>(xw + 65536);
        acc_dswrite4<48, 16384>(xw + 65536);
        acc_dswrite4<52, 17408>(xw + 65536);
        acc_dswrite4<56, 18432>(xw + 65536);
        acc_dswrite4<60, 19456>(xw + 65536);
        acc_dswrite4<176, 20480>(xw + 65536);
        acc_dswrite4<180, 21504>(xw + 65536);
        acc_dswrite4<184, 22528>(xw + 65536);
        acc_dswrite4<188, 23552>(xw + 65536);
        acc_dswrite4<80, 24576>(xw + 65536);
        acc_dswrite4<84, 25600>(xw + 65536);
        acc_dswrite4<88, 26624>(xw + 65536);
        acc_dswrite4<92, 27648>(xw + 65536);
        acc_dswrite4<208, 28672>(xw + 65536);
        acc_dswrite4<212, 29696>(xw + 65536);
        acc_dswrite4<216, 30720>(xw + 65536);
        acc_dswrite4<220, 31744>(xw + 65536);
      } break;
    }
    WAITL0();
    __builtin_amdgcn_s_barrier();
    switch (w) {
      case 0: {
        { f32x4 sum = acc_read4<16>(); sum *= fw[0][0];
          sum += xr[1536] * fw[0][1];
          sum += xr[3072] * fw[0][2];
          sum += xr[4608] * fw[0][3];
          u32x2 v = {cvtpk(sum[0], sum[1]), cvtpk(sum[2], sum[3])};
          *(u32x2*)(otile + (0 + r) * 512 + (((4) ^ ((0 + r) & 31)) * 16) + 8 * h) = v; }
        { f32x4 sum = acc_read4<20>(); sum *= fw[0][0];
          sum += xr[1600] * fw[0][1];
          sum += xr[3136] * fw[0][2];
          sum += xr[4672] * fw[0][3];
          u32x2 v = {cvtpk(sum[0], sum[1]), cvtpk(sum[2], sum[3])};
          *(u32x2*)(otile + (0 + r) * 512 + (((5) ^ ((0 + r) & 31)) * 16) + 8 * h) = v; }
        { f32x4 sum = acc_read4<24>(); sum *= fw[0][0];
          sum += xr[1664] * fw[0][1];
          sum += xr[3200] * fw[0][2];
          sum += xr[4736] * fw[0][3];
          u32x2 v = {cvtpk(sum[0], sum[1]), cvtpk(sum[2], sum[3])};
          *(u32x2*)(otile + (0 + r) * 512 + (((6) ^ ((0 + r) & 31)) * 16) + 8 * h) = v; }
        { f32x4 sum = acc_read4<28>(); sum *= fw[0][0];
          sum += xr[1728] * fw[0][1];
          sum += xr[3264] * fw[0][2];
          sum += xr[4800] * fw[0][3];
          u32x2 v = {cvtpk(sum[0], sum[1]), cvtpk(sum[2], sum[3])};
          *(u32x2*)(otile + (0 + r) * 512 + (((7) ^ ((0 + r) & 31)) * 16) + 8 * h) = v; }
        { f32x4 sum = acc_read4<144>(); sum *= fw[1][0];
          sum += xr[1792] * fw[1][1];
          sum += xr[3328] * fw[1][2];
          sum += xr[4864] * fw[1][3];
          u32x2 v = {cvtpk(sum[0], sum[1]), cvtpk(sum[2], sum[3])};
          *(u32x2*)(otile + (32 + r) * 512 + (((4) ^ ((32 + r) & 31)) * 16) + 8 * h) = v; }
        { f32x4 sum = acc_read4<148>(); sum *= fw[1][0];
          sum += xr[1856] * fw[1][1];
          sum += xr[3392] * fw[1][2];
          sum += xr[4928] * fw[1][3];
          u32x2 v = {cvtpk(sum[0], sum[1]), cvtpk(sum[2], sum[3])};
          *(u32x2*)(otile + (32 + r) * 512 + (((5) ^ ((32 + r) & 31)) * 16) + 8 * h) = v; }
        { f32x4 sum = acc_read4<152>(); sum *= fw[1][0];
          sum += xr[1920] * fw[1][1];
          sum += xr[3456] * fw[1][2];
          sum += xr[4992] * fw[1][3];
          u32x2 v = {cvtpk(sum[0], sum[1]), cvtpk(sum[2], sum[3])};
          *(u32x2*)(otile + (32 + r) * 512 + (((6) ^ ((32 + r) & 31)) * 16) + 8 * h) = v; }
        { f32x4 sum = acc_read4<156>(); sum *= fw[1][0];
          sum += xr[1984] * fw[1][1];
          sum += xr[3520] * fw[1][2];
          sum += xr[5056] * fw[1][3];
          u32x2 v = {cvtpk(sum[0], sum[1]), cvtpk(sum[2], sum[3])};
          *(u32x2*)(otile + (32 + r) * 512 + (((7) ^ ((32 + r) & 31)) * 16) + 8 * h) = v; }
      } break;
      case 1: {
        { f32x4 sum = acc_read4<48>(); sum *= fw[0][1];
          sum += xr[0] * fw[0][0];
          sum += xr[3584] * fw[0][2];
          sum += xr[5120] * fw[0][3];
          u32x2 v = {cvtpk(sum[0], sum[1]), cvtpk(sum[2], sum[3])};
          *(u32x2*)(otile + (0 + r) * 512 + (((12) ^ ((0 + r) & 31)) * 16) + 8 * h) = v; }
        { f32x4 sum = acc_read4<52>(); sum *= fw[0][1];
          sum += xr[64] * fw[0][0];
          sum += xr[3648] * fw[0][2];
          sum += xr[5184] * fw[0][3];
          u32x2 v = {cvtpk(sum[0], sum[1]), cvtpk(sum[2], sum[3])};
          *(u32x2*)(otile + (0 + r) * 512 + (((13) ^ ((0 + r) & 31)) * 16) + 8 * h) = v; }
        { f32x4 sum = acc_read4<56>(); sum *= fw[0][1];
          sum += xr[128] * fw[0][0];
          sum += xr[3712] * fw[0][2];
          sum += xr[5248] * fw[0][3];
          u32x2 v = {cvtpk(sum[0], sum[1]), cvtpk(sum[2], sum[3])};
          *(u32x2*)(otile + (0 + r) * 512 + (((14) ^ ((0 + r) & 31)) * 16) + 8 * h) = v; }
        { f32x4 sum = acc_read4<60>(); sum *= fw[0][1];
          sum += xr[192] * fw[0][0];
          sum += xr[3776] * fw[0][2];
          sum += xr[5312] * fw[0][3];
          u32x2 v = {cvtpk(sum[0], sum[1]), cvtpk(sum[2], sum[3])};
          *(u32x2*)(otile + (0 + r) * 512 + (((15) ^ ((0 + r) & 31)) * 16) + 8 * h) = v; }
        { f32x4 sum = acc_read4<176>(); sum *= fw[1][1];
          sum += xr[256] * fw[1][0];
          sum += xr[3840] * fw[1][2];
          sum += xr[5376] * fw[1][3];
          u32x2 v = {cvtpk(sum[0], sum[1]), cvtpk(sum[2], sum[3])};
          *(u32x2*)(otile + (32 + r) * 512 + (((12) ^ ((32 + r) & 31)) * 16) + 8 * h) = v; }
        { f32x4 sum = acc_read4<180>(); sum *= fw[1][1];
          sum += xr[320] * fw[1][0];
          sum += xr[3904] * fw[1][2];
          sum += xr[5440] * fw[1][3];
          u32x2 v = {cvtpk(sum[0], sum[1]), cvtpk(sum[2], sum[3])};
          *(u32x2*)(otile + (32 + r) * 512 + (((13) ^ ((32 + r) & 31)) * 16) + 8 * h) = v; }
        { f32x4 sum = acc_read4<184>(); sum *= fw[1][1];
          sum += xr[384] * fw[1][0];
          sum += xr[3968] * fw[1][2];
          sum += xr[5504] * fw[1][3];
          u32x2 v = {cvtpk(sum[0], sum[1]), cvtpk(sum[2], sum[3])};
          *(u32x2*)(otile + (32 + r) * 512 + (((14) ^ ((32 + r) & 31)) * 16) + 8 * h) = v; }
        { f32x4 sum = acc_read4<188>(); sum *= fw[1][1];
          sum += xr[448] * fw[1][0];
          sum += xr[4032] * fw[1][2];
          sum += xr[5568] * fw[1][3];
          u32x2 v = {cvtpk(sum[0], sum[1]), cvtpk(sum[2], sum[3])};
          *(u32x2*)(otile + (32 + r) * 512 + (((15) ^ ((32 + r) & 31)) * 16) + 8 * h) = v; }
      } break;
      case 2: {
        { f32x4 sum = acc_read4<80>(); sum *= fw[0][2];
          sum += xr[512] * fw[0][0];
          sum += xr[2048] * fw[0][1];
          sum += xr[5632] * fw[0][3];
          u32x2 v = {cvtpk(sum[0], sum[1]), cvtpk(sum[2], sum[3])};
          *(u32x2*)(otile + (0 + r) * 512 + (((20) ^ ((0 + r) & 31)) * 16) + 8 * h) = v; }
        { f32x4 sum = acc_read4<84>(); sum *= fw[0][2];
          sum += xr[576] * fw[0][0];
          sum += xr[2112] * fw[0][1];
          sum += xr[5696] * fw[0][3];
          u32x2 v = {cvtpk(sum[0], sum[1]), cvtpk(sum[2], sum[3])};
          *(u32x2*)(otile + (0 + r) * 512 + (((21) ^ ((0 + r) & 31)) * 16) + 8 * h) = v; }
        { f32x4 sum = acc_read4<88>(); sum *= fw[0][2];
          sum += xr[640] * fw[0][0];
          sum += xr[2176] * fw[0][1];
          sum += xr[5760] * fw[0][3];
          u32x2 v = {cvtpk(sum[0], sum[1]), cvtpk(sum[2], sum[3])};
          *(u32x2*)(otile + (0 + r) * 512 + (((22) ^ ((0 + r) & 31)) * 16) + 8 * h) = v; }
        { f32x4 sum = acc_read4<92>(); sum *= fw[0][2];
          sum += xr[704] * fw[0][0];
          sum += xr[2240] * fw[0][1];
          sum += xr[5824] * fw[0][3];
          u32x2 v = {cvtpk(sum[0], sum[1]), cvtpk(sum[2], sum[3])};
          *(u32x2*)(otile + (0 + r) * 512 + (((23) ^ ((0 + r) & 31)) * 16) + 8 * h) = v; }
        { f32x4 sum = acc_read4<208>(); sum *= fw[1][2];
          sum += xr[768] * fw[1][0];
          sum += xr[2304] * fw[1][1];
          sum += xr[5888] * fw[1][3];
          u32x2 v = {cvtpk(sum[0], sum[1]), cvtpk(sum[2], sum[3])};
          *(u32x2*)(otile + (32 + r) * 512 + (((20) ^ ((32 + r) & 31)) * 16) + 8 * h) = v; }
        { f32x4 sum = acc_read4<212>(); sum *= fw[1][2];
          sum += xr[832] * fw[1][0];
          sum += xr[2368] * fw[1][1];
          sum += xr[5952] * fw[1][3];
          u32x2 v = {cvtpk(sum[0], sum[1]), cvtpk(sum[2], sum[3])};
          *(u32x2*)(otile + (32 + r) * 512 + (((21) ^ ((32 + r) & 31)) * 16) + 8 * h) = v; }
        { f32x4 sum = acc_read4<216>(); sum *= fw[1][2];
          sum += xr[896] * fw[1][0];
          sum += xr[2432] * fw[1][1];
          sum += xr[6016] * fw[1][3];
          u32x2 v = {cvtpk(sum[0], sum[1]), cvtpk(sum[2], sum[3])};
          *(u32x2*)(otile + (32 + r) * 512 + (((22) ^ ((32 + r) & 31)) * 16) + 8 * h) = v; }
        { f32x4 sum = acc_read4<220>(); sum *= fw[1][2];
          sum += xr[960] * fw[1][0];
          sum += xr[2496] * fw[1][1];
          sum += xr[6080] * fw[1][3];
          u32x2 v = {cvtpk(sum[0], sum[1]), cvtpk(sum[2], sum[3])};
          *(u32x2*)(otile + (32 + r) * 512 + (((23) ^ ((32 + r) & 31)) * 16) + 8 * h) = v; }
      } break;
      case 3: {
        { f32x4 sum = acc_read4<112>(); sum *= fw[0][3];
          sum += xr[1024] * fw[0][0];
          sum += xr[2560] * fw[0][1];
          sum += xr[4096] * fw[0][2];
          u32x2 v = {cvtpk(sum[0], sum[1]), cvtpk(sum[2], sum[3])};
          *(u32x2*)(otile + (0 + r) * 512 + (((28) ^ ((0 + r) & 31)) * 16) + 8 * h) = v; }
        { f32x4 sum = acc_read4<116>(); sum *= fw[0][3];
          sum += xr[1088] * fw[0][0];
          sum += xr[2624] * fw[0][1];
          sum += xr[4160] * fw[0][2];
          u32x2 v = {cvtpk(sum[0], sum[1]), cvtpk(sum[2], sum[3])};
          *(u32x2*)(otile + (0 + r) * 512 + (((29) ^ ((0 + r) & 31)) * 16) + 8 * h) = v; }
        { f32x4 sum = acc_read4<120>(); sum *= fw[0][3];
          sum += xr[1152] * fw[0][0];
          sum += xr[2688] * fw[0][1];
          sum += xr[4224] * fw[0][2];
          u32x2 v = {cvtpk(sum[0], sum[1]), cvtpk(sum[2], sum[3])};
          *(u32x2*)(otile + (0 + r) * 512 + (((30) ^ ((0 + r) & 31)) * 16) + 8 * h) = v; }
        { f32x4 sum = acc_read4<124>(); sum *= fw[0][3];
          sum += xr[1216] * fw[0][0];
          sum += xr[2752] * fw[0][1];
          sum += xr[4288] * fw[0][2];
          u32x2 v = {cvtpk(sum[0], sum[1]), cvtpk(sum[2], sum[3])};
          *(u32x2*)(otile + (0 + r) * 512 + (((31) ^ ((0 + r) & 31)) * 16) + 8 * h) = v; }
        { f32x4 sum = acc_read4<240>(); sum *= fw[1][3];
          sum += xr[1280] * fw[1][0];
          sum += xr[2816] * fw[1][1];
          sum += xr[4352] * fw[1][2];
          u32x2 v = {cvtpk(sum[0], sum[1]), cvtpk(sum[2], sum[3])};
          *(u32x2*)(otile + (32 + r) * 512 + (((28) ^ ((32 + r) & 31)) * 16) + 8 * h) = v; }
        { f32x4 sum = acc_read4<244>(); sum *= fw[1][3];
          sum += xr[1344] * fw[1][0];
          sum += xr[2880] * fw[1][1];
          sum += xr[4416] * fw[1][2];
          u32x2 v = {cvtpk(sum[0], sum[1]), cvtpk(sum[2], sum[3])};
          *(u32x2*)(otile + (32 + r) * 512 + (((29) ^ ((32 + r) & 31)) * 16) + 8 * h) = v; }
        { f32x4 sum = acc_read4<248>(); sum *= fw[1][3];
          sum += xr[1408] * fw[1][0];
          sum += xr[2944] * fw[1][1];
          sum += xr[4480] * fw[1][2];
          u32x2 v = {cvtpk(sum[0], sum[1]), cvtpk(sum[2], sum[3])};
          *(u32x2*)(otile + (32 + r) * 512 + (((30) ^ ((32 + r) & 31)) * 16) + 8 * h) = v; }
        { f32x4 sum = acc_read4<252>(); sum *= fw[1][3];
          sum += xr[1472] * fw[1][0];
          sum += xr[3008] * fw[1][1];
          sum += xr[4544] * fw[1][2];
          u32x2 v = {cvtpk(sum[0], sum[1]), cvtpk(sum[2], sum[3])};
          *(u32x2*)(otile + (32 + r) * 512 + (((31) ^ ((32 + r) & 31)) * 16) + 8 * h) = v; }
      } break;
    }
  }
  __syncthreads();
  asm volatile("s_waitcnt vmcnt(0)" :: "v"(c.pfd) : "memory");
  STAMP(15);
#pragma unroll
  for (int k = 0; k < 7; ++k) {
    const int R = 14 * w + 2 * k + h;
    u32x4 v = *(const u32x4*)(otile + R * 512 + ((r ^ (R & 31)) * 16));
    *(u32x4*)(o + ((size_t)(head * 8 + (r >> 2)) * NROW + row0 + R) * 32 + (r & 3) * 8) = v;
  }
  STAMP(10);
}

__global__ __launch_bounds__(512, 2) void oproj2_kernel(const u16* __restrict__ o, const u16* __restrict__ wo,
                                                        u16* __restrict__ out) {
  extern __shared__ __attribute__((aligned(16))) char lds[];
  const int tid = threadIdx.x, lane = tid & 63;
  const int w = __builtin_amdgcn_readfirstlane(tid >> 6);
  const int r = lane & 31, h = lane >> 5;
  const int xcd = blockIdx.x & 7, idx = blockIdx.x >> 3;
  const int rt = xcd + 8 * (idx / 7), ct = idx % 7;
  if (rt >= 28) return;
  const int col0 = ct * 64;
  char* ring = lds + w * 18432;
  __amdgpu_buffer_rsrc_t rw = __builtin_amdgcn_make_buffer_rsrc((void*)(wo + (size_t)(512 * w) * DM + col0), 0,
                                                                 (512 * DM - col0) * 2, 0x00020000);
  __amdgpu_buffer_rsrc_t ra = __builtin_amdgcn_make_buffer_rsrc((void*)(o + ((size_t)(16 * w) * NROW + rt * 32) * 32), 0,
                                                                 (16 * NROW - rt * 32) * 64, 0x00020000);
  const int drow = lane >> 3;
  const int wvoff = drow * (DM * 2) + (((lane & 7) ^ (((drow >> 1) & 1) << 2)) * 16);
  const int arow = lane >> 2;
  const int avoff = arow * 64 + (((lane & 3) ^ ((arow >> 2) & 3)) * 16);
  auto issue = [&](int u) {
    char* dst = ring + (u % 3) * 6144;
#pragma unroll
    for (int p = 0; p < 4; ++p)
      __builtin_amdgcn_raw_ptr_buffer_load_lds(rw, (lds_vp)(dst + p * 1024), 16, wvoff, (32 * u + 8 * p) * (DM * 2), 0, 0);
#pragma unroll
    for (int p = 0; p < 2; ++p)
      __builtin_amdgcn_raw_ptr_buffer_load_lds(ra, (lds_vp)(dst + 4096 + p * 1024), 16, avoff, 1024 * p + u * (NROW * 64), 0, 0);
  };
  issue(0); issue(1); issue(2);
  const int g = lane >> 4, qq = (lane >> 2) & 3, pp = lane & 3;
  const unsigned rb = (unsigned)(size_t)ring;
  const unsigned trb = rb + (8 * (g >> 1) + qq) * 128 + ((32 * (g & 1) + 8 * pp) ^ ((qq >> 1) << 6));
  const unsigned ab0 = rb + 4096 + r * 64 + (((0 + h) ^ ((r >> 2) & 3)) * 16);
  const unsigned ab1 = rb + 4096 + r * 64 + (((2 + h) ^ ((r >> 2) & 3)) * 16);
  f32x16 acc[2];
#pragma unroll
  for (int b2 = 0; b2 < 2; ++b2)
#pragma unroll
    for (int i = 0; i < 16; ++i) acc[b2][i] = 0.f;
#pragma unroll
  for (int u = 0; u < 16; ++u) {
    if (u <= 13) WAITV(12); else if (u == 14) WAITV(6); else WAITV(0);
    const unsigned so = (u % 3) * 6144;
    s16x4 a[2][4];
    bf16x8 xb[2];
#pragma unroll
    for (int s = 0; s < 2; ++s) {
      a[s][0] = trread(trb + so + s * 2048);
      a[s][1] = trread(trb + so + s * 2048 + 512);
      a[s][2] = trread((trb + so + s * 2048) ^ 64);
      a[s][3] = trread((trb + so + s * 2048 + 512) ^ 64);
    }
    asm volatile("ds_read_b128 %0, %1" : "=v"(xb[0]) : "v"(ab0 + so) : "memory");
    asm volatile("ds_read_b128 %0, %1" : "=v"(xb[1]) : "v"(ab1 + so) : "memory");
    asm volatile("s_waitcnt lgkmcnt(0)"
                 : "+v"(a[0][0]), "+v"(a[0][1]), "+v"(a[0][2]), "+v"(a[0][3]), "+v"(a[1][0]), "+v"(a[1][1]),
                   "+v"(a[1][2]), "+v"(a[1][3]), "+v"(xb[0]), "+v"(xb[1])::"memory");
    if (u + 3 < 16) issue(u + 3);
#pragma unroll
    for (int s = 0; s < 2; ++s) {
      acc[0] = __builtin_amdgcn_mfma_f32_32x32x16_bf16(cat8(a[s][0], a[s][1]), xb[s], acc[0], 0, 0, 0);
      acc[1] = __builtin_amdgcn_mfma_f32_32x32x16_bf16(cat8(a[s][2], a[s][3]), xb[s], acc[1], 0, 0, 0);
    }
  }
  __syncthreads();
  float* red = (float*)lds;
#pragma unroll
  for (int cb = 0; cb < 2; ++cb)
#pragma unroll
    for (int gg = 0; gg < 4; ++gg) {
      f32x4 v = {acc[cb][4 * gg], acc[cb][4 * gg + 1], acc[cb][4 * gg + 2], acc[cb][4 * gg + 3]};
      *(f32x4*)(red + ((w * 8 + cb * 4 + gg) * 64 + lane) * 4) = v;
    }
  __syncthreads();
  {
    const int cg = w, cb = cg >> 2, gg = cg & 3;
    f32x4 s = {0.f, 0.f, 0.f, 0.f};
#pragma unroll
    for (int ww = 0; ww < 8; ++ww) s += *(const f32x4*)(red + ((ww * 8 + cg) * 64 + lane) * 4);
    u32x2 v = {cvtpk(s[0], s[1]), cvtpk(s[2], s[3])};
    *(u32x2*)(out + (size_t)(rt * 32 + r) * DM + col0 + 32 * cb + 8 * gg + 4 * h) = v;
  }
}

extern "C" void kernel_launch(void* const* d_in, const int* in_sizes, int n_in, void* d_out, int out_size,
                              void* d_ws, size_t ws_size, hipStream_t stream) {
  const u16* x = (const u16*)d_in[0];
  const u16* kc = (const u16*)d_in[1];
  const u16* vc = (const u16*)d_in[2];
  const u16* wq = (const u16*)d_in[3];
  const u16* wo = (const u16*)d_in[4];
  u16* out = (u16*)d_out;
  u16* ows = (u16*)d_ws;
  unsigned* cnt = (unsigned*)((char*)d_ws + (8 << 20));
  (void)hipFuncSetAttribute((const void*)attn4_kernel, hipFuncAttributeMaxDynamicSharedMemorySize, 163840);
  (void)hipFuncSetAttribute((const void*)oproj2_kernel, hipFuncAttributeMaxDynamicSharedMemorySize, 147456);
  hipLaunchKernelGGL(attn4_kernel, dim3(256), dim3(256), 163840, stream, x, wq, kc, vc, wo, ows, cnt);
  hipLaunchKernelGGL(oproj2_kernel, dim3(8 * 28), dim3(512), 147456, stream, (const u16*)ows, wo, out);
}
```
